```python
import torch

def _softmax(x, axis=-1):
    return torch.softmax(x, dim=axis)
def _gelu(x):
    return torch.nn.functional.gelu(x, approximate="tanh")
def _silu(x):
    return torch.nn.functional.silu(x)
def _sigmoid(x):
    return torch.sigmoid(x)
def _layernorm(x, axis=-1, eps=1e-5):
    m = x.mean(dim=axis, keepdim=True)
    v = x.var(dim=axis, keepdim=True, unbiased=False)
    return (x - m) / torch.sqrt(v + eps)
def _scatter_add(tgt, idx, src, axis=0):
    return tgt.clone().index_add_(axis, idx, src)
def _segment_sum(src, seg, n):
    out = torch.zeros((n,) + src.shape[1:], dtype=src.dtype)
    return out.index_add_(0, seg, src)
def _pad(x, pw):
    flat = tuple(v for pair in reversed(pw) for v in pair)
    return torch.nn.functional.pad(x, flat)

kernel_name = 'recurrent_scan__linear_kernel_hedgehog__dense_activation_silu__prompt'

def setup_inputs():

    _w = (lambda a: (lambda t: t.to(torch.bfloat16) if t.is_floating_point() else t)(torch.as_tensor(a)))
    def _A_setup():
        B, S, D, H = 1, 2048, 576, 32
        Dh = D // H
        return {
            "x": _w((lambda *s: torch.randn(*s, dtype=torch.bfloat16) * 0.05)(B, S, D)),
            "wqkv": _w((lambda *s: torch.randn(*s, dtype=torch.bfloat16) * 0.4)(D, 3 * D)),
            "w_phi": _w((lambda *s: torch.randn(*s, dtype=torch.bfloat16) * 0.4)(H, Dh, Dh) * (Dh ** -0.5)),
            "wo": _w((lambda *s: torch.randn(*s, dtype=torch.bfloat16) * 0.2)(D, D)),
        }
    _A_in = _A_setup()

    def _B_setup():
        B, S, D, F = 1, 2048, 576, 4608
        return {
            "x": _w((lambda *s: torch.randn(*s, dtype=torch.bfloat16) * 0.05)(B, S, D)),
            "w1": _w((lambda *s: torch.randn(*s, dtype=torch.bfloat16) * 0.4)(D, F) * (D ** -0.5)),
            "w2": _w((lambda *s: torch.randn(*s, dtype=torch.bfloat16) * 0.1)(F, D) * (F ** -0.5)),
        }
    _B_in = _B_setup()

    return {
        "A_x": _A_in["x"],
        "A_wqkv": _A_in["wqkv"],
        "A_w_phi": _A_in["w_phi"],
        "A_wo": _A_in["wo"],
        "B_x": _B_in["x"],
        "B_w1": _B_in["w1"],
        "B_w2": _B_in["w2"],
    }

def reference(A_x, A_wqkv, A_w_phi, A_wo, B_x, B_w1, B_w2):
    def _A_ref(x, wqkv, w_phi, wo):
        B, S, D = x.shape
        H = 32; Dh = D // H
        qkv = (lambda x, *d: x.permute(*d))((x @ wqkv).reshape(B, S, 3, H, Dh), 2, 0, 3, 1, 4)
        q, k, v = qkv[0], qkv[1], qkv[2]
        pq = torch.einsum("bhsd,hdf->bhsf", q, w_phi)
        pk = torch.einsum("bhsd,hdf->bhsf", k, w_phi)
        phi_q = torch.concatenate([_softmax(pq, axis=-1), _softmax(-pq, axis=-1)], axis=-1)
        phi_k = torch.concatenate([_softmax(pk, axis=-1), _softmax(-pk, axis=-1)], axis=-1)
        kv = torch.einsum("bhsf,bhsd->bhfd", phi_k, v)
        z  = torch.einsum("bhsf->bhf", phi_k)
        num = torch.einsum("bhqf,bhfd->bhqd", phi_q, kv)
        den = torch.einsum("bhqf,bhf->bhq", phi_q, z)[..., None] + 1e-6
        o = num / den
        return (lambda x, *d: x.permute(*d))(o, 0, 2, 1, 3).reshape(B, S, D) @ wo

    def _B_ref(x, w1, w2):
        h = torch.einsum("bsd,df->bsf", x, w1)
        h = _silu(h)
        out = torch.einsum("bsf,fd->bsd", h, w2)
        return out

    x = A_x
    xn = _layernorm(x, axis=-1)
    s = _A_ref(xn, A_wqkv, A_w_phi, A_wo)
    g = _silu(_B_ref(xn, B_w1, B_w2))
    return x + s * g
```

```cpp
#include <hip/hip_runtime.h>
#define SRD(p, n) __builtin_amdgcn_make_buffer_rsrc((void*)(p), (short)0, (int)(n), 0x00020000)
#define CLOBBERS "v0","v1","v2","v3","v4","v5","v6","v7","v8","v9","v10","v11","v12","v13","v14","v15","v16","v17","v18","v19","v20","v21","v22","v23","v24","v25","v26","v27","v28","v29","v30","v31","v32","v33","v34","v35","v36","v37","v38","v39","v40","v41","v42","v43","v44","v45","v46","v47","v48","v49","v50","v51","v52","v53","v54","v55","v56","v57","v58","v59","v60","v61","v62","v63","v64","v65","v66","v67","v68","v69","v70","v71","v72","v73","v74","v75","v76","v77","v78","v79","v80","v81","v82","v83","v84","v85","v86","v87","v88","v89","v90","v91","v92","v93","v94","v95","v96","v97","v98","v99","v100","v101","v102","v103","v104","v105","v106","v107","v108","v109","v110","v111","v112","v113","v114","v115","v116","v117","v118","v119","v120","v121","v122","v123","v124","v125","v126","v127","v128","v129","v130","v131","v132","v133","v134","v135","v136","v137","v138","v139","v140","v141","v142","v143","v144","v145","v146","v147","v148","v149","v150","v151","v152","v153","v154","v155","v156","v157","v158","v159","v160","v161","v162","v163","v164","v165","v166","v167","v168","v169","v170","v171","v172","v173","v174","v175","v176","v177","v178","v179","v180","v181","v182","v183","v184","v185","v186","v187","v188","v189","v190","v191","v192","v193","v194","v195","v196","v197","v198","v199","v200","v201","v202","v203","v204","v205","v206","v207","v208","v209","v210","v211","v212","v213","v214","v215","v216","v217","v218","v219","v220","v221","v222","v223","v224","v225","v226","v227","v228","v229","v230","v231","v232","v233","v234","v235","v236","v237","v238","v239","v240","v241","v242","v243","v244","v245","v246","v247","v248","v249","v250","v251","v252","v253","v254","v255","a0","a1","a2","a3","a4","a5","a6","a7","a8","a9","a10","a11","a12","a13","a14","a15","a16","a17","a18","a19","a20","a21","a22","a23","a24","a25","a26","a27","a28","a29","a30","a31","a32","a33","a34","a35","a36","a37","a38","a39","a40","a41","a42","a43","a44","a45","a46","a47","a48","a49","a50","a51","a52","a53","a54","a55","a56","a57","a58","a59","a60","a61","a62","a63","a64","a65","a66","a67","a68","a69","a70","a71","a72","a73","a74","a75","a76","a77","a78","a79","a80","a81","a82","a83","a84","a85","a86","a87","a88","a89","a90","a91","a92","a93","a94","a95","a96","a97","a98","a99","a100","a101","a102","a103","a104","a105","a106","a107","a108","a109","a110","a111","a112","a113","a114","a115","a116","a117","a118","a119","a120","a121","a122","a123","a124","a125","a126","a127","a128","a129","a130","a131","a132","a133","a134","a135","a136","a137","a138","a139","a140","a141","a142","a143","a144","a145","a146","a147","a148","a149","a150","a151","a152","a153","a154","a155","a156","a157","a158","a159","a160","a161","a162","a163","a164","a165","a166","a167","a168","a169","a170","a171","a172","a173","a174","a175","a176","a177","a178","a179","a180","a181","a182","a183","a184","a185","a186","a187","a188","a189","a190","a191","a192","a193","a194","a195","a196","a197","a198","a199","a200","a201","a202","a203","a204","a205","a206","a207","a208","a209","a210","a211","a212","a213","a214","a215","a216","a217","a218","a219","a220","a221","a222","a223","a224","a225","a226","a227","a228","a229","a230","a231","a232","a233","a234","a235","a236","a237","a238","a239","a240","a241","a242","a243","a244","a245","a246","a247","a248","a249","a250","a251","a252","a253","a254","a255","s40","s41","s42","s43","s44","s45","s46","s47","s48","s49","s50","s51","s52","s53","s54","s55","s56","s57","s58","s59","s60","s61","s62","s63","s64","s65","s66","s67","s68","s69","s70","s71","s72","s73","s74","s75","s76","s77","s78","s79","s80","s81","s82","s83","s84","s85","s86","s87","s88","s89","s90","s91","s92","s93","s94","s95","s96","s97","s98","s99","vcc","scc","memory"

__global__ __launch_bounds__(256, 1) void fused_kernel(const char* __restrict__ x, const char* __restrict__ wqkv, const char* __restrict__ wphi, const char* __restrict__ wo,
    const char* __restrict__ w1, const char* __restrict__ w2, char* __restrict__ out, char* __restrict__ ws) {
  extern __shared__ __attribute__((aligned(16))) char lds[];
  asm volatile("s_getpc_b64 s[96:97]\n.Lfused_kernel_a:\n\ts_mov_b32 s98, .Lfused_kernel_b-.Lfused_kernel_a\n\ts_max_u32 s98, s98, .Lfused_kernel_c-.Lfused_kernel_a\n\tv_min_u32 v33, s98, %0\n\tglobal_load_dword v32, v33, s[96:97]"
    :: "v"(((threadIdx.x >> 2) + (blockIdx.x >> 3) % 13u * 64u) << 7) : "v32", "v33", "s96", "s97", "s98", "scc", "memory");
  const unsigned b = blockIdx.x, xcd = b & 7, r = b >> 3;
  const unsigned mi = (xcd >> 2) * 8 + (r >> 2), role = r & 3;
  const unsigned wv = __builtin_amdgcn_readfirstlane(threadIdx.x >> 6);
  const unsigned ldsb = (unsigned)(size_t)lds;
  const unsigned dbg = 49836032u + b * 256u; (void)dbg;
  const __amdgpu_buffer_rsrc_t rx = SRD(x, 2048 * 576 * 2), rw = SRD(ws, 49832448);
  if (role != 3) {
    const unsigned j = (xcd & 3) * 3 + role, nc0 = mi % 9, sc = (3u - (unsigned)((size_t)w1 >> 7) - 6u * j) & 7u;
    const unsigned rb0 = (mi >> 1) % 3, rb = (3 * (mi & 1) + rb0 == sc) ? (rb0 == 2 ? 0 : rb0 + 1) : rb0, slow = sc < 6 ? j * 768 + sc * 128 + 4096 : 0x7f000000u;
    const __amdgpu_buffer_rsrc_t r1 = SRD(w1 - 4096, 576 * 4608 * 2 + 4096), r2 = SRD(w2 - 4096, 576 * 4608 * 2 + 4096), rq = SRD(wqkv - 4096, 576 * 1728 * 2 + 4096), ro = SRD(wo, 576 * 576 * 2), rout = SRD(out, 2048 * 576 * 2);
    asm volatile(
"s_nop 4\n\t"
"v_mbcnt_lo_u32_b32 v245, -1, 0\n\t"
"s_lshl_b32 s40, %[mi], 2\n\t"
"v_mbcnt_hi_u32_b32 v245, -1, v245\n\t"
"s_add_u32 s40, s40, %[wv]\n\t"
"s_mul_i32 s40, s40, 36864\n\t"
"v_lshrrev_b32 v0, 3, v245\n\t"
"v_and_b32 v1, 7, v245\n\t"
"v_mul_u32_u24 v2, 1152, v0\n\t"
"v_lshl_add_u32 v2, v1, 4, v2\n\t"
"s_add_u32 s41, s40, 9216\n\t"
"s_add_u32 s42, s40, 18432\n\t"
"s_add_u32 s43, s40, 27648\n\t"
"buffer_load_dwordx4 v[32:35], v2, %[x], s40 offen offset:0\n\t"
"buffer_load_dwordx4 v[36:39], v2, %[x], s40 offen offset:128\n\t"
"buffer_load_dwordx4 v[40:43], v2, %[x], s40 offen offset:256\n\t"
"buffer_load_dwordx4 v[44:47], v2, %[x], s40 offen offset:384\n\t"
"buffer_load_dwordx4 v[48:51], v2, %[x], s40 offen offset:512\n\t"
"buffer_load_dwordx4 v[52:55], v2, %[x], s40 offen offset:640\n\t"
"buffer_load_dwordx4 v[56:59], v2, %[x], s40 offen offset:768\n\t"
"buffer_load_dwordx4 v[60:63], v2, %[x], s40 offen offset:896\n\t"
"buffer_load_dwordx4 v[64:67], v2, %[x], s40 offen offset:1024\n\t"
"s_and_b32 s72, %[mi], 1\n\t"
"s_mul_i32 s66, s72, 384\n\t"
"s_sub_u32 s67, 384, s66\n\t"
"s_mul_i32 s69, %[j], 768\n\t"
"s_add_u32 s64, %[rb], 0\n\t"
"s_sub_u32 s65, s64, 3\n\t"
"s_min_u32 s64, s64, s65\n\t"
"s_lshl_b32 s64, s64, 7\n\t"
"s_add_u32 s64, s64, s69\n\t"
"s_add_u32 s44, s64, s66\n\t"
"s_add_u32 s47, s64, s67\n\t"
"s_add_u32 s64, %[rb], 1\n\t"
"s_sub_u32 s65, s64, 3\n\t"
"s_min_u32 s64, s64, s65\n\t"
"buffer_load_dwordx4 v[68:71], v2, %[x], s41 offen offset:0\n\t"
"s_lshl_b32 s64, s64, 7\n\t"
"s_add_u32 s64, s64, s69\n\t"
"s_add_u32 s45, s64, s66\n\t"
"s_add_u32 s48, s64, s67\n\t"
"s_add_u32 s64, %[rb], 2\n\t"
"s_sub_u32 s65, s64, 3\n\t"
"s_min_u32 s64, s64, s65\n\t"
"s_lshl_b32 s64, s64, 7\n\t"
"s_add_u32 s64, s64, s69\n\t"
"s_add_u32 s46, s64, s66\n\t"
"s_add_u32 s49, s64, s67\n\t"
"s_mul_i32 s63, %[wv], 6144\n\t"
"s_add_u32 s63, s63, %[lds]\n\t"
"v_and_b32 v3, 3, v0\n\t"
"buffer_load_dwordx4 v[72:75], v2, %[x], s41 offen offset:128\n\t"
"v_lshrrev_b32 v4, 2, v0\n\t"
"v_lshl_add_u32 v3, v4, 3, v3\n\t"
"v_and_b32 v4, 6, v0\n\t"
"v_xor_b32 v4, v4, v1\n\t"
"v_lshlrev_b32 v4, 4, v4\n\t"
"s_movk_i32 s66, 9216\n\t"
"v_mad_u32_u24 v5, v3, s66, v4\n\t"
"s_mul_i32 s64, %[wv], 6\n\t"
"s_add_u32 s65, s64, 0\n\t"
"s_lshr_b32 s67, s65, 2\n\t"
"s_lshl_b32 s67, s67, 4\n\t"
"s_and_b32 s65, s65, 2\n\t"
"s_add_u32 s65, s65, s67\n\t"
"s_mul_i32 s65, s65, 18432\n\t"
"buffer_load_dwordx4 v[76:79], v2, %[x], s41 offen offset:256\n\t"
"s_add_u32 s65, s65, 4096\n\t"
"v_add_u32 v200, s65, v5\n\t"
"s_add_u32 s65, s64, 1\n\t"
"s_lshr_b32 s67, s65, 2\n\t"
"s_lshl_b32 s67, s67, 4\n\t"
"s_and_b32 s65, s65, 2\n\t"
"s_add_u32 s65, s65, s67\n\t"
"s_mul_i32 s65, s65, 18432\n\t"
"s_add_u32 s65, s65, 150528\n\t"
"v_add_u32 v201, s65, v5\n\t"
"s_add_u32 s65, s64, 2\n\t"
"s_lshr_b32 s67, s65, 2\n\t"
"s_lshl_b32 s67, s67, 4\n\t"
"s_and_b32 s65, s65, 2\n\t"
"buffer_load_dwordx4 v[80:83], v2, %[x], s41 offen offset:384\n\t"
"s_add_u32 s65, s65, s67\n\t"
"s_mul_i32 s65, s65, 18432\n\t"
"s_add_u32 s65, s65, 2048\n\t"
"v_add_u32 v202, s65, v5\n\t"
"s_add_u32 s65, s64, 3\n\t"
"s_lshr_b32 s67, s65, 2\n\t"
"s_lshl_b32 s67, s67, 4\n\t"
"s_and_b32 s65, s65, 2\n\t"
"s_add_u32 s65, s65, s67\n\t"
"s_mul_i32 s65, s65, 18432\n\t"
"s_add_u32 s65, s65, 148480\n\t"
"v_add_u32 v203, s65, v5\n\t"
"s_add_u32 s65, s64, 4\n\t"
"s_lshr_b32 s67, s65, 2\n\t"
"buffer_load_dwordx4 v[84:87], v2, %[x], s41 offen offset:512\n\t"
"s_lshl_b32 s67, s67, 4\n\t"
"s_and_b32 s65, s65, 2\n\t"
"s_add_u32 s65, s65, s67\n\t"
"s_mul_i32 s65, s65, 18432\n\t"
"s_add_u32 s65, s65, 4096\n\t"
"v_add_u32 v204, s65, v5\n\t"
"s_add_u32 s65, s64, 5\n\t"
"s_lshr_b32 s67, s65, 2\n\t"
"s_lshl_b32 s67, s67, 4\n\t"
"s_and_b32 s65, s65, 2\n\t"
"s_add_u32 s65, s65, s67\n\t"
"s_mul_i32 s65, s65, 18432\n\t"
"s_add_u32 s65, s65, 150528\n\t"
"v_add_u32 v205, s65, v5\n\t"
"buffer_load_dwordx4 v[88:91], v2, %[x], s41 offen offset:640\n\t"
"s_lshl_b32 s64, %[mi], 7\n\t"
"s_add_u32 s64, s64, 49829888\n\t"
"v_mov_b32 v9, 0\n\t"
"buffer_load_dwordx2 v[254:255], v9, %[ws], s64 offen sc1\n\t"
"v_xor_b32 v5, v0, v1\n\t"
"v_lshlrev_b32 v5, 4, v5\n\t"
"v_and_b32 v6, 3, v245\n\t"
"v_lshl_add_u32 v5, v6, 8, v5\n\t"
"v_and_b32 v6, 4, v245\n\t"
"v_lshl_add_u32 v5, v6, 10, v5\n\t"
"s_lshl_b32 s65, %[wv], 10\n\t"
"s_add_u32 s66, %[lds], 73728\n\t"
"s_add_u32 s65, s65, s66\n\t"
"v_add_u32 v176, s65, v5\n\t"
"buffer_load_dwordx4 v[92:95], v2, %[x], s41 offen offset:768\n\t"
"v_add_u32 v177, 32768, v176\n\t"
"v_lshrrev_b32 v5, 4, v245\n\t"
"s_xor_b32 s67, %[wv], 1\n\t"
"s_lshl_b32 s67, s67, 10\n\t"
"s_add_u32 s67, s67, s66\n\t"
"v_add_u32 v6, 0, v5\n\t"
"v_xor_b32 v6, v6, v245\n\t"
"v_lshlrev_b32 v6, 4, v6\n\t"
"v_add_u32 v246, s65, v6\n\t"
"v_add_u32 v250, 32768, v246\n\t"
"v_add_u32 v247, s67, v6\n\t"
"v_add_u32 v251, 32768, v247\n\t"
"v_add_u32 v6, 4, v5\n\t"
"v_xor_b32 v6, v6, v245\n\t"
"buffer_load_dwordx4 v[96:99], v2, %[x], s41 offen offset:896\n\t"
"v_lshlrev_b32 v6, 4, v6\n\t"
"v_add_u32 v248, s65, v6\n\t"
"v_add_u32 v252, 32768, v248\n\t"
"v_add_u32 v249, s67, v6\n\t"
"v_add_u32 v253, 32768, v249\n\t"
"s_mov_b32 s70, 0x3f803f80\n\t"
"s_mov_b32 s71, 0xffff0000\n\t"
"v_lshrrev_b32 v5, 2, v245\n\t"
"v_bfe_u32 v6, v245, 3, 2\n\t"
"v_and_b32 v7, 3, v245\n\t"
"v_lshlrev_b32 v7, 3, v7\n\t"
"v_lshl_add_u32 v7, v5, 7, v7\n\t"
"v_add_u32 v7, %[lds], v7\n\t"
"s_and_b32 s64, %[wv], 1\n\t"
"buffer_load_dwordx4 v[100:103], v2, %[x], s41 offen offset:1024\n\t"
"s_lshl_b32 s65, s64, 1\n\t"
"s_add_u32 s66, s65, 0\n\t"
"v_xor_b32 v8, s66, v6\n\t"
"v_lshl_add_u32 v212, v8, 5, v7\n\t"
"v_add_u32 v213, 49152, v212\n\t"
"v_add_u32 v214, 98304, v212\n\t"
"s_add_u32 s66, s65, 1\n\t"
"v_xor_b32 v8, s66, v6\n\t"
"v_lshl_add_u32 v215, v8, 5, v7\n\t"
"v_add_u32 v216, 49152, v215\n\t"
"v_add_u32 v217, 98304, v215\n\t"
"s_lshl_b32 s65, s64, 12\n\t"
"s_sub_u32 s66, 4096, s65\n\t"
"v_xor_b32 v8, 0, v6\n\t"
"buffer_load_dwordx4 v[104:107], v2, %[x], s42 offen offset:0\n\t"
"v_lshl_add_u32 v8, v8, 5, v7\n\t"
"v_add_u32 v218, s65, v8\n\t"
"v_add_u32 v219, 49152, v218\n\t"
"v_add_u32 v220, 98304, v218\n\t"
"v_add_u32 v230, s66, v8\n\t"
"v_add_u32 v231, 49152, v230\n\t"
"v_add_u32 v232, 98304, v230\n\t"
"v_xor_b32 v8, 1, v6\n\t"
"v_lshl_add_u32 v8, v8, 5, v7\n\t"
"v_add_u32 v221, s65, v8\n\t"
"v_add_u32 v222, 49152, v221\n\t"
"v_add_u32 v223, 98304, v221\n\t"
"v_add_u32 v233, s66, v8\n\t"
"v_add_u32 v234, 49152, v233\n\t"
"buffer_load_dwordx4 v[108:111], v2, %[x], s42 offen offset:128\n\t"
"v_add_u32 v235, 98304, v233\n\t"
"v_xor_b32 v8, 2, v6\n\t"
"v_lshl_add_u32 v8, v8, 5, v7\n\t"
"v_add_u32 v224, s65, v8\n\t"
"v_add_u32 v225, 49152, v224\n\t"
"v_add_u32 v226, 98304, v224\n\t"
"v_add_u32 v236, s66, v8\n\t"
"v_add_u32 v237, 49152, v236\n\t"
"v_add_u32 v238, 98304, v236\n\t"
"v_xor_b32 v8, 3, v6\n\t"
"v_lshl_add_u32 v8, v8, 5, v7\n\t"
"v_add_u32 v227, s65, v8\n\t"
"v_add_u32 v228, 49152, v227\n\t"
"v_add_u32 v229, 98304, v227\n\t"
"buffer_load_dwordx4 v[112:115], v2, %[x], s42 offen offset:256\n\t"
"v_add_u32 v239, s66, v8\n\t"
"v_add_u32 v240, 49152, v239\n\t"
"v_add_u32 v241, 98304, v239\n\t"
"v_lshlrev_b32 v244, 4, v245\n\t"
"s_add_u32 s66, %[lds], 147456\n\t"
"s_lshl_b32 s65, %[wv], 11\n\t"
"s_add_u32 s65, s65, s66\n\t"
"v_add_u32 v242, s65, v244\n\t"
"s_xor_b32 s65, %[wv], 1\n\t"
"s_lshl_b32 s65, s65, 11\n\t"
"s_add_u32 s65, s65, s66\n\t"
"v_add_u32 v243, s65, v244\n\t"
"s_mul_i32 s64, s72, 221184\n\t"
"s_mul_i32 s65, %[j], 442368\n\t"
"buffer_load_dwordx4 v[116:119], v2, %[x], s42 offen offset:384\n\t"
"s_add_u32 s50, s65, s64\n\t"
"s_sub_u32 s64, 221184, s64\n\t"
"s_add_u32 s51, s65, s64\n\t"
"s_movk_i32 s66, 1152\n\t"
"v_mad_u32_u24 v5, v0, s66, v4\n\t"
"s_mul_i32 s64, %[wv], 6\n\t"
"s_lshl_b32 s66, %[rb], 3\n\t"
"s_add_u32 s64, s64, s66\n\t"
"s_add_u32 s65, s64, 0\n\t"
"s_sub_u32 s67, s65, 24\n\t"
"s_min_u32 s65, s65, s67\n\t"
"s_mul_i32 s65, s65, 9216\n\t"
"s_add_u32 s65, s65, 4096\n\t"
"v_add_u32 v206, s65, v5\n\t"
"buffer_load_dwordx4 v[120:123], v2, %[x], s42 offen offset:512\n\t"
"s_add_u32 s65, s64, 1\n\t"
"s_sub_u32 s67, s65, 24\n\t"
"s_min_u32 s65, s65, s67\n\t"
"s_mul_i32 s65, s65, 9216\n\t"
"s_add_u32 s65, s65, 3072\n\t"
"v_add_u32 v207, s65, v5\n\t"
"s_add_u32 s65, s64, 2\n\t"
"s_sub_u32 s67, s65, 24\n\t"
"s_min_u32 s65, s65, s67\n\t"
"s_mul_i32 s65, s65, 9216\n\t"
"s_add_u32 s65, s65, 2048\n\t"
"v_add_u32 v208, s65, v5\n\t"
"s_add_u32 s65, s64, 3\n\t"
"s_sub_u32 s67, s65, 24\n\t"
"buffer_load_dwordx4 v[124:127], v2, %[x], s42 offen offset:640\n\t"
"s_min_u32 s65, s65, s67\n\t"
"s_mul_i32 s65, s65, 9216\n\t"
"s_add_u32 s65, s65, 1024\n\t"
"v_add_u32 v209, s65, v5\n\t"
"s_add_u32 s65, s64, 4\n\t"
"s_sub_u32 s67, s65, 24\n\t"
"s_min_u32 s65, s65, s67\n\t"
"s_mul_i32 s65, s65, 9216\n\t"
"s_add_u32 s65, s65, 4096\n\t"
"v_add_u32 v210, s65, v5\n\t"
"s_add_u32 s65, s64, 5\n\t"
"s_sub_u32 s67, s65, 24\n\t"
"s_min_u32 s65, s65, s67\n\t"
"s_mul_i32 s65, s65, 9216\n\t"
"buffer_load_dwordx4 v[128:131], v2, %[x], s42 offen offset:768\n\t"
"s_add_u32 s65, s65, 3072\n\t"
"v_add_u32 v211, s65, v5\n\t"
"buffer_load_dwordx4 v[132:135], v2, %[x], s42 offen offset:896\n\t"
"buffer_load_dwordx4 v[136:139], v2, %[x], s42 offen offset:1024\n\t"
"buffer_load_dwordx4 v[140:143], v2, %[x], s43 offen offset:0\n\t"
"buffer_load_dwordx4 v[144:147], v2, %[x], s43 offen offset:128\n\t"
"v_mov_b32 v188, 0\n\t"
"v_mov_b32 v189, 0\n\t"
"v_mov_b32 v190, 0\n\t"
"v_mov_b32 v191, 0\n\t"
"s_waitcnt vmcnt(29)\n\t"
"v_dot2c_f32_bf16 v188, s70, v32\n\t"
"v_dot2c_f32_bf16 v189, v32, v32\n\t"
"v_dot2c_f32_bf16 v190, s70, v33\n\t"
"v_dot2c_f32_bf16 v191, v33, v33\n\t"
"v_dot2c_f32_bf16 v188, s70, v34\n\t"
"v_dot2c_f32_bf16 v189, v34, v34\n\t"
"v_dot2c_f32_bf16 v190, s70, v35\n\t"
"v_dot2c_f32_bf16 v191, v35, v35\n\t"
"s_waitcnt vmcnt(28)\n\t"
"v_dot2c_f32_bf16 v188, s70, v36\n\t"
"v_dot2c_f32_bf16 v189, v36, v36\n\t"
"v_dot2c_f32_bf16 v190, s70, v37\n\t"
"v_dot2c_f32_bf16 v191, v37, v37\n\t"
"v_dot2c_f32_bf16 v188, s70, v38\n\t"
"v_dot2c_f32_bf16 v189, v38, v38\n\t"
"buffer_load_dwordx4 v[148:151], v2, %[x], s43 offen offset:256\n\t"
"v_dot2c_f32_bf16 v190, s70, v39\n\t"
"v_dot2c_f32_bf16 v191, v39, v39\n\t"
"s_waitcnt vmcnt(28)\n\t"
"v_dot2c_f32_bf16 v188, s70, v40\n\t"
"v_dot2c_f32_bf16 v189, v40, v40\n\t"
"v_dot2c_f32_bf16 v190, s70, v41\n\t"
"v_dot2c_f32_bf16 v191, v41, v41\n\t"
"v_dot2c_f32_bf16 v188, s70, v42\n\t"
"v_dot2c_f32_bf16 v189, v42, v42\n\t"
"v_dot2c_f32_bf16 v190, s70, v43\n\t"
"v_dot2c_f32_bf16 v191, v43, v43\n\t"
"s_waitcnt vmcnt(27)\n\t"
"v_dot2c_f32_bf16 v188, s70, v44\n\t"
"v_dot2c_f32_bf16 v189, v44, v44\n\t"
"v_dot2c_f32_bf16 v190, s70, v45\n\t"
"v_dot2c_f32_bf16 v191, v45, v45\n\t"
"v_dot2c_f32_bf16 v188, s70, v46\n\t"
"v_dot2c_f32_bf16 v189, v46, v46\n\t"
"v_dot2c_f32_bf16 v190, s70, v47\n\t"
"v_dot2c_f32_bf16 v191, v47, v47\n\t"
"buffer_load_dwordx4 v[152:155], v2, %[x], s43 offen offset:384\n\t"
"s_waitcnt vmcnt(27)\n\t"
"v_dot2c_f32_bf16 v188, s70, v48\n\t"
"v_dot2c_f32_bf16 v189, v48, v48\n\t"
"v_dot2c_f32_bf16 v190, s70, v49\n\t"
"v_dot2c_f32_bf16 v191, v49, v49\n\t"
"v_dot2c_f32_bf16 v188, s70, v50\n\t"
"v_dot2c_f32_bf16 v189, v50, v50\n\t"
"v_dot2c_f32_bf16 v190, s70, v51\n\t"
"v_dot2c_f32_bf16 v191, v51, v51\n\t"
"s_waitcnt vmcnt(26)\n\t"
"v_dot2c_f32_bf16 v188, s70, v52\n\t"
"v_dot2c_f32_bf16 v189, v52, v52\n\t"
"v_dot2c_f32_bf16 v190, s70, v53\n\t"
"v_dot2c_f32_bf16 v191, v53, v53\n\t"
"v_dot2c_f32_bf16 v188, s70, v54\n\t"
"v_dot2c_f32_bf16 v189, v54, v54\n\t"
"v_dot2c_f32_bf16 v190, s70, v55\n\t"
"v_dot2c_f32_bf16 v191, v55, v55\n\t"
"s_waitcnt vmcnt(25)\n\t"
"v_dot2c_f32_bf16 v188, s70, v56\n\t"
"v_dot2c_f32_bf16 v189, v56, v56\n\t"
"buffer_load_dwordx4 v[156:159], v2, %[x], s43 offen offset:512\n\t"
"v_dot2c_f32_bf16 v190, s70, v57\n\t"
"v_dot2c_f32_bf16 v191, v57, v57\n\t"
"v_dot2c_f32_bf16 v188, s70, v58\n\t"
"v_dot2c_f32_bf16 v189, v58, v58\n\t"
"v_dot2c_f32_bf16 v190, s70, v59\n\t"
"v_dot2c_f32_bf16 v191, v59, v59\n\t"
"s_waitcnt vmcnt(25)\n\t"
"v_dot2c_f32_bf16 v188, s70, v60\n\t"
"v_dot2c_f32_bf16 v189, v60, v60\n\t"
"v_dot2c_f32_bf16 v190, s70, v61\n\t"
"v_dot2c_f32_bf16 v191, v61, v61\n\t"
"v_dot2c_f32_bf16 v188, s70, v62\n\t"
"v_dot2c_f32_bf16 v189, v62, v62\n\t"
"v_dot2c_f32_bf16 v190, s70, v63\n\t"
"v_dot2c_f32_bf16 v191, v63, v63\n\t"
"s_waitcnt vmcnt(24)\n\t"
"v_dot2c_f32_bf16 v188, s70, v64\n\t"
"v_dot2c_f32_bf16 v189, v64, v64\n\t"
"v_dot2c_f32_bf16 v190, s70, v65\n\t"
"v_dot2c_f32_bf16 v191, v65, v65\n\t"
"buffer_load_dwordx4 v[160:163], v2, %[x], s43 offen offset:640\n\t"
"v_dot2c_f32_bf16 v188, s70, v66\n\t"
"v_dot2c_f32_bf16 v189, v66, v66\n\t"
"v_dot2c_f32_bf16 v190, s70, v67\n\t"
"v_dot2c_f32_bf16 v191, v67, v67\n\t"
"s_nop 1\n\t"
"v_add_f32 v188, v188, v190\n\t"
"v_add_f32 v189, v189, v191\n\t"
"s_nop 0\n\t"
"v_add_f32_dpp v188, v188, v188 quad_perm:[1,0,3,2] row_mask:0xf bank_mask:0xf\n\t"
"v_add_f32_dpp v189, v189, v189 quad_perm:[1,0,3,2] row_mask:0xf bank_mask:0xf\n\t"
"s_nop 0\n\t"
"v_add_f32_dpp v188, v188, v188 quad_perm:[2,3,0,1] row_mask:0xf bank_mask:0xf\n\t"
"v_add_f32_dpp v189, v189, v189 quad_perm:[2,3,0,1] row_mask:0xf bank_mask:0xf\n\t"
"s_nop 0\n\t"
"v_add_f32_dpp v188, v188, v188 row_half_mirror row_mask:0xf bank_mask:0xf\n\t"
"v_add_f32_dpp v189, v189, v189 row_half_mirror row_mask:0xf bank_mask:0xf\n\t"
"v_mul_f32 v188, 0x3ae38e39, v188\n\t"
"v_mul_f32 v189, 0x3ae38e39, v189\n\t"
"v_fma_f32 v189, -v188, v188, v189\n\t"
"v_max_f32 v189, 0, v189\n\t"
"v_cvt_pk_bf16_f32 v188, v188, v189\n\t"
"v_and_b32 v189, s71, v188\n\t"
"buffer_load_dwordx4 v[164:167], v2, %[x], s43 offen offset:768\n\t"
"v_lshlrev_b32 v188, 16, v188\n\t"
"v_add_f32 v189, 0x3727c5ac, v189\n\t"
"v_cvt_pk_bf16_f32 v189, v189, v189\n\t"
"v_lshlrev_b32 v189, 16, v189\n\t"
"v_sqrt_f32 v189, v189\n\t"
"s_nop 0\n\t"
"v_cvt_pk_bf16_f32 v189, v189, v189\n\t"
"v_lshlrev_b32 v189, 16, v189\n\t"
"v_rcp_f32 v189, v189\n\t"
"s_nop 0\n\t"
"v_mul_f32 v192, 0xbfb8aa3b, v189\n\t"
"v_mul_f32 v193, -v188, v192\n\t"
"v_lshlrev_b32 v180, 16, v32\n\t"
"v_and_b32 v181, s71, v32\n\t"
"v_lshlrev_b32 v182, 16, v33\n\t"
"v_and_b32 v183, s71, v33\n\t"
"v_lshlrev_b32 v184, 16, v34\n\t"
"v_and_b32 v185, s71, v34\n\t"
"v_lshlrev_b32 v186, 16, v35\n\t"
"v_and_b32 v187, s71, v35\n\t"
"buffer_load_dwordx4 v[168:171], v2, %[x], s43 offen offset:896\n\t"
"v_pk_fma_f32 v[180:181], v[180:181], v[192:193], v[192:193] op_sel:[0,0,1] op_sel_hi:[1,0,1]\n\t"
"v_pk_fma_f32 v[182:183], v[182:183], v[192:193], v[192:193] op_sel:[0,0,1] op_sel_hi:[1,0,1]\n\t"
"v_pk_fma_f32 v[184:185], v[184:185], v[192:193], v[192:193] op_sel:[0,0,1] op_sel_hi:[1,0,1]\n\t"
"v_pk_fma_f32 v[186:187], v[186:187], v[192:193], v[192:193] op_sel:[0,0,1] op_sel_hi:[1,0,1]\n\t"
"v_cvt_pk_bf16_f32 v32, v180, v181\n\t"
"v_cvt_pk_bf16_f32 v33, v182, v183\n\t"
"v_cvt_pk_bf16_f32 v34, v184, v185\n\t"
"v_cvt_pk_bf16_f32 v35, v186, v187\n\t"
"ds_write_b128 v176, v[32:35] offset:0\n\t"
"v_lshlrev_b32 v180, 16, v36\n\t"
"v_and_b32 v181, s71, v36\n\t"
"v_lshlrev_b32 v182, 16, v37\n\t"
"v_and_b32 v183, s71, v37\n\t"
"v_lshlrev_b32 v184, 16, v38\n\t"
"v_and_b32 v185, s71, v38\n\t"
"v_lshlrev_b32 v186, 16, v39\n\t"
"v_and_b32 v187, s71, v39\n\t"
"v_pk_fma_f32 v[180:181], v[180:181], v[192:193], v[192:193] op_sel:[0,0,1] op_sel_hi:[1,0,1]\n\t"
"buffer_load_dwordx4 v[172:175], v2, %[x], s43 offen offset:1024\n\t"
"v_pk_fma_f32 v[182:183], v[182:183], v[192:193], v[192:193] op_sel:[0,0,1] op_sel_hi:[1,0,1]\n\t"
"v_pk_fma_f32 v[184:185], v[184:185], v[192:193], v[192:193] op_sel:[0,0,1] op_sel_hi:[1,0,1]\n\t"
"v_pk_fma_f32 v[186:187], v[186:187], v[192:193], v[192:193] op_sel:[0,0,1] op_sel_hi:[1,0,1]\n\t"
"v_cvt_pk_bf16_f32 v36, v180, v181\n\t"
"v_cvt_pk_bf16_f32 v37, v182, v183\n\t"
"v_cvt_pk_bf16_f32 v38, v184, v185\n\t"
"v_cvt_pk_bf16_f32 v39, v186, v187\n\t"
"ds_write_b128 v176, v[36:39] offset:8192\n\t"
"v_lshlrev_b32 v180, 16, v40\n\t"
"v_and_b32 v181, s71, v40\n\t"
"v_lshlrev_b32 v182, 16, v41\n\t"
"v_and_b32 v183, s71, v41\n\t"
"v_lshlrev_b32 v184, 16, v42\n\t"
"v_and_b32 v185, s71, v42\n\t"
"v_lshlrev_b32 v186, 16, v43\n\t"
"v_and_b32 v187, s71, v43\n\t"
"v_pk_fma_f32 v[180:181], v[180:181], v[192:193], v[192:193] op_sel:[0,0,1] op_sel_hi:[1,0,1]\n\t"
"v_pk_fma_f32 v[182:183], v[182:183], v[192:193], v[192:193] op_sel:[0,0,1] op_sel_hi:[1,0,1]\n\t"
"s_add_u32 m0, s63, 0\n\t"
"s_nop 0\n\t"
"buffer_load_dwordx4 v200, %[w1], s44 offen offset:0 lds\n\t"
"v_pk_fma_f32 v[184:185], v[184:185], v[192:193], v[192:193] op_sel:[0,0,1] op_sel_hi:[1,0,1]\n\t"
"v_pk_fma_f32 v[186:187], v[186:187], v[192:193], v[192:193] op_sel:[0,0,1] op_sel_hi:[1,0,1]\n\t"
"v_cvt_pk_bf16_f32 v40, v180, v181\n\t"
"v_cvt_pk_bf16_f32 v41, v182, v183\n\t"
"v_cvt_pk_bf16_f32 v42, v184, v185\n\t"
"v_cvt_pk_bf16_f32 v43, v186, v187\n\t"
"ds_write_b128 v176, v[40:43] offset:16384\n\t"
"v_lshlrev_b32 v180, 16, v44\n\t"
"v_and_b32 v181, s71, v44\n\t"
"v_lshlrev_b32 v182, 16, v45\n\t"
"v_and_b32 v183, s71, v45\n\t"
"v_lshlrev_b32 v184, 16, v46\n\t"
"v_and_b32 v185, s71, v46\n\t"
"v_lshlrev_b32 v186, 16, v47\n\t"
"v_and_b32 v187, s71, v47\n\t"
"v_pk_fma_f32 v[180:181], v[180:181], v[192:193], v[192:193] op_sel:[0,0,1] op_sel_hi:[1,0,1]\n\t"
"v_pk_fma_f32 v[182:183], v[182:183], v[192:193], v[192:193] op_sel:[0,0,1] op_sel_hi:[1,0,1]\n\t"
"v_pk_fma_f32 v[184:185], v[184:185], v[192:193], v[192:193] op_sel:[0,0,1] op_sel_hi:[1,0,1]\n\t"
"buffer_load_dwordx4 v201, %[w1], s44 offen offset:1024 lds\n\t"
"v_pk_fma_f32 v[186:187], v[186:187], v[192:193], v[192:193] op_sel:[0,0,1] op_sel_hi:[1,0,1]\n\t"
"v_cvt_pk_bf16_f32 v44, v180, v181\n\t"
"v_cvt_pk_bf16_f32 v45, v182, v183\n\t"
"v_cvt_pk_bf16_f32 v46, v184, v185\n\t"
"v_cvt_pk_bf16_f32 v47, v186, v187\n\t"
"ds_write_b128 v176, v[44:47] offset:24576\n\t"
"v_lshlrev_b32 v180, 16, v48\n\t"
"v_and_b32 v181, s71, v48\n\t"
"v_lshlrev_b32 v182, 16, v49\n\t"
"v_and_b32 v183, s71, v49\n\t"
"v_lshlrev_b32 v184, 16, v50\n\t"
"v_and_b32 v185, s71, v50\n\t"
"v_lshlrev_b32 v186, 16, v51\n\t"
"v_and_b32 v187, s71, v51\n\t"
"v_mov_b32 v188, 0\n\t"
"v_mov_b32 v189, 0\n\t"
"v_mov_b32 v190, 0\n\t"
"v_mov_b32 v191, 0\n\t"
"buffer_load_dwordx4 v202, %[w1], s44 offen offset:2048 lds\n\t"
"s_waitcnt vmcnt(30)\n\t"
"v_dot2c_f32_bf16 v188, s70, v68\n\t"
"v_dot2c_f32_bf16 v189, v68, v68\n\t"
"v_dot2c_f32_bf16 v190, s70, v69\n\t"
"v_dot2c_f32_bf16 v191, v69, v69\n\t"
"v_dot2c_f32_bf16 v188, s70, v70\n\t"
"v_dot2c_f32_bf16 v189, v70, v70\n\t"
"v_dot2c_f32_bf16 v190, s70, v71\n\t"
"v_dot2c_f32_bf16 v191, v71, v71\n\t"
"s_waitcnt vmcnt(29)\n\t"
"v_dot2c_f32_bf16 v188, s70, v72\n\t"
"v_dot2c_f32_bf16 v189, v72, v72\n\t"
"v_dot2c_f32_bf16 v190, s70, v73\n\t"
"v_dot2c_f32_bf16 v191, v73, v73\n\t"
"v_dot2c_f32_bf16 v188, s70, v74\n\t"
"v_dot2c_f32_bf16 v189, v74, v74\n\t"
"v_dot2c_f32_bf16 v190, s70, v75\n\t"
"v_dot2c_f32_bf16 v191, v75, v75\n\t"
"s_waitcnt vmcnt(28)\n\t"
"v_dot2c_f32_bf16 v188, s70, v76\n\t"
"v_dot2c_f32_bf16 v189, v76, v76\n\t"
"buffer_load_dwordx4 v203, %[w1], s44 offen offset:3072 lds\n\t"
"v_dot2c_f32_bf16 v190, s70, v77\n\t"
"v_dot2c_f32_bf16 v191, v77, v77\n\t"
"v_dot2c_f32_bf16 v188, s70, v78\n\t"
"v_dot2c_f32_bf16 v189, v78, v78\n\t"
"v_dot2c_f32_bf16 v190, s70, v79\n\t"
"v_dot2c_f32_bf16 v191, v79, v79\n\t"
"s_waitcnt vmcnt(28)\n\t"
"v_dot2c_f32_bf16 v188, s70, v80\n\t"
"v_dot2c_f32_bf16 v189, v80, v80\n\t"
"v_dot2c_f32_bf16 v190, s70, v81\n\t"
"v_dot2c_f32_bf16 v191, v81, v81\n\t"
"v_dot2c_f32_bf16 v188, s70, v82\n\t"
"v_dot2c_f32_bf16 v189, v82, v82\n\t"
"v_dot2c_f32_bf16 v190, s70, v83\n\t"
"v_dot2c_f32_bf16 v191, v83, v83\n\t"
"s_waitcnt vmcnt(27)\n\t"
"v_dot2c_f32_bf16 v188, s70, v84\n\t"
"v_dot2c_f32_bf16 v189, v84, v84\n\t"
"v_dot2c_f32_bf16 v190, s70, v85\n\t"
"v_dot2c_f32_bf16 v191, v85, v85\n\t"
"s_add_u32 m0, s63, 4096\n\t"
"s_nop 0\n\t"
"buffer_load_dwordx4 v204, %[w1], s44 offen offset:0 lds\n\t"
"v_dot2c_f32_bf16 v188, s70, v86\n\t"
"v_dot2c_f32_bf16 v189, v86, v86\n\t"
"v_dot2c_f32_bf16 v190, s70, v87\n\t"
"v_dot2c_f32_bf16 v191, v87, v87\n\t"
"s_waitcnt vmcnt(27)\n\t"
"v_dot2c_f32_bf16 v188, s70, v88\n\t"
"v_dot2c_f32_bf16 v189, v88, v88\n\t"
"v_dot2c_f32_bf16 v190, s70, v89\n\t"
"v_dot2c_f32_bf16 v191, v89, v89\n\t"
"v_dot2c_f32_bf16 v188, s70, v90\n\t"
"v_dot2c_f32_bf16 v189, v90, v90\n\t"
"v_dot2c_f32_bf16 v190, s70, v91\n\t"
"v_dot2c_f32_bf16 v191, v91, v91\n\t"
"s_waitcnt vmcnt(25)\n\t"
"v_dot2c_f32_bf16 v188, s70, v92\n\t"
"v_dot2c_f32_bf16 v189, v92, v92\n\t"
"v_dot2c_f32_bf16 v190, s70, v93\n\t"
"v_dot2c_f32_bf16 v191, v93, v93\n\t"
"v_dot2c_f32_bf16 v188, s70, v94\n\t"
"v_dot2c_f32_bf16 v189, v94, v94\n\t"
"buffer_load_dwordx4 v205, %[w1], s44 offen offset:1024 lds\n\t"
"v_dot2c_f32_bf16 v190, s70, v95\n\t"
"v_dot2c_f32_bf16 v191, v95, v95\n\t"
"s_waitcnt vmcnt(25)\n\t"
"v_dot2c_f32_bf16 v188, s70, v96\n\t"
"v_dot2c_f32_bf16 v189, v96, v96\n\t"
"v_dot2c_f32_bf16 v190, s70, v97\n\t"
"v_dot2c_f32_bf16 v191, v97, v97\n\t"
"v_dot2c_f32_bf16 v188, s70, v98\n\t"
"v_dot2c_f32_bf16 v189, v98, v98\n\t"
"v_dot2c_f32_bf16 v190, s70, v99\n\t"
"v_dot2c_f32_bf16 v191, v99, v99\n\t"
"s_waitcnt vmcnt(24)\n\t"
"v_dot2c_f32_bf16 v188, s70, v100\n\t"
"v_dot2c_f32_bf16 v189, v100, v100\n\t"
"v_dot2c_f32_bf16 v190, s70, v101\n\t"
"v_dot2c_f32_bf16 v191, v101, v101\n\t"
"v_dot2c_f32_bf16 v188, s70, v102\n\t"
"v_dot2c_f32_bf16 v189, v102, v102\n\t"
"v_dot2c_f32_bf16 v190, s70, v103\n\t"
"v_dot2c_f32_bf16 v191, v103, v103\n\t"
"s_add_u32 s68, s44, 1769472\n\t"
"s_add_u32 m0, s63, 24576\n\t"
"s_nop 0\n\t"
"buffer_load_dwordx4 v200, %[w1], s68 offen offset:0 lds\n\t"
"v_pk_fma_f32 v[180:181], v[180:181], v[192:193], v[192:193] op_sel:[0,0,1] op_sel_hi:[1,0,1]\n\t"
"v_add_f32 v188, v188, v190\n\t"
"v_pk_fma_f32 v[182:183], v[182:183], v[192:193], v[192:193] op_sel:[0,0,1] op_sel_hi:[1,0,1]\n\t"
"v_pk_fma_f32 v[184:185], v[184:185], v[192:193], v[192:193] op_sel:[0,0,1] op_sel_hi:[1,0,1]\n\t"
"v_pk_fma_f32 v[186:187], v[186:187], v[192:193], v[192:193] op_sel:[0,0,1] op_sel_hi:[1,0,1]\n\t"
"v_add_f32 v189, v189, v191\n\t"
"v_cvt_pk_bf16_f32 v48, v180, v181\n\t"
"v_cvt_pk_bf16_f32 v49, v182, v183\n\t"
"v_cvt_pk_bf16_f32 v50, v184, v185\n\t"
"v_add_f32_dpp v188, v188, v188 quad_perm:[1,0,3,2] row_mask:0xf bank_mask:0xf\n\t"
"v_cvt_pk_bf16_f32 v51, v186, v187\n\t"
"ds_write_b128 v177, v[48:51] offset:0\n\t"
"v_lshlrev_b32 v180, 16, v52\n\t"
"v_add_f32_dpp v189, v189, v189 quad_perm:[1,0,3,2] row_mask:0xf bank_mask:0xf\n\t"
"v_and_b32 v181, s71, v52\n\t"
"v_lshlrev_b32 v182, 16, v53\n\t"
"v_and_b32 v183, s71, v53\n\t"
"v_add_f32_dpp v188, v188, v188 quad_perm:[2,3,0,1] row_mask:0xf bank_mask:0xf\n\t"
"buffer_load_dwordx4 v201, %[w1], s68 offen offset:1024 lds\n\t"
"v_lshlrev_b32 v184, 16, v54\n\t"
"v_and_b32 v185, s71, v54\n\t"
"v_lshlrev_b32 v186, 16, v55\n\t"
"v_and_b32 v187, s71, v55\n\t"
"v_add_f32_dpp v189, v189, v189 quad_perm:[2,3,0,1] row_mask:0xf bank_mask:0xf\n\t"
"v_pk_fma_f32 v[180:181], v[180:181], v[192:193], v[192:193] op_sel:[0,0,1] op_sel_hi:[1,0,1]\n\t"
"v_pk_fma_f32 v[182:183], v[182:183], v[192:193], v[192:193] op_sel:[0,0,1] op_sel_hi:[1,0,1]\n\t"
"v_pk_fma_f32 v[184:185], v[184:185], v[192:193], v[192:193] op_sel:[0,0,1] op_sel_hi:[1,0,1]\n\t"
"v_add_f32_dpp v188, v188, v188 row_half_mirror row_mask:0xf bank_mask:0xf\n\t"
"v_pk_fma_f32 v[186:187], v[186:187], v[192:193], v[192:193] op_sel:[0,0,1] op_sel_hi:[1,0,1]\n\t"
"v_cvt_pk_bf16_f32 v52, v180, v181\n\t"
"v_cvt_pk_bf16_f32 v53, v182, v183\n\t"
"v_add_f32_dpp v189, v189, v189 row_half_mirror row_mask:0xf bank_mask:0xf\n\t"
"v_cvt_pk_bf16_f32 v54, v184, v185\n\t"
"v_cvt_pk_bf16_f32 v55, v186, v187\n\t"
"ds_write_b128 v177, v[52:55] offset:8192\n\t"
"v_mul_f32 v188, 0x3ae38e39, v188\n\t"
"v_lshlrev_b32 v180, 16, v56\n\t"
"buffer_load_dwordx4 v202, %[w1], s68 offen offset:2048 lds\n\t"
"v_and_b32 v181, s71, v56\n\t"
"v_lshlrev_b32 v182, 16, v57\n\t"
"v_mul_f32 v189, 0x3ae38e39, v189\n\t"
"v_and_b32 v183, s71, v57\n\t"
"v_lshlrev_b32 v184, 16, v58\n\t"
"v_and_b32 v185, s71, v58\n\t"
"v_lshlrev_b32 v186, 16, v59\n\t"
"v_fma_f32 v189, -v188, v188, v189\n\t"
"v_and_b32 v187, s71, v59\n\t"
"v_pk_fma_f32 v[180:181], v[180:181], v[192:193], v[192:193] op_sel:[0,0,1] op_sel_hi:[1,0,1]\n\t"
"v_pk_fma_f32 v[182:183], v[182:183], v[192:193], v[192:193] op_sel:[0,0,1] op_sel_hi:[1,0,1]\n\t"
"v_max_f32 v189, 0, v189\n\t"
"v_pk_fma_f32 v[184:185], v[184:185], v[192:193], v[192:193] op_sel:[0,0,1] op_sel_hi:[1,0,1]\n\t"
"v_pk_fma_f32 v[186:187], v[186:187], v[192:193], v[192:193] op_sel:[0,0,1] op_sel_hi:[1,0,1]\n\t"
"v_cvt_pk_bf16_f32 v56, v180, v181\n\t"
"v_cvt_pk_bf16_f32 v188, v188, v189\n\t"
"v_cvt_pk_bf16_f32 v57, v182, v183\n\t"
"v_cvt_pk_bf16_f32 v58, v184, v185\n\t"
"buffer_load_dwordx4 v203, %[w1], s68 offen offset:3072 lds\n\t"
"v_cvt_pk_bf16_f32 v59, v186, v187\n\t"
"v_and_b32 v189, s71, v188\n\t"
"ds_write_b128 v177, v[56:59] offset:16384\n\t"
"v_lshlrev_b32 v180, 16, v60\n\t"
"v_and_b32 v181, s71, v60\n\t"
"v_lshlrev_b32 v188, 16, v188\n\t"
"v_lshlrev_b32 v182, 16, v61\n\t"
"v_and_b32 v183, s71, v61\n\t"
"v_lshlrev_b32 v184, 16, v62\n\t"
"v_and_b32 v185, s71, v62\n\t"
"v_add_f32 v189, 0x3727c5ac, v189\n\t"
"v_lshlrev_b32 v186, 16, v63\n\t"
"v_and_b32 v187, s71, v63\n\t"
"v_pk_fma_f32 v[180:181], v[180:181], v[192:193], v[192:193] op_sel:[0,0,1] op_sel_hi:[1,0,1]\n\t"
"v_cvt_pk_bf16_f32 v189, v189, v189\n\t"
"v_pk_fma_f32 v[182:183], v[182:183], v[192:193], v[192:193] op_sel:[0,0,1] op_sel_hi:[1,0,1]\n\t"
"v_pk_fma_f32 v[184:185], v[184:185], v[192:193], v[192:193] op_sel:[0,0,1] op_sel_hi:[1,0,1]\n\t"
"v_pk_fma_f32 v[186:187], v[186:187], v[192:193], v[192:193] op_sel:[0,0,1] op_sel_hi:[1,0,1]\n\t"
"s_add_u32 m0, s63, 28672\n\t"
"s_nop 0\n\t"
"buffer_load_dwordx4 v204, %[w1], s68 offen offset:0 lds\n\t"
"v_lshlrev_b32 v189, 16, v189\n\t"
"v_cvt_pk_bf16_f32 v60, v180, v181\n\t"
"v_cvt_pk_bf16_f32 v61, v182, v183\n\t"
"v_cvt_pk_bf16_f32 v62, v184, v185\n\t"
"v_sqrt_f32 v189, v189\n\t"
"v_cvt_pk_bf16_f32 v63, v186, v187\n\t"
"ds_write_b128 v177, v[60:63] offset:24576\n\t"
"v_lshlrev_b32 v180, 16, v64\n\t"
"v_cvt_pk_bf16_f32 v189, v189, v189\n\t"
"v_and_b32 v181, s71, v64\n\t"
"v_lshlrev_b32 v182, 16, v65\n\t"
"v_and_b32 v183, s71, v65\n\t"
"v_lshlrev_b32 v184, 16, v66\n\t"
"v_lshlrev_b32 v189, 16, v189\n\t"
"v_and_b32 v185, s71, v66\n\t"
"v_lshlrev_b32 v186, 16, v67\n\t"
"v_and_b32 v187, s71, v67\n\t"
"v_rcp_f32 v189, v189\n\t"
"buffer_load_dwordx4 v205, %[w1], s68 offen offset:1024 lds\n\t"
"v_pk_fma_f32 v[180:181], v[180:181], v[192:193], v[192:193] op_sel:[0,0,1] op_sel_hi:[1,0,1]\n\t"
"v_pk_fma_f32 v[182:183], v[182:183], v[192:193], v[192:193] op_sel:[0,0,1] op_sel_hi:[1,0,1]\n\t"
"v_pk_fma_f32 v[184:185], v[184:185], v[192:193], v[192:193] op_sel:[0,0,1] op_sel_hi:[1,0,1]\n\t"
"v_mul_f32 v194, 0xbfb8aa3b, v189\n\t"
"v_pk_fma_f32 v[186:187], v[186:187], v[192:193], v[192:193] op_sel:[0,0,1] op_sel_hi:[1,0,1]\n\t"
"v_cvt_pk_bf16_f32 v64, v180, v181\n\t"
"v_cvt_pk_bf16_f32 v65, v182, v183\n\t"
"v_mul_f32 v195, -v188, v194\n\t"
"v_cvt_pk_bf16_f32 v66, v184, v185\n\t"
"v_cvt_pk_bf16_f32 v67, v186, v187\n\t"
"ds_write_b128 v177, v[64:67] offset:32768\n\t"
"v_lshlrev_b32 v180, 16, v68\n\t"
"v_and_b32 v181, s71, v68\n\t"
"v_lshlrev_b32 v182, 16, v69\n\t"
"v_and_b32 v183, s71, v69\n\t"
"v_lshlrev_b32 v184, 16, v70\n\t"
"v_and_b32 v185, s71, v70\n\t"
"v_lshlrev_b32 v186, 16, v71\n\t"
"s_add_u32 s68, s44, 3538944\n\t"
"s_add_u32 m0, s63, 49152\n\t"
"s_nop 0\n\t"
"buffer_load_dwordx4 v200, %[w1], s68 offen offset:0 lds\n\t"
"v_and_b32 v187, s71, v71\n\t"
"v_pk_fma_f32 v[180:181], v[180:181], v[194:195], v[194:195] op_sel:[0,0,1] op_sel_hi:[1,0,1]\n\t"
"v_pk_fma_f32 v[182:183], v[182:183], v[194:195], v[194:195] op_sel:[0,0,1] op_sel_hi:[1,0,1]\n\t"
"v_pk_fma_f32 v[184:185], v[184:185], v[194:195], v[194:195] op_sel:[0,0,1] op_sel_hi:[1,0,1]\n\t"
"v_pk_fma_f32 v[186:187], v[186:187], v[194:195], v[194:195] op_sel:[0,0,1] op_sel_hi:[1,0,1]\n\t"
"v_cvt_pk_bf16_f32 v68, v180, v181\n\t"
"v_cvt_pk_bf16_f32 v69, v182, v183\n\t"
"v_cvt_pk_bf16_f32 v70, v184, v185\n\t"
"v_cvt_pk_bf16_f32 v71, v186, v187\n\t"
"ds_write_b128 v176, v[68:71] offset:128\n\t"
"v_lshlrev_b32 v180, 16, v72\n\t"
"v_and_b32 v181, s71, v72\n\t"
"v_lshlrev_b32 v182, 16, v73\n\t"
"v_and_b32 v183, s71, v73\n\t"
"v_lshlrev_b32 v184, 16, v74\n\t"
"v_and_b32 v185, s71, v74\n\t"
"v_lshlrev_b32 v186, 16, v75\n\t"
"v_and_b32 v187, s71, v75\n\t"
"buffer_load_dwordx4 v201, %[w1], s68 offen offset:1024 lds\n\t"
"v_pk_fma_f32 v[180:181], v[180:181], v[194:195], v[194:195] op_sel:[0,0,1] op_sel_hi:[1,0,1]\n\t"
"v_pk_fma_f32 v[182:183], v[182:183], v[194:195], v[194:195] op_sel:[0,0,1] op_sel_hi:[1,0,1]\n\t"
"v_pk_fma_f32 v[184:185], v[184:185], v[194:195], v[194:195] op_sel:[0,0,1] op_sel_hi:[1,0,1]\n\t"
"v_pk_fma_f32 v[186:187], v[186:187], v[194:195], v[194:195] op_sel:[0,0,1] op_sel_hi:[1,0,1]\n\t"
"v_cvt_pk_bf16_f32 v72, v180, v181\n\t"
"v_cvt_pk_bf16_f32 v73, v182, v183\n\t"
"v_cvt_pk_bf16_f32 v74, v184, v185\n\t"
"v_cvt_pk_bf16_f32 v75, v186, v187\n\t"
"ds_write_b128 v176, v[72:75] offset:8320\n\t"
"v_lshlrev_b32 v180, 16, v76\n\t"
"v_and_b32 v181, s71, v76\n\t"
"v_lshlrev_b32 v182, 16, v77\n\t"
"v_and_b32 v183, s71, v77\n\t"
"v_lshlrev_b32 v184, 16, v78\n\t"
"v_and_b32 v185, s71, v78\n\t"
"v_lshlrev_b32 v186, 16, v79\n\t"
"v_and_b32 v187, s71, v79\n\t"
"v_pk_fma_f32 v[180:181], v[180:181], v[194:195], v[194:195] op_sel:[0,0,1] op_sel_hi:[1,0,1]\n\t"
"buffer_load_dwordx4 v202, %[w1], s68 offen offset:2048 lds\n\t"
"v_pk_fma_f32 v[182:183], v[182:183], v[194:195], v[194:195] op_sel:[0,0,1] op_sel_hi:[1,0,1]\n\t"
"v_pk_fma_f32 v[184:185], v[184:185], v[194:195], v[194:195] op_sel:[0,0,1] op_sel_hi:[1,0,1]\n\t"
"v_pk_fma_f32 v[186:187], v[186:187], v[194:195], v[194:195] op_sel:[0,0,1] op_sel_hi:[1,0,1]\n\t"
"v_cvt_pk_bf16_f32 v76, v180, v181\n\t"
"v_cvt_pk_bf16_f32 v77, v182, v183\n\t"
"v_cvt_pk_bf16_f32 v78, v184, v185\n\t"
"v_cvt_pk_bf16_f32 v79, v186, v187\n\t"
"ds_write_b128 v176, v[76:79] offset:16512\n\t"
"v_lshlrev_b32 v180, 16, v80\n\t"
"v_and_b32 v181, s71, v80\n\t"
"v_lshlrev_b32 v182, 16, v81\n\t"
"v_and_b32 v183, s71, v81\n\t"
"v_lshlrev_b32 v184, 16, v82\n\t"
"v_and_b32 v185, s71, v82\n\t"
"v_lshlrev_b32 v186, 16, v83\n\t"
"v_and_b32 v187, s71, v83\n\t"
"v_pk_fma_f32 v[180:181], v[180:181], v[194:195], v[194:195] op_sel:[0,0,1] op_sel_hi:[1,0,1]\n\t"
"v_pk_fma_f32 v[182:183], v[182:183], v[194:195], v[194:195] op_sel:[0,0,1] op_sel_hi:[1,0,1]\n\t"
"buffer_load_dwordx4 v203, %[w1], s68 offen offset:3072 lds\n\t"
"v_pk_fma_f32 v[184:185], v[184:185], v[194:195], v[194:195] op_sel:[0,0,1] op_sel_hi:[1,0,1]\n\t"
"v_pk_fma_f32 v[186:187], v[186:187], v[194:195], v[194:195] op_sel:[0,0,1] op_sel_hi:[1,0,1]\n\t"
"v_cvt_pk_bf16_f32 v80, v180, v181\n\t"
"v_cvt_pk_bf16_f32 v81, v182, v183\n\t"
"v_cvt_pk_bf16_f32 v82, v184, v185\n\t"
"v_cvt_pk_bf16_f32 v83, v186, v187\n\t"
"ds_write_b128 v176, v[80:83] offset:24704\n\t"
"v_lshlrev_b32 v180, 16, v84\n\t"
"v_and_b32 v181, s71, v84\n\t"
"v_lshlrev_b32 v182, 16, v85\n\t"
"v_and_b32 v183, s71, v85\n\t"
"v_lshlrev_b32 v184, 16, v86\n\t"
"v_and_b32 v185, s71, v86\n\t"
"v_lshlrev_b32 v186, 16, v87\n\t"
"v_and_b32 v187, s71, v87\n\t"
"v_mov_b32 v188, 0\n\t"
"v_mov_b32 v189, 0\n\t"
"v_mov_b32 v190, 0\n\t"
"s_add_u32 m0, s63, 53248\n\t"
"s_nop 0\n\t"
"buffer_load_dwordx4 v204, %[w1], s68 offen offset:0 lds\n\t"
"v_mov_b32 v191, 0\n\t"
"s_waitcnt vmcnt(34)\n\t"
"v_dot2c_f32_bf16 v188, s70, v104\n\t"
"v_dot2c_f32_bf16 v189, v104, v104\n\t"
"v_dot2c_f32_bf16 v190, s70, v105\n\t"
"v_dot2c_f32_bf16 v191, v105, v105\n\t"
"v_dot2c_f32_bf16 v188, s70, v106\n\t"
"v_dot2c_f32_bf16 v189, v106, v106\n\t"
"v_dot2c_f32_bf16 v190, s70, v107\n\t"
"v_dot2c_f32_bf16 v191, v107, v107\n\t"
"s_waitcnt vmcnt(33)\n\t"
"v_dot2c_f32_bf16 v188, s70, v108\n\t"
"v_dot2c_f32_bf16 v189, v108, v108\n\t"
"v_dot2c_f32_bf16 v190, s70, v109\n\t"
"v_dot2c_f32_bf16 v191, v109, v109\n\t"
"v_dot2c_f32_bf16 v188, s70, v110\n\t"
"v_dot2c_f32_bf16 v189, v110, v110\n\t"
"v_dot2c_f32_bf16 v190, s70, v111\n\t"
"v_dot2c_f32_bf16 v191, v111, v111\n\t"
"s_waitcnt vmcnt(32)\n\t"
"v_dot2c_f32_bf16 v188, s70, v112\n\t"
"buffer_load_dwordx4 v205, %[w1], s68 offen offset:1024 lds\n\t"
"v_dot2c_f32_bf16 v189, v112, v112\n\t"
"v_dot2c_f32_bf16 v190, s70, v113\n\t"
"v_dot2c_f32_bf16 v191, v113, v113\n\t"
"v_dot2c_f32_bf16 v188, s70, v114\n\t"
"v_dot2c_f32_bf16 v189, v114, v114\n\t"
"v_dot2c_f32_bf16 v190, s70, v115\n\t"
"v_dot2c_f32_bf16 v191, v115, v115\n\t"
"s_waitcnt vmcnt(32)\n\t"
"v_dot2c_f32_bf16 v188, s70, v116\n\t"
"v_dot2c_f32_bf16 v189, v116, v116\n\t"
"v_dot2c_f32_bf16 v190, s70, v117\n\t"
"v_dot2c_f32_bf16 v191, v117, v117\n\t"
"v_dot2c_f32_bf16 v188, s70, v118\n\t"
"v_dot2c_f32_bf16 v189, v118, v118\n\t"
"v_dot2c_f32_bf16 v190, s70, v119\n\t"
"v_dot2c_f32_bf16 v191, v119, v119\n\t"
"s_waitcnt vmcnt(31)\n\t"
"v_dot2c_f32_bf16 v188, s70, v120\n\t"
"v_dot2c_f32_bf16 v189, v120, v120\n\t"
"v_dot2c_f32_bf16 v190, s70, v121\n\t"
"s_and_b32 s73, %[mi], 7\n\t"
"s_lshl_b32 s73, s73, 2\n\t"
"s_add_u32 s73, s73, %[wv]\n\t"
"s_mul_i32 s73, s73, 165888\n\t"
"s_add_u32 s73, s73, %[slow]\n\t"
"v_min_u32 v12, 17, v245\n\t"
"v_mul_u32_u24 v12, 9216, v12\n\t"
"buffer_load_dword v64, v12, %[w1], s73 offen\n\t"
"v_dot2c_f32_bf16 v191, v121, v121\n\t"
"v_dot2c_f32_bf16 v188, s70, v122\n\t"
"v_dot2c_f32_bf16 v189, v122, v122\n\t"
"v_dot2c_f32_bf16 v190, s70, v123\n\t"
"v_dot2c_f32_bf16 v191, v123, v123\n\t"
"s_waitcnt vmcnt(31)\n\t"
"v_dot2c_f32_bf16 v188, s70, v124\n\t"
"v_dot2c_f32_bf16 v189, v124, v124\n\t"
"v_dot2c_f32_bf16 v190, s70, v125\n\t"
"v_dot2c_f32_bf16 v191, v125, v125\n\t"
"v_dot2c_f32_bf16 v188, s70, v126\n\t"
"v_dot2c_f32_bf16 v189, v126, v126\n\t"
"v_dot2c_f32_bf16 v190, s70, v127\n\t"
"v_dot2c_f32_bf16 v191, v127, v127\n\t"
"s_waitcnt vmcnt(30)\n\t"
"v_dot2c_f32_bf16 v188, s70, v128\n\t"
"v_dot2c_f32_bf16 v189, v128, v128\n\t"
"v_dot2c_f32_bf16 v190, s70, v129\n\t"
"v_dot2c_f32_bf16 v191, v129, v129\n\t"
"v_dot2c_f32_bf16 v188, s70, v130\n\t"
"s_mul_i32 s73, %[mi], 48\n\t"
"s_lshl_b32 s74, %[j], 2\n\t"
"s_add_u32 s73, s73, s74\n\t"
"s_add_u32 s73, s73, %[wv]\n\t"
"s_mul_i32 s73, s73, 3072\n\t"
"v_min_u32 v14, 23, v245\n\t"
"v_lshlrev_b32 v14, 7, v14\n\t"
"buffer_load_dword v64, v14, %[out], s73 offen sc1\n\t"
"v_dot2c_f32_bf16 v189, v130, v130\n\t"
"v_dot2c_f32_bf16 v190, s70, v131\n\t"
"v_dot2c_f32_bf16 v191, v131, v131\n\t"
"s_waitcnt vmcnt(30)\n\t"
"v_dot2c_f32_bf16 v188, s70, v132\n\t"
"v_dot2c_f32_bf16 v189, v132, v132\n\t"
"v_dot2c_f32_bf16 v190, s70, v133\n\t"
"v_dot2c_f32_bf16 v191, v133, v133\n\t"
"v_dot2c_f32_bf16 v188, s70, v134\n\t"
"v_dot2c_f32_bf16 v189, v134, v134\n\t"
"v_dot2c_f32_bf16 v190, s70, v135\n\t"
"v_dot2c_f32_bf16 v191, v135, v135\n\t"
"s_waitcnt vmcnt(29)\n\t"
"v_dot2c_f32_bf16 v188, s70, v136\n\t"
"v_dot2c_f32_bf16 v189, v136, v136\n\t"
"v_dot2c_f32_bf16 v190, s70, v137\n\t"
"v_dot2c_f32_bf16 v191, v137, v137\n\t"
"v_dot2c_f32_bf16 v188, s70, v138\n\t"
"v_dot2c_f32_bf16 v189, v138, v138\n\t"
"v_dot2c_f32_bf16 v190, s70, v139\n\t"
"s_lshl_b32 s73, %[mi], 11\n\t"
"s_add_u32 s73, s73, 49758208\n\t"
"v_min_u32 v14, 1536, v14\n\t"
"buffer_load_dword v64, v14, %[ws], s73 offen sc1\n\t"
"v_dot2c_f32_bf16 v191, v139, v139\n\t"
"v_pk_fma_f32 v[180:181], v[180:181], v[194:195], v[194:195] op_sel:[0,0,1] op_sel_hi:[1,0,1]\n\t"
"v_add_f32 v188, v188, v190\n\t"
"v_pk_fma_f32 v[182:183], v[182:183], v[194:195], v[194:195] op_sel:[0,0,1] op_sel_hi:[1,0,1]\n\t"
"v_pk_fma_f32 v[184:185], v[184:185], v[194:195], v[194:195] op_sel:[0,0,1] op_sel_hi:[1,0,1]\n\t"
"v_pk_fma_f32 v[186:187], v[186:187], v[194:195], v[194:195] op_sel:[0,0,1] op_sel_hi:[1,0,1]\n\t"
"v_add_f32 v189, v189, v191\n\t"
"v_cvt_pk_bf16_f32 v84, v180, v181\n\t"
"v_cvt_pk_bf16_f32 v85, v182, v183\n\t"
"v_cvt_pk_bf16_f32 v86, v184, v185\n\t"
"v_add_f32_dpp v188, v188, v188 quad_perm:[1,0,3,2] row_mask:0xf bank_mask:0xf\n\t"
"v_cvt_pk_bf16_f32 v87, v186, v187\n\t"
"ds_write_b128 v177, v[84:87] offset:128\n\t"
"v_lshlrev_b32 v180, 16, v88\n\t"
"v_add_f32_dpp v189, v189, v189 quad_perm:[1,0,3,2] row_mask:0xf bank_mask:0xf\n\t"
"v_and_b32 v181, s71, v88\n\t"
"v_lshlrev_b32 v182, 16, v89\n\t"
"v_and_b32 v183, s71, v89\n\t"
"v_add_f32_dpp v188, v188, v188 quad_perm:[2,3,0,1] row_mask:0xf bank_mask:0xf\n\t"
"v_lshlrev_b32 v184, 16, v90\n\t"
"v_and_b32 v185, s71, v90\n\t"
"v_lshlrev_b32 v186, 16, v91\n\t"
"v_and_b32 v187, s71, v91\n\t"
"v_add_f32_dpp v189, v189, v189 quad_perm:[2,3,0,1] row_mask:0xf bank_mask:0xf\n\t"
"v_pk_fma_f32 v[180:181], v[180:181], v[194:195], v[194:195] op_sel:[0,0,1] op_sel_hi:[1,0,1]\n\t"
"v_pk_fma_f32 v[182:183], v[182:183], v[194:195], v[194:195] op_sel:[0,0,1] op_sel_hi:[1,0,1]\n\t"
"v_pk_fma_f32 v[184:185], v[184:185], v[194:195], v[194:195] op_sel:[0,0,1] op_sel_hi:[1,0,1]\n\t"
"v_add_f32_dpp v188, v188, v188 row_half_mirror row_mask:0xf bank_mask:0xf\n\t"
"v_pk_fma_f32 v[186:187], v[186:187], v[194:195], v[194:195] op_sel:[0,0,1] op_sel_hi:[1,0,1]\n\t"
"v_cvt_pk_bf16_f32 v88, v180, v181\n\t"
"v_cvt_pk_bf16_f32 v89, v182, v183\n\t"
"v_add_f32_dpp v189, v189, v189 row_half_mirror row_mask:0xf bank_mask:0xf\n\t"
"v_cvt_pk_bf16_f32 v90, v184, v185\n\t"
"v_cvt_pk_bf16_f32 v91, v186, v187\n\t"
"ds_write_b128 v177, v[88:91] offset:8320\n\t"
"v_mul_f32 v188, 0x3ae38e39, v188\n\t"
"v_lshlrev_b32 v180, 16, v92\n\t"
"v_and_b32 v181, s71, v92\n\t"
"v_lshlrev_b32 v182, 16, v93\n\t"
"v_mul_f32 v189, 0x3ae38e39, v189\n\t"
"v_and_b32 v183, s71, v93\n\t"
"v_lshlrev_b32 v184, 16, v94\n\t"
"v_and_b32 v185, s71, v94\n\t"
"v_lshlrev_b32 v186, 16, v95\n\t"
"v_fma_f32 v189, -v188, v188, v189\n\t"
"v_and_b32 v187, s71, v95\n\t"
"v_pk_fma_f32 v[180:181], v[180:181], v[194:195], v[194:195] op_sel:[0,0,1] op_sel_hi:[1,0,1]\n\t"
"v_pk_fma_f32 v[182:183], v[182:183], v[194:195], v[194:195] op_sel:[0,0,1] op_sel_hi:[1,0,1]\n\t"
"v_max_f32 v189, 0, v189\n\t"
"v_pk_fma_f32 v[184:185], v[184:185], v[194:195], v[194:195] op_sel:[0,0,1] op_sel_hi:[1,0,1]\n\t"
"v_pk_fma_f32 v[186:187], v[186:187], v[194:195], v[194:195] op_sel:[0,0,1] op_sel_hi:[1,0,1]\n\t"
"v_cvt_pk_bf16_f32 v92, v180, v181\n\t"
"v_cvt_pk_bf16_f32 v188, v188, v189\n\t"
"v_cvt_pk_bf16_f32 v93, v182, v183\n\t"
"v_cvt_pk_bf16_f32 v94, v184, v185\n\t"
"v_cvt_pk_bf16_f32 v95, v186, v187\n\t"
"v_and_b32 v189, s71, v188\n\t"
"ds_write_b128 v177, v[92:95] offset:16512\n\t"
"v_lshlrev_b32 v180, 16, v96\n\t"
"v_and_b32 v181, s71, v96\n\t"
"v_lshlrev_b32 v188, 16, v188\n\t"
"v_lshlrev_b32 v182, 16, v97\n\t"
"v_and_b32 v183, s71, v97\n\t"
"v_lshlrev_b32 v184, 16, v98\n\t"
"v_and_b32 v185, s71, v98\n\t"
"v_add_f32 v189, 0x3727c5ac, v189\n\t"
"v_lshlrev_b32 v186, 16, v99\n\t"
"v_and_b32 v187, s71, v99\n\t"
"v_pk_fma_f32 v[180:181], v[180:181], v[194:195], v[194:195] op_sel:[0,0,1] op_sel_hi:[1,0,1]\n\t"
"v_cvt_pk_bf16_f32 v189, v189, v189\n\t"
"v_pk_fma_f32 v[182:183], v[182:183], v[194:195], v[194:195] op_sel:[0,0,1] op_sel_hi:[1,0,1]\n\t"
"v_pk_fma_f32 v[184:185], v[184:185], v[194:195], v[194:195] op_sel:[0,0,1] op_sel_hi:[1,0,1]\n\t"
"v_pk_fma_f32 v[186:187], v[186:187], v[194:195], v[194:195] op_sel:[0,0,1] op_sel_hi:[1,0,1]\n\t"
"v_lshlrev_b32 v189, 16, v189\n\t"
"v_cvt_pk_bf16_f32 v96, v180, v181\n\t"
"v_cvt_pk_bf16_f32 v97, v182, v183\n\t"
"v_cvt_pk_bf16_f32 v98, v184, v185\n\t"
"v_sqrt_f32 v189, v189\n\t"
"v_cvt_pk_bf16_f32 v99, v186, v187\n\t"
"ds_write_b128 v177, v[96:99] offset:24704\n\t"
"v_lshlrev_b32 v180, 16, v100\n\t"
"v_cvt_pk_bf16_f32 v189, v189, v189\n\t"
"v_and_b32 v181, s71, v100\n\t"
"v_lshlrev_b32 v182, 16, v101\n\t"
"v_and_b32 v183, s71, v101\n\t"
"v_lshlrev_b32 v184, 16, v102\n\t"
"v_lshlrev_b32 v189, 16, v189\n\t"
"v_and_b32 v185, s71, v102\n\t"
"v_lshlrev_b32 v186, 16, v103\n\t"
"v_and_b32 v187, s71, v103\n\t"
"v_rcp_f32 v189, v189\n\t"
"v_pk_fma_f32 v[180:181], v[180:181], v[194:195], v[194:195] op_sel:[0,0,1] op_sel_hi:[1,0,1]\n\t"
"v_pk_fma_f32 v[182:183], v[182:183], v[194:195], v[194:195] op_sel:[0,0,1] op_sel_hi:[1,0,1]\n\t"
"v_pk_fma_f32 v[184:185], v[184:185], v[194:195], v[194:195] op_sel:[0,0,1] op_sel_hi:[1,0,1]\n\t"
"v_mul_f32 v196, 0xbfb8aa3b, v189\n\t"
"v_pk_fma_f32 v[186:187], v[186:187], v[194:195], v[194:195] op_sel:[0,0,1] op_sel_hi:[1,0,1]\n\t"
"v_cvt_pk_bf16_f32 v100, v180, v181\n\t"
"v_cvt_pk_bf16_f32 v101, v182, v183\n\t"
"v_mul_f32 v197, -v188, v196\n\t"
"v_cvt_pk_bf16_f32 v102, v184, v185\n\t"
"v_cvt_pk_bf16_f32 v103, v186, v187\n\t"
"ds_write_b128 v177, v[100:103] offset:32896\n\t"
"s_waitcnt lgkmcnt(0)\n\t"
"s_barrier\n\t"
"ds_read_b128 v[0:3], v246 offset:0\n\t"
"v_lshlrev_b32 v180, 16, v104\n\t"
"v_and_b32 v181, s71, v104\n\t"
"v_lshlrev_b32 v182, 16, v105\n\t"
"v_and_b32 v183, s71, v105\n\t"
"v_lshlrev_b32 v184, 16, v106\n\t"
"ds_read_b128 v[8:11], v247 offset:0\n\t"
"v_and_b32 v185, s71, v106\n\t"
"v_lshlrev_b32 v186, 16, v107\n\t"
"v_and_b32 v187, s71, v107\n\t"
"v_pk_fma_f32 v[180:181], v[180:181], v[196:197], v[196:197] op_sel:[0,0,1] op_sel_hi:[1,0,1]\n\t"
"v_pk_fma_f32 v[182:183], v[182:183], v[196:197], v[196:197] op_sel:[0,0,1] op_sel_hi:[1,0,1]\n\t"
"ds_read_b128 v[16:19], v248 offset:4096\n\t"
"v_pk_fma_f32 v[184:185], v[184:185], v[196:197], v[196:197] op_sel:[0,0,1] op_sel_hi:[1,0,1]\n\t"
"v_pk_fma_f32 v[186:187], v[186:187], v[196:197], v[196:197] op_sel:[0,0,1] op_sel_hi:[1,0,1]\n\t"
"v_cvt_pk_bf16_f32 v104, v180, v181\n\t"
"v_cvt_pk_bf16_f32 v105, v182, v183\n\t"
"v_cvt_pk_bf16_f32 v106, v184, v185\n\t"
"ds_read_b128 v[24:27], v249 offset:4096\n\t"
"v_cvt_pk_bf16_f32 v107, v186, v187\n\t"
"v_lshlrev_b32 v180, 16, v108\n\t"
"v_and_b32 v181, s71, v108\n\t"
"v_lshlrev_b32 v182, 16, v109\n\t"
"v_and_b32 v183, s71, v109\n\t"
"ds_read_b128 a[0:3], v246 offset:8192\n\t"
"v_lshlrev_b32 v184, 16, v110\n\t"
"v_and_b32 v185, s71, v110\n\t"
"v_lshlrev_b32 v186, 16, v111\n\t"
"v_and_b32 v187, s71, v111\n\t"
"v_pk_fma_f32 v[180:181], v[180:181], v[196:197], v[196:197] op_sel:[0,0,1] op_sel_hi:[1,0,1]\n\t"
"ds_read_b128 a[8:11], v247 offset:8192\n\t"
"v_pk_fma_f32 v[182:183], v[182:183], v[196:197], v[196:197] op_sel:[0,0,1] op_sel_hi:[1,0,1]\n\t"
"v_pk_fma_f32 v[184:185], v[184:185], v[196:197], v[196:197] op_sel:[0,0,1] op_sel_hi:[1,0,1]\n\t"
"v_pk_fma_f32 v[186:187], v[186:187], v[196:197], v[196:197] op_sel:[0,0,1] op_sel_hi:[1,0,1]\n\t"
"v_cvt_pk_bf16_f32 v108, v180, v181\n\t"
"v_cvt_pk_bf16_f32 v109, v182, v183\n\t"
"ds_read_b128 a[16:19], v248 offset:12288\n\t"
"v_cvt_pk_bf16_f32 v110, v184, v185\n\t"
"v_cvt_pk_bf16_f32 v111, v186, v187\n\t"
"v_lshlrev_b32 v180, 16, v112\n\t"
"v_and_b32 v181, s71, v112\n\t"
"v_lshlrev_b32 v182, 16, v113\n\t"
"ds_read_b128 a[24:27], v249 offset:12288\n\t"
"v_and_b32 v183, s71, v113\n\t"
"v_lshlrev_b32 v184, 16, v114\n\t"
"v_and_b32 v185, s71, v114\n\t"
"v_lshlrev_b32 v186, 16, v115\n\t"
"v_and_b32 v187, s71, v115\n\t"
"ds_read_b128 a[32:35], v246 offset:16384\n\t"
"v_pk_fma_f32 v[180:181], v[180:181], v[196:197], v[196:197] op_sel:[0,0,1] op_sel_hi:[1,0,1]\n\t"
"v_pk_fma_f32 v[182:183], v[182:183], v[196:197], v[196:197] op_sel:[0,0,1] op_sel_hi:[1,0,1]\n\t"
"v_pk_fma_f32 v[184:185], v[184:185], v[196:197], v[196:197] op_sel:[0,0,1] op_sel_hi:[1,0,1]\n\t"
"v_pk_fma_f32 v[186:187], v[186:187], v[196:197], v[196:197] op_sel:[0,0,1] op_sel_hi:[1,0,1]\n\t"
"v_cvt_pk_bf16_f32 v112, v180, v181\n\t"
"ds_read_b128 a[40:43], v247 offset:16384\n\t"
"v_cvt_pk_bf16_f32 v113, v182, v183\n\t"
"v_cvt_pk_bf16_f32 v114, v184, v185\n\t"
"v_cvt_pk_bf16_f32 v115, v186, v187\n\t"
"v_lshlrev_b32 v180, 16, v116\n\t"
"v_and_b32 v181, s71, v116\n\t"
"ds_read_b128 a[48:51], v248 offset:20480\n\t"
"v_lshlrev_b32 v182, 16, v117\n\t"
"v_and_b32 v183, s71, v117\n\t"
"v_lshlrev_b32 v184, 16, v118\n\t"
"v_and_b32 v185, s71, v118\n\t"
"v_lshlrev_b32 v186, 16, v119\n\t"
"ds_read_b128 a[56:59], v249 offset:20480\n\t"
"v_and_b32 v187, s71, v119\n\t"
"v_pk_fma_f32 v[180:181], v[180:181], v[196:197], v[196:197] op_sel:[0,0,1] op_sel_hi:[1,0,1]\n\t"
"v_pk_fma_f32 v[182:183], v[182:183], v[196:197], v[196:197] op_sel:[0,0,1] op_sel_hi:[1,0,1]\n\t"
"v_pk_fma_f32 v[184:185], v[184:185], v[196:197], v[196:197] op_sel:[0,0,1] op_sel_hi:[1,0,1]\n\t"
"v_pk_fma_f32 v[186:187], v[186:187], v[196:197], v[196:197] op_sel:[0,0,1] op_sel_hi:[1,0,1]\n\t"
"ds_read_b128 a[64:67], v246 offset:24576\n\t"
"v_cvt_pk_bf16_f32 v116, v180, v181\n\t"
"v_cvt_pk_bf16_f32 v117, v182, v183\n\t"
"v_cvt_pk_bf16_f32 v118, v184, v185\n\t"
"v_cvt_pk_bf16_f32 v119, v186, v187\n\t"
"v_lshlrev_b32 v180, 16, v120\n\t"
"ds_read_b128 a[72:75], v247 offset:24576\n\t"
"v_and_b32 v181, s71, v120\n\t"
"v_lshlrev_b32 v182, 16, v121\n\t"
"v_and_b32 v183, s71, v121\n\t"
"v_lshlrev_b32 v184, 16, v122\n\t"
"v_and_b32 v185, s71, v122\n\t"
"ds_read_b128 a[80:83], v248 offset:28672\n\t"
"v_lshlrev_b32 v186, 16, v123\n\t"
"v_and_b32 v187, s71, v123\n\t"
"v_mov_b32 v188, 0\n\t"
"v_mov_b32 v189, 0\n\t"
"v_mov_b32 v190, 0\n\t"
"ds_read_b128 a[88:91], v249 offset:28672\n\t"
"v_mov_b32 v191, 0\n\t"
"s_waitcnt vmcnt(29)\n\t"
"v_dot2c_f32_bf16 v188, s70, v140\n\t"
"v_dot2c_f32_bf16 v189, v140, v140\n\t"
"v_dot2c_f32_bf16 v190, s70, v141\n\t"
"v_dot2c_f32_bf16 v191, v141, v141\n\t"
"ds_read_b128 a[96:99], v250 offset:0\n\t"
"v_dot2c_f32_bf16 v188, s70, v142\n\t"
"v_dot2c_f32_bf16 v189, v142, v142\n\t"
"v_dot2c_f32_bf16 v190, s70, v143\n\t"
"v_dot2c_f32_bf16 v191, v143, v143\n\t"
"s_waitcnt vmcnt(28)\n\t"
"v_dot2c_f32_bf16 v188, s70, v144\n\t"
"ds_read_b128 a[104:107], v251 offset:0\n\t"
"v_dot2c_f32_bf16 v189, v144, v144\n\t"
"v_dot2c_f32_bf16 v190, s70, v145\n\t"
"v_dot2c_f32_bf16 v191, v145, v145\n\t"
"v_dot2c_f32_bf16 v188, s70, v146\n\t"
"v_dot2c_f32_bf16 v189, v146, v146\n\t"
"ds_read_b128 a[112:115], v252 offset:4096\n\t"
"v_dot2c_f32_bf16 v190, s70, v147\n\t"
"v_dot2c_f32_bf16 v191, v147, v147\n\t"
"s_waitcnt vmcnt(27)\n\t"
"v_dot2c_f32_bf16 v188, s70, v148\n\t"
"v_dot2c_f32_bf16 v189, v148, v148\n\t"
"v_dot2c_f32_bf16 v190, s70, v149\n\t"
"ds_read_b128 a[120:123], v253 offset:4096\n\t"
"v_dot2c_f32_bf16 v191, v149, v149\n\t"
"v_dot2c_f32_bf16 v188, s70, v150\n\t"
"v_dot2c_f32_bf16 v189, v150, v150\n\t"
"v_dot2c_f32_bf16 v190, s70, v151\n\t"
"v_dot2c_f32_bf16 v191, v151, v151\n\t"
"ds_read_b128 a[128:131], v250 offset:8192\n\t"
"s_waitcnt vmcnt(26)\n\t"
"v_dot2c_f32_bf16 v188, s70, v152\n\t"
"v_dot2c_f32_bf16 v189, v152, v152\n\t"
"v_dot2c_f32_bf16 v190, s70, v153\n\t"
"v_dot2c_f32_bf16 v191, v153, v153\n\t"
"v_dot2c_f32_bf16 v188, s70, v154\n\t"
"ds_read_b128 a[136:139], v251 offset:8192\n\t"
"v_dot2c_f32_bf16 v189, v154, v154\n\t"
"v_dot2c_f32_bf16 v190, s70, v155\n\t"
"v_dot2c_f32_bf16 v191, v155, v155\n\t"
"s_waitcnt vmcnt(25)\n\t"
"v_dot2c_f32_bf16 v188, s70, v156\n\t"
"v_dot2c_f32_bf16 v189, v156, v156\n\t"
"ds_read_b128 a[144:147], v252 offset:12288\n\t"
"v_dot2c_f32_bf16 v190, s70, v157\n\t"
"v_dot2c_f32_bf16 v191, v157, v157\n\t"
"v_dot2c_f32_bf16 v188, s70, v158\n\t"
"v_dot2c_f32_bf16 v189, v158, v158\n\t"
"v_dot2c_f32_bf16 v190, s70, v159\n\t"
"ds_read_b128 a[152:155], v253 offset:12288\n\t"
"v_dot2c_f32_bf16 v191, v159, v159\n\t"
"s_waitcnt vmcnt(24)\n\t"
"v_dot2c_f32_bf16 v188, s70, v160\n\t"
"v_dot2c_f32_bf16 v189, v160, v160\n\t"
"v_dot2c_f32_bf16 v190, s70, v161\n\t"
"v_dot2c_f32_bf16 v191, v161, v161\n\t"
"ds_read_b128 a[160:163], v250 offset:16384\n\t"
"v_dot2c_f32_bf16 v188, s70, v162\n\t"
"v_dot2c_f32_bf16 v189, v162, v162\n\t"
"v_dot2c_f32_bf16 v190, s70, v163\n\t"
"v_dot2c_f32_bf16 v191, v163, v163\n\t"
"s_waitcnt vmcnt(23)\n\t"
"v_dot2c_f32_bf16 v188, s70, v164\n\t"
"ds_read_b128 a[168:171], v251 offset:16384\n\t"
"v_dot2c_f32_bf16 v189, v164, v164\n\t"
"v_dot2c_f32_bf16 v190, s70, v165\n\t"
"v_dot2c_f32_bf16 v191, v165, v165\n\t"
"v_dot2c_f32_bf16 v188, s70, v166\n\t"
"v_dot2c_f32_bf16 v189, v166, v166\n\t"
"ds_read_b128 a[176:179], v252 offset:20480\n\t"
"v_dot2c_f32_bf16 v190, s70, v167\n\t"
"v_dot2c_f32_bf16 v191, v167, v167\n\t"
"s_waitcnt vmcnt(22)\n\t"
"v_dot2c_f32_bf16 v188, s70, v168\n\t"
"v_dot2c_f32_bf16 v189, v168, v168\n\t"
"v_dot2c_f32_bf16 v190, s70, v169\n\t"
"ds_read_b128 a[184:187], v253 offset:20480\n\t"
"v_dot2c_f32_bf16 v191, v169, v169\n\t"
"v_dot2c_f32_bf16 v188, s70, v170\n\t"
"v_dot2c_f32_bf16 v189, v170, v170\n\t"
"v_dot2c_f32_bf16 v190, s70, v171\n\t"
"v_dot2c_f32_bf16 v191, v171, v171\n\t"
"ds_read_b128 a[192:195], v250 offset:24576\n\t"
"s_waitcnt vmcnt(21)\n\t"
"v_dot2c_f32_bf16 v188, s70, v172\n\t"
"v_dot2c_f32_bf16 v189, v172, v172\n\t"
"v_dot2c_f32_bf16 v190, s70, v173\n\t"
"v_dot2c_f32_bf16 v191, v173, v173\n\t"
"v_dot2c_f32_bf16 v188, s70, v174\n\t"
"ds_read_b128 a[200:203], v251 offset:24576\n\t"
"v_dot2c_f32_bf16 v189, v174, v174\n\t"
"v_dot2c_f32_bf16 v190, s70, v175\n\t"
"v_dot2c_f32_bf16 v191, v175, v175\n\t"
"v_pk_fma_f32 v[180:181], v[180:181], v[196:197], v[196:197] op_sel:[0,0,1] op_sel_hi:[1,0,1]\n\t"
"s_nop 0\n\t"
"v_add_f32 v188, v188, v190\n\t"
"ds_read_b128 a[208:211], v252 offset:28672\n\t"
"v_pk_fma_f32 v[182:183], v[182:183], v[196:197], v[196:197] op_sel:[0,0,1] op_sel_hi:[1,0,1]\n\t"
"v_pk_fma_f32 v[184:185], v[184:185], v[196:197], v[196:197] op_sel:[0,0,1] op_sel_hi:[1,0,1]\n\t"
"v_pk_fma_f32 v[186:187], v[186:187], v[196:197], v[196:197] op_sel:[0,0,1] op_sel_hi:[1,0,1]\n\t"
"v_add_f32 v189, v189, v191\n\t"
"v_cvt_pk_bf16_f32 v120, v180, v181\n\t"
"ds_read_b128 a[216:219], v253 offset:28672\n\t"
"v_cvt_pk_bf16_f32 v121, v182, v183\n\t"
"v_cvt_pk_bf16_f32 v122, v184, v185\n\t"
"v_add_f32_dpp v188, v188, v188 quad_perm:[1,0,3,2] row_mask:0xf bank_mask:0xf\n\t"
"v_cvt_pk_bf16_f32 v123, v186, v187\n\t"
"v_lshlrev_b32 v180, 16, v124\n\t"
"ds_read_b128 a[224:227], v250 offset:32768\n\t"
"v_and_b32 v181, s71, v124\n\t"
"v_add_f32_dpp v189, v189, v189 quad_perm:[1,0,3,2] row_mask:0xf bank_mask:0xf\n\t"
"v_lshlrev_b32 v182, 16, v125\n\t"
"v_and_b32 v183, s71, v125\n\t"
"v_lshlrev_b32 v184, 16, v126\n\t"
"ds_read_b128 a[232:235], v251 offset:32768\n\t"
"v_add_f32_dpp v188, v188, v188 quad_perm:[2,3,0,1] row_mask:0xf bank_mask:0xf\n\t"
"v_and_b32 v185, s71, v126\n\t"
"v_lshlrev_b32 v186, 16, v127\n\t"
"v_and_b32 v187, s71, v127\n\t"
"v_add_f32_dpp v189, v189, v189 quad_perm:[2,3,0,1] row_mask:0xf bank_mask:0xf\n\t"
"ds_read_b128 a[240:243], v252 offset:36864\n\t"
"v_pk_fma_f32 v[180:181], v[180:181], v[196:197], v[196:197] op_sel:[0,0,1] op_sel_hi:[1,0,1]\n\t"
"v_pk_fma_f32 v[182:183], v[182:183], v[196:197], v[196:197] op_sel:[0,0,1] op_sel_hi:[1,0,1]\n\t"
"v_pk_fma_f32 v[184:185], v[184:185], v[196:197], v[196:197] op_sel:[0,0,1] op_sel_hi:[1,0,1]\n\t"
"v_add_f32_dpp v188, v188, v188 row_half_mirror row_mask:0xf bank_mask:0xf\n\t"
"v_pk_fma_f32 v[186:187], v[186:187], v[196:197], v[196:197] op_sel:[0,0,1] op_sel_hi:[1,0,1]\n\t"
"ds_read_b128 a[248:251], v253 offset:36864\n\t"
"v_cvt_pk_bf16_f32 v124, v180, v181\n\t"
"v_cvt_pk_bf16_f32 v125, v182, v183\n\t"
"v_add_f32_dpp v189, v189, v189 row_half_mirror row_mask:0xf bank_mask:0xf\n\t"
"v_cvt_pk_bf16_f32 v126, v184, v185\n\t"
"v_cvt_pk_bf16_f32 v127, v186, v187\n\t"
"v_lshlrev_b32 v180, 16, v128\n\t"
"v_mul_f32 v188, 0x3ae38e39, v188\n\t"
"v_and_b32 v181, s71, v128\n\t"
"v_lshlrev_b32 v182, 16, v129\n\t"
"v_and_b32 v183, s71, v129\n\t"
"v_mul_f32 v189, 0x3ae38e39, v189\n\t"
"v_lshlrev_b32 v184, 16, v130\n\t"
"v_and_b32 v185, s71, v130\n\t"
"v_lshlrev_b32 v186, 16, v131\n\t"
"v_fma_f32 v189, -v188, v188, v189\n\t"
"v_and_b32 v187, s71, v131\n\t"
"v_pk_fma_f32 v[180:181], v[180:181], v[196:197], v[196:197] op_sel:[0,0,1] op_sel_hi:[1,0,1]\n\t"
"v_pk_fma_f32 v[182:183], v[182:183], v[196:197], v[196:197] op_sel:[0,0,1] op_sel_hi:[1,0,1]\n\t"
"v_max_f32 v189, 0, v189\n\t"
"v_pk_fma_f32 v[184:185], v[184:185], v[196:197], v[196:197] op_sel:[0,0,1] op_sel_hi:[1,0,1]\n\t"
"v_pk_fma_f32 v[186:187], v[186:187], v[196:197], v[196:197] op_sel:[0,0,1] op_sel_hi:[1,0,1]\n\t"
"v_cvt_pk_bf16_f32 v128, v180, v181\n\t"
"v_cvt_pk_bf16_f32 v188, v188, v189\n\t"
"v_cvt_pk_bf16_f32 v129, v182, v183\n\t"
"v_cvt_pk_bf16_f32 v130, v184, v185\n\t"
"v_cvt_pk_bf16_f32 v131, v186, v187\n\t"
"v_and_b32 v189, s71, v188\n\t"
"v_lshlrev_b32 v180, 16, v132\n\t"
"v_and_b32 v181, s71, v132\n\t"
"v_lshlrev_b32 v182, 16, v133\n\t"
"v_lshlrev_b32 v188, 16, v188\n\t"
"v_and_b32 v183, s71, v133\n\t"
"v_lshlrev_b32 v184, 16, v134\n\t"
"v_and_b32 v185, s71, v134\n\t"
"v_add_f32 v189, 0x3727c5ac, v189\n\t"
"v_lshlrev_b32 v186, 16, v135\n\t"
"v_and_b32 v187, s71, v135\n\t"
"v_pk_fma_f32 v[180:181], v[180:181], v[196:197], v[196:197] op_sel:[0,0,1] op_sel_hi:[1,0,1]\n\t"
"v_cvt_pk_bf16_f32 v189, v189, v189\n\t"
"v_pk_fma_f32 v[182:183], v[182:183], v[196:197], v[196:197] op_sel:[0,0,1] op_sel_hi:[1,0,1]\n\t"
"v_pk_fma_f32 v[184:185], v[184:185], v[196:197], v[196:197] op_sel:[0,0,1] op_sel_hi:[1,0,1]\n\t"
"v_pk_fma_f32 v[186:187], v[186:187], v[196:197], v[196:197] op_sel:[0,0,1] op_sel_hi:[1,0,1]\n\t"
"v_lshlrev_b32 v189, 16, v189\n\t"
"v_cvt_pk_bf16_f32 v132, v180, v181\n\t"
"v_cvt_pk_bf16_f32 v133, v182, v183\n\t"
"v_cvt_pk_bf16_f32 v134, v184, v185\n\t"
"v_sqrt_f32 v189, v189\n\t"
"v_cvt_pk_bf16_f32 v135, v186, v187\n\t"
"v_lshlrev_b32 v180, 16, v136\n\t"
"v_and_b32 v181, s71, v136\n\t"
"v_cvt_pk_bf16_f32 v189, v189, v189\n\t"
"v_lshlrev_b32 v182, 16, v137\n\t"
"v_and_b32 v183, s71, v137\n\t"
"v_lshlrev_b32 v184, 16, v138\n\t"
"v_lshlrev_b32 v189, 16, v189\n\t"
"v_and_b32 v185, s71, v138\n\t"
"v_lshlrev_b32 v186, 16, v139\n\t"
"v_and_b32 v187, s71, v139\n\t"
"v_rcp_f32 v189, v189\n\t"
"v_pk_fma_f32 v[180:181], v[180:181], v[196:197], v[196:197] op_sel:[0,0,1] op_sel_hi:[1,0,1]\n\t"
"v_pk_fma_f32 v[182:183], v[182:183], v[196:197], v[196:197] op_sel:[0,0,1] op_sel_hi:[1,0,1]\n\t"
"v_pk_fma_f32 v[184:185], v[184:185], v[196:197], v[196:197] op_sel:[0,0,1] op_sel_hi:[1,0,1]\n\t"
"v_mul_f32 v198, 0xbfb8aa3b, v189\n\t"
"v_pk_fma_f32 v[186:187], v[186:187], v[196:197], v[196:197] op_sel:[0,0,1] op_sel_hi:[1,0,1]\n\t"
"v_cvt_pk_bf16_f32 v136, v180, v181\n\t"
"v_cvt_pk_bf16_f32 v137, v182, v183\n\t"
"v_mul_f32 v199, -v188, v198\n\t"
"v_cvt_pk_bf16_f32 v138, v184, v185\n\t"
"v_cvt_pk_bf16_f32 v139, v186, v187\n\t"
"s_waitcnt lgkmcnt(0)\n\t"
"s_barrier\n\t"
"v_lshlrev_b32 v180, 16, v140\n\t"
"v_and_b32 v181, s71, v140\n\t"
"v_lshlrev_b32 v182, 16, v141\n\t"
"v_and_b32 v183, s71, v141\n\t"
"v_lshlrev_b32 v184, 16, v142\n\t"
"v_and_b32 v185, s71, v142\n\t"
"v_lshlrev_b32 v186, 16, v143\n\t"
"v_and_b32 v187, s71, v143\n\t"
"ds_write_b128 v176, v[104:107] offset:0\n\t"
"v_pk_fma_f32 v[180:181], v[180:181], v[198:199], v[198:199] op_sel:[0,0,1] op_sel_hi:[1,0,1]\n\t"
"v_pk_fma_f32 v[182:183], v[182:183], v[198:199], v[198:199] op_sel:[0,0,1] op_sel_hi:[1,0,1]\n\t"
"v_pk_fma_f32 v[184:185], v[184:185], v[198:199], v[198:199] op_sel:[0,0,1] op_sel_hi:[1,0,1]\n\t"
"v_pk_fma_f32 v[186:187], v[186:187], v[198:199], v[198:199] op_sel:[0,0,1] op_sel_hi:[1,0,1]\n\t"
"v_cvt_pk_bf16_f32 v140, v180, v181\n\t"
"v_cvt_pk_bf16_f32 v141, v182, v183\n\t"
"v_cvt_pk_bf16_f32 v142, v184, v185\n\t"
"v_cvt_pk_bf16_f32 v143, v186, v187\n\t"
"ds_write_b128 v176, v[140:143] offset:128\n\t"
"v_lshlrev_b32 v180, 16, v144\n\t"
"v_and_b32 v181, s71, v144\n\t"
"v_lshlrev_b32 v182, 16, v145\n\t"
"v_and_b32 v183, s71, v145\n\t"
"v_lshlrev_b32 v184, 16, v146\n\t"
"v_and_b32 v185, s71, v146\n\t"
"v_lshlrev_b32 v186, 16, v147\n\t"
"v_and_b32 v187, s71, v147\n\t"
"ds_write_b128 v176, v[108:111] offset:8192\n\t"
"v_pk_fma_f32 v[180:181], v[180:181], v[198:199], v[198:199] op_sel:[0,0,1] op_sel_hi:[1,0,1]\n\t"
"v_pk_fma_f32 v[182:183], v[182:183], v[198:199], v[198:199] op_sel:[0,0,1] op_sel_hi:[1,0,1]\n\t"
"v_pk_fma_f32 v[184:185], v[184:185], v[198:199], v[198:199] op_sel:[0,0,1] op_sel_hi:[1,0,1]\n\t"
"v_pk_fma_f32 v[186:187], v[186:187], v[198:199], v[198:199] op_sel:[0,0,1] op_sel_hi:[1,0,1]\n\t"
"v_cvt_pk_bf16_f32 v144, v180, v181\n\t"
"v_cvt_pk_bf16_f32 v145, v182, v183\n\t"
"v_cvt_pk_bf16_f32 v146, v184, v185\n\t"
"v_cvt_pk_bf16_f32 v147, v186, v187\n\t"
"ds_write_b128 v176, v[144:147] offset:8320\n\t"
"v_lshlrev_b32 v180, 16, v148\n\t"
"v_and_b32 v181, s71, v148\n\t"
"v_lshlrev_b32 v182, 16, v149\n\t"
"v_and_b32 v183, s71, v149\n\t"
"v_lshlrev_b32 v184, 16, v150\n\t"
"v_and_b32 v185, s71, v150\n\t"
"v_lshlrev_b32 v186, 16, v151\n\t"
"v_and_b32 v187, s71, v151\n\t"
"ds_write_b128 v176, v[112:115] offset:16384\n\t"
"v_pk_fma_f32 v[180:181], v[180:181], v[198:199], v[198:199] op_sel:[0,0,1] op_sel_hi:[1,0,1]\n\t"
"v_pk_fma_f32 v[182:183], v[182:183], v[198:199], v[198:199] op_sel:[0,0,1] op_sel_hi:[1,0,1]\n\t"
"v_pk_fma_f32 v[184:185], v[184:185], v[198:199], v[198:199] op_sel:[0,0,1] op_sel_hi:[1,0,1]\n\t"
"v_pk_fma_f32 v[186:187], v[186:187], v[198:199], v[198:199] op_sel:[0,0,1] op_sel_hi:[1,0,1]\n\t"
"v_cvt_pk_bf16_f32 v148, v180, v181\n\t"
"v_cvt_pk_bf16_f32 v149, v182, v183\n\t"
"v_cvt_pk_bf16_f32 v150, v184, v185\n\t"
"v_cvt_pk_bf16_f32 v151, v186, v187\n\t"
"ds_write_b128 v176, v[148:151] offset:16512\n\t"
"v_lshlrev_b32 v180, 16, v152\n\t"
"v_and_b32 v181, s71, v152\n\t"
"v_lshlrev_b32 v182, 16, v153\n\t"
"v_and_b32 v183, s71, v153\n\t"
"v_lshlrev_b32 v184, 16, v154\n\t"
"v_and_b32 v185, s71, v154\n\t"
"v_lshlrev_b32 v186, 16, v155\n\t"
"v_and_b32 v187, s71, v155\n\t"
"ds_write_b128 v176, v[116:119] offset:24576\n\t"
"v_pk_fma_f32 v[180:181], v[180:181], v[198:199], v[198:199] op_sel:[0,0,1] op_sel_hi:[1,0,1]\n\t"
"v_pk_fma_f32 v[182:183], v[182:183], v[198:199], v[198:199] op_sel:[0,0,1] op_sel_hi:[1,0,1]\n\t"
"v_pk_fma_f32 v[184:185], v[184:185], v[198:199], v[198:199] op_sel:[0,0,1] op_sel_hi:[1,0,1]\n\t"
"v_pk_fma_f32 v[186:187], v[186:187], v[198:199], v[198:199] op_sel:[0,0,1] op_sel_hi:[1,0,1]\n\t"
"v_cvt_pk_bf16_f32 v152, v180, v181\n\t"
"v_cvt_pk_bf16_f32 v153, v182, v183\n\t"
"v_cvt_pk_bf16_f32 v154, v184, v185\n\t"
"v_cvt_pk_bf16_f32 v155, v186, v187\n\t"
"ds_write_b128 v176, v[152:155] offset:24704\n\t"
"v_lshlrev_b32 v180, 16, v156\n\t"
"v_and_b32 v181, s71, v156\n\t"
"v_lshlrev_b32 v182, 16, v157\n\t"
"v_and_b32 v183, s71, v157\n\t"
"v_lshlrev_b32 v184, 16, v158\n\t"
"v_and_b32 v185, s71, v158\n\t"
"v_lshlrev_b32 v186, 16, v159\n\t"
"v_and_b32 v187, s71, v159\n\t"
"ds_write_b128 v177, v[120:123] offset:0\n\t"
"v_pk_fma_f32 v[180:181], v[180:181], v[198:199], v[198:199] op_sel:[0,0,1] op_sel_hi:[1,0,1]\n\t"
"v_pk_fma_f32 v[182:183], v[182:183], v[198:199], v[198:199] op_sel:[0,0,1] op_sel_hi:[1,0,1]\n\t"
"v_pk_fma_f32 v[184:185], v[184:185], v[198:199], v[198:199] op_sel:[0,0,1] op_sel_hi:[1,0,1]\n\t"
"v_pk_fma_f32 v[186:187], v[186:187], v[198:199], v[198:199] op_sel:[0,0,1] op_sel_hi:[1,0,1]\n\t"
"v_cvt_pk_bf16_f32 v156, v180, v181\n\t"
"v_cvt_pk_bf16_f32 v157, v182, v183\n\t"
"v_cvt_pk_bf16_f32 v158, v184, v185\n\t"
"v_cvt_pk_bf16_f32 v159, v186, v187\n\t"
"ds_write_b128 v177, v[156:159] offset:128\n\t"
"v_lshlrev_b32 v180, 16, v160\n\t"
"v_and_b32 v181, s71, v160\n\t"
"v_lshlrev_b32 v182, 16, v161\n\t"
"v_and_b32 v183, s71, v161\n\t"
"v_lshlrev_b32 v184, 16, v162\n\t"
"v_and_b32 v185, s71, v162\n\t"
"v_lshlrev_b32 v186, 16, v163\n\t"
"v_and_b32 v187, s71, v163\n\t"
"ds_write_b128 v177, v[124:127] offset:8192\n\t"
"v_pk_fma_f32 v[180:181], v[180:181], v[198:199], v[198:199] op_sel:[0,0,1] op_sel_hi:[1,0,1]\n\t"
"v_pk_fma_f32 v[182:183], v[182:183], v[198:199], v[198:199] op_sel:[0,0,1] op_sel_hi:[1,0,1]\n\t"
"v_pk_fma_f32 v[184:185], v[184:185], v[198:199], v[198:199] op_sel:[0,0,1] op_sel_hi:[1,0,1]\n\t"
"v_pk_fma_f32 v[186:187], v[186:187], v[198:199], v[198:199] op_sel:[0,0,1] op_sel_hi:[1,0,1]\n\t"
"v_cvt_pk_bf16_f32 v160, v180, v181\n\t"
"v_cvt_pk_bf16_f32 v161, v182, v183\n\t"
"v_cvt_pk_bf16_f32 v162, v184, v185\n\t"
"v_cvt_pk_bf16_f32 v163, v186, v187\n\t"
"ds_write_b128 v177, v[160:163] offset:8320\n\t"
"v_lshlrev_b32 v180, 16, v164\n\t"
"v_and_b32 v181, s71, v164\n\t"
"v_lshlrev_b32 v182, 16, v165\n\t"
"v_and_b32 v183, s71, v165\n\t"
"v_lshlrev_b32 v184, 16, v166\n\t"
"v_and_b32 v185, s71, v166\n\t"
"v_lshlrev_b32 v186, 16, v167\n\t"
"v_and_b32 v187, s71, v167\n\t"
"ds_write_b128 v177, v[128:131] offset:16384\n\t"
"v_pk_fma_f32 v[180:181], v[180:181], v[198:199], v[198:199] op_sel:[0,0,1] op_sel_hi:[1,0,1]\n\t"
"v_pk_fma_f32 v[182:183], v[182:183], v[198:199], v[198:199] op_sel:[0,0,1] op_sel_hi:[1,0,1]\n\t"
"v_pk_fma_f32 v[184:185], v[184:185], v[198:199], v[198:199] op_sel:[0,0,1] op_sel_hi:[1,0,1]\n\t"
"v_pk_fma_f32 v[186:187], v[186:187], v[198:199], v[198:199] op_sel:[0,0,1] op_sel_hi:[1,0,1]\n\t"
"v_cvt_pk_bf16_f32 v164, v180, v181\n\t"
"v_cvt_pk_bf16_f32 v165, v182, v183\n\t"
"v_cvt_pk_bf16_f32 v166, v184, v185\n\t"
"v_cvt_pk_bf16_f32 v167, v186, v187\n\t"
"ds_write_b128 v177, v[164:167] offset:16512\n\t"
"v_lshlrev_b32 v180, 16, v168\n\t"
"v_and_b32 v181, s71, v168\n\t"
"v_lshlrev_b32 v182, 16, v169\n\t"
"v_and_b32 v183, s71, v169\n\t"
"v_lshlrev_b32 v184, 16, v170\n\t"
"v_and_b32 v185, s71, v170\n\t"
"v_lshlrev_b32 v186, 16, v171\n\t"
"v_and_b32 v187, s71, v171\n\t"
"ds_write_b128 v177, v[132:135] offset:24576\n\t"
"v_pk_fma_f32 v[180:181], v[180:181], v[198:199], v[198:199] op_sel:[0,0,1] op_sel_hi:[1,0,1]\n\t"
"v_pk_fma_f32 v[182:183], v[182:183], v[198:199], v[198:199] op_sel:[0,0,1] op_sel_hi:[1,0,1]\n\t"
"v_pk_fma_f32 v[184:185], v[184:185], v[198:199], v[198:199] op_sel:[0,0,1] op_sel_hi:[1,0,1]\n\t"
"v_pk_fma_f32 v[186:187], v[186:187], v[198:199], v[198:199] op_sel:[0,0,1] op_sel_hi:[1,0,1]\n\t"
"v_cvt_pk_bf16_f32 v168, v180, v181\n\t"
"v_cvt_pk_bf16_f32 v169, v182, v183\n\t"
"v_cvt_pk_bf16_f32 v170, v184, v185\n\t"
"v_cvt_pk_bf16_f32 v171, v186, v187\n\t"
"ds_write_b128 v177, v[168:171] offset:24704\n\t"
"v_lshlrev_b32 v180, 16, v172\n\t"
"v_and_b32 v181, s71, v172\n\t"
"v_lshlrev_b32 v182, 16, v173\n\t"
"v_and_b32 v183, s71, v173\n\t"
"v_lshlrev_b32 v184, 16, v174\n\t"
"v_and_b32 v185, s71, v174\n\t"
"v_lshlrev_b32 v186, 16, v175\n\t"
"v_and_b32 v187, s71, v175\n\t"
"ds_write_b128 v177, v[136:139] offset:32768\n\t"
"v_pk_fma_f32 v[180:181], v[180:181], v[198:199], v[198:199] op_sel:[0,0,1] op_sel_hi:[1,0,1]\n\t"
"v_pk_fma_f32 v[182:183], v[182:183], v[198:199], v[198:199] op_sel:[0,0,1] op_sel_hi:[1,0,1]\n\t"
"v_pk_fma_f32 v[184:185], v[184:185], v[198:199], v[198:199] op_sel:[0,0,1] op_sel_hi:[1,0,1]\n\t"
"v_pk_fma_f32 v[186:187], v[186:187], v[198:199], v[198:199] op_sel:[0,0,1] op_sel_hi:[1,0,1]\n\t"
"v_cvt_pk_bf16_f32 v172, v180, v181\n\t"
"v_cvt_pk_bf16_f32 v173, v182, v183\n\t"
"v_cvt_pk_bf16_f32 v174, v184, v185\n\t"
"v_cvt_pk_bf16_f32 v175, v186, v187\n\t"
"ds_write_b128 v177, v[172:175] offset:32896\n\t"
"s_waitcnt lgkmcnt(0)\n\t"
"s_waitcnt vmcnt(9)\n\t"
"s_barrier\n\t"
"ds_read_b64_tr_b16 v[168:169], v212 offset:0\n\t"
"ds_read_b64_tr_b16 v[170:171], v212 offset:2048\n\t"
"ds_read_b128 v[4:7], v246 offset:0\n\t"
"ds_read_b128 v[12:15], v247 offset:0\n\t"
"ds_read_b128 v[20:23], v248 offset:4096\n\t"
"ds_read_b128 v[28:31], v249 offset:4096\n\t"
"ds_read_b64_tr_b16 v[172:173], v215 offset:0\n\t"
"ds_read_b64_tr_b16 v[174:175], v215 offset:2048\n\t"
"ds_read_b64_tr_b16 v[176:177], v212 offset:4096\n\t"
"ds_read_b64_tr_b16 v[178:179], v212 offset:6144\n\t"
"ds_read_b64_tr_b16 v[180:181], v215 offset:4096\n\t"
"ds_read_b64_tr_b16 v[182:183], v215 offset:6144\n\t"
"s_waitcnt lgkmcnt(8)\n\t"
"v_mfma_f32_16x16x32_bf16 v[32:35], v[168:171], v[0:3], 0\n\t"
"ds_read_b64_tr_b16 v[184:185], v212 offset:8192\n\t"
"v_mfma_f32_16x16x32_bf16 v[40:43], v[168:171], v[8:11], 0\n\t"
"ds_read_b64_tr_b16 v[186:187], v212 offset:10240\n\t"
"v_mfma_f32_16x16x32_bf16 v[36:39], v[168:171], v[4:7], 0\n\t"
"ds_read_b64_tr_b16 v[188:189], v215 offset:8192\n\t"
"v_mfma_f32_16x16x32_bf16 v[44:47], v[168:171], v[12:15], 0\n\t"
"s_waitcnt lgkmcnt(7)\n\t"
"ds_read_b64_tr_b16 v[190:191], v215 offset:10240\n\t"
"v_mfma_f32_16x16x32_bf16 v[48:51], v[172:175], v[0:3], 0\n\t"
"ds_read_b128 a[4:7], v246 offset:8192\n\t"
"v_mfma_f32_16x16x32_bf16 v[56:59], v[172:175], v[8:11], 0\n\t"
"ds_read_b64_tr_b16 v[168:169], v212 offset:12288\n\t"
"v_mfma_f32_16x16x32_bf16 v[52:55], v[172:175], v[4:7], 0\n\t"
"s_waitcnt lgkmcnt(8)\n\t"
"ds_read_b64_tr_b16 v[170:171], v212 offset:14336\n\t"
"v_mfma_f32_16x16x32_bf16 v[60:63], v[172:175], v[12:15], 0\n\t"
"ds_read_b64_tr_b16 v[172:173], v215 offset:12288\n\t"
"v_mfma_f32_16x16x32_bf16 v[32:35], v[176:179], v[16:19], v[32:35]\n\t"
"ds_read_b128 a[12:15], v247 offset:8192\n\t"
"v_mfma_f32_16x16x32_bf16 v[40:43], v[176:179], v[24:27], v[40:43]\n\t"
"ds_read_b64_tr_b16 v[174:175], v215 offset:14336\n\t"
"v_mfma_f32_16x16x32_bf16 v[36:39], v[176:179], v[20:23], v[36:39]\n\t"
"s_waitcnt lgkmcnt(10)\n\t"
"ds_read_b128 a[20:23], v248 offset:12288\n\t"
"v_mfma_f32_16x16x32_bf16 v[44:47], v[176:179], v[28:31], v[44:47]\n\t"
"ds_read_b64_tr_b16 v[176:177], v212 offset:16384\n\t"
"v_mfma_f32_16x16x32_bf16 v[48:51], v[180:183], v[16:19], v[48:51]\n\t"
"ds_read_b64_tr_b16 v[178:179], v212 offset:18432\n\t"
"v_mfma_f32_16x16x32_bf16 v[56:59], v[180:183], v[24:27], v[56:59]\n\t"
"ds_read_b128 a[28:31], v249 offset:12288\n\t"
"v_mfma_f32_16x16x32_bf16 v[52:55], v[180:183], v[20:23], v[52:55]\n\t"
"s_waitcnt lgkmcnt(5)\n\t"
"ds_read_b128 a[36:39], v246 offset:16384\n\t"
"v_mfma_f32_16x16x32_bf16 v[60:63], v[180:183], v[28:31], v[60:63]\n\t"
"ds_read_b64_tr_b16 v[180:181], v215 offset:16384\n\t"
"v_mfma_f32_16x16x32_bf16 v[32:35], v[184:187], a[0:3], v[32:35]\n\t"
"ds_read_b64_tr_b16 v[182:183], v215 offset:18432\n\t"
"v_mfma_f32_16x16x32_bf16 v[40:43], v[184:187], a[8:11], v[40:43]\n\t"
"ds_read_b128 a[44:47], v247 offset:16384\n\t"
"v_mfma_f32_16x16x32_bf16 v[36:39], v[184:187], a[4:7], v[36:39]\n\t"
"ds_read_b128 a[52:55], v248 offset:20480\n\t"
"v_mfma_f32_16x16x32_bf16 v[44:47], v[184:187], a[12:15], v[44:47]\n\t"
"ds_read_b64_tr_b16 v[184:185], v212 offset:20480\n\t"
"v_mfma_f32_16x16x32_bf16 v[48:51], v[188:191], a[0:3], v[48:51]\n\t"
"ds_read_b64_tr_b16 v[186:187], v212 offset:22528\n\t"
"v_mfma_f32_16x16x32_bf16 v[56:59], v[188:191], a[8:11], v[56:59]\n\t"
"ds_read_b128 a[60:63], v249 offset:20480\n\t"
"v_mfma_f32_16x16x32_bf16 v[52:55], v[188:191], a[4:7], v[52:55]\n\t"
"s_waitcnt lgkmcnt(8)\n\t"
"ds_read_b128 a[68:71], v246 offset:24576\n\t"
"v_mfma_f32_16x16x32_bf16 v[60:63], v[188:191], a[12:15], v[60:63]\n\t"
"ds_read_b64_tr_b16 v[188:189], v215 offset:20480\n\t"
"v_mfma_f32_16x16x32_bf16 v[32:35], v[168:171], a[16:19], v[32:35]\n\t"
"ds_read_b64_tr_b16 v[190:191], v215 offset:22528\n\t"
"v_mfma_f32_16x16x32_bf16 v[40:43], v[168:171], a[24:27], v[40:43]\n\t"
"ds_read_b128 a[76:79], v247 offset:24576\n\t"
"v_mfma_f32_16x16x32_bf16 v[36:39], v[168:171], a[20:23], v[36:39]\n\t"
"ds_read_b128 a[84:87], v248 offset:28672\n\t"
"v_mfma_f32_16x16x32_bf16 v[44:47], v[168:171], a[28:31], v[44:47]\n\t"
"ds_read_b128 a[92:95], v249 offset:28672\n\t"
"v_mfma_f32_16x16x32_bf16 v[48:51], v[172:175], a[16:19], v[48:51]\n\t"
"ds_read_b128 a[100:103], v250 offset:0\n\t"
"v_mfma_f32_16x16x32_bf16 v[56:59], v[172:175], a[24:27], v[56:59]\n\t"
"ds_read_b128 a[108:111], v251 offset:0\n\t"
"v_mfma_f32_16x16x32_bf16 v[52:55], v[172:175], a[20:23], v[52:55]\n\t"
"s_waitcnt lgkmcnt(12)\n\t"
"ds_read_b128 a[116:119], v252 offset:4096\n\t"
"v_mfma_f32_16x16x32_bf16 v[60:63], v[172:175], a[28:31], v[60:63]\n\t"
"ds_read_b128 a[124:127], v253 offset:4096\n\t"
"v_mfma_f32_16x16x32_bf16 v[32:35], v[176:179], a[32:35], v[32:35]\n\t"
"ds_read_b128 a[132:135], v250 offset:8192\n\t"
"v_mfma_f32_16x16x32_bf16 v[40:43], v[176:179], a[40:43], v[40:43]\n\t"
"ds_read_b128 a[140:143], v251 offset:8192\n\t"
"v_mfma_f32_16x16x32_bf16 v[36:39], v[176:179], a[36:39], v[36:39]\n\t"
"ds_read_b128 a[148:151], v252 offset:12288\n\t"
"v_mfma_f32_16x16x32_bf16 v[44:47], v[176:179], a[44:47], v[44:47]\n\t"
"ds_read_b128 a[156:159], v253 offset:12288\n\t"
"v_mfma_f32_16x16x32_bf16 v[48:51], v[180:183], a[32:35], v[48:51]\n\t"
"ds_read_b128 a[164:167], v250 offset:16384\n\t"
"ds_read_b128 a[172:175], v251 offset:16384\n\t"
"ds_read_b128 a[180:183], v252 offset:20480\n\t"
"ds_read_b128 a[188:191], v253 offset:20480\n\t"
"ds_read_b128 a[196:199], v250 offset:24576\n\t"
"ds_read_b128 a[204:207], v251 offset:24576\n\t"
"ds_read_b128 a[212:215], v252 offset:28672\n\t"
"ds_read_b128 a[220:223], v253 offset:28672\n\t"
"ds_read_b128 a[228:231], v250 offset:32768\n\t"
"ds_read_b128 a[236:239], v251 offset:32768\n\t"
"ds_read_b128 a[244:247], v252 offset:36864\n\t"
"ds_read_b128 a[252:255], v253 offset:36864\n\t"
"v_mfma_f32_16x16x32_bf16 v[56:59], v[180:183], a[40:43], v[56:59]\n\t"
"ds_read_b64_tr_b16 v[168:169], v212 offset:24576\n\t"
"v_mfma_f32_16x16x32_bf16 v[52:55], v[180:183], a[36:39], v[52:55]\n\t"
"ds_read_b64_tr_b16 v[170:171], v212 offset:26624\n\t"
"v_mfma_f32_16x16x32_bf16 v[60:63], v[180:183], a[44:47], v[60:63]\n\t"
"s_waitcnt lgkmcnt(15)\n\t"
"ds_read_b64_tr_b16 v[172:173], v215 offset:24576\n\t"
"v_mfma_f32_16x16x32_bf16 v[32:35], v[184:187], a[48:51], v[32:35]\n\t"
"ds_read_b64_tr_b16 v[174:175], v215 offset:26624\n\t"
"v_mfma_f32_16x16x32_bf16 v[40:43], v[184:187], a[56:59], v[40:43]\n\t"
"ds_read_b64_tr_b16 v[176:177], v212 offset:28672\n\t"
"v_mfma_f32_16x16x32_bf16 v[36:39], v[184:187], a[52:55], v[36:39]\n\t"
"s_waitcnt lgkmcnt(5)\n\t"
"ds_read_b64_tr_b16 v[178:179], v212 offset:30720\n\t"
"v_mfma_f32_16x16x32_bf16 v[44:47], v[184:187], a[60:63], v[44:47]\n\t"
"ds_read_b64_tr_b16 v[180:181], v215 offset:28672\n\t"
"v_mfma_f32_16x16x32_bf16 v[48:51], v[188:191], a[48:51], v[48:51]\n\t"
"s_barrier\n\t"
"ds_read_b64_tr_b16 v[182:183], v215 offset:30720\n\t"
"v_mfma_f32_16x16x32_bf16 v[56:59], v[188:191], a[56:59], v[56:59]\n\t"
"s_add_u32 m0, s63, 73728\n\t"
"ds_read_b64_tr_b16 v[184:185], v212 offset:32768\n\t"
"v_mfma_f32_16x16x32_bf16 v[52:55], v[188:191], a[52:55], v[52:55]\n\t"
"buffer_load_dwordx4 v200, %[w1], s45 offen offset:0 lds\n\t"
"v_mfma_f32_16x16x32_bf16 v[60:63], v[188:191], a[60:63], v[60:63]\n\t"
"s_waitcnt lgkmcnt(7)\n\t"
"ds_read_b64_tr_b16 v[186:187], v212 offset:34816\n\t"
"v_mfma_f32_16x16x32_bf16 v[32:35], v[168:171], a[64:67], v[32:35]\n\t"
"v_lshrrev_b32 v246, 7, v244\n\t"
"v_and_b32 v247, 4, v246\n\t"
"v_mfma_f32_16x16x32_bf16 v[40:43], v[168:171], a[72:75], v[40:43]\n\t"
"buffer_load_dwordx4 v201, %[w1], s45 offen offset:1024 lds\n\t"
"v_mfma_f32_16x16x32_bf16 v[36:39], v[168:171], a[68:71], v[36:39]\n\t"
"s_waitcnt lgkmcnt(6)\n\t"
"ds_read_b64_tr_b16 v[188:189], v215 offset:32768\n\t"
"v_add_u32 v246, v246, v247\n\t"
"v_mfma_f32_16x16x32_bf16 v[44:47], v[168:171], a[76:79], v[44:47]\n\t"
"ds_read_b64_tr_b16 v[190:191], v215 offset:34816\n\t"
"v_mul_u32_u24 v246, 5760, v246\n\t"
"v_mfma_f32_16x16x32_bf16 v[48:51], v[172:175], a[64:67], v[48:51]\n\t"
"s_lshl_b32 s41, %[j], 7\n\t"
"s_cmp_lt_u32 %[j], 9\n\t"
"s_cselect_b32 s41, s41, 2130706432\n\t"
"v_mfma_f32_16x16x32_bf16 v[56:59], v[172:175], a[72:75], v[56:59]\n\t"
"s_mul_i32 s64, %[wv], 6\n\t"
"s_add_u32 s65, s64, 0\n\t"
"s_lshr_b32 s67, s65, 2\n\t"
"v_mfma_f32_16x16x32_bf16 v[52:55], v[172:175], a[68:71], v[52:55]\n\t"
"s_lshl_b32 s67, s67, 4\n\t"
"ds_read_b64_tr_b16 v[168:169], v212 offset:36864\n\t"
"v_sub_u32 v144, v200, v246\n\t"
"v_mfma_f32_16x16x32_bf16 v[60:63], v[172:175], a[76:79], v[60:63]\n\t"
"s_waitcnt lgkmcnt(7)\n\t"
"ds_read_b64_tr_b16 v[170:171], v212 offset:38912\n\t"
"v_mfma_f32_16x16x32_bf16 v[32:35], v[176:179], a[80:83], v[32:35]\n\t"
"s_and_b32 s65, s65, 2\n\t"
"s_add_u32 s65, s65, s67\n\t"
"s_mul_i32 s65, s65, 11520\n\t"
"v_mfma_f32_16x16x32_bf16 v[40:43], v[176:179], a[88:91], v[40:43]\n\t"
"s_add_u32 s65, s65, 0\n\t"
"ds_read_b64_tr_b16 v[172:173], v215 offset:36864\n\t"
"v_subrev_u32 v144, s65, v144\n\t"
"v_mfma_f32_16x16x32_bf16 v[36:39], v[176:179], a[84:87], v[36:39]\n\t"
"s_waitcnt lgkmcnt(7)\n\t"
"s_add_u32 s65, s64, 1\n\t"
"s_lshr_b32 s67, s65, 2\n\t"
"v_mfma_f32_16x16x32_bf16 v[44:47], v[176:179], a[92:95], v[44:47]\n\t"
"s_lshl_b32 s67, s67, 4\n\t"
"ds_read_b64_tr_b16 v[174:175], v215 offset:38912\n\t"
"v_sub_u32 v145, v201, v246\n\t"
"v_mfma_f32_16x16x32_bf16 v[48:51], v[180:183], a[80:83], v[48:51]\n\t"
"buffer_load_dwordx4 v202, %[w1], s45 offen offset:2048 lds\n\t"
"v_mfma_f32_16x16x32_bf16 v[56:59], v[180:183], a[88:91], v[56:59]\n\t"
"s_and_b32 s65, s65, 2\n\t"
"s_add_u32 s65, s65, s67\n\t"
"s_mul_i32 s65, s65, 11520\n\t"
"v_mfma_f32_16x16x32_bf16 v[52:55], v[180:183], a[84:87], v[52:55]\n\t"
"s_add_u32 s65, s65, 92160\n\t"
"ds_read_b64_tr_b16 v[176:177], v212 offset:40960\n\t"
"v_subrev_u32 v145, s65, v145\n\t"
"v_mfma_f32_16x16x32_bf16 v[60:63], v[180:183], a[92:95], v[60:63]\n\t"
"s_waitcnt lgkmcnt(7)\n\t"
"s_add_u32 s65, s64, 2\n\t"
"s_lshr_b32 s67, s65, 2\n\t"
"v_mfma_f32_16x16x32_bf16 v[32:35], v[184:187], a[96:99], v[32:35]\n\t"
"s_lshl_b32 s67, s67, 4\n\t"
"ds_read_b64_tr_b16 v[178:179], v212 offset:43008\n\t"
"v_sub_u32 v146, v202, v246\n\t"
"v_mfma_f32_16x16x32_bf16 v[40:43], v[184:187], a[104:107], v[40:43]\n\t"
"buffer_load_dwordx4 v203, %[w1], s45 offen offset:3072 lds\n\t"
"v_mfma_f32_16x16x32_bf16 v[36:39], v[184:187], a[100:103], v[36:39]\n\t"
"s_waitcnt lgkmcnt(6)\n\t"
"s_add_u32 m0, s63, 77824\n\t"
"s_and_b32 s65, s65, 2\n\t"
"v_mfma_f32_16x16x32_bf16 v[44:47], v[184:187], a[108:111], v[44:47]\n\t"
"buffer_load_dwordx4 v204, %[w1], s45 offen offset:0 lds\n\t"
"v_mfma_f32_16x16x32_bf16 v[48:51], v[188:191], a[96:99], v[48:51]\n\t"
"buffer_load_dwordx4 v205, %[w1], s45 offen offset:1024 lds\n\t"
"v_mfma_f32_16x16x32_bf16 v[56:59], v[188:191], a[104:107], v[56:59]\n\t"
"s_add_u32 s65, s65, s67\n\t"
"s_mul_i32 s65, s65, 11520\n\t"
"s_add_u32 s65, s65, 0\n\t"
"v_mfma_f32_16x16x32_bf16 v[52:55], v[188:191], a[100:103], v[52:55]\n\t"
"s_waitcnt lgkmcnt(4)\n\t"
"ds_read_b64_tr_b16 v[180:181], v215 offset:40960\n\t"
"v_subrev_u32 v146, s65, v146\n\t"
"v_mfma_f32_16x16x32_bf16 v[60:63], v[188:191], a[108:111], v[60:63]\n\t"
"s_add_u32 s65, s64, 3\n\t"
"s_lshr_b32 s67, s65, 2\n\t"
"s_lshl_b32 s67, s67, 4\n\t"
"v_mfma_f32_16x16x32_bf16 v[32:35], v[168:171], a[112:115], v[32:35]\n\t"
"s_and_b32 s65, s65, 2\n\t"
"ds_read_b64_tr_b16 v[182:183], v215 offset:43008\n\t"
"v_sub_u32 v147, v203, v246\n\t"
"v_mfma_f32_16x16x32_bf16 v[40:43], v[168:171], a[120:123], v[40:43]\n\t"
"s_add_u32 s65, s65, s67\n\t"
"s_mul_i32 s65, s65, 11520\n\t"
"s_add_u32 s65, s65, 92160\n\t"
"v_mfma_f32_16x16x32_bf16 v[36:39], v[168:171], a[116:119], v[36:39]\n\t"
"s_waitcnt lgkmcnt(4)\n\t"
"ds_read_b64_tr_b16 v[184:185], v212 offset:45056\n\t"
"v_subrev_u32 v147, s65, v147\n\t"
"v_mfma_f32_16x16x32_bf16 v[44:47], v[168:171], a[124:127], v[44:47]\n\t"
"s_add_u32 s65, s64, 4\n\t"
"s_lshr_b32 s67, s65, 2\n\t"
"s_lshl_b32 s67, s67, 4\n\t"
"v_mfma_f32_16x16x32_bf16 v[48:51], v[172:175], a[112:115], v[48:51]\n\t"
"s_and_b32 s65, s65, 2\n\t"
"ds_read_b64_tr_b16 v[186:187], v212 offset:47104\n\t"
"v_sub_u32 v148, v204, v246\n\t"
"v_mfma_f32_16x16x32_bf16 v[56:59], v[172:175], a[120:123], v[56:59]\n\t"
"s_add_u32 s65, s65, s67\n\t"
"s_mul_i32 s65, s65, 11520\n\t"
"s_add_u32 s65, s65, 0\n\t"
"v_mfma_f32_16x16x32_bf16 v[52:55], v[172:175], a[116:119], v[52:55]\n\t"
"s_waitcnt lgkmcnt(4)\n\t"
"ds_read_b64_tr_b16 v[188:189], v215 offset:45056\n\t"
"v_subrev_u32 v148, s65, v148\n\t"
"v_mfma_f32_16x16x32_bf16 v[60:63], v[172:175], a[124:127], v[60:63]\n\t"
"s_add_u32 s65, s64, 5\n\t"
"ds_read_b64_tr_b16 v[190:191], v215 offset:47104\n\t"
"v_mfma_f32_16x16x32_bf16 v[32:35], v[176:179], a[128:131], v[32:35]\n\t"
"s_add_u32 s68, s45, 1769472\n\t"
"s_add_u32 m0, s63, 98304\n\t"
"s_lshr_b32 s67, s65, 2\n\t"
"v_mfma_f32_16x16x32_bf16 v[40:43], v[176:179], a[136:139], v[40:43]\n\t"
"buffer_load_dwordx4 v200, %[w1], s68 offen offset:0 lds\n\t"
"v_mfma_f32_16x16x32_bf16 v[36:39], v[176:179], a[132:135], v[36:39]\n\t"
"s_waitcnt lgkmcnt(4)\n\t"
"s_lshl_b32 s67, s67, 4\n\t"
"v_sub_u32 v149, v205, v246\n\t"
"v_mfma_f32_16x16x32_bf16 v[44:47], v[176:179], a[140:143], v[44:47]\n\t"
"buffer_load_dwordx4 v201, %[w1], s68 offen offset:1024 lds\n\t"
"v_mfma_f32_16x16x32_bf16 v[48:51], v[180:183], a[128:131], v[48:51]\n\t"
"s_waitcnt vmcnt(11)\n\t"
"s_waitcnt lgkmcnt(0)\n\t"
"s_barrier\n\t"
"v_mfma_f32_16x16x32_bf16 v[56:59], v[180:183], a[136:139], v[56:59]\n\t"
"buffer_load_dwordx4 v202, %[w1], s68 offen offset:2048 lds\n\t"
"v_mfma_f32_16x16x32_bf16 v[52:55], v[180:183], a[132:135], v[52:55]\n\t"
"ds_read_b64_tr_b16 v[168:169], v213 offset:0\n\t"
"v_mfma_f32_16x16x32_bf16 v[60:63], v[180:183], a[140:143], v[60:63]\n\t"
"s_and_b32 s65, s65, 2\n\t"
"ds_read_b64_tr_b16 v[170:171], v213 offset:2048\n\t"
"v_mfma_f32_16x16x32_bf16 v[32:35], v[184:187], a[144:147], v[32:35]\n\t"
"s_add_u32 s65, s65, s67\n\t"
"ds_read_b64_tr_b16 v[172:173], v216 offset:0\n\t"
"v_mfma_f32_16x16x32_bf16 v[40:43], v[184:187], a[152:155], v[40:43]\n\t"
"s_mul_i32 s65, s65, 11520\n\t"
"ds_read_b64_tr_b16 v[174:175], v216 offset:2048\n\t"
"v_mfma_f32_16x16x32_bf16 v[36:39], v[184:187], a[148:151], v[36:39]\n\t"
"s_add_u32 s65, s65, 92160\n\t"
"ds_read_b64_tr_b16 v[176:177], v213 offset:4096\n\t"
"v_subrev_u32 v149, s65, v149\n\t"
"v_mfma_f32_16x16x32_bf16 v[44:47], v[184:187], a[156:159], v[44:47]\n\t"
"ds_read_b64_tr_b16 v[178:179], v213 offset:6144\n\t"
"v_mfma_f32_16x16x32_bf16 v[48:51], v[188:191], a[144:147], v[48:51]\n\t"
"s_lshl_b32 s64, %[wv], 6\n\t"
"ds_read_b64_tr_b16 v[180:181], v216 offset:4096\n\t"
"v_add_u32 v246, s64, v245\n\t"
"v_mfma_f32_16x16x32_bf16 v[56:59], v[188:191], a[152:155], v[56:59]\n\t"
"ds_read_b64_tr_b16 v[182:183], v216 offset:6144\n\t"
"v_cmp_eq_u32 vcc, 0, v246\n\t"
"v_mfma_f32_16x16x32_bf16 v[52:55], v[188:191], a[148:151], v[52:55]\n\t"
"s_waitcnt lgkmcnt(6)\n\t"
"ds_read_b64_tr_b16 v[184:185], v213 offset:8192\n\t"
"v_mov_b32 v247, 2130706432\n\t"
"v_mfma_f32_16x16x32_bf16 v[60:63], v[188:191], a[156:159], v[60:63]\n\t"
"ds_read_b64_tr_b16 v[186:187], v213 offset:10240\n\t"
"v_add_u32 v254, 1, v254\n\t"
"v_mfma_f32_16x16x32_bf16 v[32:35], v[168:171], a[160:163], v[32:35]\n\t"
"buffer_load_dwordx4 v203, %[w1], s68 offen offset:3072 lds\n\t"
"v_mfma_f32_16x16x32_bf16 v[40:43], v[168:171], a[168:171], v[40:43]\n\t"
"s_add_u32 m0, s63, 102400\n\t"
"ds_read_b64_tr_b16 v[188:189], v216 offset:8192\n\t"
"v_cndmask_b32 v252, v247, v246, vcc\n\t"
"v_mfma_f32_16x16x32_bf16 v[36:39], v[168:171], a[164:167], v[36:39]\n\t"
"buffer_load_dwordx4 v204, %[w1], s68 offen offset:0 lds\n\t"
"v_mfma_f32_16x16x32_bf16 v[44:47], v[168:171], a[172:175], v[44:47]\n\t"
"s_waitcnt lgkmcnt(7)\n\t"
"s_lshl_b32 s78, %[mi], 11\n\t"
"s_add_u32 s78, s78, 49758208\n\t"
"v_mfma_f32_16x16x32_bf16 v[48:51], v[172:175], a[160:163], v[48:51]\n\t"
"s_lshl_b32 s64, %[j], 3\n\t"
"ds_read_b64_tr_b16 v[190:191], v216 offset:10240\n\t"
"v_mfma_f32_16x16x32_bf16 v[56:59], v[172:175], a[168:171], v[56:59]\n\t"
"s_add_u32 s79, s78, s64\n\t"
"ds_read_b64_tr_b16 v[168:169], v213 offset:12288\n\t"
"v_mfma_f32_16x16x32_bf16 v[52:55], v[172:175], a[164:167], v[52:55]\n\t"
"s_and_b32 s64, %[wv], 1\n\t"
"s_lshl_b32 s65, %[j], 1\n\t"
"s_add_u32 s64, s64, s65\n\t"
"v_mfma_f32_16x16x32_bf16 v[60:63], v[172:175], a[172:175], v[60:63]\n\t"
"s_waitcnt lgkmcnt(7)\n\t"
"ds_read_b64_tr_b16 v[170:171], v213 offset:14336\n\t"
"v_mfma_f32_16x16x32_bf16 v[32:35], v[176:179], a[176:179], v[32:35]\n\t"
"s_lshl_b32 s64, s64, 10\n\t"
"ds_read_b64_tr_b16 v[172:173], v216 offset:12288\n\t"
"v_mfma_f32_16x16x32_bf16 v[40:43], v[176:179], a[184:187], v[40:43]\n\t"
"s_add_u32 s64, s64, 37748736\n\t"
"s_cmp_lt_u32 %[j], 9\n\t"
"s_cselect_b32 s64, s64, 2130706432\n\t"
"v_mfma_f32_16x16x32_bf16 v[36:39], v[176:179], a[180:183], v[36:39]\n\t"
"buffer_load_dwordx4 v205, %[w1], s68 offen offset:1024 lds\n\t"
"v_mfma_f32_16x16x32_bf16 v[44:47], v[176:179], a[188:191], v[44:47]\n\t"
"s_waitcnt lgkmcnt(7)\n\t"
"s_lshl_b32 s65, %[mi], 2\n\t"
"s_xor_b32 s66, %[wv], 0\n\t"
"v_mfma_f32_16x16x32_bf16 v[48:51], v[180:183], a[176:179], v[48:51]\n\t"
"s_add_u32 s66, s66, s65\n\t"
"ds_read_b64_tr_b16 v[174:175], v216 offset:14336\n\t"
"v_mfma_f32_16x16x32_bf16 v[56:59], v[180:183], a[184:187], v[56:59]\n\t"
"s_mul_i32 s66, s66, 36864\n\t"
"ds_read_b64_tr_b16 v[176:177], v213 offset:16384\n\t"
"v_mfma_f32_16x16x32_bf16 v[52:55], v[180:183], a[180:183], v[52:55]\n\t"
"s_add_u32 s42, s66, s64\n\t"
"s_xor_b32 s66, %[wv], 1\n\t"
"s_add_u32 s66, s66, s65\n\t"
"v_mfma_f32_16x16x32_bf16 v[60:63], v[180:183], a[188:191], v[60:63]\n\t"
"s_waitcnt lgkmcnt(7)\n\t"
"s_mul_i32 s66, s66, 36864\n\t"
"s_add_u32 s43, s66, s64\n\t"
"v_mfma_f32_16x16x32_bf16 v[32:35], v[184:187], a[192:195], v[32:35]\n\t"
"s_mul_i32 s58, %[j], 43691\n\t"
"s_lshr_b32 s58, s58, 17\n\t"
"s_mul_i32 s64, s58, 3\n\t"
"v_mfma_f32_16x16x32_bf16 v[40:43], v[184:187], a[200:203], v[40:43]\n\t"
"s_sub_u32 s62, %[j], s64\n\t"
"ds_read_b64_tr_b16 v[178:179], v213 offset:18432\n\t"
"v_min_u32 v248, 2, v245\n\t"
"v_mfma_f32_16x16x32_bf16 v[36:39], v[184:187], a[196:199], v[36:39]\n\t"
"s_waitcnt lgkmcnt(6)\n\t"
"ds_read_b64_tr_b16 v[180:181], v216 offset:16384\n\t"
"v_add_u32 v248, s64, v248\n\t"
"v_mfma_f32_16x16x32_bf16 v[44:47], v[184:187], a[204:207], v[44:47]\n\t"
"s_add_u32 s68, s45, 3538944\n\t"
"ds_read_b64_tr_b16 v[182:183], v216 offset:18432\n\t"
"v_lshlrev_b32 v248, 3, v248\n\t"
"v_mfma_f32_16x16x32_bf16 v[48:51], v[188:191], a[192:195], v[48:51]\n\t"
"s_add_u32 m0, s63, 122880\n\t"
"ds_read_b64_tr_b16 v[184:185], v213 offset:20480\n\t"
"buffer_load_dwordx4 v200, %[w1], s68 offen offset:0 lds\n\t"
"v_mfma_f32_16x16x32_bf16 v[56:59], v[188:191], a[200:203], v[56:59]\n\t"
"buffer_load_dwordx4 v201, %[w1], s68 offen offset:1024 lds\n\t"
"v_mfma_f32_16x16x32_bf16 v[52:55], v[188:191], a[196:199], v[52:55]\n\t"
"s_add_u32 s64, %[nc0], s62\n\t"
"s_add_u32 s64, s64, 0\n\t"
"s_sub_u32 s65, s64, 9\n\t"
"v_mfma_f32_16x16x32_bf16 v[60:63], v[188:191], a[204:207], v[60:63]\n\t"
"s_waitcnt lgkmcnt(7)\n\t"
"s_waitcnt vmcnt(8)\n\t"
"s_min_u32 s64, s64, s65\n\t"
"v_mfma_f32_16x16x32_bf16 v[32:35], v[168:171], a[208:211], v[32:35]\n\t"
"s_barrier\n\t"
"s_lshl_b32 s53, s64, 7\n\t"
"s_add_u32 s64, %[nc0], s62\n\t"
"v_mfma_f32_16x16x32_bf16 v[40:43], v[168:171], a[216:219], v[40:43]\n\t"
"buffer_load_dwordx4 v202, %[w1], s68 offen offset:2048 lds\n\t"
"v_mfma_f32_16x16x32_bf16 v[36:39], v[168:171], a[212:215], v[36:39]\n\t"
"s_add_u32 s64, s64, 3\n\t"
"ds_read_b64_tr_b16 v[186:187], v213 offset:22528\n\t"
"v_mfma_f32_16x16x32_bf16 v[44:47], v[168:171], a[220:223], v[44:47]\n\t"
"s_waitcnt lgkmcnt(6)\n\t"
"ds_read_b64_tr_b16 v[188:189], v216 offset:20480\n\t"
"v_mfma_f32_16x16x32_bf16 v[48:51], v[172:175], a[208:211], v[48:51]\n\t"
"s_sub_u32 s65, s64, 9\n\t"
"s_min_u32 s64, s64, s65\n\t"
"s_lshl_b32 s54, s64, 7\n\t"
"v_mfma_f32_16x16x32_bf16 v[56:59], v[172:175], a[216:219], v[56:59]\n\t"
"s_add_u32 s64, %[nc0], s62\n\t"
"ds_read_b64_tr_b16 v[190:191], v216 offset:22528\n\t"
"v_mfma_f32_16x16x32_bf16 v[52:55], v[172:175], a[212:215], v[52:55]\n\t"
"s_waitcnt lgkmcnt(6)\n\t"
"s_add_u32 s64, s64, 6\n\t"
"s_sub_u32 s65, s64, 9\n\t"
"v_mfma_f32_16x16x32_bf16 v[60:63], v[172:175], a[220:223], v[60:63]\n\t"
"buffer_load_dwordx4 v203, %[w1], s68 offen offset:3072 lds\n\t"
"v_mfma_f32_16x16x32_bf16 v[32:35], v[176:179], a[224:227], v[32:35]\n\t"
"s_min_u32 s64, s64, s65\n\t"
"ds_read_b64_tr_b16 v[168:169], v213 offset:24576\n\t"
"v_mfma_f32_16x16x32_bf16 v[40:43], v[176:179], a[232:235], v[40:43]\n\t"
"s_lshl_b32 s55, s64, 7\n\t"
"s_mul_i32 s66, %[mi], 36\n\t"
"s_add_u32 s66, s66, %[wv]\n\t"
"v_mfma_f32_16x16x32_bf16 v[36:39], v[176:179], a[228:231], v[36:39]\n\t"
"s_mul_i32 s66, s66, 16384\n\t"
"ds_read_b64_tr_b16 v[170:171], v213 offset:26624\n\t"
"v_mfma_f32_16x16x32_bf16 v[44:47], v[176:179], a[236:239], v[44:47]\n\t"
"s_waitcnt lgkmcnt(6)\n\t"
"ds_read_b64_tr_b16 v[172:173], v216 offset:24576\n\t"
"v_mfma_f32_16x16x32_bf16 v[48:51], v[180:183], a[224:227], v[48:51]\n\t"
"s_lshl_b32 s67, s58, 10\n\t"
"s_add_u32 s52, s66, s67\n\t"
"s_add_u32 s64, s62, 1\n\t"
"v_mfma_f32_16x16x32_bf16 v[56:59], v[180:183], a[232:235], v[56:59]\n\t"
"s_add_u32 m0, s63, 126976\n\t"
"ds_read_b64_tr_b16 v[174:175], v216 offset:26624\n\t"
"buffer_load_dwordx4 v204, %[w1], s68 offen offset:0 lds\n\t"
"v_mfma_f32_16x16x32_bf16 v[52:55], v[180:183], a[228:231], v[52:55]\n\t"
"s_waitcnt lgkmcnt(6)\n\t"
"s_sub_u32 s65, s64, 3\n\t"
"s_min_u32 s64, s64, s65\n\t"
"v_mfma_f32_16x16x32_bf16 v[60:63], v[180:183], a[236:239], v[60:63]\n\t"
"s_sub_u32 s94, s64, s62\n\t"
"ds_read_b64_tr_b16 v[176:177], v213 offset:28672\n\t"
"v_mfma_f32_16x16x32_bf16 v[32:35], v[184:187], a[240:243], v[32:35]\n\t"
"s_add_u32 s64, s62, 2\n\t"
"ds_read_b64_tr_b16 v[178:179], v213 offset:30720\n\t"
"v_mfma_f32_16x16x32_bf16 v[40:43], v[184:187], a[248:251], v[40:43]\n\t"
"s_sub_u32 s65, s64, 3\n\t"
"s_min_u32 s64, s64, s65\n\t"
"s_sub_u32 s95, s64, s62\n\t"
"v_mfma_f32_16x16x32_bf16 v[36:39], v[184:187], a[244:247], v[36:39]\n\t"
"s_waitcnt lgkmcnt(6)\n\t"
"s_lshl_b32 s65, %[mi], 2\n\t"
"s_xor_b32 s66, %[wv], 0\n\t"
"v_mfma_f32_16x16x32_bf16 v[44:47], v[184:187], a[252:255], v[44:47]\n\t"
"buffer_load_dwordx4 v205, %[w1], s68 offen offset:1024 lds\n\t"
"v_mfma_f32_16x16x32_bf16 v[48:51], v[188:191], a[240:243], v[48:51]\n\t"
"s_add_u32 s66, s66, s65\n\t"
"ds_read_b64_tr_b16 v[180:181], v216 offset:28672\n\t"
"v_mfma_f32_16x16x32_bf16 v[56:59], v[188:191], a[248:251], v[56:59]\n\t"
"s_mul_i32 s66, s66, 294912\n\t"
"ds_read_b64_tr_b16 v[182:183], v216 offset:30720\n\t"
"v_mfma_f32_16x16x32_bf16 v[52:55], v[188:191], a[244:247], v[52:55]\n\t"
"s_add_u32 s56, s66, 9437184\n\t"
"s_xor_b32 s66, %[wv], 1\n\t"
"s_add_u32 s66, s66, s65\n\t"
"v_mfma_f32_16x16x32_bf16 v[60:63], v[188:191], a[252:255], v[60:63]\n\t"
"s_waitcnt lgkmcnt(6)\n\t"
"ds_read_b64_tr_b16 v[184:185], v213 offset:32768\n\t"
"v_mfma_f32_16x16x32_bf16 v[64:67], v[168:171], v[0:3], 0\n\t"
"s_mul_i32 s66, s66, 294912\n\t"
"s_add_u32 s57, s66, 9437184\n\t"
"s_and_b32 s64, %[wv], 1\n\t"
"v_mfma_f32_16x16x32_bf16 v[72:75], v[168:171], v[8:11], 0\n\t"
"s_lshl_b32 s64, s64, 10\n\t"
"ds_read_b64_tr_b16 v[186:187], v213 offset:34816\n\t"
"v_add_u32 v246, s64, v244\n\t"
"v_mfma_f32_16x16x32_bf16 v[68:71], v[168:171], v[4:7], 0\n\t"
"s_waitcnt lgkmcnt(6)\n\t"
"ds_read_b64_tr_b16 v[188:189], v216 offset:32768\n\t"
"v_xor_b32 v247, 1024, v246\n\t"
"v_mfma_f32_16x16x32_bf16 v[76:79], v[168:171], v[12:15], 0\n\t"
"s_add_u32 s68, s41, 0\n\t"
"v_exp_f32 v192, v40\n\t"
"v_mfma_f32_16x16x32_bf16 v[80:83], v[172:175], v[0:3], 0\n\t"
"s_add_u32 m0, s63, 0\n\t"
"v_exp_f32 v193, v41\n\t"
"v_mfma_f32_16x16x32_bf16 v[88:91], v[172:175], v[8:11], 0\n\t"
"buffer_load_dwordx4 v144, %[wq], s68 offen offset:0 lds\n\t"
"v_mfma_f32_16x16x32_bf16 v[84:87], v[172:175], v[4:7], 0\n\t"
"s_waitcnt lgkmcnt(5)\n\t"
"ds_read_b64_tr_b16 v[190:191], v216 offset:34816\n\t"
"v_add_f32 v192, 1.0, v192\n\t"
"v_mfma_f32_16x16x32_bf16 v[92:95], v[172:175], v[12:15], 0\n\t"
"v_exp_f32 v194, v42\n\t"
"v_mfma_f32_16x16x32_bf16 v[64:67], v[176:179], v[16:19], v[64:67]\n\t"
"buffer_load_dwordx4 v145, %[wq], s68 offen offset:1024 lds\n\t"
"v_mfma_f32_16x16x32_bf16 v[72:75], v[176:179], v[24:27], v[72:75]\n\t"
"ds_read_b64_tr_b16 v[168:169], v213 offset:36864\n\t"
"v_add_f32 v193, 1.0, v193\n\t"
"v_mfma_f32_16x16x32_bf16 v[68:71], v[176:179], v[20:23], v[68:71]\n\t"
"s_waitcnt lgkmcnt(5)\n\t"
"v_rcp_f32 v192, v192\n\t"
"v_mfma_f32_16x16x32_bf16 v[76:79], v[176:179], v[28:31], v[76:79]\n\t"
"v_exp_f32 v195, v43\n\t"
"v_mfma_f32_16x16x32_bf16 v[80:83], v[180:183], v[16:19], v[80:83]\n\t"
"ds_read_b64_tr_b16 v[170:171], v213 offset:38912\n\t"
"v_add_f32 v194, 1.0, v194\n\t"
"v_mfma_f32_16x16x32_bf16 v[88:91], v[180:183], v[24:27], v[88:91]\n\t"
"buffer_load_dwordx4 v146, %[wq], s68 offen offset:2048 lds\n\t"
"v_mfma_f32_16x16x32_bf16 v[84:87], v[180:183], v[20:23], v[84:87]\n\t"
"s_waitcnt lgkmcnt(4)\n\t"
"v_rcp_f32 v193, v193\n\t"
"v_mfma_f32_16x16x32_bf16 v[92:95], v[180:183], v[28:31], v[92:95]\n\t"
"ds_read_b64_tr_b16 v[172:173], v216 offset:36864\n\t"
"v_mul_f32 v192, v40, v192\n\t"
"v_mfma_f32_16x16x32_bf16 v[64:67], v[184:187], a[0:3], v[64:67]\n\t"
"v_exp_f32 v196, v56\n\t"
"v_mfma_f32_16x16x32_bf16 v[72:75], v[184:187], a[8:11], v[72:75]\n\t"
"ds_read_b64_tr_b16 v[174:175], v216 offset:38912\n\t"
"v_add_f32 v195, 1.0, v195\n\t"
"v_mfma_f32_16x16x32_bf16 v[68:71], v[184:187], a[4:7], v[68:71]\n\t"
"s_waitcnt lgkmcnt(4)\n\t"
"v_rcp_f32 v194, v194\n\t"
"v_mfma_f32_16x16x32_bf16 v[76:79], v[184:187], a[12:15], v[76:79]\n\t"
"buffer_load_dwordx4 v147, %[wq], s68 offen offset:3072 lds\n\t"
"v_mfma_f32_16x16x32_bf16 v[80:83], v[188:191], a[0:3], v[80:83]\n\t"
"ds_read_b64_tr_b16 v[176:177], v213 offset:40960\n\t"
"v_mul_f32 v193, v41, v193\n\t"
"v_mfma_f32_16x16x32_bf16 v[88:91], v[188:191], a[8:11], v[88:91]\n\t"
"v_exp_f32 v197, v57\n\t"
"v_mfma_f32_16x16x32_bf16 v[84:87], v[188:191], a[4:7], v[84:87]\n\t"
"s_waitcnt lgkmcnt(3)\n\t"
"s_getpc_b64 s[96:97]\n\t"
"70:\n\t"
"s_and_b32 s98, %[mi], 3\n\t"
"s_lshl_b32 s98, s98, 2\n\t"
"s_add_u32 s98, s98, %[wv]\n\t"
"s_lshl_b32 s98, s98, 10\n\t"
"v_add_u32 v253, s98, v244\n\t"
"v_min_u32 v253, 97f-70b, v253\n\t"
"global_load_dword v253, v253, s[96:97]\n\t"
"v_mfma_f32_16x16x32_bf16 v[92:95], v[188:191], a[12:15], v[92:95]\n\t"
"ds_read_b64_tr_b16 v[178:179], v213 offset:43008\n\t"
"v_add_f32 v196, 1.0, v196\n\t"
"v_mfma_f32_16x16x32_bf16 v[64:67], v[168:171], a[16:19], v[64:67]\n\t"
"s_add_u32 m0, s63, 4096\n\t"
"v_rcp_f32 v195, v195\n\t"
"v_mfma_f32_16x16x32_bf16 v[72:75], v[168:171], a[24:27], v[72:75]\n\t"
"ds_read_b64_tr_b16 v[180:181], v216 offset:40960\n\t"
"v_mul_f32 v194, v42, v194\n\t"
"v_mfma_f32_16x16x32_bf16 v[68:71], v[168:171], a[20:23], v[68:71]\n\t"
"s_waitcnt lgkmcnt(3)\n\t"
"ds_read_b64_tr_b16 v[182:183], v216 offset:43008\n\t"
"v_cvt_pk_bf16_f32 v160, v192, v193\n\t"
"v_mfma_f32_16x16x32_bf16 v[76:79], v[168:171], a[28:31], v[76:79]\n\t"
"v_exp_f32 v198, v58\n\t"
"v_mfma_f32_16x16x32_bf16 v[80:83], v[172:175], a[16:19], v[80:83]\n\t"
"buffer_load_dwordx4 v148, %[wq], s68 offen offset:0 lds\n\t"
"v_mfma_f32_16x16x32_bf16 v[88:91], v[172:175], a[24:27], v[88:91]\n\t"
"ds_read_b64_tr_b16 v[184:185], v213 offset:45056\n\t"
"v_add_f32 v197, 1.0, v197\n\t"
"v_mfma_f32_16x16x32_bf16 v[84:87], v[172:175], a[20:23], v[84:87]\n\t"
"s_waitcnt lgkmcnt(3)\n\t"
"v_rcp_f32 v196, v196\n\t"
"v_mfma_f32_16x16x32_bf16 v[92:95], v[172:175], a[28:31], v[92:95]\n\t"
"ds_read_b64_tr_b16 v[186:187], v213 offset:47104\n\t"
"ds_read_b64_tr_b16 v[188:189], v216 offset:45056\n\t"
"ds_read_b64_tr_b16 v[190:191], v216 offset:47104\n\t"
"v_mul_f32 v195, v43, v195\n\t"
"v_mfma_f32_16x16x32_bf16 v[64:67], v[176:179], a[32:35], v[64:67]\n\t"
"buffer_load_dwordx4 v149, %[wq], s68 offen offset:1024 lds\n\t"
"v_mfma_f32_16x16x32_bf16 v[72:75], v[176:179], a[40:43], v[72:75]\n\t"
"v_exp_f32 v199, v59\n\t"
"v_mfma_f32_16x16x32_bf16 v[68:71], v[176:179], a[36:39], v[68:71]\n\t"
"s_waitcnt vmcnt(7)\n\t"
"v_add_f32 v198, 1.0, v198\n\t"
"v_rcp_f32 v197, v197\n\t"
"v_mfma_f32_16x16x32_bf16 v[76:79], v[176:179], a[44:47], v[76:79]\n\t"
"s_waitcnt lgkmcnt(4)\n\t"
"v_mul_f32 v196, v56, v196\n\t"
"v_cvt_pk_bf16_f32 v161, v194, v195\n\t"
"v_mfma_f32_16x16x32_bf16 v[80:83], v[180:183], a[32:35], v[80:83]\n\t"
"s_waitcnt lgkmcnt(0)\n\t"
"v_exp_f32 v192, v44\n\t"
"v_mfma_f32_16x16x32_bf16 v[88:91], v[180:183], a[40:43], v[88:91]\n\t"
"s_barrier\n\t"
"v_add_f32 v199, 1.0, v199\n\t"
"v_rcp_f32 v198, v198\n\t"
"v_mfma_f32_16x16x32_bf16 v[84:87], v[180:183], a[36:39], v[84:87]\n\t"
"s_add_u32 s68, s41, 663552\n\t"
"ds_read_b64_tr_b16 v[168:169], v214 offset:0\n\t"
"v_mul_f32 v197, v57, v197\n\t"
"v_mfma_f32_16x16x32_bf16 v[92:95], v[180:183], a[44:47], v[92:95]\n\t"
"v_exp_f32 v193, v45\n\t"
"v_mfma_f32_16x16x32_bf16 v[64:67], v[184:187], a[48:51], v[64:67]\n\t"
"s_add_u32 m0, s63, 24576\n\t"
"ds_read_b64_tr_b16 v[170:171], v214 offset:2048\n\t"
"v_add_f32 v192, 1.0, v192\n\t"
"v_mfma_f32_16x16x32_bf16 v[72:75], v[184:187], a[56:59], v[72:75]\n\t"
"buffer_load_dwordx4 v144, %[wq], s68 offen offset:0 lds\n\t"
"v_mfma_f32_16x16x32_bf16 v[68:71], v[184:187], a[52:55], v[68:71]\n\t"
"v_rcp_f32 v199, v199\n\t"
"v_mfma_f32_16x16x32_bf16 v[76:79], v[184:187], a[60:63], v[76:79]\n\t"
"ds_read_b64_tr_b16 v[172:173], v217 offset:0\n\t"
"ds_read_b64_tr_b16 v[174:175], v217 offset:2048\n\t"
"v_mul_f32 v198, v58, v198\n\t"
"v_mfma_f32_16x16x32_bf16 v[80:83], v[188:191], a[48:51], v[80:83]\n\t"
"ds_read_b64_tr_b16 v[176:177], v214 offset:4096\n\t"
"v_cvt_pk_bf16_f32 v162, v196, v197\n\t"
"v_mfma_f32_16x16x32_bf16 v[88:91], v[188:191], a[56:59], v[88:91]\n\t"
"buffer_load_dwordx4 v145, %[wq], s68 offen offset:1024 lds\n\t"
"v_mfma_f32_16x16x32_bf16 v[84:87], v[188:191], a[52:55], v[84:87]\n\t"
"s_waitcnt lgkmcnt(3)\n\t"
"v_exp_f32 v194, v46\n\t"
"v_mfma_f32_16x16x32_bf16 v[92:95], v[188:191], a[60:63], v[92:95]\n\t"
"ds_read_b64_tr_b16 v[178:179], v214 offset:6144\n\t"
"v_add_f32 v193, 1.0, v193\n\t"
"v_mfma_f32_16x16x32_bf16 v[64:67], v[168:171], a[64:67], v[64:67]\n\t"
"v_rcp_f32 v192, v192\n\t"
"v_mfma_f32_16x16x32_bf16 v[72:75], v[168:171], a[72:75], v[72:75]\n\t"
"s_lshl_b32 s58, s44, 4\n\t"
"ds_read_b64_tr_b16 v[180:181], v217 offset:4096\n\t"
"v_mul_f32 v199, v59, v199\n\t"
"v_mfma_f32_16x16x32_bf16 v[68:71], v[168:171], a[68:71], v[68:71]\n\t"
"s_add_u32 s69, s58, s57\n\t"
"v_exp_f32 v195, v47\n\t"
"v_mfma_f32_16x16x32_bf16 v[76:79], v[168:171], a[76:79], v[76:79]\n\t"
"s_waitcnt lgkmcnt(3)\n\t"
"ds_read_b64_tr_b16 v[182:183], v217 offset:6144\n\t"
"v_add_f32 v194, 1.0, v194\n\t"
"v_mfma_f32_16x16x32_bf16 v[80:83], v[172:175], a[64:67], v[80:83]\n\t"
"v_rcp_f32 v193, v193\n\t"
"v_mfma_f32_16x16x32_bf16 v[88:91], v[172:175], a[72:75], v[88:91]\n\t"
"buffer_load_dwordx4 v146, %[wq], s68 offen offset:2048 lds\n\t"
"v_mfma_f32_16x16x32_bf16 v[84:87], v[172:175], a[68:71], v[84:87]\n\t"
"s_waitcnt lgkmcnt(2)\n\t"
"ds_read_b64_tr_b16 v[184:185], v214 offset:8192\n\t"
"v_mul_f32 v192, v44, v192\n\t"
"v_mfma_f32_16x16x32_bf16 v[92:95], v[172:175], a[76:79], v[92:95]\n\t"
"ds_read_b64_tr_b16 v[186:187], v214 offset:10240\n\t"
"v_cvt_pk_bf16_f32 v163, v198, v199\n\t"
"v_mfma_f32_16x16x32_bf16 v[64:67], v[176:179], a[80:83], v[64:67]\n\t"
"v_exp_f32 v196, v60\n\t"
"v_mfma_f32_16x16x32_bf16 v[72:75], v[176:179], a[88:91], v[72:75]\n\t"
"ds_read_b64_tr_b16 v[188:189], v217 offset:8192\n\t"
"v_add_f32 v195, 1.0, v195\n\t"
"v_mfma_f32_16x16x32_bf16 v[68:71], v[176:179], a[84:87], v[68:71]\n\t"
"s_waitcnt lgkmcnt(3)\n\t"
"v_rcp_f32 v194, v194\n\t"
"v_mfma_f32_16x16x32_bf16 v[76:79], v[176:179], a[92:95], v[76:79]\n\t"
"ds_read_b64_tr_b16 v[190:191], v217 offset:10240\n\t"
"buffer_load_dwordx4 v147, %[wq], s68 offen offset:3072 lds\n\t"
"v_mfma_f32_16x16x32_bf16 v[80:83], v[180:183], a[80:83], v[80:83]\n\t"
"s_add_u32 m0, s63, 28672\n\t"
"ds_read_b64_tr_b16 v[168:169], v214 offset:12288\n\t"
"v_mul_f32 v193, v45, v193\n\t"
"v_mfma_f32_16x16x32_bf16 v[88:91], v[180:183], a[88:91], v[88:91]\n\t"
"buffer_store_dwordx4 v[160:163], v246, %[ws], s69 offen sc1\n\t"
"ds_write_b128 v242, v[160:163] offset:0\n\t"
"v_mfma_f32_16x16x32_bf16 v[84:87], v[180:183], a[84:87], v[84:87]\n\t"
"s_waitcnt lgkmcnt(4)\n\t"
"v_exp_f32 v197, v61\n\t"
"v_mfma_f32_16x16x32_bf16 v[92:95], v[180:183], a[92:95], v[92:95]\n\t"
"ds_read_b64_tr_b16 v[170:171], v214 offset:14336\n\t"
"v_add_f32 v196, 1.0, v196\n\t"
"v_mfma_f32_16x16x32_bf16 v[64:67], v[184:187], a[96:99], v[64:67]\n\t"
"buffer_load_dwordx4 v148, %[wq], s68 offen offset:0 lds\n\t"
"v_mfma_f32_16x16x32_bf16 v[72:75], v[184:187], a[104:107], v[72:75]\n\t"
"buffer_load_dwordx4 v149, %[wq], s68 offen offset:1024 lds\n\t"
"v_mfma_f32_16x16x32_bf16 v[68:71], v[184:187], a[100:103], v[68:71]\n\t"
"s_waitcnt lgkmcnt(3)\n\t"
"v_rcp_f32 v195, v195\n\t"
"v_mfma_f32_16x16x32_bf16 v[76:79], v[184:187], a[108:111], v[76:79]\n\t"
"ds_read_b64_tr_b16 v[172:173], v217 offset:12288\n\t"
"ds_read_b64_tr_b16 v[174:175], v217 offset:14336\n\t"
"v_mul_f32 v194, v46, v194\n\t"
"v_mfma_f32_16x16x32_bf16 v[80:83], v[188:191], a[96:99], v[80:83]\n\t"
"s_add_u32 s68, s41, 1327104\n\t"
"ds_read_b64_tr_b16 v[176:177], v214 offset:16384\n\t"
"v_cvt_pk_bf16_f32 v164, v192, v193\n\t"
"v_mfma_f32_16x16x32_bf16 v[88:91], v[188:191], a[104:107], v[88:91]\n\t"
"s_add_u32 m0, s63, 49152\n\t"
"v_exp_f32 v198, v62\n\t"
"v_mfma_f32_16x16x32_bf16 v[84:87], v[188:191], a[100:103], v[84:87]\n\t"
"s_waitcnt lgkmcnt(3)\n\t"
"ds_read_b64_tr_b16 v[178:179], v214 offset:18432\n\t"
"v_add_f32 v197, 1.0, v197\n\t"
"v_mfma_f32_16x16x32_bf16 v[92:95], v[188:191], a[108:111], v[92:95]\n\t"
"v_rcp_f32 v196, v196\n\t"
"v_mfma_f32_16x16x32_bf16 v[64:67], v[168:171], a[112:115], v[64:67]\n\t"
"ds_read_b64_tr_b16 v[180:181], v217 offset:16384\n\t"
"v_mul_f32 v195, v47, v195\n\t"
"v_mfma_f32_16x16x32_bf16 v[72:75], v[168:171], a[120:123], v[72:75]\n\t"
"v_exp_f32 v199, v63\n\t"
"v_mfma_f32_16x16x32_bf16 v[68:71], v[168:171], a[116:119], v[68:71]\n\t"
"s_waitcnt lgkmcnt(3)\n\t"
"ds_read_b64_tr_b16 v[182:183], v217 offset:18432\n\t"
"v_add_f32 v198, 1.0, v198\n\t"
"v_mfma_f32_16x16x32_bf16 v[76:79], v[168:171], a[124:127], v[76:79]\n\t"
"v_rcp_f32 v197, v197\n\t"
"v_mfma_f32_16x16x32_bf16 v[80:83], v[172:175], a[112:115], v[80:83]\n\t"
"buffer_load_dwordx4 v144, %[wq], s68 offen offset:0 lds\n\t"
"v_mul_f32 v196, v60, v196\n\t"
"v_mfma_f32_16x16x32_bf16 v[88:91], v[172:175], a[120:123], v[88:91]\n\t"
"ds_read_b64_tr_b16 v[184:185], v214 offset:20480\n\t"
"v_cvt_pk_bf16_f32 v165, v194, v195\n\t"
"v_mfma_f32_16x16x32_bf16 v[84:87], v[172:175], a[116:119], v[84:87]\n\t"
"s_waitcnt lgkmcnt(3)\n\t"
"v_exp_f32 v192, v32\n\t"
"v_mfma_f32_16x16x32_bf16 v[92:95], v[172:175], a[124:127], v[92:95]\n\t"
"ds_read_b64_tr_b16 v[186:187], v214 offset:22528\n\t"
"v_add_f32 v199, 1.0, v199\n\t"
"v_mfma_f32_16x16x32_bf16 v[64:67], v[176:179], a[128:131], v[64:67]\n\t"
"v_rcp_f32 v198, v198\n\t"
"v_mfma_f32_16x16x32_bf16 v[72:75], v[176:179], a[136:139], v[72:75]\n\t"
"buffer_load_dwordx4 v145, %[wq], s68 offen offset:1024 lds\n\t"
"v_mul_f32 v197, v61, v197\n\t"
"v_mfma_f32_16x16x32_bf16 v[68:71], v[176:179], a[132:135], v[68:71]\n\t"
"s_waitcnt lgkmcnt(2)\n\t"
"v_exp_f32 v193, v33\n\t"
"v_add_f32 v192, 1.0, v192\n\t"
"v_mfma_f32_16x16x32_bf16 v[76:79], v[176:179], a[140:143], v[76:79]\n\t"
"ds_read_b64_tr_b16 v[188:189], v217 offset:20480\n\t"
"ds_read_b64_tr_b16 v[190:191], v217 offset:22528\n\t"
"v_rcp_f32 v199, v199\n\t"
"v_mfma_f32_16x16x32_bf16 v[80:83], v[180:183], a[128:131], v[80:83]\n\t"
"v_mul_f32 v198, v62, v198\n\t"
"v_cvt_pk_bf16_f32 v166, v196, v197\n\t"
"v_mfma_f32_16x16x32_bf16 v[88:91], v[180:183], a[136:139], v[88:91]\n\t"
"buffer_load_dwordx4 v146, %[wq], s68 offen offset:2048 lds\n\t"
"v_mfma_f32_16x16x32_bf16 v[84:87], v[180:183], a[132:135], v[84:87]\n\t"
"s_waitcnt lgkmcnt(2)\n\t"
"v_exp_f32 v194, v34\n\t"
"v_mfma_f32_16x16x32_bf16 v[92:95], v[180:183], a[140:143], v[92:95]\n\t"
"ds_read_b64_tr_b16 v[168:169], v214 offset:24576\n\t"
"ds_read_b64_tr_b16 v[170:171], v214 offset:26624\n\t"
"v_add_f32 v193, 1.0, v193\n\t"
"v_mfma_f32_16x16x32_bf16 v[64:67], v[184:187], a[144:147], v[64:67]\n\t"
"v_rcp_f32 v192, v192\n\t"
"v_mfma_f32_16x16x32_bf16 v[72:75], v[184:187], a[152:155], v[72:75]\n\t"
"s_add_u32 s69, s69, 147456\n\t"
"ds_read_b64_tr_b16 v[172:173], v217 offset:24576\n\t"
"v_mul_f32 v199, v63, v199\n\t"
"v_mfma_f32_16x16x32_bf16 v[68:71], v[184:187], a[148:151], v[68:71]\n\t"
"s_waitcnt lgkmcnt(3)\n\t"
"v_exp_f32 v195, v35\n\t"
"v_mfma_f32_16x16x32_bf16 v[76:79], v[184:187], a[156:159], v[76:79]\n\t"
"ds_read_b64_tr_b16 v[174:175], v217 offset:26624\n\t"
"v_add_f32 v194, 1.0, v194\n\t"
"v_mfma_f32_16x16x32_bf16 v[80:83], v[188:191], a[144:147], v[80:83]\n\t"
"buffer_load_dwordx4 v147, %[wq], s68 offen offset:3072 lds\n\t"
"v_mfma_f32_16x16x32_bf16 v[88:91], v[188:191], a[152:155], v[88:91]\n\t"
"s_add_u32 m0, s63, 53248\n\t"
"v_rcp_f32 v193, v193\n\t"
"v_mul_f32 v192, v32, v192\n\t"
"v_mfma_f32_16x16x32_bf16 v[84:87], v[188:191], a[148:151], v[84:87]\n\t"
"s_waitcnt lgkmcnt(2)\n\t"
"ds_read_b64_tr_b16 v[176:177], v214 offset:28672\n\t"
"v_cvt_pk_bf16_f32 v167, v198, v199\n\t"
"v_mfma_f32_16x16x32_bf16 v[92:95], v[188:191], a[156:159], v[92:95]\n\t"
"ds_read_b64_tr_b16 v[178:179], v214 offset:30720\n\t"
"v_exp_f32 v196, v48\n\t"
"v_mfma_f32_16x16x32_bf16 v[64:67], v[168:171], a[160:163], v[64:67]\n\t"
"v_add_f32 v195, 1.0, v195\n\t"
"v_rcp_f32 v194, v194\n\t"
"v_mfma_f32_16x16x32_bf16 v[72:75], v[168:171], a[168:171], v[72:75]\n\t"
"buffer_load_dwordx4 v148, %[wq], s68 offen offset:0 lds\n\t"
"v_mfma_f32_16x16x32_bf16 v[68:71], v[168:171], a[164:167], v[68:71]\n\t"
"s_waitcnt lgkmcnt(2)\n\t"
"buffer_store_dwordx4 v[164:167], v246, %[ws], s69 offen sc1\n\t"
"ds_write_b128 v242, v[164:167] offset:1024\n\t"
"v_mul_f32 v193, v33, v193\n\t"
"v_mfma_f32_16x16x32_bf16 v[76:79], v[168:171], a[172:175], v[76:79]\n\t"
"ds_read_b64_tr_b16 v[180:181], v217 offset:28672\n\t"
"ds_read_b64_tr_b16 v[182:183], v217 offset:30720\n\t"
"buffer_load_dwordx4 v149, %[wq], s68 offen offset:1024 lds\n\t"
"v_mfma_f32_16x16x32_bf16 v[80:83], v[172:175], a[160:163], v[80:83]\n\t"
"s_add_u32 m0, s63, 73728\n\t"
"v_exp_f32 v197, v49\n\t"
"v_mfma_f32_16x16x32_bf16 v[88:91], v[172:175], a[168:171], v[88:91]\n\t"
"ds_read_b64_tr_b16 v[184:185], v214 offset:32768\n\t"
"v_add_f32 v196, 1.0, v196\n\t"
"v_mfma_f32_16x16x32_bf16 v[84:87], v[172:175], a[164:167], v[84:87]\n\t"
"s_waitcnt lgkmcnt(4)\n\t"
"v_rcp_f32 v195, v195\n\t"
"v_mfma_f32_16x16x32_bf16 v[92:95], v[172:175], a[172:175], v[92:95]\n\t"
"ds_read_b64_tr_b16 v[186:187], v214 offset:34816\n\t"
"v_mul_f32 v194, v34, v194\n\t"
"v_cvt_pk_bf16_f32 v96, v192, v193\n\t"
"v_mfma_f32_16x16x32_bf16 v[64:67], v[176:179], a[176:179], v[64:67]\n\t"
"v_exp_f32 v198, v50\n\t"
"v_mfma_f32_16x16x32_bf16 v[72:75], v[176:179], a[184:187], v[72:75]\n\t"
"ds_read_b64_tr_b16 v[188:189], v217 offset:32768\n\t"
"v_add_f32 v197, 1.0, v197\n\t"
"v_mfma_f32_16x16x32_bf16 v[68:71], v[176:179], a[180:183], v[68:71]\n\t"
"s_waitcnt lgkmcnt(3)\n\t"
"v_rcp_f32 v196, v196\n\t"
"v_mfma_f32_16x16x32_bf16 v[76:79], v[176:179], a[188:191], v[76:79]\n\t"
"ds_read_b64_tr_b16 v[190:191], v217 offset:34816\n\t"
"v_mul_f32 v195, v35, v195\n\t"
"v_mfma_f32_16x16x32_bf16 v[80:83], v[180:183], a[176:179], v[80:83]\n\t"
"buffer_load_dwordx4 v200, %[w1], s46 offen offset:0 lds\n\t"
"v_exp_f32 v199, v51\n\t"
"v_mfma_f32_16x16x32_bf16 v[88:91], v[180:183], a[184:187], v[88:91]\n\t"
"ds_read_b64_tr_b16 v[168:169], v214 offset:36864\n\t"
"v_add_f32 v198, 1.0, v198\n\t"
"v_mfma_f32_16x16x32_bf16 v[84:87], v[180:183], a[180:183], v[84:87]\n\t"
"s_waitcnt lgkmcnt(3)\n\t"
"v_rcp_f32 v197, v197\n\t"
"v_mfma_f32_16x16x32_bf16 v[92:95], v[180:183], a[188:191], v[92:95]\n\t"
"ds_read_b64_tr_b16 v[170:171], v214 offset:38912\n\t"
"v_mul_f32 v196, v48, v196\n\t"
"v_mfma_f32_16x16x32_bf16 v[64:67], v[184:187], a[192:195], v[64:67]\n\t"
"v_cvt_pk_bf16_f32 v97, v194, v195\n\t"
"v_exp_f32 v192, v36\n\t"
"v_mfma_f32_16x16x32_bf16 v[72:75], v[184:187], a[200:203], v[72:75]\n\t"
"buffer_load_dwordx4 v201, %[w1], s46 offen offset:1024 lds\n\t"
"v_mfma_f32_16x16x32_bf16 v[68:71], v[184:187], a[196:199], v[68:71]\n\t"
"s_waitcnt lgkmcnt(2)\n\t"
"ds_read_b64_tr_b16 v[172:173], v217 offset:36864\n\t"
"v_add_f32 v199, 1.0, v199\n\t"
"v_mfma_f32_16x16x32_bf16 v[76:79], v[184:187], a[204:207], v[76:79]\n\t"
"ds_read_b64_tr_b16 v[174:175], v217 offset:38912\n\t"
"v_rcp_f32 v198, v198\n\t"
"v_mul_f32 v197, v49, v197\n\t"
"v_mfma_f32_16x16x32_bf16 v[80:83], v[188:191], a[192:195], v[80:83]\n\t"
"v_exp_f32 v193, v37\n\t"
"v_mfma_f32_16x16x32_bf16 v[88:91], v[188:191], a[200:203], v[88:91]\n\t"
"buffer_load_dwordx4 v202, %[w1], s46 offen offset:2048 lds\n\t"
"v_add_f32 v192, 1.0, v192\n\t"
"v_mfma_f32_16x16x32_bf16 v[84:87], v[188:191], a[196:199], v[84:87]\n\t"
"s_waitcnt lgkmcnt(2)\n\t"
"v_rcp_f32 v199, v199\n\t"
"v_mfma_f32_16x16x32_bf16 v[92:95], v[188:191], a[204:207], v[92:95]\n\t"
"ds_read_b64_tr_b16 v[176:177], v214 offset:40960\n\t"
"ds_read_b64_tr_b16 v[178:179], v214 offset:43008\n\t"
"v_mul_f32 v198, v50, v198\n\t"
"v_mfma_f32_16x16x32_bf16 v[64:67], v[168:171], a[208:211], v[64:67]\n\t"
"ds_read_b64_tr_b16 v[180:181], v217 offset:40960\n\t"
"v_cvt_pk_bf16_f32 v98, v196, v197\n\t"
"v_mfma_f32_16x16x32_bf16 v[72:75], v[168:171], a[216:219], v[72:75]\n\t"
"v_exp_f32 v194, v38\n\t"
"v_mfma_f32_16x16x32_bf16 v[68:71], v[168:171], a[212:215], v[68:71]\n\t"
"s_waitcnt lgkmcnt(3)\n\t"
"v_add_f32 v193, 1.0, v193\n\t"
"v_rcp_f32 v192, v192\n\t"
"v_mfma_f32_16x16x32_bf16 v[76:79], v[168:171], a[220:223], v[76:79]\n\t"
"s_add_u32 s69, s58, s56\n\t"
"ds_read_b64_tr_b16 v[182:183], v217 offset:43008\n\t"
"v_mul_f32 v199, v51, v199\n\t"
"v_exp_f32 v195, v39\n\t"
"v_mfma_f32_16x16x32_bf16 v[80:83], v[172:175], a[208:211], v[80:83]\n\t"
"buffer_load_dwordx4 v203, %[w1], s46 offen offset:3072 lds\n\t"
"v_add_f32 v194, 1.0, v194\n\t"
"v_rcp_f32 v193, v193\n\t"
"v_mul_f32 v192, v36, v192\n\t"
"v_mfma_f32_16x16x32_bf16 v[88:91], v[172:175], a[216:219], v[88:91]\n\t"
"s_add_u32 m0, s63, 77824\n\t"
"v_cvt_pk_bf16_f32 v99, v198, v199\n\t"
"v_exp_f32 v196, v52\n\t"
"v_add_f32 v195, 1.0, v195\n\t"
"v_mfma_f32_16x16x32_bf16 v[84:87], v[172:175], a[212:215], v[84:87]\n\t"
"buffer_store_dwordx4 v[96:99], v246, %[ws], s69 offen sc1\n\t"
"ds_read_b64_tr_b16 v[184:185], v214 offset:45056\n\t"
"v_rcp_f32 v194, v194\n\t"
"v_mul_f32 v193, v37, v193\n\t"
"v_mfma_f32_16x16x32_bf16 v[92:95], v[172:175], a[220:223], v[92:95]\n\t"
"s_waitcnt lgkmcnt(3)\n\t"
"ds_read_b64_tr_b16 v[186:187], v214 offset:47104\n\t"
"ds_read_b64_tr_b16 v[188:189], v217 offset:45056\n\t"
"ds_read_b64_tr_b16 v[190:191], v217 offset:47104\n\t"
"v_exp_f32 v197, v53\n\t"
"v_mfma_f32_16x16x32_bf16 v[64:67], v[176:179], a[224:227], v[64:67]\n\t"
"buffer_load_dwordx4 v204, %[w1], s46 offen offset:0 lds\n\t"
"v_mfma_f32_16x16x32_bf16 v[72:75], v[176:179], a[232:235], v[72:75]\n\t"
"buffer_load_dwordx4 v205, %[w1], s46 offen offset:1024 lds\n\t"
"v_mfma_f32_16x16x32_bf16 v[68:71], v[176:179], a[228:231], v[68:71]\n\t"
"s_waitcnt vmcnt(14)\n\t"
"v_add_f32 v196, 1.0, v196\n\t"
"v_rcp_f32 v195, v195\n\t"
"v_mfma_f32_16x16x32_bf16 v[76:79], v[176:179], a[236:239], v[76:79]\n\t"
"s_waitcnt lgkmcnt(4)\n\t"
"v_mul_f32 v194, v38, v194\n\t"
"v_cvt_pk_bf16_f32 v100, v192, v193\n\t"
"v_mfma_f32_16x16x32_bf16 v[80:83], v[180:183], a[224:227], v[80:83]\n\t"
"s_waitcnt lgkmcnt(0)\n\t"
"v_exp_f32 v198, v54\n\t"
"v_mfma_f32_16x16x32_bf16 v[88:91], v[180:183], a[232:235], v[88:91]\n\t"
"s_barrier\n\t"
"v_add_f32 v197, 1.0, v197\n\t"
"v_rcp_f32 v196, v196\n\t"
"v_mfma_f32_16x16x32_bf16 v[84:87], v[180:183], a[228:231], v[84:87]\n\t"
"s_add_u32 s68, s46, 1769472\n\t"
"ds_read_b64_tr_b16 v[168:169], v212 offset:0\n\t"
"v_mul_f32 v195, v39, v195\n\t"
"v_mfma_f32_16x16x32_bf16 v[92:95], v[180:183], a[236:239], v[92:95]\n\t"
"v_exp_f32 v199, v55\n\t"
"v_mfma_f32_16x16x32_bf16 v[64:67], v[184:187], a[240:243], v[64:67]\n\t"
"s_add_u32 m0, s63, 98304\n\t"
"ds_read_b64_tr_b16 v[170:171], v212 offset:2048\n\t"
"v_add_f32 v198, 1.0, v198\n\t"
"v_mfma_f32_16x16x32_bf16 v[72:75], v[184:187], a[248:251], v[72:75]\n\t"
"v_rcp_f32 v197, v197\n\t"
"v_mfma_f32_16x16x32_bf16 v[68:71], v[184:187], a[244:247], v[68:71]\n\t"
"v_mul_f32 v196, v52, v196\n\t"
"v_cvt_pk_bf16_f32 v101, v194, v195\n\t"
"v_add_f32 v199, 1.0, v199\n\t"
"v_mfma_f32_16x16x32_bf16 v[76:79], v[184:187], a[252:255], v[76:79]\n\t"
"ds_read_b64_tr_b16 v[172:173], v215 offset:0\n\t"
"ds_read_b64_tr_b16 v[174:175], v215 offset:2048\n\t"
"v_rcp_f32 v198, v198\n\t"
"v_mul_f32 v197, v53, v197\n\t"
"v_mfma_f32_16x16x32_bf16 v[80:83], v[188:191], a[240:243], v[80:83]\n\t"
"v_rcp_f32 v199, v199\n\t"
"v_mul_f32 v198, v54, v198\n\t"
"v_cvt_pk_bf16_f32 v102, v196, v197\n\t"
"v_mfma_f32_16x16x32_bf16 v[88:91], v[188:191], a[248:251], v[88:91]\n\t"
"s_add_u32 s69, s69, 147456\n\t"
"v_mul_f32 v199, v55, v199\n\t"
"v_cvt_pk_bf16_f32 v103, v198, v199\n\t"
"v_mfma_f32_16x16x32_bf16 v[84:87], v[188:191], a[244:247], v[84:87]\n\t"
"buffer_store_dwordx4 v[100:103], v246, %[ws], s69 offen sc1\n\t"
"ds_read_b64_tr_b16 v[176:177], v212 offset:4096\n\t"
"ds_read_b64_tr_b16 v[178:179], v212 offset:6144\n\t"
"ds_read_b64_tr_b16 v[180:181], v215 offset:4096\n\t"
"v_mfma_f32_16x16x32_bf16 v[92:95], v[188:191], a[252:255], v[92:95]\n\t"
"s_waitcnt lgkmcnt(5)\n\t"
"ds_read_b64_tr_b16 v[182:183], v215 offset:6144\n\t"
"v_mfma_f32_16x16x32_bf16 v[32:35], v[168:171], v[0:3], 0\n\t"
"buffer_load_dwordx4 v200, %[w1], s68 offen offset:0 lds\n\t"
"v_mfma_f32_16x16x32_bf16 v[40:43], v[168:171], v[8:11], 0\n\t"
"buffer_load_dwordx4 v201, %[w1], s68 offen offset:1024 lds\n\t"
"v_mfma_f32_16x16x32_bf16 v[36:39], v[168:171], v[4:7], 0\n\t"
"ds_read_b64_tr_b16 v[184:185], v212 offset:8192\n\t"
"v_mfma_f32_16x16x32_bf16 v[44:47], v[168:171], v[12:15], 0\n\t"
"s_waitcnt lgkmcnt(5)\n\t"
"v_exp_f32 v192, v72\n\t"
"v_mfma_f32_16x16x32_bf16 v[48:51], v[172:175], v[0:3], 0\n\t"
"v_exp_f32 v193, v73\n\t"
"v_mfma_f32_16x16x32_bf16 v[56:59], v[172:175], v[8:11], 0\n\t"
"buffer_load_dwordx4 v202, %[w1], s68 offen offset:2048 lds\n\t"
"v_mfma_f32_16x16x32_bf16 v[52:55], v[172:175], v[4:7], 0\n\t"
"s_waitcnt lgkmcnt(3)\n\t"
"ds_read_b64_tr_b16 v[186:187], v212 offset:10240\n\t"
"v_add_f32 v192, 1.0, v192\n\t"
"v_mfma_f32_16x16x32_bf16 v[60:63], v[172:175], v[12:15], 0\n\t"
"v_exp_f32 v194, v74\n\t"
"v_mfma_f32_16x16x32_bf16 v[32:35], v[176:179], v[16:19], v[32:35]\n\t"
"ds_read_b64_tr_b16 v[188:189], v215 offset:8192\n\t"
"v_add_f32 v193, 1.0, v193\n\t"
"v_mfma_f32_16x16x32_bf16 v[40:43], v[176:179], v[24:27], v[40:43]\n\t"
"v_rcp_f32 v192, v192\n\t"
"v_mfma_f32_16x16x32_bf16 v[36:39], v[176:179], v[20:23], v[36:39]\n\t"
"s_waitcnt lgkmcnt(3)\n\t"
"v_exp_f32 v195, v75\n\t"
"v_mfma_f32_16x16x32_bf16 v[44:47], v[176:179], v[28:31], v[44:47]\n\t"
"ds_read_b64_tr_b16 v[190:191], v215 offset:10240\n\t"
"v_add_f32 v194, 1.0, v194\n\t"
"v_mfma_f32_16x16x32_bf16 v[48:51], v[180:183], v[16:19], v[48:51]\n\t"
"v_rcp_f32 v193, v193\n\t"
"v_mfma_f32_16x16x32_bf16 v[56:59], v[180:183], v[24:27], v[56:59]\n\t"
"ds_read_b64_tr_b16 v[168:169], v212 offset:12288\n\t"
"v_mul_f32 v192, v72, v192\n\t"
"v_mfma_f32_16x16x32_bf16 v[52:55], v[180:183], v[20:23], v[52:55]\n\t"
"s_waitcnt lgkmcnt(3)\n\t"
"v_exp_f32 v196, v88\n\t"
"v_mfma_f32_16x16x32_bf16 v[60:63], v[180:183], v[28:31], v[60:63]\n\t"
"ds_read_b64_tr_b16 v[170:171], v212 offset:14336\n\t"
"v_add_f32 v195, 1.0, v195\n\t"
"v_mfma_f32_16x16x32_bf16 v[32:35], v[184:187], a[0:3], v[32:35]\n\t"
"v_rcp_f32 v194, v194\n\t"
"v_mfma_f32_16x16x32_bf16 v[40:43], v[184:187], a[8:11], v[40:43]\n\t"
"ds_read_b64_tr_b16 v[172:173], v215 offset:12288\n\t"
"v_mul_f32 v193, v73, v193\n\t"
"v_mfma_f32_16x16x32_bf16 v[36:39], v[184:187], a[4:7], v[36:39]\n\t"
"s_waitcnt lgkmcnt(3)\n\t"
"v_exp_f32 v197, v89\n\t"
"v_mfma_f32_16x16x32_bf16 v[44:47], v[184:187], a[12:15], v[44:47]\n\t"
"ds_read_b128 v[104:107], v243 offset:0\n\t"
"ds_read_b128 v[108:111], v243 offset:1024\n\t"
"ds_read_b64_tr_b16 v[174:175], v215 offset:14336\n\t"
"v_add_f32 v196, 1.0, v196\n\t"
"v_mfma_f32_16x16x32_bf16 v[48:51], v[188:191], a[0:3], v[48:51]\n\t"
"buffer_load_dwordx4 v203, %[w1], s68 offen offset:3072 lds\n\t"
"v_mfma_f32_16x16x32_bf16 v[56:59], v[188:191], a[8:11], v[56:59]\n\t"
"s_add_u32 m0, s63, 102400\n\t"
"v_rcp_f32 v195, v195\n\t"
"v_mfma_f32_16x16x32_bf16 v[52:55], v[188:191], a[4:7], v[52:55]\n\t"
"s_waitcnt lgkmcnt(4)\n\t"
"ds_read_b64_tr_b16 v[176:177], v212 offset:16384\n\t"
"v_mul_f32 v194, v74, v194\n\t"
"v_mfma_f32_16x16x32_bf16 v[60:63], v[188:191], a[12:15], v[60:63]\n\t"
"ds_read_b64_tr_b16 v[178:179], v212 offset:18432\n\t"
"v_cvt_pk_bf16_f32 v160, v192, v193\n\t"
"v_mfma_f32_16x16x32_bf16 v[32:35], v[168:171], a[16:19], v[32:35]\n\t"
"v_exp_f32 v198, v90\n\t"
"v_mfma_f32_16x16x32_bf16 v[40:43], v[168:171], a[24:27], v[40:43]\n\t"
"ds_read_b64_tr_b16 v[180:181], v215 offset:16384\n\t"
"v_add_f32 v197, 1.0, v197\n\t"
"v_mfma_f32_16x16x32_bf16 v[36:39], v[168:171], a[20:23], v[36:39]\n\t"
"s_waitcnt lgkmcnt(3)\n\t"
"v_rcp_f32 v196, v196\n\t"
"v_mfma_f32_16x16x32_bf16 v[44:47], v[168:171], a[28:31], v[44:47]\n\t"
"ds_read_b64_tr_b16 v[182:183], v215 offset:18432\n\t"
"v_mul_f32 v195, v75, v195\n\t"
"v_mfma_f32_16x16x32_bf16 v[48:51], v[172:175], a[16:19], v[48:51]\n\t"
"buffer_load_dwordx4 v204, %[w1], s68 offen offset:0 lds\n\t"
"v_mfma_f32_16x16x32_bf16 v[56:59], v[172:175], a[24:27], v[56:59]\n\t"
"buffer_load_dwordx4 v205, %[w1], s68 offen offset:1024 lds\n\t"
"v_mfma_f32_16x16x32_bf16 v[52:55], v[172:175], a[20:23], v[52:55]\n\t"
"s_waitcnt lgkmcnt(2)\n\t"
"v_exp_f32 v199, v91\n\t"
"v_mfma_f32_16x16x32_bf16 v[60:63], v[172:175], a[28:31], v[60:63]\n\t"
"ds_read_b64_tr_b16 v[184:185], v212 offset:20480\n\t"
"ds_read_b64_tr_b16 v[186:187], v212 offset:22528\n\t"
"v_add_f32 v198, 1.0, v198\n\t"
"v_mfma_f32_16x16x32_bf16 v[32:35], v[176:179], a[32:35], v[32:35]\n\t"
"v_rcp_f32 v197, v197\n\t"
"v_mfma_f32_16x16x32_bf16 v[40:43], v[176:179], a[40:43], v[40:43]\n\t"
"ds_read_b64_tr_b16 v[188:189], v215 offset:20480\n\t"
"v_mul_f32 v196, v88, v196\n\t"
"v_mfma_f32_16x16x32_bf16 v[36:39], v[176:179], a[36:39], v[36:39]\n\t"
"s_waitcnt lgkmcnt(3)\n\t"
"ds_read_b64_tr_b16 v[190:191], v215 offset:22528\n\t"
"v_cvt_pk_bf16_f32 v161, v194, v195\n\t"
"v_mfma_f32_16x16x32_bf16 v[44:47], v[176:179], a[44:47], v[44:47]\n\t"
"s_add_u32 s68, s46, 3538944\n\t"
"v_exp_f32 v192, v76\n\t"
"v_mfma_f32_16x16x32_bf16 v[48:51], v[180:183], a[32:35], v[48:51]\n\t"
"s_add_u32 m0, s63, 122880\n\t"
"ds_read_b64_tr_b16 v[168:169], v212 offset:24576\n\t"
"v_add_f32 v199, 1.0, v199\n\t"
"v_mfma_f32_16x16x32_bf16 v[56:59], v[180:183], a[40:43], v[56:59]\n\t"
"buffer_load_dwordx4 v200, %[w1], s68 offen offset:0 lds\n\t"
"v_mfma_f32_16x16x32_bf16 v[52:55], v[180:183], a[36:39], v[52:55]\n\t"
"s_waitcnt lgkmcnt(3)\n\t"
"v_rcp_f32 v198, v198\n\t"
"v_mfma_f32_16x16x32_bf16 v[60:63], v[180:183], a[44:47], v[60:63]\n\t"
"ds_read_b64_tr_b16 v[170:171], v212 offset:26624\n\t"
"v_mul_f32 v197, v89, v197\n\t"
"v_mfma_f32_16x16x32_bf16 v[32:35], v[184:187], a[48:51], v[32:35]\n\t"
"v_exp_f32 v193, v77\n\t"
"v_mfma_f32_16x16x32_bf16 v[40:43], v[184:187], a[56:59], v[40:43]\n\t"
"ds_read_b64_tr_b16 v[172:173], v215 offset:24576\n\t"
"v_add_f32 v192, 1.0, v192\n\t"
"v_mfma_f32_16x16x32_bf16 v[36:39], v[184:187], a[52:55], v[36:39]\n\t"
"s_waitcnt lgkmcnt(3)\n\t"
"v_rcp_f32 v199, v199\n\t"
"v_mfma_f32_16x16x32_bf16 v[44:47], v[184:187], a[60:63], v[44:47]\n\t"
"ds_read_b64_tr_b16 v[174:175], v215 offset:26624\n\t"
"v_mul_f32 v198, v90, v198\n\t"
"v_mfma_f32_16x16x32_bf16 v[48:51], v[188:191], a[48:51], v[48:51]\n\t"
"ds_read_b64_tr_b16 v[176:177], v212 offset:28672\n\t"
"v_cvt_pk_bf16_f32 v162, v196, v197\n\t"
"v_mfma_f32_16x16x32_bf16 v[56:59], v[188:191], a[56:59], v[56:59]\n\t"
"v_exp_f32 v194, v78\n\t"
"v_mfma_f32_16x16x32_bf16 v[52:55], v[188:191], a[52:55], v[52:55]\n\t"
"s_waitcnt lgkmcnt(3)\n\t"
"ds_read_b64_tr_b16 v[178:179], v212 offset:30720\n\t"
"v_add_f32 v193, 1.0, v193\n\t"
"v_mfma_f32_16x16x32_bf16 v[60:63], v[188:191], a[60:63], v[60:63]\n\t"
"v_rcp_f32 v192, v192\n\t"
"v_mfma_f32_16x16x32_bf16 v[32:35], v[168:171], a[64:67], v[32:35]\n\t"
"s_lshl_b32 s58, s45, 4\n\t"
"ds_read_b64_tr_b16 v[180:181], v215 offset:28672\n\t"
"v_mul_f32 v199, v91, v199\n\t"
"v_mfma_f32_16x16x32_bf16 v[40:43], v[168:171], a[72:75], v[40:43]\n\t"
"s_add_u32 s69, s58, s57\n\t"
"v_exp_f32 v195, v79\n\t"
"v_mfma_f32_16x16x32_bf16 v[36:39], v[168:171], a[68:71], v[36:39]\n\t"
"s_waitcnt lgkmcnt(3)\n\t"
"ds_read_b64_tr_b16 v[182:183], v215 offset:30720\n\t"
"v_add_f32 v194, 1.0, v194\n\t"
"v_mfma_f32_16x16x32_bf16 v[44:47], v[168:171], a[76:79], v[44:47]\n\t"
"v_rcp_f32 v193, v193\n\t"
"v_mfma_f32_16x16x32_bf16 v[48:51], v[172:175], a[64:67], v[48:51]\n\t"
"buffer_load_dwordx4 v201, %[w1], s68 offen offset:1024 lds\n\t"
"v_mfma_f32_16x16x32_bf16 v[56:59], v[172:175], a[72:75], v[56:59]\n\t"
"ds_read_b64_tr_b16 v[184:185], v212 offset:32768\n\t"
"v_mul_f32 v192, v76, v192\n\t"
"v_mfma_f32_16x16x32_bf16 v[52:55], v[172:175], a[68:71], v[52:55]\n\t"
"s_waitcnt lgkmcnt(3)\n\t"
"ds_read_b64_tr_b16 v[186:187], v212 offset:34816\n\t"
"v_cvt_pk_bf16_f32 v163, v198, v199\n\t"
"v_mfma_f32_16x16x32_bf16 v[60:63], v[172:175], a[76:79], v[60:63]\n\t"
"v_exp_f32 v196, v92\n\t"
"v_mfma_f32_16x16x32_bf16 v[32:35], v[176:179], a[80:83], v[32:35]\n\t"
"ds_read_b64_tr_b16 v[188:189], v215 offset:32768\n\t"
"v_add_f32 v195, 1.0, v195\n\t"
"v_mfma_f32_16x16x32_bf16 v[40:43], v[176:179], a[88:91], v[40:43]\n\t"
"v_rcp_f32 v194, v194\n\t"
"v_mfma_f32_16x16x32_bf16 v[36:39], v[176:179], a[84:87], v[36:39]\n\t"
"s_waitcnt lgkmcnt(3)\n\t"
"ds_read_b64_tr_b16 v[190:191], v215 offset:34816\n\t"
"v_mul_f32 v193, v77, v193\n\t"
"v_mfma_f32_16x16x32_bf16 v[44:47], v[176:179], a[92:95], v[44:47]\n\t"
"buffer_store_dwordx4 v[160:163], v246, %[ws], s69 offen sc1\n\t"
"ds_write_b128 v242, v[160:163] offset:8192\n\t"
"v_mfma_f32_16x16x32_bf16 v[48:51], v[180:183], a[80:83], v[48:51]\n\t"
"buffer_load_dwordx4 v202, %[w1], s68 offen offset:2048 lds\n\t"
"v_mfma_f32_16x16x32_bf16 v[56:59], v[180:183], a[88:91], v[56:59]\n\t"
"buffer_load_dwordx4 v203, %[w1], s68 offen offset:3072 lds\n\t"
"v_mfma_f32_16x16x32_bf16 v[52:55], v[180:183], a[84:87], v[52:55]\n\t"
"s_waitcnt lgkmcnt(3)\n\t"
"v_exp_f32 v197, v93\n\t"
"v_mfma_f32_16x16x32_bf16 v[60:63], v[180:183], a[92:95], v[60:63]\n\t"
"ds_read_b64_tr_b16 v[168:169], v212 offset:36864\n\t"
"ds_read_b64_tr_b16 v[170:171], v212 offset:38912\n\t"
"v_add_f32 v196, 1.0, v196\n\t"
"v_mfma_f32_16x16x32_bf16 v[32:35], v[184:187], a[96:99], v[32:35]\n\t"
"v_rcp_f32 v195, v195\n\t"
"v_mfma_f32_16x16x32_bf16 v[40:43], v[184:187], a[104:107], v[40:43]\n\t"
"s_add_u32 m0, s63, 126976\n\t"
"ds_read_b64_tr_b16 v[172:173], v215 offset:36864\n\t"
"v_mul_f32 v194, v78, v194\n\t"
"v_mfma_f32_16x16x32_bf16 v[36:39], v[184:187], a[100:103], v[36:39]\n\t"
"s_waitcnt lgkmcnt(4)\n\t"
"ds_read_b64_tr_b16 v[174:175], v215 offset:38912\n\t"
"v_cvt_pk_bf16_f32 v164, v192, v193\n\t"
"v_mfma_f32_16x16x32_bf16 v[44:47], v[184:187], a[108:111], v[44:47]\n\t"
"v_exp_f32 v198, v94\n\t"
"v_mfma_f32_16x16x32_bf16 v[48:51], v[188:191], a[96:99], v[48:51]\n\t"
"buffer_load_dwordx4 v204, %[w1], s68 offen offset:0 lds\n\t"
"v_mfma_f32_16x16x32_bf16 v[56:59], v[188:191], a[104:107], v[56:59]\n\t"
"ds_read_b64_tr_b16 v[176:177], v212 offset:40960\n\t"
"v_add_f32 v197, 1.0, v197\n\t"
"v_mfma_f32_16x16x32_bf16 v[52:55], v[188:191], a[100:103], v[52:55]\n\t"
"s_waitcnt lgkmcnt(3)\n\t"
"v_rcp_f32 v196, v196\n\t"
"v_mfma_f32_16x16x32_bf16 v[60:63], v[188:191], a[108:111], v[60:63]\n\t"
"ds_read_b64_tr_b16 v[178:179], v212 offset:43008\n\t"
"v_mul_f32 v195, v79, v195\n\t"
"v_mfma_f32_16x16x32_bf16 v[32:35], v[168:171], a[112:115], v[32:35]\n\t"
"v_exp_f32 v199, v95\n\t"
"v_mfma_f32_16x16x32_bf16 v[40:43], v[168:171], a[120:123], v[40:43]\n\t"
"ds_read_b64_tr_b16 v[180:181], v215 offset:40960\n\t"
"v_add_f32 v198, 1.0, v198\n\t"
"v_mfma_f32_16x16x32_bf16 v[36:39], v[168:171], a[116:119], v[36:39]\n\t"
"s_waitcnt lgkmcnt(3)\n\t"
"v_rcp_f32 v197, v197\n\t"
"v_mfma_f32_16x16x32_bf16 v[44:47], v[168:171], a[124:127], v[44:47]\n\t"
"ds_read_b64_tr_b16 v[182:183], v215 offset:43008\n\t"
"v_mul_f32 v196, v92, v196\n\t"
"v_mfma_f32_16x16x32_bf16 v[48:51], v[172:175], a[112:115], v[48:51]\n\t"
"buffer_load_dwordx4 v205, %[w1], s68 offen offset:1024 lds\n\t"
"v_mfma_f32_16x16x32_bf16 v[56:59], v[172:175], a[120:123], v[56:59]\n\t"
"ds_read_b64_tr_b16 v[184:185], v212 offset:45056\n\t"
"v_cvt_pk_bf16_f32 v165, v194, v195\n\t"
"v_mfma_f32_16x16x32_bf16 v[52:55], v[172:175], a[116:119], v[52:55]\n\t"
"s_waitcnt lgkmcnt(3)\n\t"
"v_exp_f32 v192, v64\n\t"
"v_mfma_f32_16x16x32_bf16 v[60:63], v[172:175], a[124:127], v[60:63]\n\t"
"ds_read_b64_tr_b16 v[186:187], v212 offset:47104\n\t"
"ds_read_b64_tr_b16 v[188:189], v215 offset:45056\n\t"
"ds_read_b64_tr_b16 v[190:191], v215 offset:47104\n\t"
"v_add_f32 v199, 1.0, v199\n\t"
"v_mfma_f32_16x16x32_bf16 v[32:35], v[176:179], a[128:131], v[32:35]\n\t"
"v_rcp_f32 v198, v198\n\t"
"v_mfma_f32_16x16x32_bf16 v[40:43], v[176:179], a[136:139], v[40:43]\n\t"
"v_mul_f32 v197, v93, v197\n\t"
"v_exp_f32 v193, v65\n\t"
"v_mfma_f32_16x16x32_bf16 v[36:39], v[176:179], a[132:135], v[36:39]\n\t"
"s_waitcnt vmcnt(14)\n\t"
"v_add_f32 v192, 1.0, v192\n\t"
"v_rcp_f32 v199, v199\n\t"
"v_mfma_f32_16x16x32_bf16 v[44:47], v[176:179], a[140:143], v[44:47]\n\t"
"s_waitcnt lgkmcnt(4)\n\t"
"v_mul_f32 v198, v94, v198\n\t"
"v_cvt_pk_bf16_f32 v166, v196, v197\n\t"
"v_mfma_f32_16x16x32_bf16 v[48:51], v[180:183], a[128:131], v[48:51]\n\t"
"s_waitcnt lgkmcnt(0)\n\t"
"v_exp_f32 v194, v66\n\t"
"v_mfma_f32_16x16x32_bf16 v[56:59], v[180:183], a[136:139], v[56:59]\n\t"
"s_barrier\n\t"
"v_add_f32 v193, 1.0, v193\n\t"
"v_rcp_f32 v192, v192\n\t"
"v_mfma_f32_16x16x32_bf16 v[52:55], v[180:183], a[132:135], v[52:55]\n\t"
"s_add_u32 s69, s69, 147456\n\t"
"ds_read_b64_tr_b16 v[168:169], v213 offset:0\n\t"
"v_mul_f32 v199, v95, v199\n\t"
"v_mfma_f32_16x16x32_bf16 v[60:63], v[180:183], a[140:143], v[60:63]\n\t"
"v_exp_f32 v195, v67\n\t"
"v_mfma_f32_16x16x32_bf16 v[32:35], v[184:187], a[144:147], v[32:35]\n\t"
"s_add_u32 m0, s63, 0\n\t"
"ds_read_b64_tr_b16 v[170:171], v213 offset:2048\n\t"
"v_add_f32 v194, 1.0, v194\n\t"
"v_mfma_f32_16x16x32_bf16 v[40:43], v[184:187], a[152:155], v[40:43]\n\t"
"v_rcp_f32 v193, v193\n\t"
"v_mfma_f32_16x16x32_bf16 v[36:39], v[184:187], a[148:151], v[36:39]\n\t"
"ds_read_b64_tr_b16 v[172:173], v216 offset:0\n\t"
"v_mul_f32 v192, v64, v192\n\t"
"v_mfma_f32_16x16x32_bf16 v[44:47], v[184:187], a[156:159], v[44:47]\n\t"
"ds_read_b64_tr_b16 v[174:175], v216 offset:2048\n\t"
"v_cvt_pk_bf16_f32 v167, v198, v199\n\t"
"v_mfma_f32_16x16x32_bf16 v[48:51], v[188:191], a[144:147], v[48:51]\n\t"
"v_exp_f32 v196, v80\n\t"
"v_mfma_f32_16x16x32_bf16 v[56:59], v[188:191], a[152:155], v[56:59]\n\t"
"ds_read_b64_tr_b16 v[176:177], v213 offset:4096\n\t"
"v_add_f32 v195, 1.0, v195\n\t"
"v_mfma_f32_16x16x32_bf16 v[52:55], v[188:191], a[148:151], v[52:55]\n\t"
"s_waitcnt lgkmcnt(3)\n\t"
"v_rcp_f32 v194, v194\n\t"
"v_mfma_f32_16x16x32_bf16 v[60:63], v[188:191], a[156:159], v[60:63]\n\t"
"ds_read_b64_tr_b16 v[178:179], v213 offset:6144\n\t"
"v_mul_f32 v193, v65, v193\n\t"
"v_mfma_f32_16x16x32_bf16 v[32:35], v[168:171], a[160:163], v[32:35]\n\t"
"buffer_load_dwordx4 v200, %[w1], s47 offen offset:0 lds\n\t"
"v_mfma_f32_16x16x32_bf16 v[40:43], v[168:171], a[168:171], v[40:43]\n\t"
"buffer_store_dwordx4 v[164:167], v246, %[ws], s69 offen sc1\n\t"
"ds_write_b128 v242, v[164:167] offset:9216\n\t"
"v_mfma_f32_16x16x32_bf16 v[36:39], v[168:171], a[164:167], v[36:39]\n\t"
"s_waitcnt lgkmcnt(3)\n\t"
"v_exp_f32 v197, v81\n\t"
"v_mfma_f32_16x16x32_bf16 v[44:47], v[168:171], a[172:175], v[44:47]\n\t"
"ds_read_b64_tr_b16 v[180:181], v216 offset:4096\n\t"
"ds_read_b64_tr_b16 v[182:183], v216 offset:6144\n\t"
"v_add_f32 v196, 1.0, v196\n\t"
"v_mfma_f32_16x16x32_bf16 v[48:51], v[172:175], a[160:163], v[48:51]\n\t"
"v_rcp_f32 v195, v195\n\t"
"v_mfma_f32_16x16x32_bf16 v[56:59], v[172:175], a[168:171], v[56:59]\n\t"
"buffer_load_dwordx4 v201, %[w1], s47 offen offset:1024 lds\n\t"
"v_mfma_f32_16x16x32_bf16 v[52:55], v[172:175], a[164:167], v[52:55]\n\t"
"s_waitcnt lgkmcnt(3)\n\t"
"ds_read_b64_tr_b16 v[184:185], v213 offset:8192\n\t"
"v_mul_f32 v194, v66, v194\n\t"
"v_mfma_f32_16x16x32_bf16 v[60:63], v[172:175], a[172:175], v[60:63]\n\t"
"ds_read_b64_tr_b16 v[186:187], v213 offset:10240\n\t"
"v_cvt_pk_bf16_f32 v112, v192, v193\n\t"
"v_mfma_f32_16x16x32_bf16 v[32:35], v[176:179], a[176:179], v[32:35]\n\t"
"v_exp_f32 v198, v82\n\t"
"v_mfma_f32_16x16x32_bf16 v[40:43], v[176:179], a[184:187], v[40:43]\n\t"
"ds_read_b64_tr_b16 v[188:189], v216 offset:8192\n\t"
"v_add_f32 v197, 1.0, v197\n\t"
"v_mfma_f32_16x16x32_bf16 v[36:39], v[176:179], a[180:183], v[36:39]\n\t"
"s_waitcnt lgkmcnt(3)\n\t"
"v_rcp_f32 v196, v196\n\t"
"v_mfma_f32_16x16x32_bf16 v[44:47], v[176:179], a[188:191], v[44:47]\n\t"
"ds_read_b64_tr_b16 v[190:191], v216 offset:10240\n\t"
"v_mul_f32 v195, v67, v195\n\t"
"v_mfma_f32_16x16x32_bf16 v[48:51], v[180:183], a[176:179], v[48:51]\n\t"
"v_exp_f32 v199, v83\n\t"
"v_mfma_f32_16x16x32_bf16 v[56:59], v[180:183], a[184:187], v[56:59]\n\t"
"ds_read_b64_tr_b16 v[168:169], v213 offset:12288\n\t"
"v_add_f32 v198, 1.0, v198\n\t"
"v_mfma_f32_16x16x32_bf16 v[52:55], v[180:183], a[180:183], v[52:55]\n\t"
"s_waitcnt lgkmcnt(3)\n\t"
"v_rcp_f32 v197, v197\n\t"
"v_mfma_f32_16x16x32_bf16 v[60:63], v[180:183], a[188:191], v[60:63]\n\t"
"ds_read_b64_tr_b16 v[170:171], v213 offset:14336\n\t"
"buffer_load_dwordx4 v202, %[w1], s47 offen offset:2048 lds\n\t"
"v_mul_f32 v196, v80, v196\n\t"
"v_mfma_f32_16x16x32_bf16 v[32:35], v[184:187], a[192:195], v[32:35]\n\t"
"ds_read_b64_tr_b16 v[172:173], v216 offset:12288\n\t"
"v_cvt_pk_bf16_f32 v113, v194, v195\n\t"
"v_mfma_f32_16x16x32_bf16 v[40:43], v[184:187], a[200:203], v[40:43]\n\t"
"v_exp_f32 v192, v68\n\t"
"v_mfma_f32_16x16x32_bf16 v[36:39], v[184:187], a[196:199], v[36:39]\n\t"
"s_waitcnt lgkmcnt(3)\n\t"
"ds_read_b64_tr_b16 v[174:175], v216 offset:14336\n\t"
"v_add_f32 v199, 1.0, v199\n\t"
"v_mfma_f32_16x16x32_bf16 v[44:47], v[184:187], a[204:207], v[44:47]\n\t"
"v_rcp_f32 v198, v198\n\t"
"v_mul_f32 v197, v81, v197\n\t"
"v_mfma_f32_16x16x32_bf16 v[48:51], v[188:191], a[192:195], v[48:51]\n\t"
"buffer_load_dwordx4 v203, %[w1], s47 offen offset:3072 lds\n\t"
"v_mfma_f32_16x16x32_bf16 v[56:59], v[188:191], a[200:203], v[56:59]\n\t"
"s_add_u32 m0, s63, 4096\n\t"
"v_exp_f32 v193, v69\n\t"
"v_add_f32 v192, 1.0, v192\n\t"
"v_mfma_f32_16x16x32_bf16 v[52:55], v[188:191], a[196:199], v[52:55]\n\t"
"s_waitcnt lgkmcnt(2)\n\t"
"v_rcp_f32 v199, v199\n\t"
"v_mfma_f32_16x16x32_bf16 v[60:63], v[188:191], a[204:207], v[60:63]\n\t"
"ds_read_b64_tr_b16 v[176:177], v213 offset:16384\n\t"
"ds_read_b64_tr_b16 v[178:179], v213 offset:18432\n\t"
"v_mul_f32 v198, v82, v198\n\t"
"v_mfma_f32_16x16x32_bf16 v[32:35], v[168:171], a[208:211], v[32:35]\n\t"
"ds_read_b64_tr_b16 v[180:181], v216 offset:16384\n\t"
"v_cvt_pk_bf16_f32 v114, v196, v197\n\t"
"v_mfma_f32_16x16x32_bf16 v[40:43], v[168:171], a[216:219], v[40:43]\n\t"
"v_exp_f32 v194, v70\n\t"
"v_mfma_f32_16x16x32_bf16 v[36:39], v[168:171], a[212:215], v[36:39]\n\t"
"s_waitcnt lgkmcnt(3)\n\t"
"ds_read_b64_tr_b16 v[182:183], v216 offset:18432\n\t"
"v_add_f32 v193, 1.0, v193\n\t"
"v_mfma_f32_16x16x32_bf16 v[44:47], v[168:171], a[220:223], v[44:47]\n\t"
"v_rcp_f32 v192, v192\n\t"
"v_mfma_f32_16x16x32_bf16 v[48:51], v[172:175], a[208:211], v[48:51]\n\t"
"s_add_u32 s69, s58, s56\n\t"
"ds_read_b64_tr_b16 v[184:185], v213 offset:20480\n\t"
"v_mul_f32 v199, v83, v199\n\t"
"v_mfma_f32_16x16x32_bf16 v[56:59], v[172:175], a[216:219], v[56:59]\n\t"
"v_exp_f32 v195, v71\n\t"
"v_mfma_f32_16x16x32_bf16 v[52:55], v[172:175], a[212:215], v[52:55]\n\t"
"buffer_load_dwordx4 v204, %[w1], s47 offen offset:0 lds\n\t"
"v_add_f32 v194, 1.0, v194\n\t"
"v_mfma_f32_16x16x32_bf16 v[60:63], v[172:175], a[220:223], v[60:63]\n\t"
"s_waitcnt lgkmcnt(3)\n\t"
"ds_read_b64_tr_b16 v[186:187], v213 offset:22528\n\t"
"v_rcp_f32 v193, v193\n\t"
"v_mfma_f32_16x16x32_bf16 v[32:35], v[176:179], a[224:227], v[32:35]\n\t"
"ds_read_b64_tr_b16 v[188:189], v216 offset:20480\n\t"
"v_mul_f32 v192, v68, v192\n\t"
"v_mfma_f32_16x16x32_bf16 v[40:43], v[176:179], a[232:235], v[40:43]\n\t"
"ds_read_b64_tr_b16 v[190:191], v216 offset:22528\n\t"
"v_cvt_pk_bf16_f32 v115, v198, v199\n\t"
"v_mfma_f32_16x16x32_bf16 v[36:39], v[176:179], a[228:231], v[36:39]\n\t"
"s_waitcnt lgkmcnt(4)\n\t"
"v_exp_f32 v196, v84\n\t"
"v_mfma_f32_16x16x32_bf16 v[44:47], v[176:179], a[236:239], v[44:47]\n\t"
"ds_read_b64_tr_b16 v[168:169], v213 offset:24576\n\t"
"v_add_f32 v195, 1.0, v195\n\t"
"v_mfma_f32_16x16x32_bf16 v[48:51], v[180:183], a[224:227], v[48:51]\n\t"
"buffer_store_dwordx4 v[112:115], v246, %[ws], s69 offen sc1\n\t"
"ds_read_b64_tr_b16 v[170:171], v213 offset:26624\n\t"
"v_rcp_f32 v194, v194\n\t"
"v_mul_f32 v193, v69, v193\n\t"
"v_mfma_f32_16x16x32_bf16 v[56:59], v[180:183], a[232:235], v[56:59]\n\t"
"buffer_load_dwordx4 v205, %[w1], s47 offen offset:1024 lds\n\t"
"v_mfma_f32_16x16x32_bf16 v[52:55], v[180:183], a[228:231], v[52:55]\n\t"
"s_add_u32 s68, s47, 1769472\n\t"
"v_exp_f32 v197, v85\n\t"
"v_mfma_f32_16x16x32_bf16 v[60:63], v[180:183], a[236:239], v[60:63]\n\t"
"s_waitcnt lgkmcnt(4)\n\t"
"ds_read_b64_tr_b16 v[172:173], v216 offset:24576\n\t"
"v_add_f32 v196, 1.0, v196\n\t"
"v_mfma_f32_16x16x32_bf16 v[32:35], v[184:187], a[240:243], v[32:35]\n\t"
"s_add_u32 m0, s63, 24576\n\t"
"v_rcp_f32 v195, v195\n\t"
"v_mfma_f32_16x16x32_bf16 v[40:43], v[184:187], a[248:251], v[40:43]\n\t"
"v_mul_f32 v194, v70, v194\n\t"
"v_cvt_pk_bf16_f32 v116, v192, v193\n\t"
"v_mfma_f32_16x16x32_bf16 v[36:39], v[184:187], a[244:247], v[36:39]\n\t"
"s_waitcnt lgkmcnt(3)\n\t"
"v_exp_f32 v198, v86\n\t"
"v_mfma_f32_16x16x32_bf16 v[44:47], v[184:187], a[252:255], v[44:47]\n\t"
"ds_read_b64_tr_b16 v[174:175], v216 offset:26624\n\t"
"v_add_f32 v197, 1.0, v197\n\t"
"v_mfma_f32_16x16x32_bf16 v[48:51], v[188:191], a[240:243], v[48:51]\n\t"
"v_rcp_f32 v196, v196\n\t"
"v_mfma_f32_16x16x32_bf16 v[56:59], v[188:191], a[248:251], v[56:59]\n\t"
"buffer_load_dwordx4 v200, %[w1], s68 offen offset:0 lds\n\t"
"v_mul_f32 v195, v71, v195\n\t"
"v_mfma_f32_16x16x32_bf16 v[52:55], v[188:191], a[244:247], v[52:55]\n\t"
"s_add_u32 s69, s69, 147456\n\t"
"v_exp_f32 v199, v87\n\t"
"v_add_f32 v198, 1.0, v198\n\t"
"v_rcp_f32 v197, v197\n\t"
"v_mul_f32 v196, v84, v196\n\t"
"v_cvt_pk_bf16_f32 v117, v194, v195\n\t"
"v_add_f32 v199, 1.0, v199\n\t"
"v_rcp_f32 v198, v198\n\t"
"v_mul_f32 v197, v85, v197\n\t"
"v_rcp_f32 v199, v199\n\t"
"v_mul_f32 v198, v86, v198\n\t"
"v_cvt_pk_bf16_f32 v118, v196, v197\n\t"
"v_mul_f32 v199, v87, v199\n\t"
"v_cvt_pk_bf16_f32 v119, v198, v199\n\t"
"buffer_store_dwordx4 v[116:119], v246, %[ws], s69 offen sc1\n\t"
"v_mfma_f32_16x16x32_bf16 v[60:63], v[188:191], a[252:255], v[60:63]\n\t"
"s_waitcnt lgkmcnt(2)\n\t"
"ds_read_b64_tr_b16 v[176:177], v213 offset:28672\n\t"
"ds_read_b64_tr_b16 v[178:179], v213 offset:30720\n\t"
"v_mfma_f32_16x16x32_bf16 v[64:67], v[168:171], v[0:3], 0\n\t"
"buffer_load_dwordx4 v201, %[w1], s68 offen offset:1024 lds\n\t"
"v_mfma_f32_16x16x32_bf16 v[72:75], v[168:171], v[8:11], 0\n\t"
"ds_read_b64_tr_b16 v[180:181], v216 offset:28672\n\t"
"v_mfma_f32_16x16x32_bf16 v[68:71], v[168:171], v[4:7], 0\n\t"
"buffer_load_dwordx4 v202, %[w1], s68 offen offset:2048 lds\n\t"
"v_mfma_f32_16x16x32_bf16 v[76:79], v[168:171], v[12:15], 0\n\t"
"s_waitcnt lgkmcnt(3)\n\t"
"ds_read_b64_tr_b16 v[182:183], v216 offset:30720\n\t"
"v_cvt_pk_bf16_f32 v160, v32, v33\n\t"
"v_mfma_f32_16x16x32_bf16 v[80:83], v[172:175], v[0:3], 0\n\t"
"ds_read_b64_tr_b16 v[184:185], v213 offset:32768\n\t"
"v_cvt_pk_bf16_f32 v161, v34, v35\n\t"
"v_mfma_f32_16x16x32_bf16 v[88:91], v[172:175], v[8:11], 0\n\t"
"ds_read_b64_tr_b16 v[186:187], v213 offset:34816\n\t"
"v_cvt_pk_bf16_f32 v162, v48, v49\n\t"
"v_mfma_f32_16x16x32_bf16 v[84:87], v[172:175], v[4:7], 0\n\t"
"s_waitcnt lgkmcnt(4)\n\t"
"ds_read_b64_tr_b16 v[188:189], v216 offset:32768\n\t"
"v_cvt_pk_bf16_f32 v163, v50, v51\n\t"
"v_mfma_f32_16x16x32_bf16 v[92:95], v[172:175], v[12:15], 0\n\t"
"buffer_store_dwordx4 v[160:163], v244, %[ws], s42 offen sc1\n\t"
"ds_read_b64_tr_b16 v[190:191], v216 offset:34816\n\t"
"ds_read_b64_tr_b16 v[168:169], v213 offset:36864\n\t"
"ds_read_b64_tr_b16 v[170:171], v213 offset:38912\n\t"
"v_mfma_f32_16x16x32_bf16 v[64:67], v[176:179], v[16:19], v[64:67]\n\t"
"s_add_u32 s69, s42, 18432\n\t"
"v_cvt_pk_bf16_f32 v164, v36, v37\n\t"
"v_cvt_pk_bf16_f32 v165, v38, v39\n\t"
"v_mfma_f32_16x16x32_bf16 v[72:75], v[176:179], v[24:27], v[72:75]\n\t"
"v_cvt_pk_bf16_f32 v166, v52, v53\n\t"
"v_cvt_pk_bf16_f32 v167, v54, v55\n\t"
"v_mfma_f32_16x16x32_bf16 v[68:71], v[176:179], v[20:23], v[68:71]\n\t"
"buffer_store_dwordx4 v[164:167], v244, %[ws], s69 offen sc1\n\t"
"ds_read_b64_tr_b16 v[172:173], v216 offset:36864\n\t"
"v_cvt_pk_bf16_f32 v192, v40, v41\n\t"
"v_cvt_pk_bf16_f32 v193, v42, v43\n\t"
"v_mfma_f32_16x16x32_bf16 v[76:79], v[176:179], v[28:31], v[76:79]\n\t"
"s_waitcnt lgkmcnt(7)\n\t"
"v_cvt_pk_bf16_f32 v194, v56, v57\n\t"
"v_cvt_pk_bf16_f32 v195, v58, v59\n\t"
"v_mfma_f32_16x16x32_bf16 v[80:83], v[180:183], v[16:19], v[80:83]\n\t"
"buffer_store_dwordx4 v[192:195], v244, %[ws], s43 offen sc1\n\t"
"ds_read_b64_tr_b16 v[174:175], v216 offset:38912\n\t"
"ds_read_b64_tr_b16 v[176:177], v213 offset:40960\n\t"
"ds_read_b64_tr_b16 v[178:179], v213 offset:43008\n\t"
"v_mfma_f32_16x16x32_bf16 v[88:91], v[180:183], v[24:27], v[88:91]\n\t"
"s_add_u32 s69, s43, 18432\n\t"
"v_cvt_pk_bf16_f32 v196, v44, v45\n\t"
"v_cvt_pk_bf16_f32 v197, v46, v47\n\t"
"v_mfma_f32_16x16x32_bf16 v[84:87], v[180:183], v[20:23], v[84:87]\n\t"
"buffer_load_dwordx4 v203, %[w1], s68 offen offset:3072 lds\n\t"
"v_mfma_f32_16x16x32_bf16 v[92:95], v[180:183], v[28:31], v[92:95]\n\t"
"s_waitcnt lgkmcnt(8)\n\t"
"ds_read_b64_tr_b16 v[180:181], v216 offset:40960\n\t"
"v_cvt_pk_bf16_f32 v198, v60, v61\n\t"
"v_mfma_f32_16x16x32_bf16 v[64:67], v[184:187], a[0:3], v[64:67]\n\t"
"ds_read_b64_tr_b16 v[182:183], v216 offset:43008\n\t"
"v_cvt_pk_bf16_f32 v199, v62, v63\n\t"
"v_mfma_f32_16x16x32_bf16 v[72:75], v[184:187], a[8:11], v[72:75]\n\t"
"s_add_u32 m0, s63, 28672\n\t"
"v_mfma_f32_16x16x32_bf16 v[68:71], v[184:187], a[4:7], v[68:71]\n\t"
"buffer_load_dwordx4 v204, %[w1], s68 offen offset:0 lds\n\t"
"v_mfma_f32_16x16x32_bf16 v[76:79], v[184:187], a[12:15], v[76:79]\n\t"
"s_waitcnt lgkmcnt(8)\n\t"
"ds_read_b64_tr_b16 v[184:185], v213 offset:45056\n\t"
"v_mfma_f32_16x16x32_bf16 v[80:83], v[188:191], a[0:3], v[80:83]\n\t"
"ds_read_b64_tr_b16 v[186:187], v213 offset:47104\n\t"
"v_mfma_f32_16x16x32_bf16 v[88:91], v[188:191], a[8:11], v[88:91]\n\t"
"v_mfma_f32_16x16x32_bf16 v[84:87], v[188:191], a[4:7], v[84:87]\n\t"
"s_waitcnt lgkmcnt(8)\n\t"
"v_mfma_f32_16x16x32_bf16 v[92:95], v[188:191], a[12:15], v[92:95]\n\t"
"ds_read_b64_tr_b16 v[188:189], v216 offset:45056\n\t"
"v_mfma_f32_16x16x32_bf16 v[64:67], v[168:171], a[16:19], v[64:67]\n\t"
"ds_read_b64_tr_b16 v[190:191], v216 offset:47104\n\t"
"v_mfma_f32_16x16x32_bf16 v[72:75], v[168:171], a[24:27], v[72:75]\n\t"
"buffer_load_dwordx4 v205, %[w1], s68 offen offset:1024 lds\n\t"
"v_mfma_f32_16x16x32_bf16 v[68:71], v[168:171], a[20:23], v[68:71]\n\t"
"s_waitcnt lgkmcnt(8)\n\t"
"v_mfma_f32_16x16x32_bf16 v[76:79], v[168:171], a[28:31], v[76:79]\n\t"
"v_mfma_f32_16x16x32_bf16 v[80:83], v[172:175], a[16:19], v[80:83]\n\t"
"v_mfma_f32_16x16x32_bf16 v[88:91], v[172:175], a[24:27], v[88:91]\n\t"
"v_mfma_f32_16x16x32_bf16 v[84:87], v[172:175], a[20:23], v[84:87]\n\t"
"v_mfma_f32_16x16x32_bf16 v[92:95], v[172:175], a[28:31], v[92:95]\n\t"
"s_waitcnt lgkmcnt(6)\n\t"
"v_mfma_f32_16x16x32_bf16 v[64:67], v[176:179], a[32:35], v[64:67]\n\t"
"v_mfma_f32_16x16x32_bf16 v[72:75], v[176:179], a[40:43], v[72:75]\n\t"
"v_mfma_f32_16x16x32_bf16 v[68:71], v[176:179], a[36:39], v[68:71]\n\t"
"v_mfma_f32_16x16x32_bf16 v[76:79], v[176:179], a[44:47], v[76:79]\n\t"
"s_waitcnt lgkmcnt(4)\n\t"
"v_mfma_f32_16x16x32_bf16 v[80:83], v[180:183], a[32:35], v[80:83]\n\t"
"s_waitcnt vmcnt(18)\n\t"
"s_waitcnt lgkmcnt(0)\n\t"
"v_mfma_f32_16x16x32_bf16 v[88:91], v[180:183], a[40:43], v[88:91]\n\t"
"s_barrier\n\t"
"v_mfma_f32_16x16x32_bf16 v[84:87], v[180:183], a[36:39], v[84:87]\n\t"
"v_mfma_f32_16x16x32_bf16 v[92:95], v[180:183], a[44:47], v[92:95]\n\t"
"v_mfma_f32_16x16x32_bf16 v[64:67], v[184:187], a[48:51], v[64:67]\n\t"
"buffer_store_dwordx4 v[196:199], v244, %[ws], s69 offen sc1\n\t"
"ds_read_b64_tr_b16 v[168:169], v214 offset:0\n\t"
"ds_read_b64_tr_b16 v[170:171], v214 offset:2048\n\t"
"v_mfma_f32_16x16x32_bf16 v[72:75], v[184:187], a[56:59], v[72:75]\n\t"
"v_mfma_f32_16x16x32_bf16 v[68:71], v[184:187], a[52:55], v[68:71]\n\t"
"ds_read_b64_tr_b16 v[172:173], v217 offset:0\n\t"
"v_mfma_f32_16x16x32_bf16 v[76:79], v[184:187], a[60:63], v[76:79]\n\t"
"ds_read_b64_tr_b16 v[174:175], v217 offset:2048\n\t"
"v_mfma_f32_16x16x32_bf16 v[80:83], v[188:191], a[48:51], v[80:83]\n\t"
"v_mfma_f32_16x16x32_bf16 v[88:91], v[188:191], a[56:59], v[88:91]\n\t"
"s_add_u32 s68, s47, 3538944\n\t"
"v_mfma_f32_16x16x32_bf16 v[84:87], v[188:191], a[52:55], v[84:87]\n\t"
"s_add_u32 m0, s63, 49152\n\t"
"ds_read_b64_tr_b16 v[176:177], v214 offset:4096\n\t"
"v_mfma_f32_16x16x32_bf16 v[92:95], v[188:191], a[60:63], v[92:95]\n\t"
"s_waitcnt lgkmcnt(3)\n\t"
"ds_read_b64_tr_b16 v[178:179], v214 offset:6144\n\t"
"v_mfma_f32_16x16x32_bf16 v[64:67], v[168:171], a[64:67], v[64:67]\n\t"
"buffer_load_dwordx4 v200, %[w1], s68 offen offset:0 lds\n\t"
"v_mfma_f32_16x16x32_bf16 v[72:75], v[168:171], a[72:75], v[72:75]\n\t"
"v_mfma_f32_16x16x32_bf16 v[68:71], v[168:171], a[68:71], v[68:71]\n\t"
"ds_read_b64_tr_b16 v[180:181], v217 offset:4096\n\t"
"v_mfma_f32_16x16x32_bf16 v[76:79], v[168:171], a[76:79], v[76:79]\n\t"
"s_waitcnt lgkmcnt(3)\n\t"
"ds_read_b64_tr_b16 v[182:183], v217 offset:6144\n\t"
"v_mfma_f32_16x16x32_bf16 v[80:83], v[172:175], a[64:67], v[80:83]\n\t"
"s_getpc_b64 s[96:97]\n\t"
"71:\n\t"
"s_and_b32 s98, %[mi], 3\n\t"
"s_lshl_b32 s98, s98, 2\n\t"
"s_add_u32 s98, s98, %[wv]\n\t"
"s_lshl_b32 s98, s98, 10\n\t"
"v_add_u32 v253, s98, v244\n\t"
"v_min_u32 v253, 97f-71b, v253\n\t"
"global_load_dword v253, v253, s[96:97]\n\t"
"v_mfma_f32_16x16x32_bf16 v[88:91], v[172:175], a[72:75], v[88:91]\n\t"
"buffer_load_dwordx4 v201, %[w1], s68 offen offset:1024 lds\n\t"
"v_mfma_f32_16x16x32_bf16 v[84:87], v[172:175], a[68:71], v[84:87]\n\t"
"ds_read_b64_tr_b16 v[184:185], v214 offset:8192\n\t"
"v_mfma_f32_16x16x32_bf16 v[92:95], v[172:175], a[76:79], v[92:95]\n\t"
"s_waitcnt lgkmcnt(3)\n\t"
"ds_read_b64_tr_b16 v[186:187], v214 offset:10240\n\t"
"v_mfma_f32_16x16x32_bf16 v[64:67], v[176:179], a[80:83], v[64:67]\n\t"
"v_mfma_f32_16x16x32_bf16 v[72:75], v[176:179], a[88:91], v[72:75]\n\t"
"v_mfma_f32_16x16x32_bf16 v[68:71], v[176:179], a[84:87], v[68:71]\n\t"
"ds_read_b64_tr_b16 v[188:189], v217 offset:8192\n\t"
"v_mfma_f32_16x16x32_bf16 v[76:79], v[176:179], a[92:95], v[76:79]\n\t"
"s_waitcnt lgkmcnt(3)\n\t"
"ds_read_b64_tr_b16 v[190:191], v217 offset:10240\n\t"
"v_mfma_f32_16x16x32_bf16 v[80:83], v[180:183], a[80:83], v[80:83]\n\t"
"v_mfma_f32_16x16x32_bf16 v[88:91], v[180:183], a[88:91], v[88:91]\n\t"
"buffer_load_dwordx4 v202, %[w1], s68 offen offset:2048 lds\n\t"
"v_mfma_f32_16x16x32_bf16 v[84:87], v[180:183], a[84:87], v[84:87]\n\t"
"ds_read_b64_tr_b16 v[168:169], v214 offset:12288\n\t"
"v_mfma_f32_16x16x32_bf16 v[92:95], v[180:183], a[92:95], v[92:95]\n\t"
"s_waitcnt lgkmcnt(3)\n\t"
"ds_read_b64_tr_b16 v[170:171], v214 offset:14336\n\t"
"v_mfma_f32_16x16x32_bf16 v[64:67], v[184:187], a[96:99], v[64:67]\n\t"
"ds_read_b64_tr_b16 v[172:173], v217 offset:12288\n\t"
"v_mfma_f32_16x16x32_bf16 v[72:75], v[184:187], a[104:107], v[72:75]\n\t"
"ds_read_b64_tr_b16 v[174:175], v217 offset:14336\n\t"
"v_mfma_f32_16x16x32_bf16 v[68:71], v[184:187], a[100:103], v[68:71]\n\t"
"ds_read_b128 v[120:123], v243 offset:8192\n\t"
"v_mfma_f32_16x16x32_bf16 v[76:79], v[184:187], a[108:111], v[76:79]\n\t"
"s_waitcnt lgkmcnt(5)\n\t"
"ds_read_b128 v[124:127], v243 offset:9216\n\t"
"v_mfma_f32_16x16x32_bf16 v[80:83], v[188:191], a[96:99], v[80:83]\n\t"
"v_mfma_f32_16x16x32_bf16 v[88:91], v[188:191], a[104:107], v[88:91]\n\t"
"buffer_load_dwordx4 v203, %[w1], s68 offen offset:3072 lds\n\t"
"v_mfma_f32_16x16x32_bf16 v[84:87], v[188:191], a[100:103], v[84:87]\n\t"
"ds_read_b64_tr_b16 v[176:177], v214 offset:16384\n\t"
"v_mfma_f32_16x16x32_bf16 v[92:95], v[188:191], a[108:111], v[92:95]\n\t"
"s_waitcnt lgkmcnt(5)\n\t"
"ds_read_b64_tr_b16 v[178:179], v214 offset:18432\n\t"
"v_mfma_f32_16x16x32_bf16 v[64:67], v[168:171], a[112:115], v[64:67]\n\t"
"v_mfma_f32_16x16x32_bf16 v[72:75], v[168:171], a[120:123], v[72:75]\n\t"
"v_mfma_f32_16x16x32_bf16 v[68:71], v[168:171], a[116:119], v[68:71]\n\t"
"ds_read_b64_tr_b16 v[180:181], v217 offset:16384\n\t"
"v_mfma_f32_16x16x32_bf16 v[76:79], v[168:171], a[124:127], v[76:79]\n\t"
"s_waitcnt lgkmcnt(5)\n\t"
"ds_read_b64_tr_b16 v[182:183], v217 offset:18432\n\t"
"v_mfma_f32_16x16x32_bf16 v[80:83], v[172:175], a[112:115], v[80:83]\n\t"
"s_add_u32 m0, s63, 53248\n\t"
"v_mfma_f32_16x16x32_bf16 v[88:91], v[172:175], a[120:123], v[88:91]\n\t"
"buffer_load_dwordx4 v204, %[w1], s68 offen offset:0 lds\n\t"
"v_mfma_f32_16x16x32_bf16 v[84:87], v[172:175], a[116:119], v[84:87]\n\t"
"ds_read_b64_tr_b16 v[184:185], v214 offset:20480\n\t"
"v_mfma_f32_16x16x32_bf16 v[92:95], v[172:175], a[124:127], v[92:95]\n\t"
"s_waitcnt lgkmcnt(3)\n\t"
"ds_read_b64_tr_b16 v[186:187], v214 offset:22528\n\t"
"v_mfma_f32_16x16x32_bf16 v[64:67], v[176:179], a[128:131], v[64:67]\n\t"
"v_mfma_f32_16x16x32_bf16 v[72:75], v[176:179], a[136:139], v[72:75]\n\t"
"v_mfma_f32_16x16x32_bf16 v[68:71], v[176:179], a[132:135], v[68:71]\n\t"
"ds_read_b64_tr_b16 v[188:189], v217 offset:20480\n\t"
"v_mfma_f32_16x16x32_bf16 v[76:79], v[176:179], a[140:143], v[76:79]\n\t"
"s_waitcnt lgkmcnt(3)\n\t"
"ds_read_b64_tr_b16 v[190:191], v217 offset:22528\n\t"
"v_mfma_f32_16x16x32_bf16 v[80:83], v[180:183], a[128:131], v[80:83]\n\t"
"v_mfma_f32_16x16x32_bf16 v[88:91], v[180:183], a[136:139], v[88:91]\n\t"
"buffer_load_dwordx4 v205, %[w1], s68 offen offset:1024 lds\n\t"
"v_mfma_f32_16x16x32_bf16 v[84:87], v[180:183], a[132:135], v[84:87]\n\t"
"ds_read_b64_tr_b16 v[168:169], v214 offset:24576\n\t"
"v_mfma_f32_16x16x32_bf16 v[92:95], v[180:183], a[140:143], v[92:95]\n\t"
"s_waitcnt lgkmcnt(3)\n\t"
"ds_read_b64_tr_b16 v[170:171], v214 offset:26624\n\t"
"v_mfma_f32_16x16x32_bf16 v[64:67], v[184:187], a[144:147], v[64:67]\n\t"
"v_mfma_f32_16x16x32_bf16 v[72:75], v[184:187], a[152:155], v[72:75]\n\t"
"v_mfma_f32_16x16x32_bf16 v[68:71], v[184:187], a[148:151], v[68:71]\n\t"
"ds_read_b64_tr_b16 v[172:173], v217 offset:24576\n\t"
"v_mfma_f32_16x16x32_bf16 v[76:79], v[184:187], a[156:159], v[76:79]\n\t"
"s_waitcnt lgkmcnt(3)\n\t"
"ds_read_b64_tr_b16 v[174:175], v217 offset:26624\n\t"
"v_mfma_f32_16x16x32_bf16 v[80:83], v[188:191], a[144:147], v[80:83]\n\t"
"s_add_u32 m0, s63, 73728\n\t"
"v_mfma_f32_16x16x32_bf16 v[88:91], v[188:191], a[152:155], v[88:91]\n\t"
"buffer_load_dwordx4 v200, %[w1], s48 offen offset:0 lds\n\t"
"v_mfma_f32_16x16x32_bf16 v[84:87], v[188:191], a[148:151], v[84:87]\n\t"
"ds_read_b64_tr_b16 v[176:177], v214 offset:28672\n\t"
"v_mfma_f32_16x16x32_bf16 v[92:95], v[188:191], a[156:159], v[92:95]\n\t"
"s_waitcnt lgkmcnt(3)\n\t"
"ds_read_b64_tr_b16 v[178:179], v214 offset:30720\n\t"
"v_mfma_f32_16x16x32_bf16 v[64:67], v[168:171], a[160:163], v[64:67]\n\t"
"v_mfma_f32_16x16x32_bf16 v[72:75], v[168:171], a[168:171], v[72:75]\n\t"
"v_mfma_f32_16x16x32_bf16 v[68:71], v[168:171], a[164:167], v[68:71]\n\t"
"ds_read_b64_tr_b16 v[180:181], v217 offset:28672\n\t"
"v_mfma_f32_16x16x32_bf16 v[76:79], v[168:171], a[172:175], v[76:79]\n\t"
"s_waitcnt lgkmcnt(3)\n\t"
"ds_read_b64_tr_b16 v[182:183], v217 offset:30720\n\t"
"v_mfma_f32_16x16x32_bf16 v[80:83], v[172:175], a[160:163], v[80:83]\n\t"
"v_mfma_f32_16x16x32_bf16 v[88:91], v[172:175], a[168:171], v[88:91]\n\t"
"buffer_load_dwordx4 v201, %[w1], s48 offen offset:1024 lds\n\t"
"v_mfma_f32_16x16x32_bf16 v[84:87], v[172:175], a[164:167], v[84:87]\n\t"
"ds_read_b64_tr_b16 v[184:185], v214 offset:32768\n\t"
"v_mfma_f32_16x16x32_bf16 v[92:95], v[172:175], a[172:175], v[92:95]\n\t"
"s_waitcnt lgkmcnt(3)\n\t"
"ds_read_b64_tr_b16 v[186:187], v214 offset:34816\n\t"
"v_mfma_f32_16x16x32_bf16 v[64:67], v[176:179], a[176:179], v[64:67]\n\t"
"v_mfma_f32_16x16x32_bf16 v[72:75], v[176:179], a[184:187], v[72:75]\n\t"
"v_mfma_f32_16x16x32_bf16 v[68:71], v[176:179], a[180:183], v[68:71]\n\t"
"ds_read_b64_tr_b16 v[188:189], v217 offset:32768\n\t"
"v_mfma_f32_16x16x32_bf16 v[76:79], v[176:179], a[188:191], v[76:79]\n\t"
"s_waitcnt lgkmcnt(3)\n\t"
"ds_read_b64_tr_b16 v[190:191], v217 offset:34816\n\t"
"v_mfma_f32_16x16x32_bf16 v[80:83], v[180:183], a[176:179], v[80:83]\n\t"
"buffer_load_dwordx4 v202, %[w1], s48 offen offset:2048 lds\n\t"
"v_mfma_f32_16x16x32_bf16 v[88:91], v[180:183], a[184:187], v[88:91]\n\t"
"v_mfma_f32_16x16x32_bf16 v[84:87], v[180:183], a[180:183], v[84:87]\n\t"
"ds_read_b64_tr_b16 v[168:169], v214 offset:36864\n\t"
"v_mfma_f32_16x16x32_bf16 v[92:95], v[180:183], a[188:191], v[92:95]\n\t"
"s_waitcnt lgkmcnt(3)\n\t"
"ds_read_b64_tr_b16 v[170:171], v214 offset:38912\n\t"
"v_mfma_f32_16x16x32_bf16 v[64:67], v[184:187], a[192:195], v[64:67]\n\t"
"v_mfma_f32_16x16x32_bf16 v[72:75], v[184:187], a[200:203], v[72:75]\n\t"
"v_mfma_f32_16x16x32_bf16 v[68:71], v[184:187], a[196:199], v[68:71]\n\t"
"ds_read_b64_tr_b16 v[172:173], v217 offset:36864\n\t"
"v_mfma_f32_16x16x32_bf16 v[76:79], v[184:187], a[204:207], v[76:79]\n\t"
"s_waitcnt lgkmcnt(3)\n\t"
"ds_read_b64_tr_b16 v[174:175], v217 offset:38912\n\t"
"v_mfma_f32_16x16x32_bf16 v[80:83], v[188:191], a[192:195], v[80:83]\n\t"
"buffer_load_dwordx4 v203, %[w1], s48 offen offset:3072 lds\n\t"
"v_mfma_f32_16x16x32_bf16 v[88:91], v[188:191], a[200:203], v[88:91]\n\t"
"v_mfma_f32_16x16x32_bf16 v[84:87], v[188:191], a[196:199], v[84:87]\n\t"
"ds_read_b64_tr_b16 v[176:177], v214 offset:40960\n\t"
"v_mfma_f32_16x16x32_bf16 v[92:95], v[188:191], a[204:207], v[92:95]\n\t"
"s_waitcnt lgkmcnt(3)\n\t"
"ds_read_b64_tr_b16 v[178:179], v214 offset:43008\n\t"
"v_mfma_f32_16x16x32_bf16 v[64:67], v[168:171], a[208:211], v[64:67]\n\t"
"s_add_u32 m0, s63, 77824\n\t"
"v_mfma_f32_16x16x32_bf16 v[72:75], v[168:171], a[216:219], v[72:75]\n\t"
"buffer_load_dwordx4 v204, %[w1], s48 offen offset:0 lds\n\t"
"v_mfma_f32_16x16x32_bf16 v[68:71], v[168:171], a[212:215], v[68:71]\n\t"
"ds_read_b64_tr_b16 v[180:181], v217 offset:40960\n\t"
"v_mfma_f32_16x16x32_bf16 v[76:79], v[168:171], a[220:223], v[76:79]\n\t"
"s_waitcnt lgkmcnt(3)\n\t"
"ds_read_b64_tr_b16 v[182:183], v217 offset:43008\n\t"
"v_mfma_f32_16x16x32_bf16 v[80:83], v[172:175], a[208:211], v[80:83]\n\t"
"ds_read_b64_tr_b16 v[184:185], v214 offset:45056\n\t"
"v_mfma_f32_16x16x32_bf16 v[88:91], v[172:175], a[216:219], v[88:91]\n\t"
"ds_read_b64_tr_b16 v[186:187], v214 offset:47104\n\t"
"v_mfma_f32_16x16x32_bf16 v[84:87], v[172:175], a[212:215], v[84:87]\n\t"
"ds_read_b64_tr_b16 v[188:189], v217 offset:45056\n\t"
"v_mfma_f32_16x16x32_bf16 v[92:95], v[172:175], a[220:223], v[92:95]\n\t"
"s_waitcnt lgkmcnt(5)\n\t"
"ds_read_b64_tr_b16 v[190:191], v217 offset:47104\n\t"
"v_mfma_f32_16x16x32_bf16 v[64:67], v[176:179], a[224:227], v[64:67]\n\t"
"v_mfma_f32_16x16x32_bf16 v[72:75], v[176:179], a[232:235], v[72:75]\n\t"
"buffer_load_dwordx4 v205, %[w1], s48 offen offset:1024 lds\n\t"
"v_mfma_f32_16x16x32_bf16 v[68:71], v[176:179], a[228:231], v[68:71]\n\t"
"v_mfma_f32_16x16x32_bf16 v[76:79], v[176:179], a[236:239], v[76:79]\n\t"
"s_waitcnt lgkmcnt(4)\n\t"
"v_mfma_f32_16x16x32_bf16 v[80:83], v[180:183], a[224:227], v[80:83]\n\t"
"s_waitcnt vmcnt(13)\n\t"
"s_waitcnt lgkmcnt(0)\n\t"
"v_mfma_f32_16x16x32_bf16 v[88:91], v[180:183], a[232:235], v[88:91]\n\t"
"s_barrier\n\t"
"v_mfma_f32_16x16x32_bf16 v[84:87], v[180:183], a[228:231], v[84:87]\n\t"
"s_add_u32 s68, s48, 1769472\n\t"
"ds_read_b64_tr_b16 v[168:169], v212 offset:0\n\t"
"v_mfma_f32_16x16x32_bf16 v[92:95], v[180:183], a[236:239], v[92:95]\n\t"
"ds_read_b64_tr_b16 v[170:171], v212 offset:2048\n\t"
"v_mfma_f32_16x16x32_bf16 v[64:67], v[184:187], a[240:243], v[64:67]\n\t"
"s_add_u32 m0, s63, 98304\n\t"
"ds_read_b64_tr_b16 v[172:173], v215 offset:0\n\t"
"v_mfma_f32_16x16x32_bf16 v[72:75], v[184:187], a[248:251], v[72:75]\n\t"
"buffer_load_dwordx4 v200, %[w1], s68 offen offset:0 lds\n\t"
"v_mfma_f32_16x16x32_bf16 v[68:71], v[184:187], a[244:247], v[68:71]\n\t"
"buffer_store_dwordx2 v[254:255], v252, %[ws], s79 offen offset:128 sc1\n\t"
"ds_read_b64_tr_b16 v[174:175], v215 offset:2048\n\t"
"v_mfma_f32_16x16x32_bf16 v[76:79], v[184:187], a[252:255], v[76:79]\n\t"
"ds_read_b64_tr_b16 v[176:177], v212 offset:4096\n\t"
"v_mfma_f32_16x16x32_bf16 v[80:83], v[188:191], a[240:243], v[80:83]\n\t"
"ds_read_b64_tr_b16 v[178:179], v212 offset:6144\n\t"
"v_mfma_f32_16x16x32_bf16 v[88:91], v[188:191], a[248:251], v[88:91]\n\t"
"buffer_load_dwordx4 v201, %[w1], s68 offen offset:1024 lds\n\t"
"v_mfma_f32_16x16x32_bf16 v[84:87], v[188:191], a[244:247], v[84:87]\n\t"
"ds_read_b64_tr_b16 v[180:181], v215 offset:4096\n\t"
"v_mfma_f32_16x16x32_bf16 v[92:95], v[188:191], a[252:255], v[92:95]\n\t"
"s_waitcnt lgkmcnt(5)\n\t"
"ds_read_b64_tr_b16 v[182:183], v215 offset:6144\n\t"
"v_mfma_f32_16x16x32_bf16 v[32:35], v[168:171], v[0:3], 0\n\t"
"ds_read_b64_tr_b16 v[184:185], v212 offset:8192\n\t"
"v_mfma_f32_16x16x32_bf16 v[40:43], v[168:171], v[8:11], 0\n\t"
"ds_read_b64_tr_b16 v[186:187], v212 offset:10240\n\t"
"v_mfma_f32_16x16x32_bf16 v[36:39], v[168:171], v[4:7], 0\n\t"
"ds_read_b64_tr_b16 v[188:189], v215 offset:8192\n\t"
"v_mfma_f32_16x16x32_bf16 v[44:47], v[168:171], v[12:15], 0\n\t"
"s_waitcnt lgkmcnt(7)\n\t"
"v_exp_f32 v192, v72\n\t"
"v_mfma_f32_16x16x32_bf16 v[48:51], v[172:175], v[0:3], 0\n\t"
"v_exp_f32 v193, v73\n\t"
"v_mfma_f32_16x16x32_bf16 v[56:59], v[172:175], v[8:11], 0\n\t"
"buffer_load_dwordx4 v202, %[w1], s68 offen offset:2048 lds\n\t"
"v_mfma_f32_16x16x32_bf16 v[52:55], v[172:175], v[4:7], 0\n\t"
"s_waitcnt lgkmcnt(5)\n\t"
"ds_read_b64_tr_b16 v[190:191], v215 offset:10240\n\t"
"v_add_f32 v192, 1.0, v192\n\t"
"v_mfma_f32_16x16x32_bf16 v[60:63], v[172:175], v[12:15], 0\n\t"
"v_exp_f32 v194, v74\n\t"
"v_mfma_f32_16x16x32_bf16 v[32:35], v[176:179], v[16:19], v[32:35]\n\t"
"ds_read_b64_tr_b16 v[168:169], v212 offset:12288\n\t"
"v_add_f32 v193, 1.0, v193\n\t"
"v_mfma_f32_16x16x32_bf16 v[40:43], v[176:179], v[24:27], v[40:43]\n\t"
"v_rcp_f32 v192, v192\n\t"
"v_mfma_f32_16x16x32_bf16 v[36:39], v[176:179], v[20:23], v[36:39]\n\t"
"s_waitcnt lgkmcnt(5)\n\t"
"v_exp_f32 v195, v75\n\t"
"v_mfma_f32_16x16x32_bf16 v[44:47], v[176:179], v[28:31], v[44:47]\n\t"
"ds_read_b64_tr_b16 v[170:171], v212 offset:14336\n\t"
"v_add_f32 v194, 1.0, v194\n\t"
"v_mfma_f32_16x16x32_bf16 v[48:51], v[180:183], v[16:19], v[48:51]\n\t"
"v_rcp_f32 v193, v193\n\t"
"v_mfma_f32_16x16x32_bf16 v[56:59], v[180:183], v[24:27], v[56:59]\n\t"
"buffer_load_dwordx4 v203, %[w1], s68 offen offset:3072 lds\n\t"
"v_mfma_f32_16x16x32_bf16 v[52:55], v[180:183], v[20:23], v[52:55]\n\t"
"s_waitcnt lgkmcnt(4)\n\t"
"ds_read_b64_tr_b16 v[172:173], v215 offset:12288\n\t"
"v_mul_f32 v192, v72, v192\n\t"
"v_mfma_f32_16x16x32_bf16 v[60:63], v[180:183], v[28:31], v[60:63]\n\t"
"v_exp_f32 v196, v88\n\t"
"v_mfma_f32_16x16x32_bf16 v[32:35], v[184:187], a[0:3], v[32:35]\n\t"
"ds_read_b64_tr_b16 v[174:175], v215 offset:14336\n\t"
"v_add_f32 v195, 1.0, v195\n\t"
"v_mfma_f32_16x16x32_bf16 v[40:43], v[184:187], a[8:11], v[40:43]\n\t"
"v_rcp_f32 v194, v194\n\t"
"v_mfma_f32_16x16x32_bf16 v[36:39], v[184:187], a[4:7], v[36:39]\n\t"
"s_waitcnt lgkmcnt(4)\n\t"
"ds_read_b64_tr_b16 v[176:177], v212 offset:16384\n\t"
"v_mul_f32 v193, v73, v193\n\t"
"v_mfma_f32_16x16x32_bf16 v[44:47], v[184:187], a[12:15], v[44:47]\n\t"
"v_exp_f32 v197, v89\n\t"
"v_mfma_f32_16x16x32_bf16 v[48:51], v[188:191], a[0:3], v[48:51]\n\t"
"s_add_u32 m0, s63, 102400\n\t"
"ds_read_b64_tr_b16 v[178:179], v212 offset:18432\n\t"
"v_add_f32 v196, 1.0, v196\n\t"
"v_mfma_f32_16x16x32_bf16 v[56:59], v[188:191], a[8:11], v[56:59]\n\t"
"v_rcp_f32 v195, v195\n\t"
"v_mfma_f32_16x16x32_bf16 v[52:55], v[188:191], a[4:7], v[52:55]\n\t"
"s_waitcnt lgkmcnt(4)\n\t"
"ds_read_b64_tr_b16 v[180:181], v215 offset:16384\n\t"
"v_mul_f32 v194, v74, v194\n\t"
"v_mfma_f32_16x16x32_bf16 v[60:63], v[188:191], a[12:15], v[60:63]\n\t"
"ds_read_b64_tr_b16 v[182:183], v215 offset:18432\n\t"
"v_cvt_pk_bf16_f32 v160, v192, v193\n\t"
"v_mfma_f32_16x16x32_bf16 v[32:35], v[168:171], a[16:19], v[32:35]\n\t"
"v_exp_f32 v198, v90\n\t"
"v_mfma_f32_16x16x32_bf16 v[40:43], v[168:171], a[24:27], v[40:43]\n\t"
"buffer_load_dwordx4 v204, %[w1], s68 offen offset:0 lds\n\t"
"v_mfma_f32_16x16x32_bf16 v[36:39], v[168:171], a[20:23], v[36:39]\n\t"
"s_waitcnt lgkmcnt(4)\n\t"
"ds_read_b64_tr_b16 v[184:185], v212 offset:20480\n\t"
"v_add_f32 v197, 1.0, v197\n\t"
"v_mfma_f32_16x16x32_bf16 v[44:47], v[168:171], a[28:31], v[44:47]\n\t"
"v_rcp_f32 v196, v196\n\t"
"v_mfma_f32_16x16x32_bf16 v[48:51], v[172:175], a[16:19], v[48:51]\n\t"
"ds_read_b64_tr_b16 v[186:187], v212 offset:22528\n\t"
"v_mul_f32 v195, v75, v195\n\t"
"v_mfma_f32_16x16x32_bf16 v[56:59], v[172:175], a[24:27], v[56:59]\n\t"
"buffer_load_dwordx4 v205, %[w1], s68 offen offset:1024 lds\n\t"
"v_mfma_f32_16x16x32_bf16 v[52:55], v[172:175], a[20:23], v[52:55]\n\t"
"s_waitcnt lgkmcnt(4)\n\t"
"v_exp_f32 v199, v91\n\t"
"v_mfma_f32_16x16x32_bf16 v[60:63], v[172:175], a[28:31], v[60:63]\n\t"
"ds_read_b64_tr_b16 v[188:189], v215 offset:20480\n\t"
"v_add_f32 v198, 1.0, v198\n\t"
"v_mfma_f32_16x16x32_bf16 v[32:35], v[176:179], a[32:35], v[32:35]\n\t"
"v_rcp_f32 v197, v197\n\t"
"v_mfma_f32_16x16x32_bf16 v[40:43], v[176:179], a[40:43], v[40:43]\n\t"
"ds_read_b64_tr_b16 v[190:191], v215 offset:22528\n\t"
"v_mul_f32 v196, v88, v196\n\t"
"v_mfma_f32_16x16x32_bf16 v[36:39], v[176:179], a[36:39], v[36:39]\n\t"
"s_waitcnt lgkmcnt(4)\n\t"
"ds_read_b64_tr_b16 v[168:169], v212 offset:24576\n\t"
"v_cvt_pk_bf16_f32 v161, v194, v195\n\t"
"v_mfma_f32_16x16x32_bf16 v[44:47], v[176:179], a[44:47], v[44:47]\n\t"
"s_add_u32 s68, s48, 3538944\n\t"
"v_exp_f32 v192, v76\n\t"
"v_mfma_f32_16x16x32_bf16 v[48:51], v[180:183], a[32:35], v[48:51]\n\t"
"s_add_u32 m0, s63, 122880\n\t"
"ds_read_b64_tr_b16 v[170:171], v212 offset:26624\n\t"
"v_add_f32 v199, 1.0, v199\n\t"
"v_mfma_f32_16x16x32_bf16 v[56:59], v[180:183], a[40:43], v[56:59]\n\t"
"buffer_load_dwordx4 v200, %[w1], s68 offen offset:0 lds\n\t"
"v_mfma_f32_16x16x32_bf16 v[52:55], v[180:183], a[36:39], v[52:55]\n\t"
"s_waitcnt lgkmcnt(4)\n\t"
"v_rcp_f32 v198, v198\n\t"
"v_mfma_f32_16x16x32_bf16 v[60:63], v[180:183], a[44:47], v[60:63]\n\t"
"ds_read_b64_tr_b16 v[172:173], v215 offset:24576\n\t"
"v_mul_f32 v197, v89, v197\n\t"
"v_mfma_f32_16x16x32_bf16 v[32:35], v[184:187], a[48:51], v[32:35]\n\t"
"v_exp_f32 v193, v77\n\t"
"v_mfma_f32_16x16x32_bf16 v[40:43], v[184:187], a[56:59], v[40:43]\n\t"
"ds_read_b64_tr_b16 v[174:175], v215 offset:26624\n\t"
"v_add_f32 v192, 1.0, v192\n\t"
"v_mfma_f32_16x16x32_bf16 v[36:39], v[184:187], a[52:55], v[36:39]\n\t"
"s_waitcnt lgkmcnt(4)\n\t"
"v_rcp_f32 v199, v199\n\t"
"v_mfma_f32_16x16x32_bf16 v[44:47], v[184:187], a[60:63], v[44:47]\n\t"
"ds_read_b64_tr_b16 v[176:177], v212 offset:28672\n\t"
"v_mul_f32 v198, v90, v198\n\t"
"v_mfma_f32_16x16x32_bf16 v[48:51], v[188:191], a[48:51], v[48:51]\n\t"
"ds_read_b64_tr_b16 v[178:179], v212 offset:30720\n\t"
"v_cvt_pk_bf16_f32 v162, v196, v197\n\t"
"v_mfma_f32_16x16x32_bf16 v[56:59], v[188:191], a[56:59], v[56:59]\n\t"
"v_exp_f32 v194, v78\n\t"
"v_mfma_f32_16x16x32_bf16 v[52:55], v[188:191], a[52:55], v[52:55]\n\t"
"s_waitcnt lgkmcnt(4)\n\t"
"ds_read_b64_tr_b16 v[180:181], v215 offset:28672\n\t"
"v_add_f32 v193, 1.0, v193\n\t"
"v_mfma_f32_16x16x32_bf16 v[60:63], v[188:191], a[60:63], v[60:63]\n\t"
"v_rcp_f32 v192, v192\n\t"
"v_mfma_f32_16x16x32_bf16 v[32:35], v[168:171], a[64:67], v[32:35]\n\t"
"s_lshl_b32 s58, s46, 4\n\t"
"ds_read_b64_tr_b16 v[182:183], v215 offset:30720\n\t"
"v_mul_f32 v199, v91, v199\n\t"
"v_mfma_f32_16x16x32_bf16 v[40:43], v[168:171], a[72:75], v[40:43]\n\t"
"s_add_u32 s69, s58, s57\n\t"
"v_exp_f32 v195, v79\n\t"
"v_mfma_f32_16x16x32_bf16 v[36:39], v[168:171], a[68:71], v[36:39]\n\t"
"s_waitcnt lgkmcnt(4)\n\t"
"ds_read_b64_tr_b16 v[184:185], v212 offset:32768\n\t"
"v_add_f32 v194, 1.0, v194\n\t"
"v_mfma_f32_16x16x32_bf16 v[44:47], v[168:171], a[76:79], v[44:47]\n\t"
"buffer_load_dwordx4 v201, %[w1], s68 offen offset:1024 lds\n\t"
"v_mfma_f32_16x16x32_bf16 v[48:51], v[172:175], a[64:67], v[48:51]\n\t"
"v_rcp_f32 v193, v193\n\t"
"v_mfma_f32_16x16x32_bf16 v[56:59], v[172:175], a[72:75], v[56:59]\n\t"
"ds_read_b64_tr_b16 v[186:187], v212 offset:34816\n\t"
"v_mul_f32 v192, v76, v192\n\t"
"v_mfma_f32_16x16x32_bf16 v[52:55], v[172:175], a[68:71], v[52:55]\n\t"
"s_waitcnt lgkmcnt(4)\n\t"
"ds_read_b64_tr_b16 v[188:189], v215 offset:32768\n\t"
"v_cvt_pk_bf16_f32 v163, v198, v199\n\t"
"v_mfma_f32_16x16x32_bf16 v[60:63], v[172:175], a[76:79], v[60:63]\n\t"
"v_exp_f32 v196, v92\n\t"
"v_mfma_f32_16x16x32_bf16 v[32:35], v[176:179], a[80:83], v[32:35]\n\t"
"ds_read_b64_tr_b16 v[190:191], v215 offset:34816\n\t"
"v_add_f32 v195, 1.0, v195\n\t"
"v_mfma_f32_16x16x32_bf16 v[40:43], v[176:179], a[88:91], v[40:43]\n\t"
"v_rcp_f32 v194, v194\n\t"
"v_mfma_f32_16x16x32_bf16 v[36:39], v[176:179], a[84:87], v[36:39]\n\t"
"s_waitcnt lgkmcnt(4)\n\t"
"ds_read_b64_tr_b16 v[168:169], v212 offset:36864\n\t"
"v_mul_f32 v193, v77, v193\n\t"
"v_mfma_f32_16x16x32_bf16 v[44:47], v[176:179], a[92:95], v[44:47]\n\t"
"buffer_store_dwordx4 v[160:163], v246, %[ws], s69 offen sc1\n\t"
"ds_write_b128 v242, v[160:163] offset:0\n\t"
"v_mfma_f32_16x16x32_bf16 v[48:51], v[180:183], a[80:83], v[48:51]\n\t"
"buffer_load_dwordx4 v202, %[w1], s68 offen offset:2048 lds\n\t"
"v_mfma_f32_16x16x32_bf16 v[56:59], v[180:183], a[88:91], v[56:59]\n\t"
"buffer_load_dwordx4 v203, %[w1], s68 offen offset:3072 lds\n\t"
"v_mfma_f32_16x16x32_bf16 v[52:55], v[180:183], a[84:87], v[52:55]\n\t"
"s_waitcnt lgkmcnt(4)\n\t"
"v_exp_f32 v197, v93\n\t"
"v_mfma_f32_16x16x32_bf16 v[60:63], v[180:183], a[92:95], v[60:63]\n\t"
"ds_read_b64_tr_b16 v[170:171], v212 offset:38912\n\t"
"v_add_f32 v196, 1.0, v196\n\t"
"v_mfma_f32_16x16x32_bf16 v[32:35], v[184:187], a[96:99], v[32:35]\n\t"
"v_rcp_f32 v195, v195\n\t"
"v_mfma_f32_16x16x32_bf16 v[40:43], v[184:187], a[104:107], v[40:43]\n\t"
"s_add_u32 m0, s63, 126976\n\t"
"ds_read_b64_tr_b16 v[172:173], v215 offset:36864\n\t"
"v_mul_f32 v194, v78, v194\n\t"
"v_mfma_f32_16x16x32_bf16 v[36:39], v[184:187], a[100:103], v[36:39]\n\t"
"s_waitcnt lgkmcnt(4)\n\t"
"ds_read_b64_tr_b16 v[174:175], v215 offset:38912\n\t"
"v_cvt_pk_bf16_f32 v164, v192, v193\n\t"
"v_mfma_f32_16x16x32_bf16 v[44:47], v[184:187], a[108:111], v[44:47]\n\t"
"v_exp_f32 v198, v94\n\t"
"v_mfma_f32_16x16x32_bf16 v[48:51], v[188:191], a[96:99], v[48:51]\n\t"
"buffer_load_dwordx4 v204, %[w1], s68 offen offset:0 lds\n\t"
"v_mfma_f32_16x16x32_bf16 v[56:59], v[188:191], a[104:107], v[56:59]\n\t"
"ds_read_b64_tr_b16 v[176:177], v212 offset:40960\n\t"
"v_add_f32 v197, 1.0, v197\n\t"
"v_mfma_f32_16x16x32_bf16 v[52:55], v[188:191], a[100:103], v[52:55]\n\t"
"s_waitcnt lgkmcnt(3)\n\t"
"v_rcp_f32 v196, v196\n\t"
"v_mfma_f32_16x16x32_bf16 v[60:63], v[188:191], a[108:111], v[60:63]\n\t"
"ds_read_b64_tr_b16 v[178:179], v212 offset:43008\n\t"
"v_mul_f32 v195, v79, v195\n\t"
"v_mfma_f32_16x16x32_bf16 v[32:35], v[168:171], a[112:115], v[32:35]\n\t"
"v_exp_f32 v199, v95\n\t"
"v_mfma_f32_16x16x32_bf16 v[40:43], v[168:171], a[120:123], v[40:43]\n\t"
"ds_read_b64_tr_b16 v[180:181], v215 offset:40960\n\t"
"v_add_f32 v198, 1.0, v198\n\t"
"v_mfma_f32_16x16x32_bf16 v[36:39], v[168:171], a[116:119], v[36:39]\n\t"
"s_waitcnt lgkmcnt(3)\n\t"
"v_rcp_f32 v197, v197\n\t"
"v_mfma_f32_16x16x32_bf16 v[44:47], v[168:171], a[124:127], v[44:47]\n\t"
"ds_read_b64_tr_b16 v[182:183], v215 offset:43008\n\t"
"v_mul_f32 v196, v92, v196\n\t"
"v_mfma_f32_16x16x32_bf16 v[48:51], v[172:175], a[112:115], v[48:51]\n\t"
"buffer_load_dwordx4 v205, %[w1], s68 offen offset:1024 lds\n\t"
"v_mfma_f32_16x16x32_bf16 v[56:59], v[172:175], a[120:123], v[56:59]\n\t"
"ds_read_b64_tr_b16 v[184:185], v212 offset:45056\n\t"
"v_cvt_pk_bf16_f32 v165, v194, v195\n\t"
"v_mfma_f32_16x16x32_bf16 v[52:55], v[172:175], a[116:119], v[52:55]\n\t"
"s_waitcnt lgkmcnt(3)\n\t"
"v_exp_f32 v192, v64\n\t"
"v_mfma_f32_16x16x32_bf16 v[60:63], v[172:175], a[124:127], v[60:63]\n\t"
"ds_read_b64_tr_b16 v[186:187], v212 offset:47104\n\t"
"ds_read_b64_tr_b16 v[188:189], v215 offset:45056\n\t"
"ds_read_b64_tr_b16 v[190:191], v215 offset:47104\n\t"
"v_add_f32 v199, 1.0, v199\n\t"
"v_mfma_f32_16x16x32_bf16 v[32:35], v[176:179], a[128:131], v[32:35]\n\t"
"v_rcp_f32 v198, v198\n\t"
"v_mfma_f32_16x16x32_bf16 v[40:43], v[176:179], a[136:139], v[40:43]\n\t"
"v_mul_f32 v197, v93, v197\n\t"
"v_exp_f32 v193, v65\n\t"
"v_mfma_f32_16x16x32_bf16 v[36:39], v[176:179], a[132:135], v[36:39]\n\t"
"s_waitcnt vmcnt(14)\n\t"
"v_add_f32 v192, 1.0, v192\n\t"
"v_rcp_f32 v199, v199\n\t"
"v_mfma_f32_16x16x32_bf16 v[44:47], v[176:179], a[140:143], v[44:47]\n\t"
"s_waitcnt lgkmcnt(4)\n\t"
"v_mul_f32 v198, v94, v198\n\t"
"v_cvt_pk_bf16_f32 v166, v196, v197\n\t"
"v_mfma_f32_16x16x32_bf16 v[48:51], v[180:183], a[128:131], v[48:51]\n\t"
"s_waitcnt lgkmcnt(0)\n\t"
"v_exp_f32 v194, v66\n\t"
"v_mfma_f32_16x16x32_bf16 v[56:59], v[180:183], a[136:139], v[56:59]\n\t"
"s_barrier\n\t"
"v_add_f32 v193, 1.0, v193\n\t"
"v_rcp_f32 v192, v192\n\t"
"v_mfma_f32_16x16x32_bf16 v[52:55], v[180:183], a[132:135], v[52:55]\n\t"
"s_add_u32 s69, s69, 147456\n\t"
"ds_read_b64_tr_b16 v[168:169], v213 offset:0\n\t"
"v_mul_f32 v199, v95, v199\n\t"
"v_mfma_f32_16x16x32_bf16 v[60:63], v[180:183], a[140:143], v[60:63]\n\t"
"v_exp_f32 v195, v67\n\t"
"v_mfma_f32_16x16x32_bf16 v[32:35], v[184:187], a[144:147], v[32:35]\n\t"
"s_add_u32 m0, s63, 0\n\t"
"ds_read_b64_tr_b16 v[170:171], v213 offset:2048\n\t"
"v_add_f32 v194, 1.0, v194\n\t"
"v_mfma_f32_16x16x32_bf16 v[40:43], v[184:187], a[152:155], v[40:43]\n\t"
"v_rcp_f32 v193, v193\n\t"
"v_mfma_f32_16x16x32_bf16 v[36:39], v[184:187], a[148:151], v[36:39]\n\t"
"ds_read_b64_tr_b16 v[172:173], v216 offset:0\n\t"
"v_mul_f32 v192, v64, v192\n\t"
"v_mfma_f32_16x16x32_bf16 v[44:47], v[184:187], a[156:159], v[44:47]\n\t"
"ds_read_b64_tr_b16 v[174:175], v216 offset:2048\n\t"
"v_cvt_pk_bf16_f32 v167, v198, v199\n\t"
"v_mfma_f32_16x16x32_bf16 v[48:51], v[188:191], a[144:147], v[48:51]\n\t"
"v_exp_f32 v196, v80\n\t"
"v_mfma_f32_16x16x32_bf16 v[56:59], v[188:191], a[152:155], v[56:59]\n\t"
"ds_read_b64_tr_b16 v[176:177], v213 offset:4096\n\t"
"v_add_f32 v195, 1.0, v195\n\t"
"v_mfma_f32_16x16x32_bf16 v[52:55], v[188:191], a[148:151], v[52:55]\n\t"
"s_waitcnt lgkmcnt(3)\n\t"
"v_rcp_f32 v194, v194\n\t"
"v_mfma_f32_16x16x32_bf16 v[60:63], v[188:191], a[156:159], v[60:63]\n\t"
"ds_read_b64_tr_b16 v[178:179], v213 offset:6144\n\t"
"v_mul_f32 v193, v65, v193\n\t"
"v_mfma_f32_16x16x32_bf16 v[32:35], v[168:171], a[160:163], v[32:35]\n\t"
"buffer_load_dwordx4 v200, %[w1], s49 offen offset:0 lds\n\t"
"v_mfma_f32_16x16x32_bf16 v[40:43], v[168:171], a[168:171], v[40:43]\n\t"
"buffer_store_dwordx4 v[164:167], v246, %[ws], s69 offen sc1\n\t"
"ds_write_b128 v242, v[164:167] offset:1024\n\t"
"v_mfma_f32_16x16x32_bf16 v[36:39], v[168:171], a[164:167], v[36:39]\n\t"
"s_waitcnt lgkmcnt(3)\n\t"
"v_exp_f32 v197, v81\n\t"
"v_mfma_f32_16x16x32_bf16 v[44:47], v[168:171], a[172:175], v[44:47]\n\t"
"ds_read_b64_tr_b16 v[180:181], v216 offset:4096\n\t"
"ds_read_b64_tr_b16 v[182:183], v216 offset:6144\n\t"
"v_add_f32 v196, 1.0, v196\n\t"
"v_mfma_f32_16x16x32_bf16 v[48:51], v[172:175], a[160:163], v[48:51]\n\t"
"v_rcp_f32 v195, v195\n\t"
"v_mfma_f32_16x16x32_bf16 v[56:59], v[172:175], a[168:171], v[56:59]\n\t"
"buffer_load_dwordx4 v201, %[w1], s49 offen offset:1024 lds\n\t"
"v_mfma_f32_16x16x32_bf16 v[52:55], v[172:175], a[164:167], v[52:55]\n\t"
"s_waitcnt lgkmcnt(3)\n\t"
"ds_read_b64_tr_b16 v[184:185], v213 offset:8192\n\t"
"v_mul_f32 v194, v66, v194\n\t"
"v_mfma_f32_16x16x32_bf16 v[60:63], v[172:175], a[172:175], v[60:63]\n\t"
"ds_read_b64_tr_b16 v[186:187], v213 offset:10240\n\t"
"v_cvt_pk_bf16_f32 v128, v192, v193\n\t"
"v_mfma_f32_16x16x32_bf16 v[32:35], v[176:179], a[176:179], v[32:35]\n\t"
"v_exp_f32 v198, v82\n\t"
"v_mfma_f32_16x16x32_bf16 v[40:43], v[176:179], a[184:187], v[40:43]\n\t"
"ds_read_b64_tr_b16 v[188:189], v216 offset:8192\n\t"
"v_add_f32 v197, 1.0, v197\n\t"
"v_mfma_f32_16x16x32_bf16 v[36:39], v[176:179], a[180:183], v[36:39]\n\t"
"s_waitcnt lgkmcnt(3)\n\t"
"v_rcp_f32 v196, v196\n\t"
"v_mfma_f32_16x16x32_bf16 v[44:47], v[176:179], a[188:191], v[44:47]\n\t"
"ds_read_b64_tr_b16 v[190:191], v216 offset:10240\n\t"
"v_mul_f32 v195, v67, v195\n\t"
"v_mfma_f32_16x16x32_bf16 v[48:51], v[180:183], a[176:179], v[48:51]\n\t"
"v_exp_f32 v199, v83\n\t"
"v_mfma_f32_16x16x32_bf16 v[56:59], v[180:183], a[184:187], v[56:59]\n\t"
"ds_read_b64_tr_b16 v[168:169], v213 offset:12288\n\t"
"v_add_f32 v198, 1.0, v198\n\t"
"v_mfma_f32_16x16x32_bf16 v[52:55], v[180:183], a[180:183], v[52:55]\n\t"
"s_waitcnt lgkmcnt(3)\n\t"
"v_rcp_f32 v197, v197\n\t"
"v_mfma_f32_16x16x32_bf16 v[60:63], v[180:183], a[188:191], v[60:63]\n\t"
"ds_read_b64_tr_b16 v[170:171], v213 offset:14336\n\t"
"buffer_load_dwordx4 v202, %[w1], s49 offen offset:2048 lds\n\t"
"v_mul_f32 v196, v80, v196\n\t"
"v_mfma_f32_16x16x32_bf16 v[32:35], v[184:187], a[192:195], v[32:35]\n\t"
"ds_read_b64_tr_b16 v[172:173], v216 offset:12288\n\t"
"v_cvt_pk_bf16_f32 v129, v194, v195\n\t"
"v_mfma_f32_16x16x32_bf16 v[40:43], v[184:187], a[200:203], v[40:43]\n\t"
"v_exp_f32 v192, v68\n\t"
"v_mfma_f32_16x16x32_bf16 v[36:39], v[184:187], a[196:199], v[36:39]\n\t"
"s_waitcnt lgkmcnt(3)\n\t"
"ds_read_b64_tr_b16 v[174:175], v216 offset:14336\n\t"
"v_add_f32 v199, 1.0, v199\n\t"
"v_mfma_f32_16x16x32_bf16 v[44:47], v[184:187], a[204:207], v[44:47]\n\t"
"v_rcp_f32 v198, v198\n\t"
"v_mul_f32 v197, v81, v197\n\t"
"v_mfma_f32_16x16x32_bf16 v[48:51], v[188:191], a[192:195], v[48:51]\n\t"
"buffer_load_dwordx4 v203, %[w1], s49 offen offset:3072 lds\n\t"
"v_mfma_f32_16x16x32_bf16 v[56:59], v[188:191], a[200:203], v[56:59]\n\t"
"s_add_u32 m0, s63, 4096\n\t"
"v_exp_f32 v193, v69\n\t"
"v_add_f32 v192, 1.0, v192\n\t"
"v_mfma_f32_16x16x32_bf16 v[52:55], v[188:191], a[196:199], v[52:55]\n\t"
"s_waitcnt lgkmcnt(2)\n\t"
"v_rcp_f32 v199, v199\n\t"
"v_mfma_f32_16x16x32_bf16 v[60:63], v[188:191], a[204:207], v[60:63]\n\t"
"ds_read_b64_tr_b16 v[176:177], v213 offset:16384\n\t"
"ds_read_b64_tr_b16 v[178:179], v213 offset:18432\n\t"
"v_mul_f32 v198, v82, v198\n\t"
"v_mfma_f32_16x16x32_bf16 v[32:35], v[168:171], a[208:211], v[32:35]\n\t"
"ds_read_b64_tr_b16 v[180:181], v216 offset:16384\n\t"
"v_cvt_pk_bf16_f32 v130, v196, v197\n\t"
"v_mfma_f32_16x16x32_bf16 v[40:43], v[168:171], a[216:219], v[40:43]\n\t"
"v_exp_f32 v194, v70\n\t"
"v_mfma_f32_16x16x32_bf16 v[36:39], v[168:171], a[212:215], v[36:39]\n\t"
"s_waitcnt lgkmcnt(3)\n\t"
"ds_read_b64_tr_b16 v[182:183], v216 offset:18432\n\t"
"v_add_f32 v193, 1.0, v193\n\t"
"v_mfma_f32_16x16x32_bf16 v[44:47], v[168:171], a[220:223], v[44:47]\n\t"
"v_rcp_f32 v192, v192\n\t"
"v_mfma_f32_16x16x32_bf16 v[48:51], v[172:175], a[208:211], v[48:51]\n\t"
"s_add_u32 s69, s58, s56\n\t"
"ds_read_b64_tr_b16 v[184:185], v213 offset:20480\n\t"
"v_mul_f32 v199, v83, v199\n\t"
"v_mfma_f32_16x16x32_bf16 v[56:59], v[172:175], a[216:219], v[56:59]\n\t"
"v_exp_f32 v195, v71\n\t"
"v_mfma_f32_16x16x32_bf16 v[52:55], v[172:175], a[212:215], v[52:55]\n\t"
"buffer_load_dwordx4 v204, %[w1], s49 offen offset:0 lds\n\t"
"v_add_f32 v194, 1.0, v194\n\t"
"v_mfma_f32_16x16x32_bf16 v[60:63], v[172:175], a[220:223], v[60:63]\n\t"
"s_waitcnt lgkmcnt(3)\n\t"
"ds_read_b64_tr_b16 v[186:187], v213 offset:22528\n\t"
"v_rcp_f32 v193, v193\n\t"
"v_mfma_f32_16x16x32_bf16 v[32:35], v[176:179], a[224:227], v[32:35]\n\t"
"ds_read_b64_tr_b16 v[188:189], v216 offset:20480\n\t"
"v_mul_f32 v192, v68, v192\n\t"
"v_mfma_f32_16x16x32_bf16 v[40:43], v[176:179], a[232:235], v[40:43]\n\t"
"ds_read_b64_tr_b16 v[190:191], v216 offset:22528\n\t"
"v_cvt_pk_bf16_f32 v131, v198, v199\n\t"
"v_mfma_f32_16x16x32_bf16 v[36:39], v[176:179], a[228:231], v[36:39]\n\t"
"s_waitcnt lgkmcnt(4)\n\t"
"v_exp_f32 v196, v84\n\t"
"v_mfma_f32_16x16x32_bf16 v[44:47], v[176:179], a[236:239], v[44:47]\n\t"
"ds_read_b64_tr_b16 v[168:169], v213 offset:24576\n\t"
"v_add_f32 v195, 1.0, v195\n\t"
"v_mfma_f32_16x16x32_bf16 v[48:51], v[180:183], a[224:227], v[48:51]\n\t"
"buffer_store_dwordx4 v[128:131], v246, %[ws], s69 offen sc1\n\t"
"ds_read_b64_tr_b16 v[170:171], v213 offset:26624\n\t"
"v_rcp_f32 v194, v194\n\t"
"v_mul_f32 v193, v69, v193\n\t"
"v_mfma_f32_16x16x32_bf16 v[56:59], v[180:183], a[232:235], v[56:59]\n\t"
"buffer_load_dwordx4 v205, %[w1], s49 offen offset:1024 lds\n\t"
"v_mfma_f32_16x16x32_bf16 v[52:55], v[180:183], a[228:231], v[52:55]\n\t"
"s_add_u32 s68, s49, 1769472\n\t"
"v_exp_f32 v197, v85\n\t"
"v_mfma_f32_16x16x32_bf16 v[60:63], v[180:183], a[236:239], v[60:63]\n\t"
"s_waitcnt lgkmcnt(4)\n\t"
"ds_read_b64_tr_b16 v[172:173], v216 offset:24576\n\t"
"v_add_f32 v196, 1.0, v196\n\t"
"v_mfma_f32_16x16x32_bf16 v[32:35], v[184:187], a[240:243], v[32:35]\n\t"
"s_add_u32 m0, s63, 24576\n\t"
"v_rcp_f32 v195, v195\n\t"
"v_mfma_f32_16x16x32_bf16 v[40:43], v[184:187], a[248:251], v[40:43]\n\t"
"v_mul_f32 v194, v70, v194\n\t"
"v_cvt_pk_bf16_f32 v132, v192, v193\n\t"
"v_mfma_f32_16x16x32_bf16 v[36:39], v[184:187], a[244:247], v[36:39]\n\t"
"s_waitcnt lgkmcnt(3)\n\t"
"v_exp_f32 v198, v86\n\t"
"v_mfma_f32_16x16x32_bf16 v[44:47], v[184:187], a[252:255], v[44:47]\n\t"
"ds_read_b64_tr_b16 v[174:175], v216 offset:26624\n\t"
"v_add_f32 v197, 1.0, v197\n\t"
"v_mfma_f32_16x16x32_bf16 v[48:51], v[188:191], a[240:243], v[48:51]\n\t"
"v_rcp_f32 v196, v196\n\t"
"v_mfma_f32_16x16x32_bf16 v[56:59], v[188:191], a[248:251], v[56:59]\n\t"
"buffer_load_dwordx4 v200, %[w1], s68 offen offset:0 lds\n\t"
"v_mul_f32 v195, v71, v195\n\t"
"v_mfma_f32_16x16x32_bf16 v[52:55], v[188:191], a[244:247], v[52:55]\n\t"
"s_add_u32 s69, s69, 147456\n\t"
"v_exp_f32 v199, v87\n\t"
"v_add_f32 v198, 1.0, v198\n\t"
"v_rcp_f32 v197, v197\n\t"
"v_mul_f32 v196, v84, v196\n\t"
"v_cvt_pk_bf16_f32 v133, v194, v195\n\t"
"v_add_f32 v199, 1.0, v199\n\t"
"v_rcp_f32 v198, v198\n\t"
"v_mul_f32 v197, v85, v197\n\t"
"v_rcp_f32 v199, v199\n\t"
"v_mul_f32 v198, v86, v198\n\t"
"v_cvt_pk_bf16_f32 v134, v196, v197\n\t"
"v_mul_f32 v199, v87, v199\n\t"
"v_cvt_pk_bf16_f32 v135, v198, v199\n\t"
"buffer_store_dwordx4 v[132:135], v246, %[ws], s69 offen sc1\n\t"
"v_mfma_f32_16x16x32_bf16 v[60:63], v[188:191], a[252:255], v[60:63]\n\t"
"s_waitcnt lgkmcnt(2)\n\t"
"ds_read_b64_tr_b16 v[176:177], v213 offset:28672\n\t"
"ds_read_b64_tr_b16 v[178:179], v213 offset:30720\n\t"
"v_mfma_f32_16x16x32_bf16 v[64:67], v[168:171], v[0:3], 0\n\t"
"buffer_load_dwordx4 v201, %[w1], s68 offen offset:1024 lds\n\t"
"v_mfma_f32_16x16x32_bf16 v[72:75], v[168:171], v[8:11], 0\n\t"
"ds_read_b64_tr_b16 v[180:181], v216 offset:28672\n\t"
"v_mfma_f32_16x16x32_bf16 v[68:71], v[168:171], v[4:7], 0\n\t"
"buffer_load_dwordx4 v202, %[w1], s68 offen offset:2048 lds\n\t"
"v_mfma_f32_16x16x32_bf16 v[76:79], v[168:171], v[12:15], 0\n\t"
"s_waitcnt lgkmcnt(3)\n\t"
"ds_read_b64_tr_b16 v[182:183], v216 offset:30720\n\t"
"v_exp_f32 v192, v40\n\t"
"v_mfma_f32_16x16x32_bf16 v[80:83], v[172:175], v[0:3], 0\n\t"
"v_exp_f32 v193, v41\n\t"
"v_mfma_f32_16x16x32_bf16 v[88:91], v[172:175], v[8:11], 0\n\t"
"ds_read_b64_tr_b16 v[184:185], v213 offset:32768\n\t"
"v_add_f32 v192, 1.0, v192\n\t"
"v_mfma_f32_16x16x32_bf16 v[84:87], v[172:175], v[4:7], 0\n\t"
"s_waitcnt lgkmcnt(3)\n\t"
"v_exp_f32 v194, v42\n\t"
"v_mfma_f32_16x16x32_bf16 v[92:95], v[172:175], v[12:15], 0\n\t"
"ds_read_b64_tr_b16 v[186:187], v213 offset:34816\n\t"
"v_add_f32 v193, 1.0, v193\n\t"
"v_mfma_f32_16x16x32_bf16 v[64:67], v[176:179], v[16:19], v[64:67]\n\t"
"v_rcp_f32 v192, v192\n\t"
"v_mfma_f32_16x16x32_bf16 v[72:75], v[176:179], v[24:27], v[72:75]\n\t"
"v_exp_f32 v195, v43\n\t"
"v_mfma_f32_16x16x32_bf16 v[68:71], v[176:179], v[20:23], v[68:71]\n\t"
"s_waitcnt lgkmcnt(2)\n\t"
"ds_read_b64_tr_b16 v[188:189], v216 offset:32768\n\t"
"v_add_f32 v194, 1.0, v194\n\t"
"v_mfma_f32_16x16x32_bf16 v[76:79], v[176:179], v[28:31], v[76:79]\n\t"
"ds_read_b64_tr_b16 v[190:191], v216 offset:34816\n\t"
"v_rcp_f32 v193, v193\n\t"
"v_mfma_f32_16x16x32_bf16 v[80:83], v[180:183], v[16:19], v[80:83]\n\t"
"buffer_load_dwordx4 v203, %[w1], s68 offen offset:3072 lds\n\t"
"v_mfma_f32_16x16x32_bf16 v[88:91], v[180:183], v[24:27], v[88:91]\n\t"
"ds_read_b64_tr_b16 v[168:169], v213 offset:36864\n\t"
"v_mul_f32 v192, v40, v192\n\t"
"v_mfma_f32_16x16x32_bf16 v[84:87], v[180:183], v[20:23], v[84:87]\n\t"
"s_waitcnt lgkmcnt(3)\n\t"
"v_exp_f32 v196, v56\n\t"
"v_mfma_f32_16x16x32_bf16 v[92:95], v[180:183], v[28:31], v[92:95]\n\t"
"ds_read_b64_tr_b16 v[170:171], v213 offset:38912\n\t"
"v_add_f32 v195, 1.0, v195\n\t"
"v_mfma_f32_16x16x32_bf16 v[64:67], v[184:187], a[0:3], v[64:67]\n\t"
"v_rcp_f32 v194, v194\n\t"
"v_mfma_f32_16x16x32_bf16 v[72:75], v[184:187], a[8:11], v[72:75]\n\t"
"s_add_u32 m0, s63, 28672\n\t"
"ds_read_b64_tr_b16 v[172:173], v216 offset:36864\n\t"
"v_mul_f32 v193, v41, v193\n\t"
"v_mfma_f32_16x16x32_bf16 v[68:71], v[184:187], a[4:7], v[68:71]\n\t"
"s_waitcnt lgkmcnt(3)\n\t"
"v_exp_f32 v197, v57\n\t"
"v_mfma_f32_16x16x32_bf16 v[76:79], v[184:187], a[12:15], v[76:79]\n\t"
"ds_read_b64_tr_b16 v[174:175], v216 offset:38912\n\t"
"v_add_f32 v196, 1.0, v196\n\t"
"v_mfma_f32_16x16x32_bf16 v[80:83], v[188:191], a[0:3], v[80:83]\n\t"
"v_rcp_f32 v195, v195\n\t"
"v_mfma_f32_16x16x32_bf16 v[88:91], v[188:191], a[8:11], v[88:91]\n\t"
"ds_read_b64_tr_b16 v[176:177], v213 offset:40960\n\t"
"v_mul_f32 v194, v42, v194\n\t"
"v_mfma_f32_16x16x32_bf16 v[84:87], v[188:191], a[4:7], v[84:87]\n\t"
"s_waitcnt lgkmcnt(3)\n\t"
"ds_read_b64_tr_b16 v[178:179], v213 offset:43008\n\t"
"v_cvt_pk_bf16_f32 v160, v192, v193\n\t"
"v_mfma_f32_16x16x32_bf16 v[92:95], v[188:191], a[12:15], v[92:95]\n\t"
"v_exp_f32 v198, v58\n\t"
"v_mfma_f32_16x16x32_bf16 v[64:67], v[168:171], a[16:19], v[64:67]\n\t"
"buffer_load_dwordx4 v204, %[w1], s68 offen offset:0 lds\n\t"
"v_mfma_f32_16x16x32_bf16 v[72:75], v[168:171], a[24:27], v[72:75]\n\t"
"ds_read_b64_tr_b16 v[180:181], v216 offset:40960\n\t"
"v_add_f32 v197, 1.0, v197\n\t"
"v_mfma_f32_16x16x32_bf16 v[68:71], v[168:171], a[20:23], v[68:71]\n\t"
"s_waitcnt lgkmcnt(3)\n\t"
"v_rcp_f32 v196, v196\n\t"
"v_mfma_f32_16x16x32_bf16 v[76:79], v[168:171], a[28:31], v[76:79]\n\t"
"ds_read_b64_tr_b16 v[182:183], v216 offset:43008\n\t"
"v_mul_f32 v195, v43, v195\n\t"
"v_mfma_f32_16x16x32_bf16 v[80:83], v[172:175], a[16:19], v[80:83]\n\t"
"v_exp_f32 v199, v59\n\t"
"v_mfma_f32_16x16x32_bf16 v[88:91], v[172:175], a[24:27], v[88:91]\n\t"
"ds_read_b64_tr_b16 v[184:185], v213 offset:45056\n\t"
"v_add_f32 v198, 1.0, v198\n\t"
"v_mfma_f32_16x16x32_bf16 v[84:87], v[172:175], a[20:23], v[84:87]\n\t"
"s_waitcnt lgkmcnt(3)\n\t"
"v_rcp_f32 v197, v197\n\t"
"v_mfma_f32_16x16x32_bf16 v[92:95], v[172:175], a[28:31], v[92:95]\n\t"
"ds_read_b64_tr_b16 v[186:187], v213 offset:47104\n\t"
"ds_read_b64_tr_b16 v[188:189], v216 offset:45056\n\t"
"ds_read_b64_tr_b16 v[190:191], v216 offset:47104\n\t"
"v_mul_f32 v196, v56, v196\n\t"
"v_mfma_f32_16x16x32_bf16 v[64:67], v[176:179], a[32:35], v[64:67]\n\t"
"buffer_load_dwordx4 v205, %[w1], s68 offen offset:1024 lds\n\t"
"v_mfma_f32_16x16x32_bf16 v[72:75], v[176:179], a[40:43], v[72:75]\n\t"
"v_cvt_pk_bf16_f32 v161, v194, v195\n\t"
"v_exp_f32 v192, v44\n\t"
"v_mfma_f32_16x16x32_bf16 v[68:71], v[176:179], a[36:39], v[68:71]\n\t"
"s_waitcnt vmcnt(15)\n\t"
"v_add_f32 v199, 1.0, v199\n\t"
"v_rcp_f32 v198, v198\n\t"
"v_mfma_f32_16x16x32_bf16 v[76:79], v[176:179], a[44:47], v[76:79]\n\t"
"s_waitcnt lgkmcnt(4)\n\t"
"v_mul_f32 v197, v57, v197\n\t"
"v_exp_f32 v193, v45\n\t"
"v_mfma_f32_16x16x32_bf16 v[80:83], v[180:183], a[32:35], v[80:83]\n\t"
"s_waitcnt lgkmcnt(0)\n\t"
"v_add_f32 v192, 1.0, v192\n\t"
"v_rcp_f32 v199, v199\n\t"
"v_mfma_f32_16x16x32_bf16 v[88:91], v[180:183], a[40:43], v[88:91]\n\t"
"s_barrier\n\t"
"v_mul_f32 v198, v58, v198\n\t"
"v_cvt_pk_bf16_f32 v162, v196, v197\n\t"
"v_mfma_f32_16x16x32_bf16 v[84:87], v[180:183], a[36:39], v[84:87]\n\t"
"s_add_u32 s68, s49, 3538944\n\t"
"v_exp_f32 v194, v46\n\t"
"v_mfma_f32_16x16x32_bf16 v[92:95], v[180:183], a[44:47], v[92:95]\n\t"
"ds_read_b64_tr_b16 v[168:169], v214 offset:0\n\t"
"v_add_f32 v193, 1.0, v193\n\t"
"v_mfma_f32_16x16x32_bf16 v[64:67], v[184:187], a[48:51], v[64:67]\n\t"
"s_add_u32 m0, s63, 49152\n\t"
"ds_read_b64_tr_b16 v[170:171], v214 offset:2048\n\t"
"v_rcp_f32 v192, v192\n\t"
"v_mfma_f32_16x16x32_bf16 v[72:75], v[184:187], a[56:59], v[72:75]\n\t"
"s_lshl_b32 s58, s47, 4\n\t"
"ds_read_b64_tr_b16 v[172:173], v217 offset:0\n\t"
"v_mul_f32 v199, v59, v199\n\t"
"v_mfma_f32_16x16x32_bf16 v[68:71], v[184:187], a[52:55], v[68:71]\n\t"
"s_add_u32 s69, s58, s57\n\t"
"v_exp_f32 v195, v47\n\t"
"v_mfma_f32_16x16x32_bf16 v[76:79], v[184:187], a[60:63], v[76:79]\n\t"
"ds_read_b64_tr_b16 v[174:175], v217 offset:2048\n\t"
"v_add_f32 v194, 1.0, v194\n\t"
"v_mfma_f32_16x16x32_bf16 v[80:83], v[188:191], a[48:51], v[80:83]\n\t"
"v_rcp_f32 v193, v193\n\t"
"v_mfma_f32_16x16x32_bf16 v[88:91], v[188:191], a[56:59], v[88:91]\n\t"
"ds_read_b64_tr_b16 v[176:177], v214 offset:4096\n\t"
"v_mul_f32 v192, v44, v192\n\t"
"v_mfma_f32_16x16x32_bf16 v[84:87], v[188:191], a[52:55], v[84:87]\n\t"
"s_waitcnt lgkmcnt(3)\n\t"
"ds_read_b64_tr_b16 v[178:179], v214 offset:6144\n\t"
"v_cvt_pk_bf16_f32 v163, v198, v199\n\t"
"v_mfma_f32_16x16x32_bf16 v[92:95], v[188:191], a[60:63], v[92:95]\n\t"
"v_exp_f32 v196, v60\n\t"
"v_mfma_f32_16x16x32_bf16 v[64:67], v[168:171], a[64:67], v[64:67]\n\t"
"buffer_load_dwordx4 v200, %[w1], s68 offen offset:0 lds\n\t"
"v_add_f32 v195, 1.0, v195\n\t"
"v_rcp_f32 v194, v194\n\t"
"v_mfma_f32_16x16x32_bf16 v[72:75], v[168:171], a[72:75], v[72:75]\n\t"
"buffer_store_dwordx4 v[160:163], v246, %[ws], s69 offen sc1\n\t"
"ds_write_b128 v242, v[160:163] offset:8192\n\t"
"v_mul_f32 v193, v45, v193\n\t"
"v_mfma_f32_16x16x32_bf16 v[68:71], v[168:171], a[68:71], v[68:71]\n\t"
"s_waitcnt lgkmcnt(3)\n\t"
"v_exp_f32 v197, v61\n\t"
"v_mfma_f32_16x16x32_bf16 v[76:79], v[168:171], a[76:79], v[76:79]\n\t"
"ds_read_b64_tr_b16 v[180:181], v217 offset:4096\n\t"
"ds_read_b64_tr_b16 v[182:183], v217 offset:6144\n\t"
"v_add_f32 v196, 1.0, v196\n\t"
"v_mfma_f32_16x16x32_bf16 v[80:83], v[172:175], a[64:67], v[80:83]\n\t"
"v_rcp_f32 v195, v195\n\t"
"v_mfma_f32_16x16x32_bf16 v[88:91], v[172:175], a[72:75], v[88:91]\n\t"
"ds_read_b64_tr_b16 v[184:185], v214 offset:8192\n\t"
"v_mul_f32 v194, v46, v194\n\t"
"v_mfma_f32_16x16x32_bf16 v[84:87], v[172:175], a[68:71], v[84:87]\n\t"
"s_waitcnt lgkmcnt(4)\n\t"
"ds_read_b64_tr_b16 v[186:187], v214 offset:10240\n\t"
"v_cvt_pk_bf16_f32 v164, v192, v193\n\t"
"v_mfma_f32_16x16x32_bf16 v[92:95], v[172:175], a[76:79], v[92:95]\n\t"
"buffer_load_dwordx4 v201, %[w1], s68 offen offset:1024 lds\n\t"
"v_mfma_f32_16x16x32_bf16 v[64:67], v[176:179], a[80:83], v[64:67]\n\t"
"v_exp_f32 v198, v62\n\t"
"v_mfma_f32_16x16x32_bf16 v[72:75], v[176:179], a[88:91], v[72:75]\n\t"
"ds_read_b64_tr_b16 v[188:189], v217 offset:8192\n\t"
"v_add_f32 v197, 1.0, v197\n\t"
"v_mfma_f32_16x16x32_bf16 v[68:71], v[176:179], a[84:87], v[68:71]\n\t"
"s_waitcnt lgkmcnt(3)\n\t"
"v_rcp_f32 v196, v196\n\t"
"v_mfma_f32_16x16x32_bf16 v[76:79], v[176:179], a[92:95], v[76:79]\n\t"
"ds_read_b64_tr_b16 v[190:191], v217 offset:10240\n\t"
"v_mul_f32 v195, v47, v195\n\t"
"v_mfma_f32_16x16x32_bf16 v[80:83], v[180:183], a[80:83], v[80:83]\n\t"
"v_exp_f32 v199, v63\n\t"
"v_mfma_f32_16x16x32_bf16 v[88:91], v[180:183], a[88:91], v[88:91]\n\t"
"buffer_load_dwordx4 v202, %[w1], s68 offen offset:2048 lds\n\t"
"v_mfma_f32_16x16x32_bf16 v[84:87], v[180:183], a[84:87], v[84:87]\n\t"
"s_waitcnt lgkmcnt(2)\n\t"
"ds_read_b64_tr_b16 v[168:169], v214 offset:12288\n\t"
"v_add_f32 v198, 1.0, v198\n\t"
"v_mfma_f32_16x16x32_bf16 v[92:95], v[180:183], a[92:95], v[92:95]\n\t"
"ds_read_b64_tr_b16 v[170:171], v214 offset:14336\n\t"
"v_rcp_f32 v197, v197\n\t"
"v_mfma_f32_16x16x32_bf16 v[64:67], v[184:187], a[96:99], v[64:67]\n\t"
"ds_read_b64_tr_b16 v[172:173], v217 offset:12288\n\t"
"v_mul_f32 v196, v60, v196\n\t"
"v_mfma_f32_16x16x32_bf16 v[72:75], v[184:187], a[104:107], v[72:75]\n\t"
"ds_read_b64_tr_b16 v[174:175], v217 offset:14336\n\t"
"v_cvt_pk_bf16_f32 v165, v194, v195\n\t"
"v_mfma_f32_16x16x32_bf16 v[68:71], v[184:187], a[100:103], v[68:71]\n\t"
"s_waitcnt lgkmcnt(4)\n\t"
"v_exp_f32 v192, v32\n\t"
"v_mfma_f32_16x16x32_bf16 v[76:79], v[184:187], a[108:111], v[76:79]\n\t"
"ds_read_b128 v[136:139], v243 offset:0\n\t"
"ds_read_b128 v[140:143], v243 offset:1024\n\t"
"v_add_f32 v199, 1.0, v199\n\t"
"v_mfma_f32_16x16x32_bf16 v[80:83], v[188:191], a[96:99], v[80:83]\n\t"
"buffer_load_dwordx4 v203, %[w1], s68 offen offset:3072 lds\n\t"
"v_mfma_f32_16x16x32_bf16 v[88:91], v[188:191], a[104:107], v[88:91]\n\t"
"s_add_u32 m0, s63, 53248\n\t"
"v_rcp_f32 v198, v198\n\t"
"v_mfma_f32_16x16x32_bf16 v[84:87], v[188:191], a[100:103], v[84:87]\n\t"
"s_waitcnt lgkmcnt(4)\n\t"
"ds_read_b64_tr_b16 v[176:177], v214 offset:16384\n\t"
"v_mul_f32 v197, v61, v197\n\t"
"v_mfma_f32_16x16x32_bf16 v[92:95], v[188:191], a[108:111], v[92:95]\n\t"
"ds_read_b64_tr_b16 v[178:179], v214 offset:18432\n\t"
"v_exp_f32 v193, v33\n\t"
"v_mfma_f32_16x16x32_bf16 v[64:67], v[168:171], a[112:115], v[64:67]\n\t"
"buffer_load_dwordx4 v204, %[w1], s68 offen offset:0 lds\n\t"
"v_mfma_f32_16x16x32_bf16 v[72:75], v[168:171], a[120:123], v[72:75]\n\t"
"ds_read_b64_tr_b16 v[180:181], v217 offset:16384\n\t"
"v_add_f32 v192, 1.0, v192\n\t"
"v_mfma_f32_16x16x32_bf16 v[68:71], v[168:171], a[116:119], v[68:71]\n\t"
"s_waitcnt lgkmcnt(5)\n\t"
"v_rcp_f32 v199, v199\n\t"
"v_mfma_f32_16x16x32_bf16 v[76:79], v[168:171], a[124:127], v[76:79]\n\t"
"ds_read_b64_tr_b16 v[182:183], v217 offset:18432\n\t"
"v_mul_f32 v198, v62, v198\n\t"
"v_mfma_f32_16x16x32_bf16 v[80:83], v[172:175], a[112:115], v[80:83]\n\t"
"ds_read_b64_tr_b16 v[184:185], v214 offset:20480\n\t"
"v_cvt_pk_bf16_f32 v166, v196, v197\n\t"
"v_mfma_f32_16x16x32_bf16 v[88:91], v[172:175], a[120:123], v[88:91]\n\t"
"v_exp_f32 v194, v34\n\t"
"v_mfma_f32_16x16x32_bf16 v[84:87], v[172:175], a[116:119], v[84:87]\n\t"
"s_waitcnt lgkmcnt(3)\n\t"
"ds_read_b64_tr_b16 v[186:187], v214 offset:22528\n\t"
"v_add_f32 v193, 1.0, v193\n\t"
"v_mfma_f32_16x16x32_bf16 v[92:95], v[172:175], a[124:127], v[92:95]\n\t"
"v_rcp_f32 v192, v192\n\t"
"v_mfma_f32_16x16x32_bf16 v[64:67], v[176:179], a[128:131], v[64:67]\n\t"
"s_add_u32 s69, s69, 147456\n\t"
"ds_read_b64_tr_b16 v[188:189], v217 offset:20480\n\t"
"v_mul_f32 v199, v63, v199\n\t"
"v_mfma_f32_16x16x32_bf16 v[72:75], v[176:179], a[136:139], v[72:75]\n\t"
"buffer_load_dwordx4 v205, %[w1], s68 offen offset:1024 lds\n\t"
"v_mfma_f32_16x16x32_bf16 v[68:71], v[176:179], a[132:135], v[68:71]\n\t"
"s_waitcnt lgkmcnt(3)\n\t"
"v_exp_f32 v195, v35\n\t"
"v_mfma_f32_16x16x32_bf16 v[76:79], v[176:179], a[140:143], v[76:79]\n\t"
"s_add_u32 s68, s50, s53\n\t"
"ds_read_b64_tr_b16 v[190:191], v217 offset:22528\n\t"
"v_add_f32 v194, 1.0, v194\n\t"
"v_mfma_f32_16x16x32_bf16 v[80:83], v[180:183], a[128:131], v[80:83]\n\t"
"s_add_u32 m0, s63, 73728\n\t"
"v_rcp_f32 v193, v193\n\t"
"v_mfma_f32_16x16x32_bf16 v[88:91], v[180:183], a[136:139], v[88:91]\n\t"
"ds_read_b64_tr_b16 v[168:169], v214 offset:24576\n\t"
"v_mul_f32 v192, v32, v192\n\t"
"v_mfma_f32_16x16x32_bf16 v[84:87], v[180:183], a[132:135], v[84:87]\n\t"
"s_waitcnt lgkmcnt(3)\n\t"
"s_getpc_b64 s[96:97]\n\t"
"72:\n\t"
"s_and_b32 s98, %[mi], 3\n\t"
"s_lshl_b32 s98, s98, 2\n\t"
"s_add_u32 s98, s98, %[wv]\n\t"
"s_lshl_b32 s98, s98, 10\n\t"
"v_add_u32 v253, s98, v244\n\t"
"v_min_u32 v253, 97f-72b, v253\n\t"
"global_load_dword v253, v253, s[96:97]\n\t"
"v_mfma_f32_16x16x32_bf16 v[92:95], v[180:183], a[140:143], v[92:95]\n\t"
"ds_read_b64_tr_b16 v[170:171], v214 offset:26624\n\t"
"v_cvt_pk_bf16_f32 v167, v198, v199\n\t"
"v_mfma_f32_16x16x32_bf16 v[64:67], v[184:187], a[144:147], v[64:67]\n\t"
"v_exp_f32 v196, v48\n\t"
"v_mfma_f32_16x16x32_bf16 v[72:75], v[184:187], a[152:155], v[72:75]\n\t"
"v_add_f32 v195, 1.0, v195\n\t"
"v_rcp_f32 v194, v194\n\t"
"v_mfma_f32_16x16x32_bf16 v[68:71], v[184:187], a[148:151], v[68:71]\n\t"
"s_waitcnt lgkmcnt(2)\n\t"
"buffer_store_dwordx4 v[164:167], v246, %[ws], s69 offen sc1\n\t"
"ds_write_b128 v242, v[164:167] offset:9216\n\t"
"v_mul_f32 v193, v33, v193\n\t"
"v_mfma_f32_16x16x32_bf16 v[76:79], v[184:187], a[156:159], v[76:79]\n\t"
"ds_read_b64_tr_b16 v[172:173], v217 offset:24576\n\t"
"ds_read_b64_tr_b16 v[174:175], v217 offset:26624\n\t"
"v_exp_f32 v197, v49\n\t"
"v_mfma_f32_16x16x32_bf16 v[80:83], v[188:191], a[144:147], v[80:83]\n\t"
"buffer_load_dwordx4 v206, %[w2], s68 offen offset:0 lds\n\t"
"v_mfma_f32_16x16x32_bf16 v[88:91], v[188:191], a[152:155], v[88:91]\n\t"
"ds_read_b64_tr_b16 v[176:177], v214 offset:28672\n\t"
"v_add_f32 v196, 1.0, v196\n\t"
"v_mfma_f32_16x16x32_bf16 v[84:87], v[188:191], a[148:151], v[84:87]\n\t"
"s_waitcnt lgkmcnt(4)\n\t"
"v_rcp_f32 v195, v195\n\t"
"v_mfma_f32_16x16x32_bf16 v[92:95], v[188:191], a[156:159], v[92:95]\n\t"
"ds_read_b64_tr_b16 v[178:179], v214 offset:30720\n\t"
"v_mul_f32 v194, v34, v194\n\t"
"v_mfma_f32_16x16x32_bf16 v[64:67], v[168:171], a[160:163], v[64:67]\n\t"
"buffer_load_dwordx4 v207, %[w2], s68 offen offset:1024 lds\n\t"
"v_mfma_f32_16x16x32_bf16 v[72:75], v[168:171], a[168:171], v[72:75]\n\t"
"ds_read_b64_tr_b16 v[180:181], v217 offset:28672\n\t"
"v_cvt_pk_bf16_f32 v144, v192, v193\n\t"
"v_mfma_f32_16x16x32_bf16 v[68:71], v[168:171], a[164:167], v[68:71]\n\t"
"s_waitcnt lgkmcnt(3)\n\t"
"v_exp_f32 v198, v50\n\t"
"v_mfma_f32_16x16x32_bf16 v[76:79], v[168:171], a[172:175], v[76:79]\n\t"
"ds_read_b64_tr_b16 v[182:183], v217 offset:30720\n\t"
"v_add_f32 v197, 1.0, v197\n\t"
"v_mfma_f32_16x16x32_bf16 v[80:83], v[172:175], a[160:163], v[80:83]\n\t"
"v_rcp_f32 v196, v196\n\t"
"v_mfma_f32_16x16x32_bf16 v[88:91], v[172:175], a[168:171], v[88:91]\n\t"
"ds_read_b64_tr_b16 v[184:185], v214 offset:32768\n\t"
"v_mul_f32 v195, v35, v195\n\t"
"v_mfma_f32_16x16x32_bf16 v[84:87], v[172:175], a[164:167], v[84:87]\n\t"
"s_waitcnt lgkmcnt(3)\n\t"
"v_exp_f32 v199, v51\n\t"
"v_mfma_f32_16x16x32_bf16 v[92:95], v[172:175], a[172:175], v[92:95]\n\t"
"ds_read_b64_tr_b16 v[186:187], v214 offset:34816\n\t"
"v_add_f32 v198, 1.0, v198\n\t"
"v_mfma_f32_16x16x32_bf16 v[64:67], v[176:179], a[176:179], v[64:67]\n\t"
"v_rcp_f32 v197, v197\n\t"
"v_mfma_f32_16x16x32_bf16 v[72:75], v[176:179], a[184:187], v[72:75]\n\t"
"ds_read_b64_tr_b16 v[188:189], v217 offset:32768\n\t"
"v_mul_f32 v196, v48, v196\n\t"
"v_mfma_f32_16x16x32_bf16 v[68:71], v[176:179], a[180:183], v[68:71]\n\t"
"s_waitcnt lgkmcnt(3)\n\t"
"ds_read_b64_tr_b16 v[190:191], v217 offset:34816\n\t"
"v_cvt_pk_bf16_f32 v145, v194, v195\n\t"
"v_mfma_f32_16x16x32_bf16 v[76:79], v[176:179], a[188:191], v[76:79]\n\t"
"v_exp_f32 v192, v36\n\t"
"v_mfma_f32_16x16x32_bf16 v[80:83], v[180:183], a[176:179], v[80:83]\n\t"
"buffer_load_dwordx4 v208, %[w2], s68 offen offset:2048 lds\n\t"
"v_mfma_f32_16x16x32_bf16 v[88:91], v[180:183], a[184:187], v[88:91]\n\t"
"ds_read_b64_tr_b16 v[168:169], v214 offset:36864\n\t"
"v_add_f32 v199, 1.0, v199\n\t"
"v_mfma_f32_16x16x32_bf16 v[84:87], v[180:183], a[180:183], v[84:87]\n\t"
"s_waitcnt lgkmcnt(3)\n\t"
"v_rcp_f32 v198, v198\n\t"
"v_mfma_f32_16x16x32_bf16 v[92:95], v[180:183], a[188:191], v[92:95]\n\t"
"ds_read_b64_tr_b16 v[170:171], v214 offset:38912\n\t"
"v_mul_f32 v197, v49, v197\n\t"
"v_mfma_f32_16x16x32_bf16 v[64:67], v[184:187], a[192:195], v[64:67]\n\t"
"v_exp_f32 v193, v37\n\t"
"v_mfma_f32_16x16x32_bf16 v[72:75], v[184:187], a[200:203], v[72:75]\n\t"
"ds_read_b64_tr_b16 v[172:173], v217 offset:36864\n\t"
"v_add_f32 v192, 1.0, v192\n\t"
"v_mfma_f32_16x16x32_bf16 v[68:71], v[184:187], a[196:199], v[68:71]\n\t"
"s_waitcnt lgkmcnt(3)\n\t"
"v_rcp_f32 v199, v199\n\t"
"v_mfma_f32_16x16x32_bf16 v[76:79], v[184:187], a[204:207], v[76:79]\n\t"
"ds_read_b64_tr_b16 v[174:175], v217 offset:38912\n\t"
"v_mul_f32 v198, v50, v198\n\t"
"v_mfma_f32_16x16x32_bf16 v[80:83], v[188:191], a[192:195], v[80:83]\n\t"
"buffer_load_dwordx4 v209, %[w2], s68 offen offset:3072 lds\n\t"
"v_mfma_f32_16x16x32_bf16 v[88:91], v[188:191], a[200:203], v[88:91]\n\t"
"s_add_u32 m0, s63, 77824\n\t"
"ds_read_b64_tr_b16 v[176:177], v214 offset:40960\n\t"
"v_cvt_pk_bf16_f32 v146, v196, v197\n\t"
"v_mfma_f32_16x16x32_bf16 v[84:87], v[188:191], a[196:199], v[84:87]\n\t"
"s_waitcnt lgkmcnt(3)\n\t"
"v_exp_f32 v194, v38\n\t"
"v_mfma_f32_16x16x32_bf16 v[92:95], v[188:191], a[204:207], v[92:95]\n\t"
"ds_read_b64_tr_b16 v[178:179], v214 offset:43008\n\t"
"v_add_f32 v193, 1.0, v193\n\t"
"v_mfma_f32_16x16x32_bf16 v[64:67], v[168:171], a[208:211], v[64:67]\n\t"
"v_rcp_f32 v192, v192\n\t"
"v_mfma_f32_16x16x32_bf16 v[72:75], v[168:171], a[216:219], v[72:75]\n\t"
"s_add_u32 s69, s58, s56\n\t"
"ds_read_b64_tr_b16 v[180:181], v217 offset:40960\n\t"
"v_mul_f32 v199, v51, v199\n\t"
"v_mfma_f32_16x16x32_bf16 v[68:71], v[168:171], a[212:215], v[68:71]\n\t"
"s_waitcnt lgkmcnt(3)\n\t"
"v_exp_f32 v195, v39\n\t"
"v_mfma_f32_16x16x32_bf16 v[76:79], v[168:171], a[220:223], v[76:79]\n\t"
"ds_read_b64_tr_b16 v[182:183], v217 offset:43008\n\t"
"buffer_load_dwordx4 v210, %[w2], s68 offen offset:0 lds\n\t"
"v_mfma_f32_16x16x32_bf16 v[80:83], v[172:175], a[208:211], v[80:83]\n\t"
"v_add_f32 v194, 1.0, v194\n\t"
"v_rcp_f32 v193, v193\n\t"
"v_mfma_f32_16x16x32_bf16 v[88:91], v[172:175], a[216:219], v[88:91]\n\t"
"v_mul_f32 v192, v36, v192\n\t"
"v_cvt_pk_bf16_f32 v147, v198, v199\n\t"
"v_mfma_f32_16x16x32_bf16 v[84:87], v[172:175], a[212:215], v[84:87]\n\t"
"s_waitcnt lgkmcnt(2)\n\t"
"v_exp_f32 v196, v52\n\t"
"v_add_f32 v195, 1.0, v195\n\t"
"v_rcp_f32 v194, v194\n\t"
"v_mfma_f32_16x16x32_bf16 v[92:95], v[172:175], a[220:223], v[92:95]\n\t"
"buffer_store_dwordx4 v[144:147], v246, %[ws], s69 offen sc1\n\t"
"ds_read_b64_tr_b16 v[184:185], v214 offset:45056\n\t"
"ds_read_b64_tr_b16 v[186:187], v214 offset:47104\n\t"
"ds_read_b64_tr_b16 v[188:189], v217 offset:45056\n\t"
"ds_read_b64_tr_b16 v[190:191], v217 offset:47104\n\t"
"v_mul_f32 v193, v37, v193\n\t"
"v_mfma_f32_16x16x32_bf16 v[64:67], v[176:179], a[224:227], v[64:67]\n\t"
"v_exp_f32 v197, v53\n\t"
"v_mfma_f32_16x16x32_bf16 v[72:75], v[176:179], a[232:235], v[72:75]\n\t"
"buffer_load_dwordx4 v211, %[w2], s68 offen offset:1024 lds\n\t"
"v_mfma_f32_16x16x32_bf16 v[68:71], v[176:179], a[228:231], v[68:71]\n\t"
"s_waitcnt vmcnt(16)\n\t"
"v_add_f32 v196, 1.0, v196\n\t"
"v_rcp_f32 v195, v195\n\t"
"v_mfma_f32_16x16x32_bf16 v[76:79], v[176:179], a[236:239], v[76:79]\n\t"
"s_waitcnt lgkmcnt(4)\n\t"
"v_mul_f32 v194, v38, v194\n\t"
"v_cvt_pk_bf16_f32 v148, v192, v193\n\t"
"v_mfma_f32_16x16x32_bf16 v[80:83], v[180:183], a[224:227], v[80:83]\n\t"
"s_waitcnt lgkmcnt(0)\n\t"
"v_exp_f32 v198, v54\n\t"
"v_mfma_f32_16x16x32_bf16 v[88:91], v[180:183], a[232:235], v[88:91]\n\t"
"s_barrier\n\t"
"v_add_f32 v197, 1.0, v197\n\t"
"v_rcp_f32 v196, v196\n\t"
"v_mfma_f32_16x16x32_bf16 v[84:87], v[180:183], a[228:231], v[84:87]\n\t"
"buffer_store_dwordx2 v[254:255], v252, %[ws], s79 offen offset:512 sc1\n\t"
"ds_read_b64_tr_b16 v[168:169], v212 offset:0\n\t"
"v_mul_f32 v195, v39, v195\n\t"
"v_mfma_f32_16x16x32_bf16 v[92:95], v[180:183], a[236:239], v[92:95]\n\t"
"v_exp_f32 v199, v55\n\t"
"v_mfma_f32_16x16x32_bf16 v[64:67], v[184:187], a[240:243], v[64:67]\n\t"
"s_add_u32 s68, s50, s54\n\t"
"ds_read_b64_tr_b16 v[170:171], v212 offset:2048\n\t"
"v_add_f32 v198, 1.0, v198\n\t"
"v_mfma_f32_16x16x32_bf16 v[72:75], v[184:187], a[248:251], v[72:75]\n\t"
"s_add_u32 m0, s63, 98304\n\t"
"v_rcp_f32 v197, v197\n\t"
"v_mfma_f32_16x16x32_bf16 v[68:71], v[184:187], a[244:247], v[68:71]\n\t"
"v_mul_f32 v196, v52, v196\n\t"
"v_cvt_pk_bf16_f32 v149, v194, v195\n\t"
"v_add_f32 v199, 1.0, v199\n\t"
"v_mfma_f32_16x16x32_bf16 v[76:79], v[184:187], a[252:255], v[76:79]\n\t"
"ds_read_b64_tr_b16 v[172:173], v215 offset:0\n\t"
"ds_read_b64_tr_b16 v[174:175], v215 offset:2048\n\t"
"v_rcp_f32 v198, v198\n\t"
"v_mul_f32 v197, v53, v197\n\t"
"v_mfma_f32_16x16x32_bf16 v[80:83], v[188:191], a[240:243], v[80:83]\n\t"
"v_rcp_f32 v199, v199\n\t"
"v_mul_f32 v198, v54, v198\n\t"
"v_cvt_pk_bf16_f32 v150, v196, v197\n\t"
"v_mfma_f32_16x16x32_bf16 v[88:91], v[188:191], a[248:251], v[88:91]\n\t"
"s_add_u32 s69, s69, 147456\n\t"
"v_mul_f32 v199, v55, v199\n\t"
"v_cvt_pk_bf16_f32 v151, v198, v199\n\t"
"v_mfma_f32_16x16x32_bf16 v[84:87], v[188:191], a[244:247], v[84:87]\n\t"
"buffer_store_dwordx4 v[148:151], v246, %[ws], s69 offen sc1\n\t"
"ds_read_b64_tr_b16 v[176:177], v212 offset:4096\n\t"
"ds_read_b64_tr_b16 v[178:179], v212 offset:6144\n\t"
"ds_read_b64_tr_b16 v[180:181], v215 offset:4096\n\t"
"v_mfma_f32_16x16x32_bf16 v[92:95], v[188:191], a[252:255], v[92:95]\n\t"
"s_waitcnt lgkmcnt(5)\n\t"
"ds_read_b64_tr_b16 v[182:183], v215 offset:6144\n\t"
"v_mfma_f32_16x16x32_bf16 v[32:35], v[168:171], v[0:3], 0\n\t"
"buffer_load_dwordx4 v206, %[w2], s68 offen offset:0 lds\n\t"
"v_mfma_f32_16x16x32_bf16 v[40:43], v[168:171], v[8:11], 0\n\t"
"buffer_load_dwordx4 v207, %[w2], s68 offen offset:1024 lds\n\t"
"v_mfma_f32_16x16x32_bf16 v[36:39], v[168:171], v[4:7], 0\n\t"
"ds_read_b64_tr_b16 v[184:185], v212 offset:8192\n\t"
"v_mfma_f32_16x16x32_bf16 v[44:47], v[168:171], v[12:15], 0\n\t"
"s_waitcnt lgkmcnt(5)\n\t"
"v_exp_f32 v192, v72\n\t"
"v_mfma_f32_16x16x32_bf16 v[48:51], v[172:175], v[0:3], 0\n\t"
"v_exp_f32 v193, v73\n\t"
"v_mfma_f32_16x16x32_bf16 v[56:59], v[172:175], v[8:11], 0\n\t"
"buffer_load_dwordx4 v208, %[w2], s68 offen offset:2048 lds\n\t"
"v_mfma_f32_16x16x32_bf16 v[52:55], v[172:175], v[4:7], 0\n\t"
"s_waitcnt lgkmcnt(3)\n\t"
"ds_read_b64_tr_b16 v[186:187], v212 offset:10240\n\t"
"v_add_f32 v192, 1.0, v192\n\t"
"v_mfma_f32_16x16x32_bf16 v[60:63], v[172:175], v[12:15], 0\n\t"
"v_exp_f32 v194, v74\n\t"
"v_mfma_f32_16x16x32_bf16 v[32:35], v[176:179], v[16:19], v[32:35]\n\t"
"ds_read_b64_tr_b16 v[188:189], v215 offset:8192\n\t"
"v_add_f32 v193, 1.0, v193\n\t"
"v_mfma_f32_16x16x32_bf16 v[40:43], v[176:179], v[24:27], v[40:43]\n\t"
"v_rcp_f32 v192, v192\n\t"
"v_mfma_f32_16x16x32_bf16 v[36:39], v[176:179], v[20:23], v[36:39]\n\t"
"s_waitcnt lgkmcnt(3)\n\t"
"v_exp_f32 v195, v75\n\t"
"v_mfma_f32_16x16x32_bf16 v[44:47], v[176:179], v[28:31], v[44:47]\n\t"
"ds_read_b64_tr_b16 v[190:191], v215 offset:10240\n\t"
"v_add_f32 v194, 1.0, v194\n\t"
"v_mfma_f32_16x16x32_bf16 v[48:51], v[180:183], v[16:19], v[48:51]\n\t"
"v_rcp_f32 v193, v193\n\t"
"v_mfma_f32_16x16x32_bf16 v[56:59], v[180:183], v[24:27], v[56:59]\n\t"
"ds_read_b64_tr_b16 v[168:169], v212 offset:12288\n\t"
"v_mul_f32 v192, v72, v192\n\t"
"v_mfma_f32_16x16x32_bf16 v[52:55], v[180:183], v[20:23], v[52:55]\n\t"
"s_waitcnt lgkmcnt(3)\n\t"
"v_exp_f32 v196, v88\n\t"
"v_mfma_f32_16x16x32_bf16 v[60:63], v[180:183], v[28:31], v[60:63]\n\t"
"ds_read_b64_tr_b16 v[170:171], v212 offset:14336\n\t"
"v_add_f32 v195, 1.0, v195\n\t"
"v_mfma_f32_16x16x32_bf16 v[32:35], v[184:187], a[0:3], v[32:35]\n\t"
"v_rcp_f32 v194, v194\n\t"
"v_mfma_f32_16x16x32_bf16 v[40:43], v[184:187], a[8:11], v[40:43]\n\t"
"ds_read_b64_tr_b16 v[172:173], v215 offset:12288\n\t"
"v_mul_f32 v193, v73, v193\n\t"
"v_mfma_f32_16x16x32_bf16 v[36:39], v[184:187], a[4:7], v[36:39]\n\t"
"s_waitcnt lgkmcnt(3)\n\t"
"v_exp_f32 v197, v89\n\t"
"v_mfma_f32_16x16x32_bf16 v[44:47], v[184:187], a[12:15], v[44:47]\n\t"
"ds_read_b128 v[152:155], v243 offset:8192\n\t"
"ds_read_b128 v[156:159], v243 offset:9216\n\t"
"ds_read_b64_tr_b16 v[174:175], v215 offset:14336\n\t"
"v_add_f32 v196, 1.0, v196\n\t"
"v_mfma_f32_16x16x32_bf16 v[48:51], v[188:191], a[0:3], v[48:51]\n\t"
"buffer_load_dwordx4 v209, %[w2], s68 offen offset:3072 lds\n\t"
"v_mfma_f32_16x16x32_bf16 v[56:59], v[188:191], a[8:11], v[56:59]\n\t"
"s_add_u32 m0, s63, 102400\n\t"
"v_rcp_f32 v195, v195\n\t"
"v_mfma_f32_16x16x32_bf16 v[52:55], v[188:191], a[4:7], v[52:55]\n\t"
"s_waitcnt lgkmcnt(4)\n\t"
"ds_read_b64_tr_b16 v[176:177], v212 offset:16384\n\t"
"v_mul_f32 v194, v74, v194\n\t"
"v_mfma_f32_16x16x32_bf16 v[60:63], v[188:191], a[12:15], v[60:63]\n\t"
"ds_read_b64_tr_b16 v[178:179], v212 offset:18432\n\t"
"v_cvt_pk_bf16_f32 v160, v192, v193\n\t"
"v_mfma_f32_16x16x32_bf16 v[32:35], v[168:171], a[16:19], v[32:35]\n\t"
"v_exp_f32 v198, v90\n\t"
"v_mfma_f32_16x16x32_bf16 v[40:43], v[168:171], a[24:27], v[40:43]\n\t"
"ds_read_b64_tr_b16 v[180:181], v215 offset:16384\n\t"
"v_add_f32 v197, 1.0, v197\n\t"
"v_mfma_f32_16x16x32_bf16 v[36:39], v[168:171], a[20:23], v[36:39]\n\t"
"s_waitcnt lgkmcnt(3)\n\t"
"v_rcp_f32 v196, v196\n\t"
"v_mfma_f32_16x16x32_bf16 v[44:47], v[168:171], a[28:31], v[44:47]\n\t"
"ds_read_b64_tr_b16 v[182:183], v215 offset:18432\n\t"
"v_mul_f32 v195, v75, v195\n\t"
"v_mfma_f32_16x16x32_bf16 v[48:51], v[172:175], a[16:19], v[48:51]\n\t"
"buffer_load_dwordx4 v210, %[w2], s68 offen offset:0 lds\n\t"
"v_mfma_f32_16x16x32_bf16 v[56:59], v[172:175], a[24:27], v[56:59]\n\t"
"buffer_load_dwordx4 v211, %[w2], s68 offen offset:1024 lds\n\t"
"v_mfma_f32_16x16x32_bf16 v[52:55], v[172:175], a[20:23], v[52:55]\n\t"
"s_waitcnt lgkmcnt(2)\n\t"
"v_exp_f32 v199, v91\n\t"
"v_mfma_f32_16x16x32_bf16 v[60:63], v[172:175], a[28:31], v[60:63]\n\t"
"ds_read_b64_tr_b16 v[184:185], v212 offset:20480\n\t"
"ds_read_b64_tr_b16 v[186:187], v212 offset:22528\n\t"
"v_add_f32 v198, 1.0, v198\n\t"
"v_mfma_f32_16x16x32_bf16 v[32:35], v[176:179], a[32:35], v[32:35]\n\t"
"v_rcp_f32 v197, v197\n\t"
"v_mfma_f32_16x16x32_bf16 v[40:43], v[176:179], a[40:43], v[40:43]\n\t"
"ds_read_b64_tr_b16 v[188:189], v215 offset:20480\n\t"
"v_mul_f32 v196, v88, v196\n\t"
"v_mfma_f32_16x16x32_bf16 v[36:39], v[176:179], a[36:39], v[36:39]\n\t"
"s_waitcnt lgkmcnt(3)\n\t"
"ds_read_b64_tr_b16 v[190:191], v215 offset:22528\n\t"
"v_cvt_pk_bf16_f32 v161, v194, v195\n\t"
"v_mfma_f32_16x16x32_bf16 v[44:47], v[176:179], a[44:47], v[44:47]\n\t"
"s_add_u32 s68, s51, s53\n\t"
"v_exp_f32 v192, v76\n\t"
"v_mfma_f32_16x16x32_bf16 v[48:51], v[180:183], a[32:35], v[48:51]\n\t"
"s_add_u32 m0, s63, 122880\n\t"
"ds_read_b64_tr_b16 v[168:169], v212 offset:24576\n\t"
"v_add_f32 v199, 1.0, v199\n\t"
"v_mfma_f32_16x16x32_bf16 v[56:59], v[180:183], a[40:43], v[56:59]\n\t"
"buffer_load_dwordx4 v206, %[w2], s68 offen offset:0 lds\n\t"
"v_mfma_f32_16x16x32_bf16 v[52:55], v[180:183], a[36:39], v[52:55]\n\t"
"s_waitcnt lgkmcnt(3)\n\t"
"v_rcp_f32 v198, v198\n\t"
"v_mfma_f32_16x16x32_bf16 v[60:63], v[180:183], a[44:47], v[60:63]\n\t"
"ds_read_b64_tr_b16 v[170:171], v212 offset:26624\n\t"
"v_mul_f32 v197, v89, v197\n\t"
"v_mfma_f32_16x16x32_bf16 v[32:35], v[184:187], a[48:51], v[32:35]\n\t"
"v_exp_f32 v193, v77\n\t"
"v_mfma_f32_16x16x32_bf16 v[40:43], v[184:187], a[56:59], v[40:43]\n\t"
"ds_read_b64_tr_b16 v[172:173], v215 offset:24576\n\t"
"v_add_f32 v192, 1.0, v192\n\t"
"v_mfma_f32_16x16x32_bf16 v[36:39], v[184:187], a[52:55], v[36:39]\n\t"
"s_waitcnt lgkmcnt(3)\n\t"
"v_rcp_f32 v199, v199\n\t"
"v_mfma_f32_16x16x32_bf16 v[44:47], v[184:187], a[60:63], v[44:47]\n\t"
"ds_read_b64_tr_b16 v[174:175], v215 offset:26624\n\t"
"v_mul_f32 v198, v90, v198\n\t"
"v_mfma_f32_16x16x32_bf16 v[48:51], v[188:191], a[48:51], v[48:51]\n\t"
"ds_read_b64_tr_b16 v[176:177], v212 offset:28672\n\t"
"v_cvt_pk_bf16_f32 v162, v196, v197\n\t"
"v_mfma_f32_16x16x32_bf16 v[56:59], v[188:191], a[56:59], v[56:59]\n\t"
"v_exp_f32 v194, v78\n\t"
"v_mfma_f32_16x16x32_bf16 v[52:55], v[188:191], a[52:55], v[52:55]\n\t"
"s_waitcnt lgkmcnt(3)\n\t"
"ds_read_b64_tr_b16 v[178:179], v212 offset:30720\n\t"
"v_add_f32 v193, 1.0, v193\n\t"
"v_mfma_f32_16x16x32_bf16 v[60:63], v[188:191], a[60:63], v[60:63]\n\t"
"v_rcp_f32 v192, v192\n\t"
"v_mfma_f32_16x16x32_bf16 v[32:35], v[168:171], a[64:67], v[32:35]\n\t"
"s_lshl_b32 s58, s48, 4\n\t"
"ds_read_b64_tr_b16 v[180:181], v215 offset:28672\n\t"
"v_mul_f32 v199, v91, v199\n\t"
"v_mfma_f32_16x16x32_bf16 v[40:43], v[168:171], a[72:75], v[40:43]\n\t"
"s_add_u32 s69, s58, s57\n\t"
"v_exp_f32 v195, v79\n\t"
"v_mfma_f32_16x16x32_bf16 v[36:39], v[168:171], a[68:71], v[36:39]\n\t"
"s_waitcnt lgkmcnt(3)\n\t"
"ds_read_b64_tr_b16 v[182:183], v215 offset:30720\n\t"
"v_add_f32 v194, 1.0, v194\n\t"
"v_mfma_f32_16x16x32_bf16 v[44:47], v[168:171], a[76:79], v[44:47]\n\t"
"v_rcp_f32 v193, v193\n\t"
"v_mfma_f32_16x16x32_bf16 v[48:51], v[172:175], a[64:67], v[48:51]\n\t"
"buffer_load_dwordx4 v207, %[w2], s68 offen offset:1024 lds\n\t"
"v_mfma_f32_16x16x32_bf16 v[56:59], v[172:175], a[72:75], v[56:59]\n\t"
"ds_read_b64_tr_b16 v[184:185], v212 offset:32768\n\t"
"v_mul_f32 v192, v76, v192\n\t"
"v_mfma_f32_16x16x32_bf16 v[52:55], v[172:175], a[68:71], v[52:55]\n\t"
"s_waitcnt lgkmcnt(3)\n\t"
"ds_read_b64_tr_b16 v[186:187], v212 offset:34816\n\t"
"v_cvt_pk_bf16_f32 v163, v198, v199\n\t"
"v_mfma_f32_16x16x32_bf16 v[60:63], v[172:175], a[76:79], v[60:63]\n\t"
"v_exp_f32 v196, v92\n\t"
"v_mfma_f32_16x16x32_bf16 v[32:35], v[176:179], a[80:83], v[32:35]\n\t"
"ds_read_b64_tr_b16 v[188:189], v215 offset:32768\n\t"
"v_add_f32 v195, 1.0, v195\n\t"
"v_mfma_f32_16x16x32_bf16 v[40:43], v[176:179], a[88:91], v[40:43]\n\t"
"v_rcp_f32 v194, v194\n\t"
"v_mfma_f32_16x16x32_bf16 v[36:39], v[176:179], a[84:87], v[36:39]\n\t"
"s_waitcnt lgkmcnt(3)\n\t"
"ds_read_b64_tr_b16 v[190:191], v215 offset:34816\n\t"
"v_mul_f32 v193, v77, v193\n\t"
"v_mfma_f32_16x16x32_bf16 v[44:47], v[176:179], a[92:95], v[44:47]\n\t"
"buffer_store_dwordx4 v[160:163], v246, %[ws], s69 offen sc1\n\t"
"ds_write_b128 v242, v[160:163] offset:0\n\t"
"v_mfma_f32_16x16x32_bf16 v[48:51], v[180:183], a[80:83], v[48:51]\n\t"
"buffer_load_dwordx4 v208, %[w2], s68 offen offset:2048 lds\n\t"
"v_mfma_f32_16x16x32_bf16 v[56:59], v[180:183], a[88:91], v[56:59]\n\t"
"buffer_load_dwordx2 v[204:205], v248, %[ws], s78 offen offset:512 sc1\n\t"
"v_mfma_f32_16x16x32_bf16 v[52:55], v[180:183], a[84:87], v[52:55]\n\t"
"s_waitcnt lgkmcnt(3)\n\t"
"v_exp_f32 v197, v93\n\t"
"v_mfma_f32_16x16x32_bf16 v[60:63], v[180:183], a[92:95], v[60:63]\n\t"
"ds_read_b64_tr_b16 v[168:169], v212 offset:36864\n\t"
"ds_read_b64_tr_b16 v[170:171], v212 offset:38912\n\t"
"v_add_f32 v196, 1.0, v196\n\t"
"v_mfma_f32_16x16x32_bf16 v[32:35], v[184:187], a[96:99], v[32:35]\n\t"
"buffer_load_dwordx4 v209, %[w2], s68 offen offset:3072 lds\n\t"
"v_mfma_f32_16x16x32_bf16 v[40:43], v[184:187], a[104:107], v[40:43]\n\t"
"s_add_u32 m0, s63, 126976\n\t"
"v_rcp_f32 v195, v195\n\t"
"v_mfma_f32_16x16x32_bf16 v[36:39], v[184:187], a[100:103], v[36:39]\n\t"
"s_waitcnt lgkmcnt(3)\n\t"
"ds_read_b64_tr_b16 v[172:173], v215 offset:36864\n\t"
"v_mul_f32 v194, v78, v194\n\t"
"v_mfma_f32_16x16x32_bf16 v[44:47], v[184:187], a[108:111], v[44:47]\n\t"
"ds_read_b64_tr_b16 v[174:175], v215 offset:38912\n\t"
"v_cvt_pk_bf16_f32 v164, v192, v193\n\t"
"v_mfma_f32_16x16x32_bf16 v[48:51], v[188:191], a[96:99], v[48:51]\n\t"
"v_exp_f32 v198, v94\n\t"
"v_mfma_f32_16x16x32_bf16 v[56:59], v[188:191], a[104:107], v[56:59]\n\t"
"ds_read_b64_tr_b16 v[176:177], v212 offset:40960\n\t"
"v_add_f32 v197, 1.0, v197\n\t"
"v_mfma_f32_16x16x32_bf16 v[52:55], v[188:191], a[100:103], v[52:55]\n\t"
"s_waitcnt lgkmcnt(3)\n\t"
"v_rcp_f32 v196, v196\n\t"
"v_mfma_f32_16x16x32_bf16 v[60:63], v[188:191], a[108:111], v[60:63]\n\t"
"ds_read_b64_tr_b16 v[178:179], v212 offset:43008\n\t"
"v_mul_f32 v195, v79, v195\n\t"
"v_mfma_f32_16x16x32_bf16 v[32:35], v[168:171], a[112:115], v[32:35]\n\t"
"buffer_load_dwordx4 v210, %[w2], s68 offen offset:0 lds\n\t"
"v_mfma_f32_16x16x32_bf16 v[40:43], v[168:171], a[120:123], v[40:43]\n\t"
"v_exp_f32 v199, v95\n\t"
"v_mfma_f32_16x16x32_bf16 v[36:39], v[168:171], a[116:119], v[36:39]\n\t"
"s_waitcnt lgkmcnt(2)\n\t"
"ds_read_b64_tr_b16 v[180:181], v215 offset:40960\n\t"
"v_add_f32 v198, 1.0, v198\n\t"
"v_mfma_f32_16x16x32_bf16 v[44:47], v[168:171], a[124:127], v[44:47]\n\t"
"ds_read_b64_tr_b16 v[182:183], v215 offset:43008\n\t"
"v_rcp_f32 v197, v197\n\t"
"v_mfma_f32_16x16x32_bf16 v[48:51], v[172:175], a[112:115], v[48:51]\n\t"
"ds_read_b64_tr_b16 v[184:185], v212 offset:45056\n\t"
"v_mul_f32 v196, v92, v196\n\t"
"v_mfma_f32_16x16x32_bf16 v[56:59], v[172:175], a[120:123], v[56:59]\n\t"
"ds_read_b64_tr_b16 v[186:187], v212 offset:47104\n\t"
"v_cvt_pk_bf16_f32 v165, v194, v195\n\t"
"v_mfma_f32_16x16x32_bf16 v[52:55], v[172:175], a[116:119], v[52:55]\n\t"
"s_waitcnt lgkmcnt(4)\n\t"
"v_exp_f32 v192, v64\n\t"
"v_mfma_f32_16x16x32_bf16 v[60:63], v[172:175], a[124:127], v[60:63]\n\t"
"ds_read_b64_tr_b16 v[188:189], v215 offset:45056\n\t"
"ds_read_b64_tr_b16 v[190:191], v215 offset:47104\n\t"
"v_add_f32 v199, 1.0, v199\n\t"
"v_mfma_f32_16x16x32_bf16 v[32:35], v[176:179], a[128:131], v[32:35]\n\t"
"v_rcp_f32 v198, v198\n\t"
"v_mfma_f32_16x16x32_bf16 v[40:43], v[176:179], a[136:139], v[40:43]\n\t"
"buffer_load_dwordx4 v211, %[w2], s68 offen offset:1024 lds\n\t"
"v_mul_f32 v197, v93, v197\n\t"
"v_mfma_f32_16x16x32_bf16 v[36:39], v[176:179], a[132:135], v[36:39]\n\t"
"s_waitcnt vmcnt(16)\n\t"
"v_exp_f32 v193, v65\n\t"
"v_add_f32 v192, 1.0, v192\n\t"
"v_mfma_f32_16x16x32_bf16 v[44:47], v[176:179], a[140:143], v[44:47]\n\t"
"s_waitcnt lgkmcnt(4)\n\t"
"v_rcp_f32 v199, v199\n\t"
"v_mfma_f32_16x16x32_bf16 v[48:51], v[180:183], a[128:131], v[48:51]\n\t"
"s_waitcnt lgkmcnt(0)\n\t"
"v_mul_f32 v198, v94, v198\n\t"
"v_cvt_pk_bf16_f32 v166, v196, v197\n\t"
"v_mfma_f32_16x16x32_bf16 v[56:59], v[180:183], a[136:139], v[56:59]\n\t"
"s_barrier\n\t"
"v_exp_f32 v194, v66\n\t"
"v_mfma_f32_16x16x32_bf16 v[52:55], v[180:183], a[132:135], v[52:55]\n\t"
"s_add_u32 s68, s51, s54\n\t"
"ds_read_b64_tr_b16 v[168:169], v213 offset:0\n\t"
"v_add_f32 v193, 1.0, v193\n\t"
"v_mfma_f32_16x16x32_bf16 v[60:63], v[180:183], a[140:143], v[60:63]\n\t"
"v_rcp_f32 v192, v192\n\t"
"v_mfma_f32_16x16x32_bf16 v[32:35], v[184:187], a[144:147], v[32:35]\n\t"
"s_add_u32 s69, s69, 147456\n\t"
"ds_read_b64_tr_b16 v[170:171], v213 offset:2048\n\t"
"v_mul_f32 v199, v95, v199\n\t"
"v_mfma_f32_16x16x32_bf16 v[40:43], v[184:187], a[152:155], v[40:43]\n\t"
"s_add_u32 m0, s63, 0\n\t"
"v_exp_f32 v195, v67\n\t"
"v_mfma_f32_16x16x32_bf16 v[36:39], v[184:187], a[148:151], v[36:39]\n\t"
"ds_read_b64_tr_b16 v[172:173], v216 offset:0\n\t"
"v_add_f32 v194, 1.0, v194\n\t"
"v_mfma_f32_16x16x32_bf16 v[44:47], v[184:187], a[156:159], v[44:47]\n\t"
"ds_read_b64_tr_b16 v[174:175], v216 offset:2048\n\t"
"v_rcp_f32 v193, v193\n\t"
"v_mfma_f32_16x16x32_bf16 v[48:51], v[188:191], a[144:147], v[48:51]\n\t"
"ds_read_b64_tr_b16 v[176:177], v213 offset:4096\n\t"
"v_mul_f32 v192, v64, v192\n\t"
"v_mfma_f32_16x16x32_bf16 v[56:59], v[188:191], a[152:155], v[56:59]\n\t"
"ds_read_b64_tr_b16 v[178:179], v213 offset:6144\n\t"
"v_cvt_pk_bf16_f32 v167, v198, v199\n\t"
"v_mfma_f32_16x16x32_bf16 v[52:55], v[188:191], a[148:151], v[52:55]\n\t"
"s_waitcnt lgkmcnt(4)\n\t"
"v_exp_f32 v196, v80\n\t"
"v_mfma_f32_16x16x32_bf16 v[60:63], v[188:191], a[156:159], v[60:63]\n\t"
"buffer_load_dwordx4 v206, %[w2], s68 offen offset:0 lds\n\t"
"v_mfma_f32_16x16x32_bf16 v[32:35], v[168:171], a[160:163], v[32:35]\n\t"
"ds_read_b64_tr_b16 v[180:181], v216 offset:4096\n\t"
"v_add_f32 v195, 1.0, v195\n\t"
"v_mfma_f32_16x16x32_bf16 v[40:43], v[168:171], a[168:171], v[40:43]\n\t"
"v_rcp_f32 v194, v194\n\t"
"v_mfma_f32_16x16x32_bf16 v[36:39], v[168:171], a[164:167], v[36:39]\n\t"
"s_waitcnt lgkmcnt(3)\n\t"
"buffer_store_dwordx4 v[164:167], v246, %[ws], s69 offen sc1\n\t"
"ds_write_b128 v242, v[164:167] offset:1024\n\t"
"v_mul_f32 v193, v65, v193\n\t"
"v_mfma_f32_16x16x32_bf16 v[44:47], v[168:171], a[172:175], v[44:47]\n\t"
"ds_read_b64_tr_b16 v[182:183], v216 offset:6144\n\t"
"v_exp_f32 v197, v81\n\t"
"v_mfma_f32_16x16x32_bf16 v[48:51], v[172:175], a[160:163], v[48:51]\n\t"
"ds_read_b64_tr_b16 v[184:185], v213 offset:8192\n\t"
"v_add_f32 v196, 1.0, v196\n\t"
"v_mfma_f32_16x16x32_bf16 v[56:59], v[172:175], a[168:171], v[56:59]\n\t"
"v_rcp_f32 v195, v195\n\t"
"v_mfma_f32_16x16x32_bf16 v[52:55], v[172:175], a[164:167], v[52:55]\n\t"
"s_waitcnt lgkmcnt(4)\n\t"
"v_mul_f32 v194, v66, v194\n\t"
"v_cvt_pk_bf16_f32 v0, v192, v193\n\t"
"v_mfma_f32_16x16x32_bf16 v[60:63], v[172:175], a[172:175], v[60:63]\n\t"
"ds_read_b64_tr_b16 v[186:187], v213 offset:10240\n\t"
"buffer_load_dwordx4 v207, %[w2], s68 offen offset:1024 lds\n\t"
"v_mfma_f32_16x16x32_bf16 v[32:35], v[176:179], a[176:179], v[32:35]\n\t"
"v_exp_f32 v198, v82\n\t"
"v_mfma_f32_16x16x32_bf16 v[40:43], v[176:179], a[184:187], v[40:43]\n\t"
"ds_read_b64_tr_b16 v[188:189], v216 offset:8192\n\t"
"v_add_f32 v197, 1.0, v197\n\t"
"v_mfma_f32_16x16x32_bf16 v[36:39], v[176:179], a[180:183], v[36:39]\n\t"
"s_waitcnt lgkmcnt(3)\n\t"
"v_rcp_f32 v196, v196\n\t"
"v_mfma_f32_16x16x32_bf16 v[44:47], v[176:179], a[188:191], v[44:47]\n\t"
"ds_read_b64_tr_b16 v[190:191], v216 offset:10240\n\t"
"v_mul_f32 v195, v67, v195\n\t"
"v_mfma_f32_16x16x32_bf16 v[48:51], v[180:183], a[176:179], v[48:51]\n\t"
"v_exp_f32 v199, v83\n\t"
"v_mfma_f32_16x16x32_bf16 v[56:59], v[180:183], a[184:187], v[56:59]\n\t"
"ds_read_b64_tr_b16 v[168:169], v213 offset:12288\n\t"
"v_add_f32 v198, 1.0, v198\n\t"
"v_mfma_f32_16x16x32_bf16 v[52:55], v[180:183], a[180:183], v[52:55]\n\t"
"s_waitcnt lgkmcnt(3)\n\t"
"v_rcp_f32 v197, v197\n\t"
"v_mfma_f32_16x16x32_bf16 v[60:63], v[180:183], a[188:191], v[60:63]\n\t"
"ds_read_b64_tr_b16 v[170:171], v213 offset:14336\n\t"
"buffer_load_dwordx4 v208, %[w2], s68 offen offset:2048 lds\n\t"
"v_mul_f32 v196, v80, v196\n\t"
"v_mfma_f32_16x16x32_bf16 v[32:35], v[184:187], a[192:195], v[32:35]\n\t"
"ds_read_b64_tr_b16 v[172:173], v216 offset:12288\n\t"
"v_cvt_pk_bf16_f32 v1, v194, v195\n\t"
"v_mfma_f32_16x16x32_bf16 v[40:43], v[184:187], a[200:203], v[40:43]\n\t"
"v_exp_f32 v192, v68\n\t"
"v_mfma_f32_16x16x32_bf16 v[36:39], v[184:187], a[196:199], v[36:39]\n\t"
"s_waitcnt lgkmcnt(3)\n\t"
"ds_read_b64_tr_b16 v[174:175], v216 offset:14336\n\t"
"v_add_f32 v199, 1.0, v199\n\t"
"v_mfma_f32_16x16x32_bf16 v[44:47], v[184:187], a[204:207], v[44:47]\n\t"
"v_rcp_f32 v198, v198\n\t"
"v_mul_f32 v197, v81, v197\n\t"
"v_mfma_f32_16x16x32_bf16 v[48:51], v[188:191], a[192:195], v[48:51]\n\t"
"buffer_load_dwordx4 v209, %[w2], s68 offen offset:3072 lds\n\t"
"v_mfma_f32_16x16x32_bf16 v[56:59], v[188:191], a[200:203], v[56:59]\n\t"
"s_add_u32 m0, s63, 4096\n\t"
"v_exp_f32 v193, v69\n\t"
"v_add_f32 v192, 1.0, v192\n\t"
"v_mfma_f32_16x16x32_bf16 v[52:55], v[188:191], a[196:199], v[52:55]\n\t"
"s_waitcnt lgkmcnt(2)\n\t"
"v_rcp_f32 v199, v199\n\t"
"v_mfma_f32_16x16x32_bf16 v[60:63], v[188:191], a[204:207], v[60:63]\n\t"
"ds_read_b64_tr_b16 v[176:177], v213 offset:16384\n\t"
"ds_read_b64_tr_b16 v[178:179], v213 offset:18432\n\t"
"v_mul_f32 v198, v82, v198\n\t"
"v_mfma_f32_16x16x32_bf16 v[32:35], v[168:171], a[208:211], v[32:35]\n\t"
"ds_read_b64_tr_b16 v[180:181], v216 offset:16384\n\t"
"v_cvt_pk_bf16_f32 v2, v196, v197\n\t"
"v_mfma_f32_16x16x32_bf16 v[40:43], v[168:171], a[216:219], v[40:43]\n\t"
"v_exp_f32 v194, v70\n\t"
"v_mfma_f32_16x16x32_bf16 v[36:39], v[168:171], a[212:215], v[36:39]\n\t"
"s_waitcnt lgkmcnt(3)\n\t"
"ds_read_b64_tr_b16 v[182:183], v216 offset:18432\n\t"
"v_add_f32 v193, 1.0, v193\n\t"
"v_mfma_f32_16x16x32_bf16 v[44:47], v[168:171], a[220:223], v[44:47]\n\t"
"v_rcp_f32 v192, v192\n\t"
"v_mfma_f32_16x16x32_bf16 v[48:51], v[172:175], a[208:211], v[48:51]\n\t"
"s_add_u32 s69, s58, s56\n\t"
"ds_read_b64_tr_b16 v[184:185], v213 offset:20480\n\t"
"v_mul_f32 v199, v83, v199\n\t"
"v_mfma_f32_16x16x32_bf16 v[56:59], v[172:175], a[216:219], v[56:59]\n\t"
"v_exp_f32 v195, v71\n\t"
"v_mfma_f32_16x16x32_bf16 v[52:55], v[172:175], a[212:215], v[52:55]\n\t"
"buffer_load_dwordx4 v210, %[w2], s68 offen offset:0 lds\n\t"
"v_add_f32 v194, 1.0, v194\n\t"
"v_mfma_f32_16x16x32_bf16 v[60:63], v[172:175], a[220:223], v[60:63]\n\t"
"s_waitcnt lgkmcnt(3)\n\t"
"ds_read_b64_tr_b16 v[186:187], v213 offset:22528\n\t"
"v_rcp_f32 v193, v193\n\t"
"v_mfma_f32_16x16x32_bf16 v[32:35], v[176:179], a[224:227], v[32:35]\n\t"
"v_mul_f32 v192, v68, v192\n\t"
"v_cvt_pk_bf16_f32 v3, v198, v199\n\t"
"v_mfma_f32_16x16x32_bf16 v[40:43], v[176:179], a[232:235], v[40:43]\n\t"
"v_exp_f32 v196, v84\n\t"
"v_mfma_f32_16x16x32_bf16 v[36:39], v[176:179], a[228:231], v[36:39]\n\t"
"s_waitcnt lgkmcnt(2)\n\t"
"ds_read_b64_tr_b16 v[188:189], v216 offset:20480\n\t"
"v_add_f32 v195, 1.0, v195\n\t"
"v_mfma_f32_16x16x32_bf16 v[44:47], v[176:179], a[236:239], v[44:47]\n\t"
"ds_read_b64_tr_b16 v[190:191], v216 offset:22528\n\t"
"s_waitcnt vmcnt(9)\n\t"
"v_cmp_eq_u64 vcc, v[204:205], v[254:255]\n\t"
"s_cmp_eq_u64 vcc, -1\n\t"
"s_cbranch_scc1 103f\n\t"
"s_mov_b32 s98, 65536\n\t"
"102:\n\t"
"s_sleep 3\n\t"
"buffer_load_dwordx2 v[204:205], v248, %[ws], s78 offen offset:512 sc1\n\t"
"s_waitcnt vmcnt(0)\n\t"
"v_cmp_eq_u64 vcc, v[204:205], v[254:255]\n\t"
"s_cmp_eq_u64 vcc, -1\n\t"
"s_cbranch_scc1 103f\n\t"
"s_sub_u32 s98, s98, 1\n\t"
"s_cmp_lg_u32 s98, 0\n\t"
"s_cbranch_scc1 102b\n\t"
"103:\n\t"
"v_mfma_f32_16x16x32_bf16 v[48:51], v[180:183], a[224:227], v[48:51]\n\t"
"buffer_store_dwordx4 v[0:3], v246, %[ws], s69 offen sc1\n\t"
"ds_read_b64_tr_b16 a[0:1], v219 offset:24576\n\t"
"v_rcp_f32 v194, v194\n\t"
"v_mul_f32 v193, v69, v193\n\t"
"v_mfma_f32_16x16x32_bf16 v[56:59], v[180:183], a[232:235], v[56:59]\n\t"
"s_mul_i32 s42, s94, 768\n\t"
"ds_read_b64_tr_b16 a[2:3], v219 offset:26624\n\t"
"buffer_load_dwordx4 v211, %[w2], s68 offen offset:1024 lds\n\t"
"v_mfma_f32_16x16x32_bf16 v[52:55], v[180:183], a[228:231], v[52:55]\n\t"
"s_add_u32 s68, s50, s55\n\t"
"v_exp_f32 v197, v85\n\t"
"v_mfma_f32_16x16x32_bf16 v[60:63], v[180:183], a[236:239], v[60:63]\n\t"
"s_waitcnt lgkmcnt(4)\n\t"
"ds_read_b64_tr_b16 a[4:5], v222 offset:24576\n\t"
"ds_read_b64_tr_b16 a[6:7], v222 offset:26624\n\t"
"v_add_f32 v196, 1.0, v196\n\t"
"v_mfma_f32_16x16x32_bf16 v[32:35], v[184:187], a[240:243], v[32:35]\n\t"
"s_add_u32 m0, s63, 24576\n\t"
"v_rcp_f32 v195, v195\n\t"
"v_mfma_f32_16x16x32_bf16 v[40:43], v[184:187], a[248:251], v[40:43]\n\t"
"s_add_u32 s42, s42, s44\n\t"
"ds_read_b64_tr_b16 a[8:9], v225 offset:24576\n\t"
"v_mul_f32 v194, v70, v194\n\t"
"v_mfma_f32_16x16x32_bf16 v[36:39], v[184:187], a[244:247], v[36:39]\n\t"
"s_waitcnt lgkmcnt(5)\n\t"
"ds_read_b64_tr_b16 a[10:11], v225 offset:26624\n\t"
"v_cvt_pk_bf16_f32 v4, v192, v193\n\t"
"v_mfma_f32_16x16x32_bf16 v[44:47], v[184:187], a[252:255], v[44:47]\n\t"
"ds_read_b64_tr_b16 a[12:13], v228 offset:24576\n\t"
"ds_read_b64_tr_b16 a[14:15], v228 offset:26624\n\t"
"v_exp_f32 v198, v86\n\t"
"v_mfma_f32_16x16x32_bf16 v[48:51], v[188:191], a[240:243], v[48:51]\n\t"
"ds_read_b64_tr_b16 a[16:17], v231 offset:24576\n\t"
"v_add_f32 v197, 1.0, v197\n\t"
"v_mfma_f32_16x16x32_bf16 v[56:59], v[188:191], a[248:251], v[56:59]\n\t"
"buffer_load_dwordx4 v206, %[w2], s68 offen offset:0 lds\n\t"
"v_rcp_f32 v196, v196\n\t"
"v_mul_f32 v195, v71, v195\n\t"
"v_mfma_f32_16x16x32_bf16 v[52:55], v[188:191], a[244:247], v[52:55]\n\t"
"s_add_u32 s69, s69, 147456\n\t"
"v_exp_f32 v199, v87\n\t"
"v_add_f32 v198, 1.0, v198\n\t"
"v_rcp_f32 v197, v197\n\t"
"v_mul_f32 v196, v84, v196\n\t"
"v_cvt_pk_bf16_f32 v5, v194, v195\n\t"
"v_add_f32 v199, 1.0, v199\n\t"
"v_rcp_f32 v198, v198\n\t"
"v_mul_f32 v197, v85, v197\n\t"
"v_rcp_f32 v199, v199\n\t"
"v_mul_f32 v198, v86, v198\n\t"
"v_cvt_pk_bf16_f32 v6, v196, v197\n\t"
"v_mul_f32 v199, v87, v199\n\t"
"v_cvt_pk_bf16_f32 v7, v198, v199\n\t"
"buffer_store_dwordx4 v[4:7], v246, %[ws], s69 offen sc1\n\t"
"v_mfma_f32_16x16x32_bf16 v[60:63], v[188:191], a[252:255], v[60:63]\n\t"
"s_waitcnt lgkmcnt(5)\n\t"
"ds_read_b64_tr_b16 a[18:19], v231 offset:26624\n\t"
"ds_read_b64_tr_b16 a[20:21], v234 offset:24576\n\t"
"ds_read_b64_tr_b16 a[22:23], v234 offset:26624\n\t"
"v_mfma_f32_16x16x32_bf16 v[64:67], a[0:3], v[96:99], 0\n\t"
"buffer_load_dwordx4 v207, %[w2], s68 offen offset:1024 lds\n\t"
"v_mfma_f32_16x16x32_bf16 v[68:71], a[0:3], v[100:103], 0\n\t"
"s_lshl_b32 s42, s42, 4\n\t"
"ds_read_b64_tr_b16 a[24:25], v237 offset:24576\n\t"
"v_mfma_f32_16x16x32_bf16 v[72:75], a[4:7], v[96:99], 0\n\t"
"s_waitcnt lgkmcnt(5)\n\t"
"ds_read_b64_tr_b16 a[26:27], v237 offset:26624\n\t"
"buffer_load_dwordx4 v208, %[w2], s68 offen offset:2048 lds\n\t"
"v_mfma_f32_16x16x32_bf16 v[76:79], a[4:7], v[100:103], 0\n\t"
"s_add_u32 s42, s42, s56\n\t"
"ds_read_b64_tr_b16 a[28:29], v240 offset:24576\n\t"
"ds_read_b64_tr_b16 a[30:31], v240 offset:26624\n\t"
"v_exp_f32 v192, v40\n\t"
"v_mfma_f32_16x16x32_bf16 v[80:83], a[8:11], v[96:99], 0\n\t"
"v_exp_f32 v193, v41\n\t"
"v_mfma_f32_16x16x32_bf16 v[84:87], a[8:11], v[100:103], 0\n\t"
"ds_read_b64_tr_b16 a[32:33], v219 offset:32768\n\t"
"v_add_f32 v192, 1.0, v192\n\t"
"v_mfma_f32_16x16x32_bf16 v[88:91], a[12:15], v[96:99], 0\n\t"
"s_waitcnt lgkmcnt(5)\n\t"
"v_exp_f32 v194, v42\n\t"
"v_mfma_f32_16x16x32_bf16 v[92:95], a[12:15], v[100:103], 0\n\t"
"ds_read_b64_tr_b16 a[34:35], v219 offset:34816\n\t"
"ds_read_b64_tr_b16 a[36:37], v222 offset:32768\n\t"
"ds_read_b64_tr_b16 a[38:39], v222 offset:34816\n\t"
"v_add_f32 v193, 1.0, v193\n\t"
"v_mfma_f32_16x16x32_bf16 v[64:67], a[16:19], v[104:107], v[64:67]\n\t"
"v_rcp_f32 v192, v192\n\t"
"v_mfma_f32_16x16x32_bf16 v[68:71], a[16:19], v[108:111], v[68:71]\n\t"
"buffer_load_dwordx4 a[48:51], v246, %[ws], s42 offen sc1\n\t"
"v_mfma_f32_16x16x32_bf16 v[72:75], a[20:23], v[104:107], v[72:75]\n\t"
"s_waitcnt lgkmcnt(4)\n\t"
"v_exp_f32 v195, v43\n\t"
"v_mfma_f32_16x16x32_bf16 v[76:79], a[20:23], v[108:111], v[76:79]\n\t"
"ds_read_b64_tr_b16 a[40:41], v225 offset:32768\n\t"
"ds_read_b64_tr_b16 a[42:43], v225 offset:34816\n\t"
"ds_read_b64_tr_b16 a[44:45], v228 offset:32768\n\t"
"ds_read_b64_tr_b16 a[46:47], v228 offset:34816\n\t"
"v_add_f32 v194, 1.0, v194\n\t"
"v_mfma_f32_16x16x32_bf16 v[80:83], a[24:27], v[104:107], v[80:83]\n\t"
"v_rcp_f32 v193, v193\n\t"
"v_mfma_f32_16x16x32_bf16 v[84:87], a[24:27], v[108:111], v[84:87]\n\t"
"ds_read_b64_tr_b16 a[0:1], v231 offset:32768\n\t"
"v_mul_f32 v192, v40, v192\n\t"
"v_mfma_f32_16x16x32_bf16 v[88:91], a[28:31], v[104:107], v[88:91]\n\t"
"s_waitcnt lgkmcnt(5)\n\t"
"v_exp_f32 v196, v56\n\t"
"v_mfma_f32_16x16x32_bf16 v[92:95], a[28:31], v[108:111], v[92:95]\n\t"
"ds_read_b64_tr_b16 a[2:3], v231 offset:34816\n\t"
"ds_read_b64_tr_b16 a[4:5], v234 offset:32768\n\t"
"ds_read_b64_tr_b16 a[6:7], v234 offset:34816\n\t"
"v_add_f32 v195, 1.0, v195\n\t"
"v_mfma_f32_16x16x32_bf16 v[64:67], a[32:35], v[112:115], v[64:67]\n\t"
"v_rcp_f32 v194, v194\n\t"
"v_mfma_f32_16x16x32_bf16 v[68:71], a[32:35], v[116:119], v[68:71]\n\t"
"ds_read_b64_tr_b16 a[8:9], v237 offset:32768\n\t"
"v_mul_f32 v193, v41, v193\n\t"
"v_mfma_f32_16x16x32_bf16 v[72:75], a[36:39], v[112:115], v[72:75]\n\t"
"s_waitcnt lgkmcnt(5)\n\t"
"v_exp_f32 v197, v57\n\t"
"v_add_f32 v196, 1.0, v196\n\t"
"v_mfma_f32_16x16x32_bf16 v[76:79], a[36:39], v[116:119], v[76:79]\n\t"
"ds_read_b64_tr_b16 a[10:11], v237 offset:34816\n\t"
"ds_read_b64_tr_b16 a[12:13], v240 offset:32768\n\t"
"ds_read_b64_tr_b16 a[14:15], v240 offset:34816\n\t"
"v_rcp_f32 v195, v195\n\t"
"v_mfma_f32_16x16x32_bf16 v[80:83], a[40:43], v[112:115], v[80:83]\n\t"
"buffer_load_dwordx4 v209, %[w2], s68 offen offset:3072 lds\n\t"
"v_mul_f32 v194, v42, v194\n\t"
"v_mfma_f32_16x16x32_bf16 v[84:87], a[40:43], v[116:119], v[84:87]\n\t"
"s_add_u32 m0, s63, 28672\n\t"
"ds_read_b64_tr_b16 a[16:17], v219 offset:40960\n\t"
"v_cvt_pk_bf16_f32 v160, v192, v193\n\t"
"v_mfma_f32_16x16x32_bf16 v[88:91], a[44:47], v[112:115], v[88:91]\n\t"
"s_waitcnt lgkmcnt(5)\n\t"
"v_exp_f32 v198, v58\n\t"
"v_add_f32 v197, 1.0, v197\n\t"
"v_mfma_f32_16x16x32_bf16 v[92:95], a[44:47], v[116:119], v[92:95]\n\t"
"ds_read_b64_tr_b16 a[18:19], v219 offset:43008\n\t"
"ds_read_b64_tr_b16 a[20:21], v222 offset:40960\n\t"
"ds_read_b64_tr_b16 a[22:23], v222 offset:43008\n\t"
"buffer_load_dwordx4 a[56:59], v247, %[ws], s42 offen sc1\n\t"
"v_rcp_f32 v196, v196\n\t"
"v_mfma_f32_16x16x32_bf16 v[64:67], a[0:3], v[120:123], v[64:67]\n\t"
"v_mul_f32 v195, v43, v195\n\t"
"v_exp_f32 v199, v59\n\t"
"v_mfma_f32_16x16x32_bf16 v[68:71], a[0:3], v[124:127], v[68:71]\n\t"
"s_add_u32 s42, s42, 147456\n\t"
"ds_read_b64_tr_b16 a[24:25], v225 offset:40960\n\t"
"v_add_f32 v198, 1.0, v198\n\t"
"v_mfma_f32_16x16x32_bf16 v[72:75], a[4:7], v[120:123], v[72:75]\n\t"
"s_waitcnt lgkmcnt(5)\n\t"
"v_rcp_f32 v197, v197\n\t"
"v_mfma_f32_16x16x32_bf16 v[76:79], a[4:7], v[124:127], v[76:79]\n\t"
"ds_read_b64_tr_b16 a[26:27], v225 offset:43008\n\t"
"ds_read_b64_tr_b16 a[28:29], v228 offset:40960\n\t"
"ds_read_b64_tr_b16 a[30:31], v228 offset:43008\n\t"
"buffer_load_dwordx4 v210, %[w2], s68 offen offset:0 lds\n\t"
"v_mul_f32 v196, v56, v196\n\t"
"v_mfma_f32_16x16x32_bf16 v[80:83], a[8:11], v[120:123], v[80:83]\n\t"
"v_cvt_pk_bf16_f32 v161, v194, v195\n\t"
"v_exp_f32 v192, v44\n\t"
"v_mfma_f32_16x16x32_bf16 v[84:87], a[8:11], v[124:127], v[84:87]\n\t"
"ds_read_b64_tr_b16 a[32:33], v231 offset:40960\n\t"
"v_add_f32 v199, 1.0, v199\n\t"
"v_mfma_f32_16x16x32_bf16 v[88:91], a[12:15], v[120:123], v[88:91]\n\t"
"s_waitcnt lgkmcnt(5)\n\t"
"v_rcp_f32 v198, v198\n\t"
"v_mfma_f32_16x16x32_bf16 v[92:95], a[12:15], v[124:127], v[92:95]\n\t"
"ds_read_b64_tr_b16 a[34:35], v231 offset:43008\n\t"
"ds_read_b64_tr_b16 a[36:37], v234 offset:40960\n\t"
"ds_read_b64_tr_b16 a[38:39], v234 offset:43008\n\t"
"ds_read_b64_tr_b16 a[40:41], v237 offset:40960\n\t"
"ds_read_b64_tr_b16 a[42:43], v237 offset:43008\n\t"
"ds_read_b64_tr_b16 a[44:45], v240 offset:40960\n\t"
"ds_read_b64_tr_b16 a[46:47], v240 offset:43008\n\t"
"v_mul_f32 v197, v57, v197\n\t"
"v_mfma_f32_16x16x32_bf16 v[64:67], a[16:19], v[128:131], v[64:67]\n\t"
"buffer_load_dwordx4 a[52:55], v246, %[ws], s42 offen sc1\n\t"
"v_exp_f32 v193, v45\n\t"
"v_mfma_f32_16x16x32_bf16 v[68:71], a[16:19], v[132:135], v[68:71]\n\t"
"buffer_load_dwordx4 v211, %[w2], s68 offen offset:1024 lds\n\t"
"v_add_f32 v192, 1.0, v192\n\t"
"v_mfma_f32_16x16x32_bf16 v[72:75], a[20:23], v[128:131], v[72:75]\n\t"
"s_waitcnt vmcnt(18)\n\t"
"v_rcp_f32 v199, v199\n\t"
"v_mul_f32 v198, v58, v198\n\t"
"v_mfma_f32_16x16x32_bf16 v[76:79], a[20:23], v[132:135], v[76:79]\n\t"
"s_waitcnt lgkmcnt(8)\n\t"
"v_cvt_pk_bf16_f32 v162, v196, v197\n\t"
"v_exp_f32 v194, v46\n\t"
"v_mfma_f32_16x16x32_bf16 v[80:83], a[24:27], v[128:131], v[80:83]\n\t"
"s_waitcnt lgkmcnt(0)\n\t"
"v_add_f32 v193, 1.0, v193\n\t"
"v_rcp_f32 v192, v192\n\t"
"v_mfma_f32_16x16x32_bf16 v[84:87], a[24:27], v[132:135], v[84:87]\n\t"
"s_barrier\n\t"
"s_lshl_b32 s58, s49, 4\n\t"
"v_mul_f32 v199, v59, v199\n\t"
"v_mfma_f32_16x16x32_bf16 v[88:91], a[28:31], v[128:131], v[88:91]\n\t"
"s_add_u32 s69, s58, s57\n\t"
"v_exp_f32 v195, v47\n\t"
"v_mfma_f32_16x16x32_bf16 v[92:95], a[28:31], v[132:135], v[92:95]\n\t"
"ds_read_b64_tr_b16 a[0:1], v220 offset:0\n\t"
"buffer_load_dwordx4 a[60:63], v247, %[ws], s42 offen sc1\n\t"
"v_mfma_f32_16x16x32_bf16 v[64:67], a[32:35], v[136:139], v[64:67]\n\t"
"s_mul_i32 s42, s94, 768\n\t"
"ds_read_b64_tr_b16 a[2:3], v220 offset:2048\n\t"
"v_add_f32 v194, 1.0, v194\n\t"
"v_mfma_f32_16x16x32_bf16 v[68:71], a[32:35], v[140:143], v[68:71]\n\t"
"s_add_u32 s42, s42, s45\n\t"
"v_rcp_f32 v193, v193\n\t"
"v_mfma_f32_16x16x32_bf16 v[72:75], a[36:39], v[136:139], v[72:75]\n\t"
"s_add_u32 s68, s51, s55\n\t"
"ds_read_b64_tr_b16 a[4:5], v223 offset:0\n\t"
"ds_read_b64_tr_b16 a[6:7], v223 offset:2048\n\t"
"v_mul_f32 v192, v44, v192\n\t"
"v_mfma_f32_16x16x32_bf16 v[76:79], a[36:39], v[140:143], v[76:79]\n\t"
"ds_read_b64_tr_b16 a[8:9], v226 offset:0\n\t"
"ds_read_b64_tr_b16 a[10:11], v226 offset:2048\n\t"
"v_cvt_pk_bf16_f32 v163, v198, v199\n\t"
"v_mfma_f32_16x16x32_bf16 v[80:83], a[40:43], v[136:139], v[80:83]\n\t"
"s_add_u32 m0, s63, 49152\n\t"
"ds_read_b64_tr_b16 a[12:13], v229 offset:0\n\t"
"ds_read_b64_tr_b16 a[14:15], v229 offset:2048\n\t"
"v_exp_f32 v196, v60\n\t"
"v_mfma_f32_16x16x32_bf16 v[84:87], a[40:43], v[140:143], v[84:87]\n\t"
"ds_read_b64_tr_b16 a[16:17], v232 offset:0\n\t"
"v_add_f32 v195, 1.0, v195\n\t"
"v_mfma_f32_16x16x32_bf16 v[88:91], a[44:47], v[136:139], v[88:91]\n\t"
"s_waitcnt lgkmcnt(5)\n\t"
"v_rcp_f32 v194, v194\n\t"
"v_mul_f32 v193, v45, v193\n\t"
"v_mfma_f32_16x16x32_bf16 v[92:95], a[44:47], v[140:143], v[92:95]\n\t"
"buffer_store_dwordx4 v[160:163], v246, %[ws], s69 offen sc1\n\t"
"ds_read_b64_tr_b16 a[18:19], v232 offset:2048\n\t"
"ds_read_b64_tr_b16 a[20:21], v235 offset:0\n\t"
"ds_read_b64_tr_b16 a[22:23], v235 offset:2048\n\t"
"ds_write_b128 v242, v[160:163] offset:8192\n\t"
"v_mfma_f32_16x16x32_bf16 v[168:171], a[0:3], v[96:99], 0\n\t"
"buffer_load_dwordx4 v206, %[w2], s68 offen offset:0 lds\n\t"
"v_mfma_f32_16x16x32_bf16 v[172:175], a[0:3], v[100:103], 0\n\t"
"s_lshl_b32 s42, s42, 4\n\t"
"v_exp_f32 v197, v61\n\t"
"v_add_f32 v196, 1.0, v196\n\t"
"v_mfma_f32_16x16x32_bf16 v[176:179], a[4:7], v[96:99], 0\n\t"
"s_waitcnt lgkmcnt(5)\n\t"
"v_rcp_f32 v195, v195\n\t"
"v_mfma_f32_16x16x32_bf16 v[180:183], a[4:7], v[100:103], 0\n\t"
"ds_read_b64_tr_b16 a[24:25], v238 offset:0\n\t"
"ds_read_b64_tr_b16 a[26:27], v238 offset:2048\n\t"
"ds_read_b64_tr_b16 a[28:29], v241 offset:0\n\t"
"ds_read_b64_tr_b16 a[30:31], v241 offset:2048\n\t"
"v_mul_f32 v194, v46, v194\n\t"
"v_cvt_pk_bf16_f32 v164, v192, v193\n\t"
"v_mfma_f32_16x16x32_bf16 v[184:187], a[8:11], v[96:99], 0\n\t"
"s_add_u32 s42, s42, s56\n\t"
"v_exp_f32 v198, v62\n\t"
"v_mfma_f32_16x16x32_bf16 v[188:191], a[8:11], v[100:103], 0\n\t"
"ds_read_b64_tr_b16 a[32:33], v220 offset:8192\n\t"
"v_add_f32 v197, 1.0, v197\n\t"
"v_mfma_f32_16x16x32_bf16 v[200:203], a[12:15], v[96:99], 0\n\t"
"s_waitcnt lgkmcnt(6)\n\t"
"v_rcp_f32 v196, v196\n\t"
"v_mfma_f32_16x16x32_bf16 v[212:215], a[12:15], v[100:103], 0\n\t"
"ds_read_b64_tr_b16 a[34:35], v220 offset:10240\n\t"
"ds_read_b64_tr_b16 a[36:37], v223 offset:8192\n\t"
"ds_read_b64_tr_b16 a[38:39], v223 offset:10240\n\t"
"buffer_load_dwordx4 v207, %[w2], s68 offen offset:1024 lds\n\t"
"v_mul_f32 v195, v47, v195\n\t"
"v_mfma_f32_16x16x32_bf16 v[168:171], a[16:19], v[104:107], v[168:171]\n\t"
"v_exp_f32 v199, v63\n\t"
"v_add_f32 v198, 1.0, v198\n\t"
"v_mfma_f32_16x16x32_bf16 v[172:175], a[16:19], v[108:111], v[172:175]\n\t"
"v_rcp_f32 v197, v197\n\t"
"v_mfma_f32_16x16x32_bf16 v[176:179], a[20:23], v[104:107], v[176:179]\n\t"
"s_waitcnt lgkmcnt(4)\n\t"
"ds_read_b64_tr_b16 a[40:41], v226 offset:8192\n\t"
"v_mul_f32 v196, v60, v196\n\t"
"v_mfma_f32_16x16x32_bf16 v[180:183], a[20:23], v[108:111], v[180:183]\n\t"
"ds_read_b64_tr_b16 a[42:43], v226 offset:10240\n\t"
"ds_read_b64_tr_b16 a[44:45], v229 offset:8192\n\t"
"ds_read_b64_tr_b16 a[46:47], v229 offset:10240\n\t"
"v_cvt_pk_bf16_f32 v165, v194, v195\n\t"
"v_mfma_f32_16x16x32_bf16 v[184:187], a[24:27], v[104:107], v[184:187]\n\t"
"v_exp_f32 v192, v32\n\t"
"v_add_f32 v199, 1.0, v199\n\t"
"v_mfma_f32_16x16x32_bf16 v[188:191], a[24:27], v[108:111], v[188:191]\n\t"
"v_rcp_f32 v198, v198\n\t"
"v_mfma_f32_16x16x32_bf16 v[200:203], a[28:31], v[104:107], v[200:203]\n\t"
"s_waitcnt lgkmcnt(4)\n\t"
"ds_read_b64_tr_b16 a[0:1], v232 offset:8192\n\t"
"v_mul_f32 v197, v61, v197\n\t"
"v_mfma_f32_16x16x32_bf16 v[212:215], a[28:31], v[108:111], v[212:215]\n\t"
"ds_read_b64_tr_b16 a[2:3], v232 offset:10240\n\t"
"ds_read_b64_tr_b16 a[4:5], v235 offset:8192\n\t"
"ds_read_b64_tr_b16 a[6:7], v235 offset:10240\n\t"
"buffer_load_dwordx4 v208, %[w2], s68 offen offset:2048 lds\n\t"
"v_exp_f32 v193, v33\n\t"
"v_mfma_f32_16x16x32_bf16 v[168:171], a[32:35], v[112:115], v[168:171]\n\t"
"v_add_f32 v192, 1.0, v192\n\t"
"v_rcp_f32 v199, v199\n\t"
"v_mfma_f32_16x16x32_bf16 v[172:175], a[32:35], v[116:119], v[172:175]\n\t"
"v_mul_f32 v198, v62, v198\n\t"
"v_cvt_pk_bf16_f32 v166, v196, v197\n\t"
"v_mfma_f32_16x16x32_bf16 v[176:179], a[36:39], v[112:115], v[176:179]\n\t"
"s_waitcnt lgkmcnt(4)\n\t"
"ds_read_b64_tr_b16 a[8:9], v238 offset:8192\n\t"
"buffer_load_dwordx4 a[64:67], v246, %[ws], s42 offen sc1\n\t"
"v_mfma_f32_16x16x32_bf16 v[180:183], a[36:39], v[116:119], v[180:183]\n\t"
"ds_read_b128 v[8:11], v243 offset:0\n\t"
"ds_read_b128 v[12:15], v243 offset:1024\n\t"
"ds_read_b64_tr_b16 a[10:11], v238 offset:10240\n\t"
"ds_read_b64_tr_b16 a[12:13], v241 offset:8192\n\t"
"ds_read_b64_tr_b16 a[14:15], v241 offset:10240\n\t"
"buffer_load_dwordx4 v209, %[w2], s68 offen offset:3072 lds\n\t"
"v_exp_f32 v194, v34\n\t"
"v_add_f32 v193, 1.0, v193\n\t"
"v_mfma_f32_16x16x32_bf16 v[184:187], a[40:43], v[112:115], v[184:187]\n\t"
"s_add_u32 m0, s63, 53248\n\t"
"v_rcp_f32 v192, v192\n\t"
"v_mfma_f32_16x16x32_bf16 v[188:191], a[40:43], v[116:119], v[188:191]\n\t"
"s_add_u32 s69, s69, 147456\n\t"
"ds_read_b64_tr_b16 a[16:17], v220 offset:16384\n\t"
"v_mul_f32 v199, v63, v199\n\t"
"v_mfma_f32_16x16x32_bf16 v[200:203], a[44:47], v[112:115], v[200:203]\n\t"
"s_waitcnt lgkmcnt(7)\n\t"
"v_exp_f32 v195, v35\n\t"
"v_mfma_f32_16x16x32_bf16 v[212:215], a[44:47], v[116:119], v[212:215]\n\t"
"ds_read_b64_tr_b16 a[18:19], v220 offset:18432\n\t"
"ds_read_b64_tr_b16 a[20:21], v223 offset:16384\n\t"
"ds_read_b64_tr_b16 a[22:23], v223 offset:18432\n\t"
"v_add_f32 v194, 1.0, v194\n\t"
"v_mfma_f32_16x16x32_bf16 v[168:171], a[0:3], v[120:123], v[168:171]\n\t"
"buffer_load_dwordx4 a[72:75], v247, %[ws], s42 offen sc1\n\t"
"v_rcp_f32 v193, v193\n\t"
"v_mfma_f32_16x16x32_bf16 v[172:175], a[0:3], v[124:127], v[172:175]\n\t"
"s_add_u32 s42, s42, 147456\n\t"
"v_mul_f32 v192, v32, v192\n\t"
"v_cvt_pk_bf16_f32 v167, v198, v199\n\t"
"v_mfma_f32_16x16x32_bf16 v[176:179], a[4:7], v[120:123], v[176:179]\n\t"
"s_waitcnt lgkmcnt(4)\n\t"
"v_exp_f32 v196, v48\n\t"
"v_mfma_f32_16x16x32_bf16 v[180:183], a[4:7], v[124:127], v[180:183]\n\t"
"ds_read_b64_tr_b16 a[24:25], v226 offset:16384\n\t"
"ds_read_b64_tr_b16 a[26:27], v226 offset:18432\n\t"
"ds_read_b64_tr_b16 a[28:29], v229 offset:16384\n\t"
"ds_read_b64_tr_b16 a[30:31], v229 offset:18432\n\t"
"v_add_f32 v195, 1.0, v195\n\t"
"v_mfma_f32_16x16x32_bf16 v[184:187], a[8:11], v[120:123], v[184:187]\n\t"
"v_rcp_f32 v194, v194\n\t"
"v_mfma_f32_16x16x32_bf16 v[188:191], a[8:11], v[124:127], v[188:191]\n\t"
"buffer_store_dwordx4 v[164:167], v246, %[ws], s69 offen sc1\n\t"
"ds_write_b128 v242, v[164:167] offset:9216\n\t"
"v_mul_f32 v193, v33, v193\n\t"
"v_mfma_f32_16x16x32_bf16 v[200:203], a[12:15], v[120:123], v[200:203]\n\t"
"s_waitcnt lgkmcnt(5)\n\t"
"ds_read_b64_tr_b16 a[32:33], v232 offset:16384\n\t"
"buffer_load_dwordx4 v210, %[w2], s68 offen offset:0 lds\n\t"
"v_mfma_f32_16x16x32_bf16 v[212:215], a[12:15], v[124:127], v[212:215]\n\t"
"ds_read_b64_tr_b16 a[34:35], v232 offset:18432\n\t"
"ds_read_b64_tr_b16 a[36:37], v235 offset:16384\n\t"
"ds_read_b64_tr_b16 a[38:39], v235 offset:18432\n\t"
"v_exp_f32 v197, v49\n\t"
"v_mfma_f32_16x16x32_bf16 v[168:171], a[16:19], v[128:131], v[168:171]\n\t"
"v_add_f32 v196, 1.0, v196\n\t"
"v_rcp_f32 v195, v195\n\t"
"v_mfma_f32_16x16x32_bf16 v[172:175], a[16:19], v[132:135], v[172:175]\n\t"
"v_mul_f32 v194, v34, v194\n\t"
"v_cvt_pk_bf16_f32 v16, v192, v193\n\t"
"v_mfma_f32_16x16x32_bf16 v[176:179], a[20:23], v[128:131], v[176:179]\n\t"
"s_waitcnt lgkmcnt(5)\n\t"
"ds_read_b64_tr_b16 a[40:41], v238 offset:16384\n\t"
"buffer_load_dwordx4 v211, %[w2], s68 offen offset:1024 lds\n\t"
"v_mfma_f32_16x16x32_bf16 v[180:183], a[20:23], v[132:135], v[180:183]\n\t"
"s_mul_i32 s68, s94, 442368\n\t"
"ds_read_b64_tr_b16 a[42:43], v238 offset:18432\n\t"
"ds_read_b64_tr_b16 a[44:45], v241 offset:16384\n\t"
"ds_read_b64_tr_b16 a[46:47], v241 offset:18432\n\t"
"buffer_load_dwordx4 a[68:71], v246, %[ws], s42 offen sc1\n\t"
"v_exp_f32 v198, v50\n\t"
"v_mfma_f32_16x16x32_bf16 v[184:187], a[24:27], v[128:131], v[184:187]\n\t"
"s_add_u32 s68, s68, s53\n\t"
"ds_read_b64_tr_b16 a[0:1], v220 offset:24576\n\t"
"v_add_f32 v197, 1.0, v197\n\t"
"v_mfma_f32_16x16x32_bf16 v[188:191], a[24:27], v[132:135], v[188:191]\n\t"
"s_add_u32 s68, s68, s50\n\t"
"v_rcp_f32 v196, v196\n\t"
"v_mul_f32 v195, v35, v195\n\t"
"v_mfma_f32_16x16x32_bf16 v[200:203], a[28:31], v[128:131], v[200:203]\n\t"
"s_add_u32 m0, s63, 73728\n\t"
"v_exp_f32 v199, v51\n\t"
"v_mfma_f32_16x16x32_bf16 v[212:215], a[28:31], v[132:135], v[212:215]\n\t"
"s_waitcnt lgkmcnt(5)\n\t"
"ds_read_b64_tr_b16 a[2:3], v220 offset:26624\n\t"
"ds_read_b64_tr_b16 a[4:5], v223 offset:24576\n\t"
"ds_read_b64_tr_b16 a[6:7], v223 offset:26624\n\t"
"buffer_load_dwordx4 a[76:79], v247, %[ws], s42 offen sc1\n\t"
"v_add_f32 v198, 1.0, v198\n\t"
"v_mfma_f32_16x16x32_bf16 v[168:171], a[32:35], v[136:139], v[168:171]\n\t"
"s_mul_i32 s42, s94, 768\n\t"
"v_rcp_f32 v197, v197\n\t"
"v_mul_f32 v196, v48, v196\n\t"
"v_mfma_f32_16x16x32_bf16 v[172:175], a[32:35], v[140:143], v[172:175]\n\t"
"ds_read_b64_tr_b16 a[8:9], v226 offset:24576\n\t"
"v_cvt_pk_bf16_f32 v17, v194, v195\n\t"
"v_mfma_f32_16x16x32_bf16 v[176:179], a[36:39], v[136:139], v[176:179]\n\t"
"s_add_u32 s42, s42, s46\n\t"
"v_exp_f32 v192, v36\n\t"
"v_mfma_f32_16x16x32_bf16 v[180:183], a[36:39], v[140:143], v[180:183]\n\t"
"s_waitcnt lgkmcnt(5)\n\t"
"ds_read_b64_tr_b16 a[10:11], v226 offset:26624\n\t"
"ds_read_b64_tr_b16 a[12:13], v229 offset:24576\n\t"
"ds_read_b64_tr_b16 a[14:15], v229 offset:26624\n\t"
"v_add_f32 v199, 1.0, v199\n\t"
"v_mfma_f32_16x16x32_bf16 v[184:187], a[40:43], v[136:139], v[184:187]\n\t"
"s_getpc_b64 s[96:97]\n\t"
"73:\n\t"
"s_and_b32 s98, %[mi], 3\n\t"
"s_lshl_b32 s98, s98, 2\n\t"
"s_add_u32 s98, s98, %[wv]\n\t"
"s_lshl_b32 s98, s98, 10\n\t"
"v_add_u32 v253, s98, v244\n\t"
"v_min_u32 v253, 97f-73b, v253\n\t"
"global_load_dword v253, v253, s[96:97]\n\t"
"v_rcp_f32 v198, v198\n\t"
"v_mul_f32 v197, v49, v197\n\t"
"v_mfma_f32_16x16x32_bf16 v[188:191], a[40:43], v[140:143], v[188:191]\n\t"
"buffer_load_dwordx4 v206, %[w2], s68 offen offset:0 lds\n\t"
"v_exp_f32 v193, v37\n\t"
"v_mfma_f32_16x16x32_bf16 v[200:203], a[44:47], v[136:139], v[200:203]\n\t"
"s_waitcnt lgkmcnt(4)\n\t"
"ds_read_b64_tr_b16 a[16:17], v232 offset:24576\n\t"
"v_add_f32 v192, 1.0, v192\n\t"
"v_mfma_f32_16x16x32_bf16 v[212:215], a[44:47], v[140:143], v[212:215]\n\t"
"ds_read_b64_tr_b16 a[18:19], v232 offset:26624\n\t"
"ds_read_b64_tr_b16 a[20:21], v235 offset:24576\n\t"
"ds_read_b64_tr_b16 a[22:23], v235 offset:26624\n\t"
"v_rcp_f32 v199, v199\n\t"
"v_mfma_f32_16x16x32_bf16 v[64:67], a[0:3], v[144:147], v[64:67]\n\t"
"v_mul_f32 v198, v50, v198\n\t"
"v_cvt_pk_bf16_f32 v18, v196, v197\n\t"
"v_mfma_f32_16x16x32_bf16 v[68:71], a[0:3], v[148:151], v[68:71]\n\t"
"s_lshl_b32 s42, s42, 4\n\t"
"v_exp_f32 v194, v38\n\t"
"v_mfma_f32_16x16x32_bf16 v[72:75], a[4:7], v[144:147], v[72:75]\n\t"
"s_waitcnt lgkmcnt(4)\n\t"
"ds_read_b64_tr_b16 a[24:25], v238 offset:24576\n\t"
"v_add_f32 v193, 1.0, v193\n\t"
"v_mfma_f32_16x16x32_bf16 v[76:79], a[4:7], v[148:151], v[76:79]\n\t"
"s_add_u32 s69, s58, s56\n\t"
"ds_read_b64_tr_b16 a[26:27], v238 offset:26624\n\t"
"ds_read_b64_tr_b16 a[28:29], v241 offset:24576\n\t"
"ds_read_b64_tr_b16 a[30:31], v241 offset:26624\n\t"
"v_rcp_f32 v192, v192\n\t"
"v_mul_f32 v199, v51, v199\n\t"
"v_mfma_f32_16x16x32_bf16 v[80:83], a[8:11], v[144:147], v[80:83]\n\t"
"buffer_load_dwordx4 v207, %[w2], s68 offen offset:1024 lds\n\t"
"v_mfma_f32_16x16x32_bf16 v[84:87], a[8:11], v[148:151], v[84:87]\n\t"
"s_add_u32 s42, s42, s56\n\t"
"v_exp_f32 v195, v39\n\t"
"v_mfma_f32_16x16x32_bf16 v[88:91], a[12:15], v[144:147], v[88:91]\n\t"
"s_waitcnt lgkmcnt(4)\n\t"
"ds_read_b64_tr_b16 a[32:33], v220 offset:32768\n\t"
"v_add_f32 v194, 1.0, v194\n\t"
"v_mfma_f32_16x16x32_bf16 v[92:95], a[12:15], v[148:151], v[92:95]\n\t"
"ds_read_b64_tr_b16 a[34:35], v220 offset:34816\n\t"
"ds_read_b64_tr_b16 a[36:37], v223 offset:32768\n\t"
"ds_read_b64_tr_b16 a[38:39], v223 offset:34816\n\t"
"v_rcp_f32 v193, v193\n\t"
"v_mul_f32 v192, v36, v192\n\t"
"v_mfma_f32_16x16x32_bf16 v[64:67], a[16:19], v[152:155], v[64:67]\n\t"
"ds_read_b64_tr_b16 a[40:41], v226 offset:32768\n\t"
"v_cvt_pk_bf16_f32 v19, v198, v199\n\t"
"v_mfma_f32_16x16x32_bf16 v[68:71], a[16:19], v[156:159], v[68:71]\n\t"
"v_exp_f32 v196, v52\n\t"
"v_mfma_f32_16x16x32_bf16 v[72:75], a[20:23], v[152:155], v[72:75]\n\t"
"s_waitcnt lgkmcnt(5)\n\t"
"ds_read_b64_tr_b16 a[42:43], v226 offset:34816\n\t"
"v_add_f32 v195, 1.0, v195\n\t"
"v_mfma_f32_16x16x32_bf16 v[76:79], a[20:23], v[156:159], v[76:79]\n\t"
"buffer_store_dwordx4 v[16:19], v246, %[ws], s69 offen sc1\n\t"
"ds_read_b64_tr_b16 a[44:45], v229 offset:32768\n\t"
"ds_read_b64_tr_b16 a[46:47], v229 offset:34816\n\t"
"v_rcp_f32 v194, v194\n\t"
"v_mul_f32 v193, v37, v193\n\t"
"v_mfma_f32_16x16x32_bf16 v[80:83], a[24:27], v[152:155], v[80:83]\n\t"
"buffer_load_dwordx4 v208, %[w2], s68 offen offset:2048 lds\n\t"
"v_mfma_f32_16x16x32_bf16 v[84:87], a[24:27], v[156:159], v[84:87]\n\t"
"v_exp_f32 v197, v53\n\t"
"v_add_f32 v196, 1.0, v196\n\t"
"v_mfma_f32_16x16x32_bf16 v[88:91], a[28:31], v[152:155], v[88:91]\n\t"
"s_waitcnt lgkmcnt(4)\n\t"
"v_rcp_f32 v195, v195\n\t"
"v_mfma_f32_16x16x32_bf16 v[92:95], a[28:31], v[156:159], v[92:95]\n\t"
"ds_read_b64_tr_b16 a[0:1], v232 offset:32768\n\t"
"ds_read_b64_tr_b16 a[2:3], v232 offset:34816\n\t"
"ds_read_b64_tr_b16 a[4:5], v235 offset:32768\n\t"
"ds_read_b64_tr_b16 a[6:7], v235 offset:34816\n\t"
"v_mul_f32 v194, v38, v194\n\t"
"v_mfma_f32_16x16x32_bf16 v[64:67], a[32:35], v[0:3], v[64:67]\n\t"
"s_barrier\n\t"
"v_cvt_pk_bf16_f32 v20, v192, v193\n\t"
"v_exp_f32 v198, v54\n\t"
"v_mfma_f32_16x16x32_bf16 v[68:71], a[32:35], v[4:7], v[68:71]\n\t"
"buffer_load_dwordx4 a[80:83], v246, %[ws], s42 offen sc1\n\t"
"v_add_f32 v197, 1.0, v197\n\t"
"v_mfma_f32_16x16x32_bf16 v[72:75], a[36:39], v[0:3], v[72:75]\n\t"
"s_waitcnt lgkmcnt(4)\n\t"
"v_rcp_f32 v196, v196\n\t"
"v_mfma_f32_16x16x32_bf16 v[76:79], a[36:39], v[4:7], v[76:79]\n\t"
"ds_read_b64_tr_b16 a[8:9], v238 offset:32768\n\t"
"ds_read_b64_tr_b16 a[10:11], v238 offset:34816\n\t"
"ds_read_b64_tr_b16 a[12:13], v241 offset:32768\n\t"
"ds_read_b64_tr_b16 a[14:15], v241 offset:34816\n\t"
"buffer_load_dwordx4 v209, %[w2], s68 offen offset:3072 lds\n\t"
"v_mul_f32 v195, v39, v195\n\t"
"v_mfma_f32_16x16x32_bf16 v[80:83], a[40:43], v[0:3], v[80:83]\n\t"
"s_add_u32 m0, s63, 77824\n\t"
"v_exp_f32 v199, v55\n\t"
"v_mfma_f32_16x16x32_bf16 v[84:87], a[40:43], v[4:7], v[84:87]\n\t"
"ds_read_b64_tr_b16 a[16:17], v220 offset:40960\n\t"
"v_add_f32 v198, 1.0, v198\n\t"
"v_mfma_f32_16x16x32_bf16 v[88:91], a[44:47], v[0:3], v[88:91]\n\t"
"s_waitcnt lgkmcnt(5)\n\t"
"v_rcp_f32 v197, v197\n\t"
"v_mul_f32 v196, v52, v196\n\t"
"v_mfma_f32_16x16x32_bf16 v[92:95], a[44:47], v[4:7], v[92:95]\n\t"
"ds_read_b64_tr_b16 a[18:19], v220 offset:43008\n\t"
"ds_read_b64_tr_b16 a[20:21], v223 offset:40960\n\t"
"ds_read_b64_tr_b16 a[22:23], v223 offset:43008\n\t"
"v_cvt_pk_bf16_f32 v21, v194, v195\n\t"
"v_add_f32 v199, 1.0, v199\n\t"
"v_mfma_f32_16x16x32_bf16 v[64:67], a[0:3], v[8:11], v[64:67]\n\t"
"ds_read_b128 v[24:27], v243 offset:8192\n\t"
"ds_read_b128 v[28:31], v243 offset:9216\n\t"
"buffer_load_dwordx4 a[88:91], v247, %[ws], s42 offen sc1\n\t"
"v_mfma_f32_16x16x32_bf16 v[68:71], a[0:3], v[12:15], v[68:71]\n\t"
"s_add_u32 s42, s42, 147456\n\t"
"v_rcp_f32 v198, v198\n\t"
"v_mul_f32 v197, v53, v197\n\t"
"v_rcp_f32 v199, v199\n\t"
"v_mfma_f32_16x16x32_bf16 v[72:75], a[4:7], v[8:11], v[72:75]\n\t"
"s_waitcnt lgkmcnt(6)\n\t"
"v_mul_f32 v198, v54, v198\n\t"
"v_cvt_pk_bf16_f32 v22, v196, v197\n\t"
"v_mul_f32 v199, v55, v199\n\t"
"v_mfma_f32_16x16x32_bf16 v[76:79], a[4:7], v[12:15], v[76:79]\n\t"
"s_add_u32 s69, s69, 147456\n\t"
"ds_read_b64_tr_b16 a[24:25], v226 offset:40960\n\t"
"ds_read_b64_tr_b16 a[26:27], v226 offset:43008\n\t"
"ds_read_b64_tr_b16 a[28:29], v229 offset:40960\n\t"
"ds_read_b64_tr_b16 a[30:31], v229 offset:43008\n\t"
"buffer_load_dwordx4 v210, %[w2], s68 offen offset:0 lds\n\t"
"v_cvt_pk_bf16_f32 v23, v198, v199\n\t"
"v_mfma_f32_16x16x32_bf16 v[80:83], a[8:11], v[8:11], v[80:83]\n\t"
"buffer_store_dwordx4 v[20:23], v246, %[ws], s69 offen sc1\n\t"
"ds_read_b64_tr_b16 a[32:33], v232 offset:40960\n\t"
"ds_read_b64_tr_b16 a[34:35], v232 offset:43008\n\t"
"ds_read_b64_tr_b16 a[36:37], v235 offset:40960\n\t"
"v_mfma_f32_16x16x32_bf16 v[84:87], a[8:11], v[12:15], v[84:87]\n\t"
"ds_read_b64_tr_b16 a[38:39], v235 offset:43008\n\t"
"v_mfma_f32_16x16x32_bf16 v[88:91], a[12:15], v[8:11], v[88:91]\n\t"
"ds_read_b64_tr_b16 a[40:41], v238 offset:40960\n\t"
"v_mfma_f32_16x16x32_bf16 v[92:95], a[12:15], v[12:15], v[92:95]\n\t"
"s_waitcnt lgkmcnt(11)\n\t"
"ds_read_b64_tr_b16 a[42:43], v238 offset:43008\n\t"
"ds_read_b64_tr_b16 a[44:45], v241 offset:40960\n\t"
"ds_read_b64_tr_b16 a[46:47], v241 offset:43008\n\t"
"v_mfma_f32_16x16x32_bf16 v[64:67], a[16:19], v[16:19], v[64:67]\n\t"
"buffer_load_dwordx4 a[84:87], v246, %[ws], s42 offen sc1\n\t"
"v_mfma_f32_16x16x32_bf16 v[68:71], a[16:19], v[20:23], v[68:71]\n\t"
"buffer_load_dwordx4 v211, %[w2], s68 offen offset:1024 lds\n\t"
"v_mfma_f32_16x16x32_bf16 v[72:75], a[20:23], v[16:19], v[72:75]\n\t"
"buffer_load_dwordx4 a[92:95], v247, %[ws], s42 offen sc1\n\t"
"v_mfma_f32_16x16x32_bf16 v[76:79], a[20:23], v[20:23], v[76:79]\n\t"
"s_waitcnt lgkmcnt(8)\n\t"
"v_mfma_f32_16x16x32_bf16 v[80:83], a[24:27], v[16:19], v[80:83]\n\t"
"s_waitcnt vmcnt(26)\n\t"
"s_waitcnt lgkmcnt(0)\n\t"
"v_mfma_f32_16x16x32_bf16 v[84:87], a[24:27], v[20:23], v[84:87]\n\t"
"s_barrier\n\t"
"s_mul_i32 s42, s95, 768\n\t"
"v_mfma_f32_16x16x32_bf16 v[88:91], a[28:31], v[16:19], v[88:91]\n\t"
"s_mul_i32 s68, s94, 442368\n\t"
"ds_read_b64_tr_b16 a[0:1], v218 offset:0\n\t"
"v_mfma_f32_16x16x32_bf16 v[92:95], a[28:31], v[20:23], v[92:95]\n\t"
"ds_read_b64_tr_b16 a[2:3], v218 offset:2048\n\t"
"v_mfma_f32_16x16x32_bf16 v[64:67], a[32:35], v[24:27], v[64:67]\n\t"
"s_add_u32 s68, s68, s54\n\t"
"buffer_store_dwordx2 v[254:255], v252, %[ws], s79 offen offset:640 sc1\n\t"
"ds_read_b64_tr_b16 a[4:5], v221 offset:0\n\t"
"ds_read_b64_tr_b16 a[6:7], v221 offset:2048\n\t"
"v_mfma_f32_16x16x32_bf16 v[68:71], a[32:35], v[28:31], v[68:71]\n\t"
"s_add_u32 s68, s68, s50\n\t"
"ds_read_b64_tr_b16 a[8:9], v224 offset:0\n\t"
"v_mfma_f32_16x16x32_bf16 v[72:75], a[36:39], v[24:27], v[72:75]\n\t"
"s_add_u32 m0, s63, 98304\n\t"
"ds_read_b64_tr_b16 a[10:11], v224 offset:2048\n\t"
"v_mfma_f32_16x16x32_bf16 v[76:79], a[36:39], v[28:31], v[76:79]\n\t"
"ds_read_b64_tr_b16 a[12:13], v227 offset:0\n\t"
"v_mfma_f32_16x16x32_bf16 v[80:83], a[40:43], v[24:27], v[80:83]\n\t"
"ds_read_b64_tr_b16 a[14:15], v227 offset:2048\n\t"
"v_mfma_f32_16x16x32_bf16 v[84:87], a[40:43], v[28:31], v[84:87]\n\t"
"s_add_u32 s42, s42, s44\n\t"
"ds_read_b64_tr_b16 a[16:17], v230 offset:0\n\t"
"v_mfma_f32_16x16x32_bf16 v[88:91], a[44:47], v[24:27], v[88:91]\n\t"
"s_waitcnt lgkmcnt(5)\n\t"
"ds_read_b64_tr_b16 a[18:19], v230 offset:2048\n\t"
"v_mfma_f32_16x16x32_bf16 v[92:95], a[44:47], v[28:31], v[92:95]\n\t"
"ds_read_b64_tr_b16 a[20:21], v233 offset:0\n\t"
"ds_read_b64_tr_b16 a[22:23], v233 offset:2048\n\t"
"v_mfma_f32_16x16x32_bf16 v[168:171], a[0:3], v[144:147], v[168:171]\n\t"
"buffer_load_dwordx4 v206, %[w2], s68 offen offset:0 lds\n\t"
"v_mfma_f32_16x16x32_bf16 v[172:175], a[0:3], v[148:151], v[172:175]\n\t"
"ds_read_b64_tr_b16 a[24:25], v236 offset:0\n\t"
"v_mfma_f32_16x16x32_bf16 v[176:179], a[4:7], v[144:147], v[176:179]\n\t"
"ds_read_b64_tr_b16 a[26:27], v236 offset:2048\n\t"
"v_mfma_f32_16x16x32_bf16 v[180:183], a[4:7], v[148:151], v[180:183]\n\t"
"s_waitcnt lgkmcnt(6)\n\t"
"ds_read_b64_tr_b16 a[28:29], v239 offset:0\n\t"
"ds_read_b64_tr_b16 a[30:31], v239 offset:2048\n\t"
"v_mfma_f32_16x16x32_bf16 v[184:187], a[8:11], v[144:147], v[184:187]\n\t"
"buffer_load_dwordx4 v207, %[w2], s68 offen offset:1024 lds\n\t"
"v_mfma_f32_16x16x32_bf16 v[188:191], a[8:11], v[148:151], v[188:191]\n\t"
"s_lshl_b32 s42, s42, 4\n\t"
"ds_read_b64_tr_b16 a[32:33], v218 offset:8192\n\t"
"v_mfma_f32_16x16x32_bf16 v[200:203], a[12:15], v[144:147], v[200:203]\n\t"
"s_add_u32 s42, s42, s56\n\t"
"ds_read_b64_tr_b16 a[34:35], v218 offset:10240\n\t"
"v_mfma_f32_16x16x32_bf16 v[212:215], a[12:15], v[148:151], v[212:215]\n\t"
"s_waitcnt lgkmcnt(6)\n\t"
"ds_read_b64_tr_b16 a[36:37], v221 offset:8192\n\t"
"ds_read_b64_tr_b16 a[38:39], v221 offset:10240\n\t"
"v_mfma_f32_16x16x32_bf16 v[168:171], a[16:19], v[152:155], v[168:171]\n\t"
"ds_read_b64_tr_b16 a[40:41], v224 offset:8192\n\t"
"v_mfma_f32_16x16x32_bf16 v[172:175], a[16:19], v[156:159], v[172:175]\n\t"
"ds_read_b64_tr_b16 a[42:43], v224 offset:10240\n\t"
"v_mfma_f32_16x16x32_bf16 v[176:179], a[20:23], v[152:155], v[176:179]\n\t"
"ds_read_b64_tr_b16 a[44:45], v227 offset:8192\n\t"
"v_mfma_f32_16x16x32_bf16 v[180:183], a[20:23], v[156:159], v[180:183]\n\t"
"s_waitcnt lgkmcnt(7)\n\t"
"ds_read_b64_tr_b16 a[46:47], v227 offset:10240\n\t"
"v_mfma_f32_16x16x32_bf16 v[184:187], a[24:27], v[152:155], v[184:187]\n\t"
"buffer_load_dwordx4 v208, %[w2], s68 offen offset:2048 lds\n\t"
"v_mfma_f32_16x16x32_bf16 v[188:191], a[24:27], v[156:159], v[188:191]\n\t"
"ds_read_b64_tr_b16 a[0:1], v230 offset:8192\n\t"
"v_mfma_f32_16x16x32_bf16 v[200:203], a[28:31], v[152:155], v[200:203]\n\t"
"s_waitcnt lgkmcnt(5)\n\t"
"ds_read_b64_tr_b16 a[2:3], v230 offset:10240\n\t"
"v_mfma_f32_16x16x32_bf16 v[212:215], a[28:31], v[156:159], v[212:215]\n\t"
"ds_read_b64_tr_b16 a[4:5], v233 offset:8192\n\t"
"ds_read_b64_tr_b16 a[6:7], v233 offset:10240\n\t"
"v_mfma_f32_16x16x32_bf16 v[168:171], a[32:35], v[0:3], v[168:171]\n\t"
"ds_read_b64_tr_b16 a[8:9], v236 offset:8192\n\t"
"v_mfma_f32_16x16x32_bf16 v[172:175], a[32:35], v[4:7], v[172:175]\n\t"
"ds_read_b64_tr_b16 a[10:11], v236 offset:10240\n\t"
"v_mfma_f32_16x16x32_bf16 v[176:179], a[36:39], v[0:3], v[176:179]\n\t"
"ds_read_b64_tr_b16 a[12:13], v239 offset:8192\n\t"
"v_mfma_f32_16x16x32_bf16 v[180:183], a[36:39], v[4:7], v[180:183]\n\t"
"s_waitcnt lgkmcnt(7)\n\t"
"ds_read_b64_tr_b16 a[14:15], v239 offset:10240\n\t"
"v_mfma_f32_16x16x32_bf16 v[184:187], a[40:43], v[0:3], v[184:187]\n\t"
"buffer_load_dwordx4 a[144:147], v246, %[ws], s42 offen sc1\n\t"
"v_mfma_f32_16x16x32_bf16 v[188:191], a[40:43], v[4:7], v[188:191]\n\t"
"buffer_load_dwordx4 v209, %[w2], s68 offen offset:3072 lds\n\t"
"v_mfma_f32_16x16x32_bf16 v[200:203], a[44:47], v[0:3], v[200:203]\n\t"
"s_add_u32 m0, s63, 102400\n\t"
"ds_read_b64_tr_b16 a[16:17], v218 offset:16384\n\t"
"v_mfma_f32_16x16x32_bf16 v[212:215], a[44:47], v[4:7], v[212:215]\n\t"
"s_waitcnt lgkmcnt(5)\n\t"
"ds_read_b64_tr_b16 a[18:19], v218 offset:18432\n\t"
"ds_read_b64_tr_b16 a[20:21], v221 offset:16384\n\t"
"ds_read_b64_tr_b16 a[22:23], v221 offset:18432\n\t"
"v_mfma_f32_16x16x32_bf16 v[168:171], a[0:3], v[8:11], v[168:171]\n\t"
"buffer_load_dwordx4 a[152:155], v247, %[ws], s42 offen sc1\n\t"
"v_mfma_f32_16x16x32_bf16 v[172:175], a[0:3], v[12:15], v[172:175]\n\t"
"s_add_u32 s42, s42, 147456\n\t"
"ds_read_b64_tr_b16 a[24:25], v224 offset:16384\n\t"
"v_mfma_f32_16x16x32_bf16 v[176:179], a[4:7], v[8:11], v[176:179]\n\t"
"ds_read_b64_tr_b16 a[26:27], v224 offset:18432\n\t"
"v_mfma_f32_16x16x32_bf16 v[180:183], a[4:7], v[12:15], v[180:183]\n\t"
"s_waitcnt lgkmcnt(6)\n\t"
"ds_read_b64_tr_b16 a[28:29], v227 offset:16384\n\t"
"ds_read_b64_tr_b16 a[30:31], v227 offset:18432\n\t"
"v_mfma_f32_16x16x32_bf16 v[184:187], a[8:11], v[8:11], v[184:187]\n\t"
"buffer_load_dwordx4 v210, %[w2], s68 offen offset:0 lds\n\t"
"v_mfma_f32_16x16x32_bf16 v[188:191], a[8:11], v[12:15], v[188:191]\n\t"
"ds_read_b64_tr_b16 a[32:33], v230 offset:16384\n\t"
"v_mfma_f32_16x16x32_bf16 v[200:203], a[12:15], v[8:11], v[200:203]\n\t"
"ds_read_b64_tr_b16 a[34:35], v230 offset:18432\n\t"
"v_mfma_f32_16x16x32_bf16 v[212:215], a[12:15], v[12:15], v[212:215]\n\t"
"s_waitcnt lgkmcnt(6)\n\t"
"ds_read_b64_tr_b16 a[36:37], v233 offset:16384\n\t"
"ds_read_b64_tr_b16 a[38:39], v233 offset:18432\n\t"
"v_mfma_f32_16x16x32_bf16 v[168:171], a[16:19], v[16:19], v[168:171]\n\t"
"buffer_load_dwordx4 v211, %[w2], s68 offen offset:1024 lds\n\t"
"v_mfma_f32_16x16x32_bf16 v[172:175], a[16:19], v[20:23], v[172:175]\n\t"
"ds_read_b64_tr_b16 a[40:41], v236 offset:16384\n\t"
"v_mfma_f32_16x16x32_bf16 v[176:179], a[20:23], v[16:19], v[176:179]\n\t"
"s_waitcnt lgkmcnt(5)\n\t"
"ds_read_b64_tr_b16 a[42:43], v236 offset:18432\n\t"
"v_mfma_f32_16x16x32_bf16 v[180:183], a[20:23], v[20:23], v[180:183]\n\t"
"s_mul_i32 s68, s95, 442368\n\t"
"ds_read_b64_tr_b16 a[44:45], v239 offset:16384\n\t"
"ds_read_b64_tr_b16 a[46:47], v239 offset:18432\n\t"
"v_mfma_f32_16x16x32_bf16 v[184:187], a[24:27], v[16:19], v[184:187]\n\t"
"s_add_u32 s68, s68, s53\n\t"
"ds_read_b64_tr_b16 a[0:1], v218 offset:24576\n\t"
"buffer_load_dwordx4 a[148:151], v246, %[ws], s42 offen sc1\n\t"
"v_mfma_f32_16x16x32_bf16 v[188:191], a[24:27], v[20:23], v[188:191]\n\t"
"s_add_u32 s68, s68, s50\n\t"
"ds_read_b64_tr_b16 a[2:3], v218 offset:26624\n\t"
"v_mfma_f32_16x16x32_bf16 v[200:203], a[28:31], v[16:19], v[200:203]\n\t"
"s_add_u32 m0, s63, 122880\n\t"
"ds_read_b64_tr_b16 a[4:5], v221 offset:24576\n\t"
"v_mfma_f32_16x16x32_bf16 v[212:215], a[28:31], v[20:23], v[212:215]\n\t"
"s_waitcnt lgkmcnt(7)\n\t"
"ds_read_b64_tr_b16 a[6:7], v221 offset:26624\n\t"
"v_mfma_f32_16x16x32_bf16 v[168:171], a[32:35], v[24:27], v[168:171]\n\t"
"buffer_load_dwordx4 a[156:159], v247, %[ws], s42 offen sc1\n\t"
"v_mfma_f32_16x16x32_bf16 v[172:175], a[32:35], v[28:31], v[172:175]\n\t"
"s_mul_i32 s42, s95, 768\n\t"
"ds_read_b64_tr_b16 a[8:9], v224 offset:24576\n\t"
"v_mfma_f32_16x16x32_bf16 v[176:179], a[36:39], v[24:27], v[176:179]\n\t"
"s_waitcnt lgkmcnt(5)\n\t"
"ds_read_b64_tr_b16 a[10:11], v224 offset:26624\n\t"
"v_mfma_f32_16x16x32_bf16 v[180:183], a[36:39], v[28:31], v[180:183]\n\t"
"ds_read_b64_tr_b16 a[12:13], v227 offset:24576\n\t"
"ds_read_b64_tr_b16 a[14:15], v227 offset:26624\n\t"
"v_mfma_f32_16x16x32_bf16 v[184:187], a[40:43], v[24:27], v[184:187]\n\t"
"buffer_load_dwordx4 v206, %[w2], s68 offen offset:0 lds\n\t"
"v_mfma_f32_16x16x32_bf16 v[188:191], a[40:43], v[28:31], v[188:191]\n\t"
"ds_read_b64_tr_b16 a[16:17], v230 offset:24576\n\t"
"v_mfma_f32_16x16x32_bf16 v[200:203], a[44:47], v[24:27], v[200:203]\n\t"
"ds_read_b64_tr_b16 a[18:19], v230 offset:26624\n\t"
"v_mfma_f32_16x16x32_bf16 v[212:215], a[44:47], v[28:31], v[212:215]\n\t"
"s_waitcnt lgkmcnt(6)\n\t"
"ds_read_b64_tr_b16 a[20:21], v233 offset:24576\n\t"
"ds_read_b64_tr_b16 a[22:23], v233 offset:26624\n\t"
"v_mfma_f32_16x16x32_bf16 v[32:35], a[0:3], v[96:99], 0\n\t"
"s_add_u32 s42, s42, s45\n\t"
"ds_read_b64_tr_b16 a[24:25], v236 offset:24576\n\t"
"v_mfma_f32_16x16x32_bf16 v[36:39], a[0:3], v[100:103], 0\n\t"
"s_lshl_b32 s42, s42, 4\n\t"
"ds_read_b64_tr_b16 a[26:27], v236 offset:26624\n\t"
"v_mfma_f32_16x16x32_bf16 v[40:43], a[4:7], v[96:99], 0\n\t"
"s_waitcnt lgkmcnt(6)\n\t"
"ds_read_b64_tr_b16 a[28:29], v239 offset:24576\n\t"
"v_mfma_f32_16x16x32_bf16 v[44:47], a[4:7], v[100:103], 0\n\t"
"ds_read_b64_tr_b16 a[30:31], v239 offset:26624\n\t"
"v_mfma_f32_16x16x32_bf16 v[48:51], a[8:11], v[96:99], 0\n\t"
"buffer_load_dwordx4 v207, %[w2], s68 offen offset:1024 lds\n\t"
"v_mfma_f32_16x16x32_bf16 v[52:55], a[8:11], v[100:103], 0\n\t"
"ds_read_b64_tr_b16 a[32:33], v218 offset:32768\n\t"
"v_mfma_f32_16x16x32_bf16 v[56:59], a[12:15], v[96:99], 0\n\t"
"ds_read_b64_tr_b16 a[34:35], v218 offset:34816\n\t"
"v_mfma_f32_16x16x32_bf16 v[60:63], a[12:15], v[100:103], 0\n\t"
"s_waitcnt lgkmcnt(6)\n\t"
"ds_read_b64_tr_b16 a[36:37], v221 offset:32768\n\t"
"ds_read_b64_tr_b16 a[38:39], v221 offset:34816\n\t"
"v_mfma_f32_16x16x32_bf16 v[32:35], a[16:19], v[104:107], v[32:35]\n\t"
"ds_read_b64_tr_b16 a[40:41], v224 offset:32768\n\t"
"v_mfma_f32_16x16x32_bf16 v[36:39], a[16:19], v[108:111], v[36:39]\n\t"
"ds_read_b64_tr_b16 a[42:43], v224 offset:34816\n\t"
"v_mfma_f32_16x16x32_bf16 v[40:43], a[20:23], v[104:107], v[40:43]\n\t"
"ds_read_b64_tr_b16 a[44:45], v227 offset:32768\n\t"
"v_mfma_f32_16x16x32_bf16 v[44:47], a[20:23], v[108:111], v[44:47]\n\t"
"s_waitcnt lgkmcnt(7)\n\t"
"ds_read_b64_tr_b16 a[46:47], v227 offset:34816\n\t"
"v_mfma_f32_16x16x32_bf16 v[48:51], a[24:27], v[104:107], v[48:51]\n\t"
"buffer_load_dwordx4 v208, %[w2], s68 offen offset:2048 lds\n\t"
"v_mfma_f32_16x16x32_bf16 v[52:55], a[24:27], v[108:111], v[52:55]\n\t"
"ds_read_b64_tr_b16 a[0:1], v230 offset:32768\n\t"
"v_mfma_f32_16x16x32_bf16 v[56:59], a[28:31], v[104:107], v[56:59]\n\t"
"s_add_u32 s42, s42, s56\n\t"
"ds_read_b64_tr_b16 a[2:3], v230 offset:34816\n\t"
"v_mfma_f32_16x16x32_bf16 v[60:63], a[28:31], v[108:111], v[60:63]\n\t"
"s_waitcnt lgkmcnt(6)\n\t"
"ds_read_b64_tr_b16 a[4:5], v233 offset:32768\n\t"
"ds_read_b64_tr_b16 a[6:7], v233 offset:34816\n\t"
"v_mfma_f32_16x16x32_bf16 v[32:35], a[32:35], v[112:115], v[32:35]\n\t"
"buffer_load_dwordx4 v209, %[w2], s68 offen offset:3072 lds\n\t"
"v_mfma_f32_16x16x32_bf16 v[36:39], a[32:35], v[116:119], v[36:39]\n\t"
"s_add_u32 m0, s63, 126976\n\t"
"ds_read_b64_tr_b16 a[8:9], v236 offset:32768\n\t"
"v_mfma_f32_16x16x32_bf16 v[40:43], a[36:39], v[112:115], v[40:43]\n\t"
"s_waitcnt lgkmcnt(5)\n\t"
"ds_read_b64_tr_b16 a[10:11], v236 offset:34816\n\t"
"v_mfma_f32_16x16x32_bf16 v[44:47], a[36:39], v[116:119], v[44:47]\n\t"
"ds_read_b64_tr_b16 a[12:13], v239 offset:32768\n\t"
"ds_read_b64_tr_b16 a[14:15], v239 offset:34816\n\t"
"v_mfma_f32_16x16x32_bf16 v[48:51], a[40:43], v[112:115], v[48:51]\n\t"
"buffer_load_dwordx4 a[160:163], v246, %[ws], s42 offen sc1\n\t"
"v_mfma_f32_16x16x32_bf16 v[52:55], a[40:43], v[116:119], v[52:55]\n\t"
"buffer_load_dwordx4 a[168:171], v247, %[ws], s42 offen sc1\n\t"
"v_mfma_f32_16x16x32_bf16 v[56:59], a[44:47], v[112:115], v[56:59]\n\t"
"s_waitcnt lgkmcnt(4)\n\t"
"ds_read_b64_tr_b16 a[16:17], v218 offset:40960\n\t"
"v_mfma_f32_16x16x32_bf16 v[60:63], a[44:47], v[116:119], v[60:63]\n\t"
"s_add_u32 s42, s42, 147456\n\t"
"ds_read_b64_tr_b16 a[18:19], v218 offset:43008\n\t"
"ds_read_b64_tr_b16 a[20:21], v221 offset:40960\n\t"
"ds_read_b64_tr_b16 a[22:23], v221 offset:43008\n\t"
"v_mfma_f32_16x16x32_bf16 v[32:35], a[0:3], v[120:123], v[32:35]\n\t"
"buffer_load_dwordx4 v210, %[w2], s68 offen offset:0 lds\n\t"
"v_mfma_f32_16x16x32_bf16 v[36:39], a[0:3], v[124:127], v[36:39]\n\t"
"buffer_load_dwordx2 v[204:205], v248, %[ws], s78 offen offset:640 sc1\n\t"
"v_mfma_f32_16x16x32_bf16 v[40:43], a[4:7], v[120:123], v[40:43]\n\t"
"s_waitcnt lgkmcnt(4)\n\t"
"ds_read_b64_tr_b16 a[24:25], v224 offset:40960\n\t"
"v_mfma_f32_16x16x32_bf16 v[44:47], a[4:7], v[124:127], v[44:47]\n\t"
"ds_read_b64_tr_b16 a[26:27], v224 offset:43008\n\t"
"ds_read_b64_tr_b16 a[28:29], v227 offset:40960\n\t"
"ds_read_b64_tr_b16 a[30:31], v227 offset:43008\n\t"
"v_mfma_f32_16x16x32_bf16 v[48:51], a[8:11], v[120:123], v[48:51]\n\t"
"ds_read_b64_tr_b16 a[32:33], v230 offset:40960\n\t"
"v_mfma_f32_16x16x32_bf16 v[52:55], a[8:11], v[124:127], v[52:55]\n\t"
"ds_read_b64_tr_b16 a[34:35], v230 offset:43008\n\t"
"v_mfma_f32_16x16x32_bf16 v[56:59], a[12:15], v[120:123], v[56:59]\n\t"
"ds_read_b64_tr_b16 a[36:37], v233 offset:40960\n\t"
"v_mfma_f32_16x16x32_bf16 v[60:63], a[12:15], v[124:127], v[60:63]\n\t"
"s_waitcnt lgkmcnt(7)\n\t"
"ds_read_b64_tr_b16 a[38:39], v233 offset:43008\n\t"
"ds_read_b64_tr_b16 a[40:41], v236 offset:40960\n\t"
"ds_read_b64_tr_b16 a[42:43], v236 offset:43008\n\t"
"ds_read_b64_tr_b16 a[44:45], v239 offset:40960\n\t"
"ds_read_b64_tr_b16 a[46:47], v239 offset:43008\n\t"
"v_mfma_f32_16x16x32_bf16 v[32:35], a[16:19], v[128:131], v[32:35]\n\t"
"buffer_load_dwordx4 v211, %[w2], s68 offen offset:1024 lds\n\t"
"v_mfma_f32_16x16x32_bf16 v[36:39], a[16:19], v[132:135], v[36:39]\n\t"
"buffer_load_dwordx4 a[164:167], v246, %[ws], s42 offen sc1\n\t"
"v_mfma_f32_16x16x32_bf16 v[40:43], a[20:23], v[128:131], v[40:43]\n\t"
"buffer_load_dwordx4 a[172:175], v247, %[ws], s42 offen sc1\n\t"
"v_mfma_f32_16x16x32_bf16 v[44:47], a[20:23], v[132:135], v[44:47]\n\t"
"s_waitcnt lgkmcnt(8)\n\t"
"v_mfma_f32_16x16x32_bf16 v[48:51], a[24:27], v[128:131], v[48:51]\n\t"
"s_waitcnt vmcnt(23)\n\t"
"s_waitcnt lgkmcnt(0)\n\t"
"v_mfma_f32_16x16x32_bf16 v[52:55], a[24:27], v[132:135], v[52:55]\n\t"
"s_barrier\n\t"
"v_mfma_f32_16x16x32_bf16 v[56:59], a[28:31], v[128:131], v[56:59]\n\t"
"ds_read_b64_tr_b16 a[0:1], v219 offset:0\n\t"
"v_mfma_f32_16x16x32_bf16 v[60:63], a[28:31], v[132:135], v[60:63]\n\t"
"s_mul_i32 s42, s95, 768\n\t"
"ds_read_b64_tr_b16 a[2:3], v219 offset:2048\n\t"
"v_mfma_f32_16x16x32_bf16 v[32:35], a[32:35], v[136:139], v[32:35]\n\t"
"s_mul_i32 s68, s95, 442368\n\t"
"buffer_store_dwordx2 v[254:255], v252, %[ws], s79 offen offset:768 sc1\n\t"
"ds_read_b64_tr_b16 a[4:5], v222 offset:0\n\t"
"ds_read_b64_tr_b16 a[6:7], v222 offset:2048\n\t"
"v_mfma_f32_16x16x32_bf16 v[36:39], a[32:35], v[140:143], v[36:39]\n\t"
"s_add_u32 s68, s68, s54\n\t"
"ds_read_b64_tr_b16 a[8:9], v225 offset:0\n\t"
"v_mfma_f32_16x16x32_bf16 v[40:43], a[36:39], v[136:139], v[40:43]\n\t"
"s_add_u32 s68, s68, s50\n\t"
"ds_read_b64_tr_b16 a[10:11], v225 offset:2048\n\t"
"v_mfma_f32_16x16x32_bf16 v[44:47], a[36:39], v[140:143], v[44:47]\n\t"
"ds_read_b64_tr_b16 a[12:13], v228 offset:0\n\t"
"v_mfma_f32_16x16x32_bf16 v[48:51], a[40:43], v[136:139], v[48:51]\n\t"
"s_add_u32 m0, s63, 0\n\t"
"ds_read_b64_tr_b16 a[14:15], v228 offset:2048\n\t"
"v_mfma_f32_16x16x32_bf16 v[52:55], a[40:43], v[140:143], v[52:55]\n\t"
"s_add_u32 s42, s42, s46\n\t"
"ds_read_b64_tr_b16 a[16:17], v231 offset:0\n\t"
"v_mfma_f32_16x16x32_bf16 v[56:59], a[44:47], v[136:139], v[56:59]\n\t"
"s_waitcnt lgkmcnt(5)\n\t"
"ds_read_b64_tr_b16 a[18:19], v231 offset:2048\n\t"
"v_mfma_f32_16x16x32_bf16 v[60:63], a[44:47], v[140:143], v[60:63]\n\t"
"ds_read_b64_tr_b16 a[20:21], v234 offset:0\n\t"
"ds_read_b64_tr_b16 a[22:23], v234 offset:2048\n\t"
"v_mfma_f32_16x16x32_bf16 v[32:35], a[0:3], v[144:147], v[32:35]\n\t"
"buffer_load_dwordx4 v206, %[w2], s68 offen offset:0 lds\n\t"
"v_mfma_f32_16x16x32_bf16 v[36:39], a[0:3], v[148:151], v[36:39]\n\t"
"ds_read_b64_tr_b16 a[24:25], v237 offset:0\n\t"
"v_mfma_f32_16x16x32_bf16 v[40:43], a[4:7], v[144:147], v[40:43]\n\t"
"ds_read_b64_tr_b16 a[26:27], v237 offset:2048\n\t"
"v_mfma_f32_16x16x32_bf16 v[44:47], a[4:7], v[148:151], v[44:47]\n\t"
"s_waitcnt lgkmcnt(6)\n\t"
"ds_read_b64_tr_b16 a[28:29], v240 offset:0\n\t"
"ds_read_b64_tr_b16 a[30:31], v240 offset:2048\n\t"
"v_mfma_f32_16x16x32_bf16 v[48:51], a[8:11], v[144:147], v[48:51]\n\t"
"buffer_load_dwordx4 v207, %[w2], s68 offen offset:1024 lds\n\t"
"v_mfma_f32_16x16x32_bf16 v[52:55], a[8:11], v[148:151], v[52:55]\n\t"
"ds_read_b64_tr_b16 a[32:33], v219 offset:8192\n\t"
"v_mfma_f32_16x16x32_bf16 v[56:59], a[12:15], v[144:147], v[56:59]\n\t"
"ds_read_b64_tr_b16 a[34:35], v219 offset:10240\n\t"
"v_mfma_f32_16x16x32_bf16 v[60:63], a[12:15], v[148:151], v[60:63]\n\t"
"s_waitcnt lgkmcnt(6)\n\t"
"ds_read_b64_tr_b16 a[36:37], v222 offset:8192\n\t"
"ds_read_b64_tr_b16 a[38:39], v222 offset:10240\n\t"
"v_mfma_f32_16x16x32_bf16 v[32:35], a[16:19], v[152:155], v[32:35]\n\t"
"ds_read_b64_tr_b16 a[40:41], v225 offset:8192\n\t"
"v_mfma_f32_16x16x32_bf16 v[36:39], a[16:19], v[156:159], v[36:39]\n\t"
"ds_read_b64_tr_b16 a[42:43], v225 offset:10240\n\t"
"v_mfma_f32_16x16x32_bf16 v[40:43], a[20:23], v[152:155], v[40:43]\n\t"
"s_lshl_b32 s42, s42, 4\n\t"
"ds_read_b64_tr_b16 a[44:45], v228 offset:8192\n\t"
"v_mfma_f32_16x16x32_bf16 v[44:47], a[20:23], v[156:159], v[44:47]\n\t"
"s_waitcnt lgkmcnt(7)\n\t"
"ds_read_b64_tr_b16 a[46:47], v228 offset:10240\n\t"
"v_mfma_f32_16x16x32_bf16 v[48:51], a[24:27], v[152:155], v[48:51]\n\t"
"buffer_load_dwordx4 v208, %[w2], s68 offen offset:2048 lds\n\t"
"v_mfma_f32_16x16x32_bf16 v[52:55], a[24:27], v[156:159], v[52:55]\n\t"
"s_add_u32 s42, s42, s56\n\t"
"ds_read_b64_tr_b16 a[0:1], v231 offset:8192\n\t"
"v_mfma_f32_16x16x32_bf16 v[56:59], a[28:31], v[152:155], v[56:59]\n\t"
"s_waitcnt lgkmcnt(5)\n\t"
"ds_read_b64_tr_b16 a[2:3], v231 offset:10240\n\t"
"v_mfma_f32_16x16x32_bf16 v[60:63], a[28:31], v[156:159], v[60:63]\n\t"
"ds_read_b64_tr_b16 a[4:5], v234 offset:8192\n\t"
"ds_read_b64_tr_b16 a[6:7], v234 offset:10240\n\t"
"v_mfma_f32_16x16x32_bf16 v[32:35], a[32:35], v[0:3], v[32:35]\n\t"
"ds_read_b64_tr_b16 a[8:9], v237 offset:8192\n\t"
"v_mfma_f32_16x16x32_bf16 v[36:39], a[32:35], v[4:7], v[36:39]\n\t"
"ds_read_b64_tr_b16 a[10:11], v237 offset:10240\n\t"
"v_mfma_f32_16x16x32_bf16 v[40:43], a[36:39], v[0:3], v[40:43]\n\t"
"ds_read_b64_tr_b16 a[12:13], v240 offset:8192\n\t"
"v_mfma_f32_16x16x32_bf16 v[44:47], a[36:39], v[4:7], v[44:47]\n\t"
"s_waitcnt lgkmcnt(7)\n\t"
"ds_read_b64_tr_b16 a[14:15], v240 offset:10240\n\t"
"v_mfma_f32_16x16x32_bf16 v[48:51], a[40:43], v[0:3], v[48:51]\n\t"
"buffer_load_dwordx4 a[176:179], v246, %[ws], s42 offen sc1\n\t"
"v_mfma_f32_16x16x32_bf16 v[52:55], a[40:43], v[4:7], v[52:55]\n\t"
"buffer_load_dwordx4 v209, %[w2], s68 offen offset:3072 lds\n\t"
"v_mfma_f32_16x16x32_bf16 v[56:59], a[44:47], v[0:3], v[56:59]\n\t"
"s_add_u32 m0, s63, 4096\n\t"
"ds_read_b64_tr_b16 a[16:17], v219 offset:16384\n\t"
"v_mfma_f32_16x16x32_bf16 v[60:63], a[44:47], v[4:7], v[60:63]\n\t"
"s_waitcnt lgkmcnt(5)\n\t"
"ds_read_b64_tr_b16 a[18:19], v219 offset:18432\n\t"
"ds_read_b64_tr_b16 a[20:21], v222 offset:16384\n\t"
"ds_read_b64_tr_b16 a[22:23], v222 offset:18432\n\t"
"v_mfma_f32_16x16x32_bf16 v[32:35], a[0:3], v[8:11], v[32:35]\n\t"
"buffer_load_dwordx4 a[184:187], v247, %[ws], s42 offen sc1\n\t"
"v_mfma_f32_16x16x32_bf16 v[36:39], a[0:3], v[12:15], v[36:39]\n\t"
"s_add_u32 s42, s42, 147456\n\t"
"ds_read_b64_tr_b16 a[24:25], v225 offset:16384\n\t"
"v_mfma_f32_16x16x32_bf16 v[40:43], a[4:7], v[8:11], v[40:43]\n\t"
"s_waitcnt lgkmcnt(5)\n\t"
"ds_read_b64_tr_b16 a[26:27], v225 offset:18432\n\t"
"v_mfma_f32_16x16x32_bf16 v[44:47], a[4:7], v[12:15], v[44:47]\n\t"
"ds_read_b64_tr_b16 a[28:29], v228 offset:16384\n\t"
"ds_read_b64_tr_b16 a[30:31], v228 offset:18432\n\t"
"v_mfma_f32_16x16x32_bf16 v[48:51], a[8:11], v[8:11], v[48:51]\n\t"
"buffer_load_dwordx4 v210, %[w2], s68 offen offset:0 lds\n\t"
"v_mfma_f32_16x16x32_bf16 v[52:55], a[8:11], v[12:15], v[52:55]\n\t"
"ds_read_b64_tr_b16 a[32:33], v231 offset:16384\n\t"
"v_mfma_f32_16x16x32_bf16 v[56:59], a[12:15], v[8:11], v[56:59]\n\t"
"ds_read_b64_tr_b16 a[34:35], v231 offset:18432\n\t"
"v_mfma_f32_16x16x32_bf16 v[60:63], a[12:15], v[12:15], v[60:63]\n\t"
"s_waitcnt lgkmcnt(6)\n\t"
"ds_read_b64_tr_b16 a[36:37], v234 offset:16384\n\t"
"ds_read_b64_tr_b16 a[38:39], v234 offset:18432\n\t"
"v_mfma_f32_16x16x32_bf16 v[32:35], a[16:19], v[16:19], v[32:35]\n\t"
"buffer_load_dwordx4 v211, %[w2], s68 offen offset:1024 lds\n\t"
"v_mfma_f32_16x16x32_bf16 v[36:39], a[16:19], v[20:23], v[36:39]\n\t"
"ds_read_b64_tr_b16 a[40:41], v237 offset:16384\n\t"
"v_mfma_f32_16x16x32_bf16 v[40:43], a[20:23], v[16:19], v[40:43]\n\t"
"s_waitcnt lgkmcnt(5)\n\t"
"ds_read_b64_tr_b16 a[42:43], v237 offset:18432\n\t"
"v_mfma_f32_16x16x32_bf16 v[44:47], a[20:23], v[20:23], v[44:47]\n\t"
"s_mul_i32 s68, s94, 442368\n\t"
"ds_read_b64_tr_b16 a[44:45], v240 offset:16384\n\t"
"ds_read_b64_tr_b16 a[46:47], v240 offset:18432\n\t"
"v_mfma_f32_16x16x32_bf16 v[48:51], a[24:27], v[16:19], v[48:51]\n\t"
"s_add_u32 s68, s68, s53\n\t"
"ds_read_b64_tr_b16 a[0:1], v219 offset:24576\n\t"
"buffer_load_dwordx4 a[180:183], v246, %[ws], s42 offen sc1\n\t"
"v_mfma_f32_16x16x32_bf16 v[52:55], a[24:27], v[20:23], v[52:55]\n\t"
"s_add_u32 s68, s68, s51\n\t"
"ds_read_b64_tr_b16 a[2:3], v219 offset:26624\n\t"
"v_mfma_f32_16x16x32_bf16 v[56:59], a[28:31], v[16:19], v[56:59]\n\t"
"s_add_u32 m0, s63, 24576\n\t"
"ds_read_b64_tr_b16 a[4:5], v222 offset:24576\n\t"
"v_mfma_f32_16x16x32_bf16 v[60:63], a[28:31], v[20:23], v[60:63]\n\t"
"s_waitcnt lgkmcnt(7)\n\t"
"ds_read_b64_tr_b16 a[6:7], v222 offset:26624\n\t"
"v_mfma_f32_16x16x32_bf16 v[32:35], a[32:35], v[24:27], v[32:35]\n\t"
"buffer_load_dwordx4 a[188:191], v247, %[ws], s42 offen sc1\n\t"
"v_mfma_f32_16x16x32_bf16 v[36:39], a[32:35], v[28:31], v[36:39]\n\t"
"ds_read_b64_tr_b16 a[8:9], v225 offset:24576\n\t"
"v_mfma_f32_16x16x32_bf16 v[40:43], a[36:39], v[24:27], v[40:43]\n\t"
"ds_read_b64_tr_b16 a[10:11], v225 offset:26624\n\t"
"v_mfma_f32_16x16x32_bf16 v[44:47], a[36:39], v[28:31], v[44:47]\n\t"
"s_waitcnt lgkmcnt(6)\n\t"
"ds_read_b64_tr_b16 a[12:13], v228 offset:24576\n\t"
"ds_read_b64_tr_b16 a[14:15], v228 offset:26624\n\t"
"v_mfma_f32_16x16x32_bf16 v[48:51], a[40:43], v[24:27], v[48:51]\n\t"
"buffer_load_dwordx4 v206, %[w2], s68 offen offset:0 lds\n\t"
"v_mfma_f32_16x16x32_bf16 v[52:55], a[40:43], v[28:31], v[52:55]\n\t"
"s_waitcnt vmcnt(15)\n\t"
"v_cmp_eq_u64 vcc, v[204:205], v[254:255]\n\t"
"s_cmp_eq_u64 vcc, -1\n\t"
"s_cbranch_scc1 105f\n\t"
"s_mov_b32 s98, 65536\n\t"
"104:\n\t"
"s_sleep 3\n\t"
"buffer_load_dwordx2 v[204:205], v248, %[ws], s78 offen offset:640 sc1\n\t"
"s_waitcnt vmcnt(0)\n\t"
"v_cmp_eq_u64 vcc, v[204:205], v[254:255]\n\t"
"s_cmp_eq_u64 vcc, -1\n\t"
"s_cbranch_scc1 105f\n\t"
"s_sub_u32 s98, s98, 1\n\t"
"s_cmp_lg_u32 s98, 0\n\t"
"s_cbranch_scc1 104b\n\t"
"105:\n\t"
"v_mfma_f32_16x16x32_bf16 v[56:59], a[44:47], v[24:27], v[56:59]\n\t"
"ds_read_b64_tr_b16 a[16:17], v231 offset:24576\n\t"
"v_mfma_f32_16x16x32_bf16 v[60:63], a[44:47], v[28:31], v[60:63]\n\t"
"s_waitcnt lgkmcnt(5)\n\t"
"ds_read_b64_tr_b16 a[18:19], v231 offset:26624\n\t"
"ds_read_b64_tr_b16 a[20:21], v234 offset:24576\n\t"
"ds_read_b64_tr_b16 a[22:23], v234 offset:26624\n\t"
"v_mfma_f32_16x16x32_bf16 v[64:67], a[0:3], a[48:51], v[64:67]\n\t"
"buffer_load_dwordx4 v207, %[w2], s68 offen offset:1024 lds\n\t"
"v_mfma_f32_16x16x32_bf16 v[68:71], a[0:3], a[52:55], v[68:71]\n\t"
"s_mul_i32 s42, s94, 768\n\t"
"ds_read_b64_tr_b16 a[24:25], v237 offset:24576\n\t"
"v_mfma_f32_16x16x32_bf16 v[72:75], a[4:7], a[48:51], v[72:75]\n\t"
"s_add_u32 s42, s42, s47\n\t"
"ds_read_b64_tr_b16 a[26:27], v237 offset:26624\n\t"
"v_mfma_f32_16x16x32_bf16 v[76:79], a[4:7], a[52:55], v[76:79]\n\t"
"s_waitcnt lgkmcnt(6)\n\t"
"ds_read_b64_tr_b16 a[28:29], v240 offset:24576\n\t"
"ds_read_b64_tr_b16 a[30:31], v240 offset:26624\n\t"
"v_mfma_f32_16x16x32_bf16 v[80:83], a[8:11], a[48:51], v[80:83]\n\t"
"s_mul_i32 s43, %[j], 43691\n\t"
"ds_read_b64_tr_b16 a[32:33], v219 offset:32768\n\t"
"v_mov_b32 v5, 0\n\t"
"v_mfma_f32_16x16x32_bf16 v[84:87], a[8:11], a[52:55], v[84:87]\n\t"
"s_lshr_b32 s43, s43, 17\n\t"
"ds_read_b64_tr_b16 a[34:35], v219 offset:34816\n\t"
"v_mfma_f32_16x16x32_bf16 v[88:91], a[12:15], a[48:51], v[88:91]\n\t"
"s_mul_i32 s64, s43, 3\n\t"
"ds_read_b64_tr_b16 a[36:37], v222 offset:32768\n\t"
"v_mfma_f32_16x16x32_bf16 v[92:95], a[12:15], a[52:55], v[92:95]\n\t"
"s_waitcnt lgkmcnt(7)\n\t"
"ds_read_b64_tr_b16 a[38:39], v222 offset:34816\n\t"
"v_mfma_f32_16x16x32_bf16 v[64:67], a[16:19], a[56:59], v[64:67]\n\t"
"s_sub_u32 s64, %[j], s64\n\t"
"ds_read_b64_tr_b16 a[40:41], v225 offset:32768\n\t"
"v_mfma_f32_16x16x32_bf16 v[68:71], a[16:19], a[60:63], v[68:71]\n\t"
"s_lshl_b32 s42, s42, 4\n\t"
"ds_read_b64_tr_b16 a[42:43], v225 offset:34816\n\t"
"v_mfma_f32_16x16x32_bf16 v[72:75], a[20:23], a[56:59], v[72:75]\n\t"
"s_waitcnt lgkmcnt(6)\n\t"
"ds_read_b64_tr_b16 a[44:45], v228 offset:32768\n\t"
"v_mfma_f32_16x16x32_bf16 v[76:79], a[20:23], a[60:63], v[76:79]\n\t"
"ds_read_b64_tr_b16 a[46:47], v228 offset:34816\n\t"
"v_and_b32 v0, 3, v245\n\t"
"v_mfma_f32_16x16x32_bf16 v[80:83], a[24:27], a[56:59], v[80:83]\n\t"
"buffer_load_dwordx4 v208, %[w2], s68 offen offset:2048 lds\n\t"
"v_mfma_f32_16x16x32_bf16 v[84:87], a[24:27], a[60:63], v[84:87]\n\t"
"s_add_u32 s42, s42, s56\n\t"
"ds_read_b64_tr_b16 a[0:1], v231 offset:32768\n\t"
"v_mfma_f32_16x16x32_bf16 v[88:91], a[28:31], a[56:59], v[88:91]\n\t"
"s_waitcnt lgkmcnt(5)\n\t"
"ds_read_b64_tr_b16 a[2:3], v231 offset:34816\n\t"
"v_lshlrev_b32 v31, 3, v0\n\t"
"v_mfma_f32_16x16x32_bf16 v[92:95], a[28:31], a[60:63], v[92:95]\n\t"
"ds_read_b64_tr_b16 a[4:5], v234 offset:32768\n\t"
"ds_read_b64_tr_b16 a[6:7], v234 offset:34816\n\t"
"v_mfma_f32_16x16x32_bf16 v[64:67], a[32:35], a[64:67], v[64:67]\n\t"
"ds_read_b64_tr_b16 a[8:9], v237 offset:32768\n\t"
"v_mfma_f32_16x16x32_bf16 v[68:71], a[32:35], a[68:71], v[68:71]\n\t"
"ds_read_b64_tr_b16 a[10:11], v237 offset:34816\n\t"
"v_mul_u32_u24 v0, 3, v0\n\t"
"v_mfma_f32_16x16x32_bf16 v[72:75], a[36:39], a[64:67], v[72:75]\n\t"
"s_waitcnt lgkmcnt(6)\n\t"
"ds_read_b64_tr_b16 a[12:13], v240 offset:32768\n\t"
"v_mfma_f32_16x16x32_bf16 v[76:79], a[36:39], a[68:71], v[76:79]\n\t"
"ds_read_b64_tr_b16 a[14:15], v240 offset:34816\n\t"
"v_mfma_f32_16x16x32_bf16 v[80:83], a[40:43], a[64:67], v[80:83]\n\t"
"buffer_load_dwordx4 v209, %[w2], s68 offen offset:3072 lds\n\t"
"v_mfma_f32_16x16x32_bf16 v[84:87], a[40:43], a[68:71], v[84:87]\n\t"
"s_add_u32 m0, s63, 28672\n\t"
"ds_read_b64_tr_b16 a[16:17], v219 offset:40960\n\t"
"v_add_u32 v0, s64, v0\n\t"
"v_mfma_f32_16x16x32_bf16 v[88:91], a[44:47], a[64:67], v[88:91]\n\t"
"s_waitcnt lgkmcnt(5)\n\t"
"ds_read_b64_tr_b16 a[18:19], v219 offset:43008\n\t"
"v_lshlrev_b32 v28, 3, v0\n\t"
"v_mfma_f32_16x16x32_bf16 v[92:95], a[44:47], a[68:71], v[92:95]\n\t"
"ds_read_b64_tr_b16 a[20:21], v222 offset:40960\n\t"
"ds_read_b64_tr_b16 a[22:23], v222 offset:43008\n\t"
"v_mfma_f32_16x16x32_bf16 v[64:67], a[0:3], a[72:75], v[64:67]\n\t"
"buffer_load_dwordx4 a[96:99], v246, %[ws], s42 offen sc1\n\t"
"v_mfma_f32_16x16x32_bf16 v[68:71], a[0:3], a[76:79], v[68:71]\n\t"
"buffer_load_dwordx4 v210, %[w2], s68 offen offset:0 lds\n\t"
"v_mfma_f32_16x16x32_bf16 v[72:75], a[4:7], a[72:75], v[72:75]\n\t"
"s_lshl_b32 s64, %[wv], 5\n\t"
"ds_read_b64_tr_b16 a[24:25], v225 offset:40960\n\t"
"v_mfma_f32_16x16x32_bf16 v[76:79], a[4:7], a[76:79], v[76:79]\n\t"
"s_waitcnt lgkmcnt(5)\n\t"
"ds_read_b64_tr_b16 a[26:27], v225 offset:43008\n\t"
"ds_read_b64_tr_b16 a[28:29], v228 offset:40960\n\t"
"ds_read_b64_tr_b16 a[30:31], v228 offset:43008\n\t"
"buffer_load_dwordx2 v[204:205], v248, %[ws], s78 offen offset:768 sc1\n\t"
"v_mfma_f32_16x16x32_bf16 v[80:83], a[8:11], a[72:75], v[80:83]\n\t"
"buffer_load_dwordx4 a[104:107], v247, %[ws], s42 offen sc1\n\t"
"v_mfma_f32_16x16x32_bf16 v[84:87], a[8:11], a[76:79], v[84:87]\n\t"
"s_add_u32 s42, s42, 147456\n\t"
"ds_read_b64_tr_b16 a[32:33], v231 offset:40960\n\t"
"v_add_u32 v29, s64, v31\n\t"
"v_mfma_f32_16x16x32_bf16 v[88:91], a[12:15], a[72:75], v[88:91]\n\t"
"s_waitcnt lgkmcnt(5)\n\t"
"ds_read_b64_tr_b16 a[34:35], v231 offset:43008\n\t"
"v_mfma_f32_16x16x32_bf16 v[92:95], a[12:15], a[76:79], v[92:95]\n\t"
"ds_read_b64_tr_b16 a[36:37], v234 offset:40960\n\t"
"ds_read_b64_tr_b16 a[38:39], v234 offset:43008\n\t"
"ds_read_b64_tr_b16 a[40:41], v237 offset:40960\n\t"
"ds_read_b64_tr_b16 a[42:43], v237 offset:43008\n\t"
"ds_read_b64_tr_b16 a[44:45], v240 offset:40960\n\t"
"ds_read_b64_tr_b16 a[46:47], v240 offset:43008\n\t"
"v_mfma_f32_16x16x32_bf16 v[64:67], a[16:19], a[80:83], v[64:67]\n\t"
"buffer_load_dwordx4 v211, %[w2], s68 offen offset:1024 lds\n\t"
"v_mfma_f32_16x16x32_bf16 v[68:71], a[16:19], a[84:87], v[68:71]\n\t"
"v_mfma_f32_16x16x32_bf16 v[72:75], a[20:23], a[80:83], v[72:75]\n\t"
"buffer_load_dwordx4 a[100:103], v246, %[ws], s42 offen sc1\n\t"
"v_mfma_f32_16x16x32_bf16 v[76:79], a[20:23], a[84:87], v[76:79]\n\t"
"s_waitcnt lgkmcnt(8)\n\t"
"s_mul_i32 s66, s43, 3072\n\t"
"s_add_u32 s66, s66, s52\n\t"
"v_mfma_f32_16x16x32_bf16 v[80:83], a[24:27], a[80:83], v[80:83]\n\t"
"s_waitcnt vmcnt(23)\n\t"
"s_waitcnt lgkmcnt(0)\n\t"
"s_barrier\n\t"
"v_mfma_f32_16x16x32_bf16 v[84:87], a[24:27], a[84:87], v[84:87]\n\t"
"buffer_load_dwordx4 a[108:111], v247, %[ws], s42 offen sc1\n\t"
"v_mfma_f32_16x16x32_bf16 v[88:91], a[28:31], a[80:83], v[88:91]\n\t"
"s_and_b32 s65, s43, 1\n\t"
"ds_read_b64_tr_b16 a[0:1], v220 offset:0\n\t"
"v_mfma_f32_16x16x32_bf16 v[92:95], a[28:31], a[84:87], v[92:95]\n\t"
"ds_read_b64_tr_b16 a[2:3], v220 offset:2048\n\t"
"v_mfma_f32_16x16x32_bf16 v[64:67], a[32:35], a[88:91], v[64:67]\n\t"
"s_lshl_b32 s65, s65, 6\n\t"
"ds_read_b64_tr_b16 a[4:5], v223 offset:0\n\t"
"v_mfma_f32_16x16x32_bf16 v[68:71], a[32:35], a[92:95], v[68:71]\n\t"
"s_mul_i32 s68, s95, 442368\n\t"
"ds_read_b64_tr_b16 a[6:7], v223 offset:2048\n\t"
"v_mfma_f32_16x16x32_bf16 v[72:75], a[36:39], a[88:91], v[72:75]\n\t"
"ds_read_b64_tr_b16 a[8:9], v226 offset:0\n\t"
"v_mfma_f32_16x16x32_bf16 v[76:79], a[36:39], a[92:95], v[76:79]\n\t"
"s_add_u32 s75, s53, s65\n\t"
"ds_read_b64_tr_b16 a[10:11], v226 offset:2048\n\t"
"v_mfma_f32_16x16x32_bf16 v[80:83], a[40:43], a[88:91], v[80:83]\n\t"
"s_add_u32 s68, s68, s53\n\t"
"ds_read_b64_tr_b16 a[12:13], v229 offset:0\n\t"
"ds_read_b64_tr_b16 a[14:15], v229 offset:2048\n\t"
"v_mfma_f32_16x16x32_bf16 v[84:87], a[40:43], a[92:95], v[84:87]\n\t"
"s_add_u32 s68, s68, s51\n\t"
"ds_read_b64_tr_b16 a[16:17], v232 offset:0\n\t"
"v_mfma_f32_16x16x32_bf16 v[88:91], a[44:47], a[88:91], v[88:91]\n\t"
"s_add_u32 m0, s63, 49152\n\t"
"ds_read_b64_tr_b16 a[18:19], v232 offset:2048\n\t"
"v_mfma_f32_16x16x32_bf16 v[92:95], a[44:47], a[92:95], v[92:95]\n\t"
"s_waitcnt lgkmcnt(6)\n\t"
"ds_read_b64_tr_b16 a[20:21], v235 offset:0\n\t"
"ds_read_b64_tr_b16 a[22:23], v235 offset:2048\n\t"
"v_mfma_f32_16x16x32_bf16 v[168:171], a[0:3], a[48:51], v[168:171]\n\t"
"buffer_load_dwordx4 v206, %[w2], s68 offen offset:0 lds\n\t"
"v_mfma_f32_16x16x32_bf16 v[172:175], a[0:3], a[52:55], v[172:175]\n\t"
"s_mul_i32 s67, s53, 512\n\t"
"ds_read_b64_tr_b16 a[24:25], v238 offset:0\n\t"
"v_mfma_f32_16x16x32_bf16 v[176:179], a[4:7], a[48:51], v[176:179]\n\t"
"s_mul_i32 s42, s94, 768\n\t"
"ds_read_b64_tr_b16 a[26:27], v238 offset:2048\n\t"
"v_mfma_f32_16x16x32_bf16 v[180:183], a[4:7], a[52:55], v[180:183]\n\t"
"s_waitcnt lgkmcnt(6)\n\t"
"ds_read_b64_tr_b16 a[28:29], v241 offset:0\n\t"
"ds_read_b64_tr_b16 a[30:31], v241 offset:2048\n\t"
"v_mfma_f32_16x16x32_bf16 v[184:187], a[8:11], a[48:51], v[184:187]\n\t"
"s_add_u32 s72, s67, s66\n\t"
"ds_read_b64_tr_b16 a[32:33], v220 offset:8192\n\t"
"v_mfma_f32_16x16x32_bf16 v[188:191], a[8:11], a[52:55], v[188:191]\n\t"
"s_add_u32 s76, s54, s65\n\t"
"ds_read_b64_tr_b16 a[34:35], v220 offset:10240\n\t"
"v_mfma_f32_16x16x32_bf16 v[200:203], a[12:15], a[48:51], v[200:203]\n\t"
"s_mul_i32 s67, s54, 512\n\t"
"ds_read_b64_tr_b16 a[36:37], v223 offset:8192\n\t"
"v_mfma_f32_16x16x32_bf16 v[212:215], a[12:15], a[52:55], v[212:215]\n\t"
"s_waitcnt lgkmcnt(7)\n\t"
"ds_read_b64_tr_b16 a[38:39], v223 offset:10240\n\t"
"buffer_load_dwordx4 v207, %[w2], s68 offen offset:1024 lds\n\t"
"v_mfma_f32_16x16x32_bf16 v[168:171], a[16:19], a[56:59], v[168:171]\n\t"
"s_add_u32 s42, s42, s48\n\t"
"ds_read_b64_tr_b16 a[40:41], v226 offset:8192\n\t"
"v_mfma_f32_16x16x32_bf16 v[172:175], a[16:19], a[60:63], v[172:175]\n\t"
"s_add_u32 s73, s67, s66\n\t"
"ds_read_b64_tr_b16 a[42:43], v226 offset:10240\n\t"
"v_mfma_f32_16x16x32_bf16 v[176:179], a[20:23], a[56:59], v[176:179]\n\t"
"s_lshl_b32 s42, s42, 4\n\t"
"ds_read_b64_tr_b16 a[44:45], v229 offset:8192\n\t"
"v_mfma_f32_16x16x32_bf16 v[180:183], a[20:23], a[60:63], v[180:183]\n\t"
"s_waitcnt lgkmcnt(7)\n\t"
"ds_read_b64_tr_b16 a[46:47], v229 offset:10240\n\t"
"v_mfma_f32_16x16x32_bf16 v[184:187], a[24:27], a[56:59], v[184:187]\n\t"
"s_add_u32 s77, s55, s65\n\t"
"ds_read_b64_tr_b16 a[0:1], v232 offset:8192\n\t"
"v_mfma_f32_16x16x32_bf16 v[188:191], a[24:27], a[60:63], v[188:191]\n\t"
"s_add_u32 s42, s42, s56\n\t"
"ds_read_b64_tr_b16 a[2:3], v232 offset:10240\n\t"
"v_mfma_f32_16x16x32_bf16 v[200:203], a[28:31], a[56:59], v[200:203]\n\t"
"s_waitcnt lgkmcnt(6)\n\t"
"ds_read_b64_tr_b16 a[4:5], v235 offset:8192\n\t"
"v_mfma_f32_16x16x32_bf16 v[212:215], a[28:31], a[60:63], v[212:215]\n\t"
"s_mul_i32 s67, s55, 512\n\t"
"ds_read_b64_tr_b16 a[6:7], v235 offset:10240\n\t"
"buffer_load_dwordx4 v208, %[w2], s68 offen offset:2048 lds\n\t"
"v_mfma_f32_16x16x32_bf16 v[168:171], a[32:35], a[64:67], v[168:171]\n\t"
"buffer_load_dwordx4 v209, %[w2], s68 offen offset:3072 lds\n\t"
"v_mfma_f32_16x16x32_bf16 v[172:175], a[32:35], a[68:71], v[172:175]\n\t"
"buffer_load_dwordx4 a[112:115], v246, %[ws], s42 offen sc1\n\t"
"v_mfma_f32_16x16x32_bf16 v[176:179], a[36:39], a[64:67], v[176:179]\n\t"
"s_add_u32 s74, s67, s66\n\t"
"ds_read_b64_tr_b16 a[8:9], v238 offset:8192\n\t"
"v_mfma_f32_16x16x32_bf16 v[180:183], a[36:39], a[68:71], v[180:183]\n\t"
"s_waitcnt lgkmcnt(5)\n\t"
"ds_read_b64_tr_b16 a[10:11], v238 offset:10240\n\t"
"ds_read_b64_tr_b16 a[12:13], v241 offset:8192\n\t"
"ds_read_b64_tr_b16 a[14:15], v241 offset:10240\n\t"
"buffer_load_dwordx4 a[120:123], v247, %[ws], s42 offen sc1\n\t"
"v_mfma_f32_16x16x32_bf16 v[184:187], a[40:43], a[64:67], v[184:187]\n\t"
"s_add_u32 m0, s63, 53248\n\t"
"ds_read_b64_tr_b16 a[16:17], v220 offset:16384\n\t"
"buffer_load_dwordx4 v210, %[w2], s68 offen offset:0 lds\n\t"
"v_mfma_f32_16x16x32_bf16 v[188:191], a[40:43], a[68:71], v[188:191]\n\t"
"s_lshl_b32 s64, %[mi], 2\n\t"
"ds_read_b64_tr_b16 a[18:19], v220 offset:18432\n\t"
"v_mfma_f32_16x16x32_bf16 v[200:203], a[44:47], a[64:67], v[200:203]\n\t"
"s_waitcnt lgkmcnt(6)\n\t"
"ds_read_b64_tr_b16 a[20:21], v223 offset:16384\n\t"
"v_mfma_f32_16x16x32_bf16 v[212:215], a[44:47], a[68:71], v[212:215]\n\t"
"ds_read_b64_tr_b16 a[22:23], v223 offset:18432\n\t"
"v_mfma_f32_16x16x32_bf16 v[168:171], a[0:3], a[72:75], v[168:171]\n\t"
"s_add_u32 s64, s64, %[wv]\n\t"
"ds_read_b64_tr_b16 a[24:25], v226 offset:16384\n\t"
"v_mfma_f32_16x16x32_bf16 v[172:175], a[0:3], a[76:79], v[172:175]\n\t"
"s_add_u32 s42, s42, 147456\n\t"
"ds_read_b64_tr_b16 a[26:27], v226 offset:18432\n\t"
"v_mfma_f32_16x16x32_bf16 v[176:179], a[4:7], a[72:75], v[176:179]\n\t"
"s_lshl_b32 s64, s64, 1\n\t"
"ds_read_b64_tr_b16 a[28:29], v229 offset:16384\n\t"
"v_mfma_f32_16x16x32_bf16 v[180:183], a[4:7], a[76:79], v[180:183]\n\t"
"s_waitcnt lgkmcnt(7)\n\t"
"ds_read_b64_tr_b16 a[30:31], v229 offset:18432\n\t"
"v_mfma_f32_16x16x32_bf16 v[184:187], a[8:11], a[72:75], v[184:187]\n\t"
"buffer_load_dwordx4 v211, %[w2], s68 offen offset:1024 lds\n\t"
"v_mfma_f32_16x16x32_bf16 v[188:191], a[8:11], a[76:79], v[188:191]\n\t"
"s_lshr_b32 s65, s43, 1\n\t"
"ds_read_b64_tr_b16 a[32:33], v232 offset:16384\n\t"
"v_mfma_f32_16x16x32_bf16 v[200:203], a[12:15], a[72:75], v[200:203]\n\t"
"s_waitcnt lgkmcnt(5)\n\t"
"ds_read_b64_tr_b16 a[34:35], v232 offset:18432\n\t"
"v_mfma_f32_16x16x32_bf16 v[212:215], a[12:15], a[76:79], v[212:215]\n\t"
"ds_read_b64_tr_b16 a[36:37], v235 offset:16384\n\t"
"ds_read_b64_tr_b16 a[38:39], v235 offset:18432\n\t"
"v_mfma_f32_16x16x32_bf16 v[168:171], a[16:19], a[80:83], v[168:171]\n\t"
"buffer_load_dwordx4 a[116:119], v246, %[ws], s42 offen sc1\n\t"
"v_mfma_f32_16x16x32_bf16 v[172:175], a[16:19], a[84:87], v[172:175]\n\t"
"s_add_u32 s64, s64, s65\n\t"
"ds_read_b64_tr_b16 a[40:41], v238 offset:16384\n\t"
"v_mfma_f32_16x16x32_bf16 v[176:179], a[20:23], a[80:83], v[176:179]\n\t"
"s_waitcnt lgkmcnt(5)\n\t"
"ds_read_b64_tr_b16 a[42:43], v238 offset:18432\n\t"
"v_mfma_f32_16x16x32_bf16 v[180:183], a[20:23], a[84:87], v[180:183]\n\t"
"s_mul_i32 s68, s94, 442368\n\t"
"ds_read_b64_tr_b16 a[44:45], v241 offset:16384\n\t"
"ds_read_b64_tr_b16 a[46:47], v241 offset:18432\n\t"
"buffer_load_dwordx4 a[124:127], v247, %[ws], s42 offen sc1\n\t"
"v_mfma_f32_16x16x32_bf16 v[184:187], a[24:27], a[80:83], v[184:187]\n\t"
"s_add_u32 s68, s68, s54\n\t"
"ds_read_b64_tr_b16 a[0:1], v220 offset:24576\n\t"
"v_mfma_f32_16x16x32_bf16 v[188:191], a[24:27], a[84:87], v[188:191]\n\t"
"s_add_u32 s68, s68, s51\n\t"
"ds_read_b64_tr_b16 a[2:3], v220 offset:26624\n\t"
"v_mfma_f32_16x16x32_bf16 v[200:203], a[28:31], a[80:83], v[200:203]\n\t"
"s_add_u32 m0, s63, 73728\n\t"
"ds_read_b64_tr_b16 a[4:5], v223 offset:24576\n\t"
"buffer_load_dwordx4 v206, %[w2], s68 offen offset:0 lds\n\t"
"v_mfma_f32_16x16x32_bf16 v[212:215], a[28:31], a[84:87], v[212:215]\n\t"
"s_waitcnt lgkmcnt(7)\n\t"
"ds_read_b64_tr_b16 a[6:7], v223 offset:26624\n\t"
"v_mfma_f32_16x16x32_bf16 v[168:171], a[32:35], a[88:91], v[168:171]\n\t"
"s_mul_i32 s40, s64, 20480\n\t"
"ds_read_b64_tr_b16 a[8:9], v226 offset:24576\n\t"
"v_mfma_f32_16x16x32_bf16 v[172:175], a[32:35], a[92:95], v[172:175]\n\t"
"s_add_u32 s40, s40, 40108032\n\t"
"ds_read_b64_tr_b16 a[10:11], v226 offset:26624\n\t"
"v_mfma_f32_16x16x32_bf16 v[176:179], a[36:39], a[88:91], v[176:179]\n\t"
"s_mul_i32 s64, s64, 18432\n\t"
"ds_read_b64_tr_b16 a[12:13], v229 offset:24576\n\t"
"v_mfma_f32_16x16x32_bf16 v[180:183], a[36:39], a[92:95], v[180:183]\n\t"
"s_waitcnt lgkmcnt(7)\n\t"
"ds_read_b64_tr_b16 a[14:15], v229 offset:26624\n\t"
"v_mfma_f32_16x16x32_bf16 v[184:187], a[40:43], a[88:91], v[184:187]\n\t"
"s_add_u32 s59, s64, s75\n\t"
"ds_read_b64_tr_b16 a[16:17], v232 offset:24576\n\t"
"v_mfma_f32_16x16x32_bf16 v[188:191], a[40:43], a[92:95], v[188:191]\n\t"
"s_waitcnt vmcnt(15)\n\t"
"v_cmp_eq_u64 vcc, v[204:205], v[254:255]\n\t"
"s_cmp_eq_u64 vcc, -1\n\t"
"s_cbranch_scc1 107f\n\t"
"s_mov_b32 s98, 65536\n\t"
"106:\n\t"
"s_sleep 3\n\t"
"buffer_load_dwordx2 v[204:205], v248, %[ws], s78 offen offset:768 sc1\n\t"
"s_waitcnt vmcnt(0)\n\t"
"v_cmp_eq_u64 vcc, v[204:205], v[254:255]\n\t"
"s_cmp_eq_u64 vcc, -1\n\t"
"s_cbranch_scc1 107f\n\t"
"s_sub_u32 s98, s98, 1\n\t"
"s_cmp_lg_u32 s98, 0\n\t"
"s_cbranch_scc1 106b\n\t"
"107:\n\t"
"v_mfma_f32_16x16x32_bf16 v[200:203], a[44:47], a[88:91], v[200:203]\n\t"
"s_mul_i32 s42, s94, 768\n\t"
"ds_read_b64_tr_b16 a[18:19], v232 offset:26624\n\t"
"v_mfma_f32_16x16x32_bf16 v[212:215], a[44:47], a[92:95], v[212:215]\n\t"
"s_waitcnt lgkmcnt(6)\n\t"
"ds_read_b64_tr_b16 a[20:21], v235 offset:24576\n\t"
"ds_read_b64_tr_b16 a[22:23], v235 offset:26624\n\t"
"v_mfma_f32_16x16x32_bf16 v[64:67], a[0:3], a[144:147], v[64:67]\n\t"
"s_add_u32 s60, s64, s76\n\t"
"ds_read_b64_tr_b16 a[24:25], v238 offset:24576\n\t"
"v_mfma_f32_16x16x32_bf16 v[68:71], a[0:3], a[148:151], v[68:71]\n\t"
"s_add_u32 s42, s42, s49\n\t"
"ds_read_b64_tr_b16 a[26:27], v238 offset:26624\n\t"
"v_mfma_f32_16x16x32_bf16 v[72:75], a[4:7], a[144:147], v[72:75]\n\t"
"s_add_u32 s61, s64, s77\n\t"
"ds_read_b64_tr_b16 a[28:29], v241 offset:24576\n\t"
"v_mfma_f32_16x16x32_bf16 v[76:79], a[4:7], a[148:151], v[76:79]\n\t"
"s_waitcnt lgkmcnt(7)\n\t"
"ds_read_b64_tr_b16 a[30:31], v241 offset:26624\n\t"
"v_mfma_f32_16x16x32_bf16 v[80:83], a[8:11], a[144:147], v[80:83]\n\t"
"buffer_load_dwordx4 v207, %[w2], s68 offen offset:1024 lds\n\t"
"v_mfma_f32_16x16x32_bf16 v[84:87], a[8:11], a[148:151], v[84:87]\n\t"
"s_lshl_b32 s42, s42, 4\n\t"
"ds_read_b64_tr_b16 a[32:33], v220 offset:32768\n\t"
"v_mfma_f32_16x16x32_bf16 v[88:91], a[12:15], a[144:147], v[88:91]\n\t"
"s_waitcnt lgkmcnt(5)\n\t"
"s_getpc_b64 s[96:97]\n\t"
"74:\n\t"
"s_and_b32 s98, %[mi], 3\n\t"
"s_lshl_b32 s98, s98, 2\n\t"
"s_add_u32 s98, s98, %[wv]\n\t"
"s_lshl_b32 s98, s98, 10\n\t"
"v_add_u32 v253, s98, v244\n\t"
"v_min_u32 v253, 97f-74b, v253\n\t"
"global_load_dword v253, v253, s[96:97]\n\t"
"v_mfma_f32_16x16x32_bf16 v[92:95], a[12:15], a[148:151], v[92:95]\n\t"
"s_lshl_b32 s64, %[wv], 10\n\t"
"ds_read_b64_tr_b16 a[34:35], v220 offset:34816\n\t"
"ds_read_b64_tr_b16 a[36:37], v223 offset:32768\n\t"
"ds_read_b64_tr_b16 a[38:39], v223 offset:34816\n\t"
"v_mfma_f32_16x16x32_bf16 v[64:67], a[16:19], a[152:155], v[64:67]\n\t"
"s_add_u32 s41, s64, %[lds]\n\t"
"ds_read_b64_tr_b16 a[40:41], v226 offset:32768\n\t"
"v_mfma_f32_16x16x32_bf16 v[68:71], a[16:19], a[156:159], v[68:71]\n\t"
"s_add_u32 s42, s42, s56\n\t"
"ds_read_b64_tr_b16 a[42:43], v226 offset:34816\n\t"
"v_mfma_f32_16x16x32_bf16 v[72:75], a[20:23], a[152:155], v[72:75]\n\t"
"s_add_u32 s64, s64, 42729472\n\t"
"ds_read_b64_tr_b16 a[44:45], v229 offset:32768\n\t"
"v_mfma_f32_16x16x32_bf16 v[76:79], a[20:23], a[156:159], v[76:79]\n\t"
"s_waitcnt lgkmcnt(7)\n\t"
"ds_read_b64_tr_b16 a[46:47], v229 offset:34816\n\t"
"v_mfma_f32_16x16x32_bf16 v[80:83], a[24:27], a[152:155], v[80:83]\n\t"
"buffer_load_dwordx4 v208, %[w2], s68 offen offset:2048 lds\n\t"
"v_mfma_f32_16x16x32_bf16 v[84:87], a[24:27], a[156:159], v[84:87]\n\t"
"s_mul_i32 s75, s75, 640\n\t"
"ds_read_b64_tr_b16 a[0:1], v232 offset:32768\n\t"
"v_mfma_f32_16x16x32_bf16 v[88:91], a[28:31], a[152:155], v[88:91]\n\t"
"s_waitcnt lgkmcnt(5)\n\t"
"ds_read_b64_tr_b16 a[2:3], v232 offset:34816\n\t"
"v_mfma_f32_16x16x32_bf16 v[92:95], a[28:31], a[156:159], v[92:95]\n\t"
"ds_read_b64_tr_b16 a[4:5], v235 offset:32768\n\t"
"ds_read_b64_tr_b16 a[6:7], v235 offset:34816\n\t"
"v_mfma_f32_16x16x32_bf16 v[64:67], a[32:35], a[160:163], v[64:67]\n\t"
"buffer_load_dwordx4 a[128:131], v246, %[ws], s42 offen sc1\n\t"
"v_mfma_f32_16x16x32_bf16 v[68:71], a[32:35], a[164:167], v[68:71]\n\t"
"s_add_u32 s75, s75, s64\n\t"
"ds_read_b64_tr_b16 a[8:9], v238 offset:32768\n\t"
"v_mfma_f32_16x16x32_bf16 v[72:75], a[36:39], a[160:163], v[72:75]\n\t"
"s_waitcnt lgkmcnt(5)\n\t"
"ds_read_b64_tr_b16 a[10:11], v238 offset:34816\n\t"
"v_mfma_f32_16x16x32_bf16 v[76:79], a[36:39], a[164:167], v[76:79]\n\t"
"s_mul_i32 s76, s76, 640\n\t"
"ds_read_b64_tr_b16 a[12:13], v241 offset:32768\n\t"
"ds_read_b64_tr_b16 a[14:15], v241 offset:34816\n\t"
"buffer_load_dwordx4 a[136:139], v247, %[ws], s42 offen sc1\n\t"
"v_mfma_f32_16x16x32_bf16 v[80:83], a[40:43], a[160:163], v[80:83]\n\t"
"buffer_load_dwordx4 v209, %[w2], s68 offen offset:3072 lds\n\t"
"v_mfma_f32_16x16x32_bf16 v[84:87], a[40:43], a[164:167], v[84:87]\n\t"
"s_add_u32 s42, s42, 147456\n\t"
"ds_read_b64_tr_b16 a[16:17], v220 offset:40960\n\t"
"v_mfma_f32_16x16x32_bf16 v[88:91], a[44:47], a[160:163], v[88:91]\n\t"
"s_add_u32 s76, s76, s64\n\t"
"ds_read_b64_tr_b16 a[18:19], v220 offset:43008\n\t"
"v_mfma_f32_16x16x32_bf16 v[92:95], a[44:47], a[164:167], v[92:95]\n\t"
"s_waitcnt lgkmcnt(6)\n\t"
"ds_read_b64_tr_b16 a[20:21], v223 offset:40960\n\t"
"ds_read_b64_tr_b16 a[22:23], v223 offset:43008\n\t"
"v_mfma_f32_16x16x32_bf16 v[64:67], a[0:3], a[168:171], v[64:67]\n\t"
"buffer_load_dwordx4 a[132:135], v246, %[ws], s42 offen sc1\n\t"
"v_mfma_f32_16x16x32_bf16 v[68:71], a[0:3], a[172:175], v[68:71]\n\t"
"s_mul_i32 s77, s77, 640\n\t"
"ds_read_b64_tr_b16 a[24:25], v226 offset:40960\n\t"
"v_mfma_f32_16x16x32_bf16 v[72:75], a[4:7], a[168:171], v[72:75]\n\t"
"s_add_u32 m0, s63, 77824\n\t"
"ds_read_b64_tr_b16 a[26:27], v226 offset:43008\n\t"
"v_mfma_f32_16x16x32_bf16 v[76:79], a[4:7], a[172:175], v[76:79]\n\t"
"s_waitcnt lgkmcnt(6)\n\t"
"ds_read_b64_tr_b16 a[28:29], v229 offset:40960\n\t"
"ds_read_b64_tr_b16 a[30:31], v229 offset:43008\n\t"
"buffer_load_dwordx4 v210, %[w2], s68 offen offset:0 lds\n\t"
"v_mfma_f32_16x16x32_bf16 v[80:83], a[8:11], a[168:171], v[80:83]\n\t"
"buffer_load_dwordx4 a[140:143], v247, %[ws], s42 offen sc1\n\t"
"v_mfma_f32_16x16x32_bf16 v[84:87], a[8:11], a[172:175], v[84:87]\n\t"
"s_add_u32 s77, s77, s64\n\t"
"ds_read_b64_tr_b16 a[32:33], v232 offset:40960\n\t"
"v_mfma_f32_16x16x32_bf16 v[88:91], a[12:15], a[168:171], v[88:91]\n\t"
"s_mul_i32 s42, s95, 768\n\t"
"ds_read_b64_tr_b16 a[34:35], v232 offset:43008\n\t"
"v_mfma_f32_16x16x32_bf16 v[92:95], a[12:15], a[172:175], v[92:95]\n\t"
"s_waitcnt lgkmcnt(6)\n\t"
"ds_read_b64_tr_b16 a[36:37], v235 offset:40960\n\t"
"ds_read_b64_tr_b16 a[38:39], v235 offset:43008\n\t"
"ds_read_b64_tr_b16 a[40:41], v238 offset:40960\n\t"
"ds_read_b64_tr_b16 a[42:43], v238 offset:43008\n\t"
"ds_read_b64_tr_b16 a[44:45], v241 offset:40960\n\t"
"ds_read_b64_tr_b16 a[46:47], v241 offset:43008\n\t"
"v_mfma_f32_16x16x32_bf16 v[64:67], a[16:19], a[176:179], v[64:67]\n\t"
"buffer_load_dwordx4 v211, %[w2], s68 offen offset:1024 lds\n\t"
"v_mfma_f32_16x16x32_bf16 v[68:71], a[16:19], a[180:183], v[68:71]\n\t"
"s_add_u32 s42, s42, s47\n\t"
"s_lshl_b32 s42, s42, 4\n\t"
"s_add_u32 s42, s42, s56\n\t"
"v_mfma_f32_16x16x32_bf16 v[72:75], a[20:23], a[176:179], v[72:75]\n\t"
"buffer_load_dwordx4 a[192:195], v246, %[ws], s42 offen sc1\n\t"
"v_mfma_f32_16x16x32_bf16 v[76:79], a[20:23], a[180:183], v[76:79]\n\t"
"s_waitcnt lgkmcnt(8)\n\t"
"v_and_b32 v0, 15, v245\n\t"
"v_lshrrev_b32 v1, 4, v245\n\t"
"v_mfma_f32_16x16x32_bf16 v[80:83], a[24:27], a[176:179], v[80:83]\n\t"
"s_waitcnt vmcnt(24)\n\t"
"s_waitcnt lgkmcnt(0)\n\t"
"s_barrier\n\t"
"v_mfma_f32_16x16x32_bf16 v[84:87], a[24:27], a[180:183], v[84:87]\n\t"
"buffer_load_dwordx4 a[200:203], v247, %[ws], s42 offen sc1\n\t"
"v_mfma_f32_16x16x32_bf16 v[88:91], a[28:31], a[176:179], v[88:91]\n\t"
"s_mul_i32 s68, s95, 442368\n\t"
"ds_read_b64_tr_b16 a[0:1], v218 offset:0\n\t"
"v_mul_u32_u24 v27, 1152, v0\n\t"
"v_mfma_f32_16x16x32_bf16 v[92:95], a[28:31], a[180:183], v[92:95]\n\t"
"ds_read_b64_tr_b16 a[2:3], v218 offset:2048\n\t"
"v_lshl_add_u32 v27, v1, 3, v27\n\t"
"v_mfma_f32_16x16x32_bf16 v[64:67], a[32:35], a[184:187], v[64:67]\n\t"
"s_add_u32 s68, s68, s54\n\t"
"ds_read_b64_tr_b16 a[4:5], v221 offset:0\n\t"
"v_mfma_f32_16x16x32_bf16 v[68:71], a[32:35], a[188:191], v[68:71]\n\t"
"s_add_u32 s68, s68, s51\n\t"
"ds_read_b64_tr_b16 a[6:7], v221 offset:2048\n\t"
"v_mfma_f32_16x16x32_bf16 v[72:75], a[36:39], a[184:187], v[72:75]\n\t"
"s_add_u32 m0, s63, 98304\n\t"
"ds_read_b64_tr_b16 a[8:9], v224 offset:0\n\t"
"v_add_u32 v24, %[lds], v244\n\t"
"v_mfma_f32_16x16x32_bf16 v[76:79], a[36:39], a[188:191], v[76:79]\n\t"
"ds_read_b64_tr_b16 a[10:11], v224 offset:2048\n\t"
"buffer_load_dwordx4 v206, %[w2], s68 offen offset:0 lds\n\t"
"v_mfma_f32_16x16x32_bf16 v[80:83], a[40:43], a[184:187], v[80:83]\n\t"
"s_add_u32 s42, s42, 147456\n\t"
"ds_read_b64_tr_b16 a[12:13], v227 offset:0\n\t"
"ds_read_b64_tr_b16 a[14:15], v227 offset:2048\n\t"
"v_mfma_f32_16x16x32_bf16 v[84:87], a[40:43], a[188:191], v[84:87]\n\t"
"buffer_load_dwordx4 a[196:199], v246, %[ws], s42 offen sc1\n\t"
"v_mfma_f32_16x16x32_bf16 v[88:91], a[44:47], a[184:187], v[88:91]\n\t"
"s_waitcnt lgkmcnt(4)\n\t"
"ds_read_b64_tr_b16 a[16:17], v230 offset:0\n\t"
"v_add_u32 v25, 49152, v24\n\t"
"v_mfma_f32_16x16x32_bf16 v[92:95], a[44:47], a[188:191], v[92:95]\n\t"
"ds_read_b64_tr_b16 a[18:19], v230 offset:2048\n\t"
"ds_read_b64_tr_b16 a[20:21], v233 offset:0\n\t"
"ds_read_b64_tr_b16 a[22:23], v233 offset:2048\n\t"
"v_mfma_f32_16x16x32_bf16 v[168:171], a[0:3], a[144:147], v[168:171]\n\t"
"buffer_load_dwordx4 a[204:207], v247, %[ws], s42 offen sc1\n\t"
"v_mfma_f32_16x16x32_bf16 v[172:175], a[0:3], a[148:151], v[172:175]\n\t"
"s_mul_i32 s42, s95, 768\n\t"
"ds_read_b64_tr_b16 a[24:25], v236 offset:0\n\t"
"v_add_u32 v26, 98304, v24\n\t"
"v_mfma_f32_16x16x32_bf16 v[176:179], a[4:7], a[144:147], v[176:179]\n\t"
"s_waitcnt lgkmcnt(5)\n\t"
"ds_read_b64_tr_b16 a[26:27], v236 offset:2048\n\t"
"v_mfma_f32_16x16x32_bf16 v[180:183], a[4:7], a[148:151], v[180:183]\n\t"
"ds_read_b64_tr_b16 a[28:29], v239 offset:0\n\t"
"ds_read_b64_tr_b16 a[30:31], v239 offset:2048\n\t"
"v_lshrrev_b32 v2, 2, v0\n\t"
"v_mfma_f32_16x16x32_bf16 v[184:187], a[8:11], a[144:147], v[184:187]\n\t"
"buffer_load_dwordx4 v207, %[w2], s68 offen offset:1024 lds\n\t"
"v_mfma_f32_16x16x32_bf16 v[188:191], a[8:11], a[148:151], v[188:191]\n\t"
"s_add_u32 s42, s42, s48\n\t"
"ds_read_b64_tr_b16 a[32:33], v218 offset:8192\n\t"
"v_mfma_f32_16x16x32_bf16 v[200:203], a[12:15], a[144:147], v[200:203]\n\t"
"s_waitcnt lgkmcnt(5)\n\t"
"ds_read_b64_tr_b16 a[34:35], v218 offset:10240\n\t"
"v_and_b32 v3, 1, v245\n\t"
"v_mfma_f32_16x16x32_bf16 v[212:215], a[12:15], a[148:151], v[212:215]\n\t"
"ds_read_b64_tr_b16 a[36:37], v221 offset:8192\n\t"
"ds_read_b64_tr_b16 a[38:39], v221 offset:10240\n\t"
"v_mfma_f32_16x16x32_bf16 v[168:171], a[16:19], a[152:155], v[168:171]\n\t"
"s_lshl_b32 s42, s42, 4\n\t"
"ds_read_b64_tr_b16 a[40:41], v224 offset:8192\n\t"
"v_mfma_f32_16x16x32_bf16 v[172:175], a[16:19], a[156:159], v[172:175]\n\t"
"ds_read_b64_tr_b16 a[42:43], v224 offset:10240\n\t"
"v_lshlrev_b32 v3, 4, v3\n\t"
"v_mfma_f32_16x16x32_bf16 v[176:179], a[20:23], a[152:155], v[176:179]\n\t"
"s_add_u32 s42, s42, s56\n\t"
"ds_read_b64_tr_b16 a[44:45], v227 offset:8192\n\t"
"v_mfma_f32_16x16x32_bf16 v[180:183], a[20:23], a[156:159], v[180:183]\n\t"
"s_waitcnt lgkmcnt(7)\n\t"
"ds_read_b64_tr_b16 a[46:47], v227 offset:10240\n\t"
"v_mov_b32 v4, 0x3f80\n\t"
"v_mfma_f32_16x16x32_bf16 v[184:187], a[24:27], a[152:155], v[184:187]\n\t"
"buffer_load_dwordx4 v208, %[w2], s68 offen offset:2048 lds\n\t"
"v_mfma_f32_16x16x32_bf16 v[188:191], a[24:27], a[156:159], v[188:191]\n\t"
"ds_read_b64_tr_b16 a[0:1], v230 offset:8192\n\t"
"v_mfma_f32_16x16x32_bf16 v[200:203], a[28:31], a[152:155], v[200:203]\n\t"
"s_waitcnt lgkmcnt(5)\n\t"
"ds_read_b64_tr_b16 a[2:3], v230 offset:10240\n\t"
"v_lshlrev_b32 v3, v3, v4\n\t"
"v_mfma_f32_16x16x32_bf16 v[212:215], a[28:31], a[156:159], v[212:215]\n\t"
"ds_read_b64_tr_b16 a[4:5], v233 offset:8192\n\t"
"ds_read_b64_tr_b16 a[6:7], v233 offset:10240\n\t"
"v_mfma_f32_16x16x32_bf16 v[168:171], a[32:35], a[160:163], v[168:171]\n\t"
"buffer_load_dwordx4 a[208:211], v246, %[ws], s42 offen sc1\n\t"
"v_mfma_f32_16x16x32_bf16 v[172:175], a[32:35], a[164:167], v[172:175]\n\t"
"buffer_load_dwordx4 a[216:219], v247, %[ws], s42 offen sc1\n\t"
"v_mfma_f32_16x16x32_bf16 v[176:179], a[36:39], a[160:163], v[176:179]\n\t"
"s_waitcnt lgkmcnt(4)\n\t"
"ds_read_b64_tr_b16 a[8:9], v236 offset:8192\n\t"
"v_mfma_f32_16x16x32_bf16 v[180:183], a[36:39], a[164:167], v[180:183]\n\t"
"s_add_u32 s42, s42, 147456\n\t"
"ds_read_b64_tr_b16 a[10:11], v236 offset:10240\n\t"
"ds_read_b64_tr_b16 a[12:13], v239 offset:8192\n\t"
"ds_read_b64_tr_b16 a[14:15], v239 offset:10240\n\t"
"v_mfma_f32_16x16x32_bf16 v[184:187], a[40:43], a[160:163], v[184:187]\n\t"
"buffer_load_dwordx4 v209, %[w2], s68 offen offset:3072 lds\n\t"
"v_mfma_f32_16x16x32_bf16 v[188:191], a[40:43], a[164:167], v[188:191]\n\t"
"s_add_u32 m0, s63, 102400\n\t"
"ds_read_b64_tr_b16 a[16:17], v218 offset:16384\n\t"
"v_mfma_f32_16x16x32_bf16 v[200:203], a[44:47], a[160:163], v[200:203]\n\t"
"s_waitcnt lgkmcnt(5)\n\t"
"ds_read_b64_tr_b16 a[18:19], v218 offset:18432\n\t"
"v_mfma_f32_16x16x32_bf16 v[212:215], a[44:47], a[164:167], v[212:215]\n\t"
"ds_read_b64_tr_b16 a[20:21], v221 offset:16384\n\t"
"ds_read_b64_tr_b16 a[22:23], v221 offset:18432\n\t"
"v_mfma_f32_16x16x32_bf16 v[168:171], a[0:3], a[168:171], v[168:171]\n\t"
"buffer_load_dwordx4 v210, %[w2], s68 offen offset:0 lds\n\t"
"v_mfma_f32_16x16x32_bf16 v[172:175], a[0:3], a[172:175], v[172:175]\n\t"
"buffer_load_dwordx4 a[212:215], v246, %[ws], s42 offen sc1\n\t"
"v_mfma_f32_16x16x32_bf16 v[176:179], a[4:7], a[168:171], v[176:179]\n\t"
"s_waitcnt lgkmcnt(4)\n\t"
"ds_read_b64_tr_b16 a[24:25], v224 offset:16384\n\t"
"v_mfma_f32_16x16x32_bf16 v[180:183], a[4:7], a[172:175], v[180:183]\n\t"
"ds_read_b64_tr_b16 a[26:27], v224 offset:18432\n\t"
"ds_read_b64_tr_b16 a[28:29], v227 offset:16384\n\t"
"ds_read_b64_tr_b16 a[30:31], v227 offset:18432\n\t"
"v_mfma_f32_16x16x32_bf16 v[184:187], a[8:11], a[168:171], v[184:187]\n\t"
"buffer_load_dwordx4 a[220:223], v247, %[ws], s42 offen sc1\n\t"
"v_mfma_f32_16x16x32_bf16 v[188:191], a[8:11], a[172:175], v[188:191]\n\t"
"s_mul_i32 s42, s95, 768\n\t"
"ds_read_b64_tr_b16 a[32:33], v230 offset:16384\n\t"
"v_mfma_f32_16x16x32_bf16 v[200:203], a[12:15], a[168:171], v[200:203]\n\t"
"s_waitcnt lgkmcnt(5)\n\t"
"ds_read_b64_tr_b16 a[34:35], v230 offset:18432\n\t"
"v_mfma_f32_16x16x32_bf16 v[212:215], a[12:15], a[172:175], v[212:215]\n\t"
"s_add_u32 s42, s42, s49\n\t"
"ds_read_b64_tr_b16 a[36:37], v233 offset:16384\n\t"
"ds_read_b64_tr_b16 a[38:39], v233 offset:18432\n\t"
"v_mfma_f32_16x16x32_bf16 v[168:171], a[16:19], a[176:179], v[168:171]\n\t"
"buffer_load_dwordx4 v211, %[w2], s68 offen offset:1024 lds\n\t"
"v_mfma_f32_16x16x32_bf16 v[172:175], a[16:19], a[180:183], v[172:175]\n\t"
"ds_read_b64_tr_b16 a[40:41], v236 offset:16384\n\t"
"v_mfma_f32_16x16x32_bf16 v[176:179], a[20:23], a[176:179], v[176:179]\n\t"
"s_lshl_b32 s42, s42, 4\n\t"
"ds_read_b64_tr_b16 a[42:43], v236 offset:18432\n\t"
"v_mfma_f32_16x16x32_bf16 v[180:183], a[20:23], a[180:183], v[180:183]\n\t"
"s_waitcnt lgkmcnt(6)\n\t"
"ds_read_b64_tr_b16 a[44:45], v239 offset:16384\n\t"
"ds_read_b64_tr_b16 a[46:47], v239 offset:18432\n\t"
"v_mfma_f32_16x16x32_bf16 v[184:187], a[24:27], a[176:179], v[184:187]\n\t"
"s_mul_i32 s68, s94, 442368\n\t"
"ds_read_b64_tr_b16 a[0:1], v218 offset:24576\n\t"
"v_mfma_f32_16x16x32_bf16 v[188:191], a[24:27], a[180:183], v[188:191]\n\t"
"s_add_u32 s42, s42, s56\n\t"
"ds_read_b64_tr_b16 a[2:3], v218 offset:26624\n\t"
"v_mfma_f32_16x16x32_bf16 v[200:203], a[28:31], a[176:179], v[200:203]\n\t"
"s_add_u32 s68, s68, s55\n\t"
"ds_read_b64_tr_b16 a[4:5], v221 offset:24576\n\t"
"v_mfma_f32_16x16x32_bf16 v[212:215], a[28:31], a[180:183], v[212:215]\n\t"
"s_waitcnt lgkmcnt(7)\n\t"
"ds_read_b64_tr_b16 a[6:7], v221 offset:26624\n\t"
"v_mfma_f32_16x16x32_bf16 v[168:171], a[32:35], a[184:187], v[168:171]\n\t"
"s_add_u32 s68, s68, s50\n\t"
"v_cmp_eq_u32 vcc, v2, v1\n\t"
"v_and_b32 v2, 2, v245\n\t"
"v_mov_b32 v18, 0\n\t"
"v_cndmask_b32 v3, v5, v3, vcc\n\t"
"v_mfma_f32_16x16x32_bf16 v[172:175], a[32:35], a[188:191], v[172:175]\n\t"
"s_add_u32 m0, s63, 122880\n\t"
"ds_read_b64_tr_b16 a[8:9], v224 offset:24576\n\t"
"v_mfma_f32_16x16x32_bf16 v[176:179], a[36:39], a[184:187], v[176:179]\n\t"
"s_waitcnt lgkmcnt(5)\n\t"
"ds_read_b64_tr_b16 a[10:11], v224 offset:26624\n\t"
"v_mfma_f32_16x16x32_bf16 v[180:183], a[36:39], a[188:191], v[180:183]\n\t"
"ds_read_b64_tr_b16 a[12:13], v227 offset:24576\n\t"
"ds_read_b64_tr_b16 a[14:15], v227 offset:26624\n\t"
"v_mfma_f32_16x16x32_bf16 v[184:187], a[40:43], a[184:187], v[184:187]\n\t"
"buffer_load_dwordx4 v206, %[w2], s68 offen offset:0 lds\n\t"
"v_mfma_f32_16x16x32_bf16 v[188:191], a[40:43], a[188:191], v[188:191]\n\t"
"v_cmp_eq_u32 vcc, 0, v2\n\t"
"v_mov_b32 v19, 0\n\t"
"v_mov_b32 v20, 0\n\t"
"v_cndmask_b32 v16, v5, v3, vcc\n\t"
"v_cndmask_b32 v17, v3, v5, vcc\n\t"
"v_mfma_f32_16x16x32_bf16 v[200:203], a[44:47], a[184:187], v[200:203]\n\t"
"s_waitcnt lgkmcnt(4)\n\t"
"ds_read_b64_tr_b16 a[16:17], v230 offset:24576\n\t"
"v_mov_b32 v21, 0\n\t"
"v_mfma_f32_16x16x32_bf16 v[212:215], a[44:47], a[188:191], v[212:215]\n\t"
"ds_read_b64_tr_b16 a[18:19], v230 offset:26624\n\t"
"ds_read_b64_tr_b16 a[20:21], v233 offset:24576\n\t"
"ds_read_b64_tr_b16 a[22:23], v233 offset:26624\n\t"
"v_mov_b32 v22, v16\n\t"
"v_mfma_f32_16x16x32_bf16 v[64:67], a[0:3], a[96:99], v[64:67]\n\t"
"s_mov_b32 s43, 0\n\t"
"ds_read_b64_tr_b16 a[24:25], v236 offset:24576\n\t"
"v_mov_b32 v23, v17\n\t"
"s_waitcnt vmcnt(37)\n\t"
"v_mfma_f32_16x16x32_bf16 v[68:71], a[0:3], a[100:103], v[68:71]\n\t"
"buffer_load_dwordx4 a[224:227], v246, %[ws], s42 offen sc1\n\t"
"v_mfma_f32_16x16x32_bf16 v[72:75], a[4:7], a[96:99], v[72:75]\n\t"
"s_waitcnt lgkmcnt(5)\n\t"
"ds_read_b64_tr_b16 a[26:27], v236 offset:26624\n\t"
"v_mfma_f32_16x16x32_bf16 v[76:79], a[4:7], a[100:103], v[76:79]\n\t"
"ds_read_b64_tr_b16 a[28:29], v239 offset:24576\n\t"
"ds_read_b64_tr_b16 a[30:31], v239 offset:26624\n\t"
"v_mfma_f32_16x16x32_bf16 v[80:83], a[8:11], a[96:99], v[80:83]\n\t"
"buffer_load_dwordx4 a[232:235], v247, %[ws], s42 offen sc1\n\t"
"v_mfma_f32_16x16x32_bf16 v[84:87], a[8:11], a[100:103], v[84:87]\n\t"
"buffer_load_dwordx4 v207, %[w2], s68 offen offset:1024 lds\n\t"
"v_mfma_f32_16x16x32_bf16 v[88:91], a[12:15], a[96:99], v[88:91]\n\t"
"s_add_u32 s42, s42, 147456\n\t"
"ds_read_b64_tr_b16 a[32:33], v218 offset:32768\n\t"
"v_mfma_f32_16x16x32_bf16 v[92:95], a[12:15], a[100:103], v[92:95]\n\t"
"s_waitcnt lgkmcnt(5)\n\t"
"ds_read_b64_tr_b16 a[34:35], v218 offset:34816\n\t"
"ds_read_b64_tr_b16 a[36:37], v221 offset:32768\n\t"
"ds_read_b64_tr_b16 a[38:39], v221 offset:34816\n\t"
"v_mfma_f32_16x16x32_bf16 v[64:67], a[16:19], a[104:107], v[64:67]\n\t"
"buffer_load_dwordx4 a[228:231], v246, %[ws], s42 offen sc1\n\t"
"s_waitcnt vmcnt(40)\n\t"
"v_mfma_f32_16x16x32_bf16 v[68:71], a[16:19], a[108:111], v[68:71]\n\t"
"buffer_load_dwordx4 a[236:239], v247, %[ws], s42 offen sc1\n\t"
"v_mfma_f32_16x16x32_bf16 v[72:75], a[20:23], a[104:107], v[72:75]\n\t"
"buffer_load_dwordx2 v[216:217], v31, %[ws], s78 offen offset:256 sc1\n\t"
"v_mfma_f32_16x16x32_bf16 v[76:79], a[20:23], a[108:111], v[76:79]\n\t"
"s_waitcnt lgkmcnt(4)\n\t"
"ds_read_b64_tr_b16 a[40:41], v224 offset:32768\n\t"
"ds_read_b64_tr_b16 a[42:43], v224 offset:34816\n\t"
"ds_read_b64_tr_b16 a[44:45], v227 offset:32768\n\t"
"ds_read_b64_tr_b16 a[46:47], v227 offset:34816\n\t"
"v_mfma_f32_16x16x32_bf16 v[80:83], a[24:27], a[104:107], v[80:83]\n\t"
"buffer_load_dwordx4 v208, %[w2], s68 offen offset:2048 lds\n\t"
"v_mfma_f32_16x16x32_bf16 v[84:87], a[24:27], a[108:111], v[84:87]\n\t"
"ds_read_b64_tr_b16 a[0:1], v230 offset:32768\n\t"
"v_mfma_f32_16x16x32_bf16 v[88:91], a[28:31], a[104:107], v[88:91]\n\t"
"ds_read_b64_tr_b16 a[2:3], v230 offset:34816\n\t"
"v_mfma_f32_16x16x32_bf16 v[92:95], a[28:31], a[108:111], v[92:95]\n\t"
"s_waitcnt lgkmcnt(6)\n\t"
"ds_read_b64_tr_b16 a[4:5], v233 offset:32768\n\t"
"ds_read_b64_tr_b16 a[6:7], v233 offset:34816\n\t"
"s_waitcnt vmcnt(38)\n\t"
"v_mfma_f32_16x16x32_bf16 v[64:67], a[32:35], a[112:115], v[64:67]\n\t"
"ds_read_b64_tr_b16 a[8:9], v236 offset:32768\n\t"
"s_waitcnt vmcnt(34)\n\t"
"v_mfma_f32_16x16x32_bf16 v[68:71], a[32:35], a[116:119], v[68:71]\n\t"
"ds_read_b64_tr_b16 a[10:11], v236 offset:34816\n\t"
"v_mfma_f32_16x16x32_bf16 v[72:75], a[36:39], a[112:115], v[72:75]\n\t"
"ds_read_b64_tr_b16 a[12:13], v239 offset:32768\n\t"
"v_mfma_f32_16x16x32_bf16 v[76:79], a[36:39], a[116:119], v[76:79]\n\t"
"s_waitcnt lgkmcnt(7)\n\t"
"ds_read_b64_tr_b16 a[14:15], v239 offset:34816\n\t"
"v_mfma_f32_16x16x32_bf16 v[80:83], a[40:43], a[112:115], v[80:83]\n\t"
"buffer_load_dwordx4 v209, %[w2], s68 offen offset:3072 lds\n\t"
"v_mfma_f32_16x16x32_bf16 v[84:87], a[40:43], a[116:119], v[84:87]\n\t"
"ds_read_b64_tr_b16 a[16:17], v218 offset:40960\n\t"
"v_mfma_f32_16x16x32_bf16 v[88:91], a[44:47], a[112:115], v[88:91]\n\t"
"s_add_u32 m0, s63, 126976\n\t"
"ds_read_b64_tr_b16 a[18:19], v218 offset:43008\n\t"
"v_mfma_f32_16x16x32_bf16 v[92:95], a[44:47], a[116:119], v[92:95]\n\t"
"s_waitcnt lgkmcnt(6)\n\t"
"ds_read_b64_tr_b16 a[20:21], v221 offset:40960\n\t"
"ds_read_b64_tr_b16 a[22:23], v221 offset:43008\n\t"
"v_mfma_f32_16x16x32_bf16 v[64:67], a[0:3], a[120:123], v[64:67]\n\t"
"buffer_load_dwordx4 v210, %[w2], s68 offen offset:0 lds\n\t"
"s_waitcnt vmcnt(35)\n\t"
"v_mfma_f32_16x16x32_bf16 v[68:71], a[0:3], a[124:127], v[68:71]\n\t"
"ds_read_b64_tr_b16 a[24:25], v224 offset:40960\n\t"
"v_mfma_f32_16x16x32_bf16 v[72:75], a[4:7], a[120:123], v[72:75]\n\t"
"ds_read_b64_tr_b16 a[26:27], v224 offset:43008\n\t"
"v_mfma_f32_16x16x32_bf16 v[76:79], a[4:7], a[124:127], v[76:79]\n\t"
"s_waitcnt lgkmcnt(6)\n\t"
"ds_read_b64_tr_b16 a[28:29], v227 offset:40960\n\t"
"ds_read_b64_tr_b16 a[30:31], v227 offset:43008\n\t"
"v_mfma_f32_16x16x32_bf16 v[80:83], a[8:11], a[120:123], v[80:83]\n\t"
"ds_read_b64_tr_b16 a[32:33], v230 offset:40960\n\t"
"v_mfma_f32_16x16x32_bf16 v[84:87], a[8:11], a[124:127], v[84:87]\n\t"
"ds_read_b64_tr_b16 a[34:35], v230 offset:43008\n\t"
"v_mfma_f32_16x16x32_bf16 v[88:91], a[12:15], a[120:123], v[88:91]\n\t"
"ds_read_b64_tr_b16 a[36:37], v233 offset:40960\n\t"
"v_mfma_f32_16x16x32_bf16 v[92:95], a[12:15], a[124:127], v[92:95]\n\t"
"s_waitcnt lgkmcnt(7)\n\t"
"ds_read_b64_tr_b16 a[38:39], v233 offset:43008\n\t"
"ds_read_b64_tr_b16 a[40:41], v236 offset:40960\n\t"
"ds_read_b64_tr_b16 a[42:43], v236 offset:43008\n\t"
"ds_read_b64_tr_b16 a[44:45], v239 offset:40960\n\t"
"ds_read_b64_tr_b16 a[46:47], v239 offset:43008\n\t"
"s_waitcnt vmcnt(30)\n\t"
"v_mfma_f32_16x16x32_bf16 v[64:67], a[16:19], a[128:131], v[64:67]\n\t"
"s_waitcnt vmcnt(27)\n\t"
"v_mfma_f32_16x16x32_bf16 v[68:71], a[16:19], a[132:135], v[68:71]\n\t"
"buffer_load_dwordx4 v211, %[w2], s68 offen offset:1024 lds\n\t"
"v_mfma_f32_16x16x32_bf16 v[72:75], a[20:23], a[128:131], v[72:75]\n\t"
"v_mfma_f32_16x16x32_bf16 v[76:79], a[20:23], a[132:135], v[76:79]\n\t"
"s_waitcnt lgkmcnt(8)\n\t"
"v_mfma_f32_16x16x32_bf16 v[80:83], a[24:27], a[128:131], v[80:83]\n\t"
"s_waitcnt vmcnt(25)\n\t"
"s_waitcnt lgkmcnt(0)\n\t"
"v_mfma_f32_16x16x32_bf16 v[84:87], a[24:27], a[132:135], v[84:87]\n\t"
"s_barrier\n\t"
"v_mfma_f32_16x16x32_bf16 v[88:91], a[28:31], a[128:131], v[88:91]\n\t"
"ds_read_b64_tr_b16 a[0:1], v219 offset:0\n\t"
"v_mfma_f32_16x16x32_bf16 v[92:95], a[28:31], a[132:135], v[92:95]\n\t"
"ds_read_b64_tr_b16 a[2:3], v219 offset:2048\n\t"
"v_mfma_f32_16x16x32_bf16 v[64:67], a[32:35], a[136:139], v[64:67]\n\t"
"ds_read_b64_tr_b16 a[4:5], v222 offset:0\n\t"
"v_mfma_f32_16x16x32_bf16 v[68:71], a[32:35], a[140:143], v[68:71]\n\t"
"s_mul_i32 s68, s94, 442368\n\t"
"ds_read_b64_tr_b16 a[6:7], v222 offset:2048\n\t"
"v_mfma_f32_16x16x32_bf16 v[72:75], a[36:39], a[136:139], v[72:75]\n\t"
"s_add_u32 s68, s68, s55\n\t"
"ds_read_b64_tr_b16 a[8:9], v225 offset:0\n\t"
"v_mfma_f32_16x16x32_bf16 v[76:79], a[36:39], a[140:143], v[76:79]\n\t"
"ds_read_b64_tr_b16 a[10:11], v225 offset:2048\n\t"
"v_mfma_f32_16x16x32_bf16 v[80:83], a[40:43], a[136:139], v[80:83]\n\t"
"s_add_u32 s68, s68, s51\n\t"
"ds_read_b64_tr_b16 a[12:13], v228 offset:0\n\t"
"ds_read_b64_tr_b16 a[14:15], v228 offset:2048\n\t"
"v_mfma_f32_16x16x32_bf16 v[84:87], a[40:43], a[140:143], v[84:87]\n\t"
"s_add_u32 m0, s63, 0\n\t"
"ds_read_b64_tr_b16 a[16:17], v231 offset:0\n\t"
"v_mfma_f32_16x16x32_bf16 v[88:91], a[44:47], a[136:139], v[88:91]\n\t"
"s_waitcnt lgkmcnt(5)\n\t"
"ds_read_b64_tr_b16 a[18:19], v231 offset:2048\n\t"
"v_mfma_f32_16x16x32_bf16 v[92:95], a[44:47], a[140:143], v[92:95]\n\t"
"ds_read_b64_tr_b16 a[20:21], v234 offset:0\n\t"
"ds_read_b64_tr_b16 a[22:23], v234 offset:2048\n\t"
"s_waitcnt vmcnt(24)\n\t"
"v_mfma_f32_16x16x32_bf16 v[64:67], a[0:3], a[192:195], v[64:67]\n\t"
"buffer_load_dwordx4 v206, %[w2], s68 offen offset:0 lds\n\t"
"s_waitcnt vmcnt(22)\n\t"
"v_mfma_f32_16x16x32_bf16 v[68:71], a[0:3], a[196:199], v[68:71]\n\t"
"ds_read_b64_tr_b16 a[24:25], v237 offset:0\n\t"
"v_mfma_f32_16x16x32_bf16 v[72:75], a[4:7], a[192:195], v[72:75]\n\t"
"ds_read_b64_tr_b16 a[26:27], v237 offset:2048\n\t"
"v_mfma_f32_16x16x32_bf16 v[76:79], a[4:7], a[196:199], v[76:79]\n\t"
"s_waitcnt lgkmcnt(6)\n\t"
"ds_read_b64_tr_b16 a[28:29], v240 offset:0\n\t"
"ds_read_b64_tr_b16 a[30:31], v240 offset:2048\n\t"
"v_mfma_f32_16x16x32_bf16 v[80:83], a[8:11], a[192:195], v[80:83]\n\t"
"buffer_load_dwordx4 v207, %[w2], s68 offen offset:1024 lds\n\t"
"v_mfma_f32_16x16x32_bf16 v[84:87], a[8:11], a[196:199], v[84:87]\n\t"
"ds_read_b64_tr_b16 a[32:33], v219 offset:8192\n\t"
"v_mfma_f32_16x16x32_bf16 v[88:91], a[12:15], a[192:195], v[88:91]\n\t"
"ds_read_b64_tr_b16 a[34:35], v219 offset:10240\n\t"
"v_mfma_f32_16x16x32_bf16 v[92:95], a[12:15], a[196:199], v[92:95]\n\t"
"s_waitcnt lgkmcnt(6)\n\t"
"ds_read_b64_tr_b16 a[36:37], v222 offset:8192\n\t"
"ds_read_b64_tr_b16 a[38:39], v222 offset:10240\n\t"
"v_mfma_f32_16x16x32_bf16 v[64:67], a[16:19], a[200:203], v[64:67]\n\t"
"ds_read_b64_tr_b16 a[40:41], v225 offset:8192\n\t"
"s_waitcnt vmcnt(22)\n\t"
"v_mfma_f32_16x16x32_bf16 v[68:71], a[16:19], a[204:207], v[68:71]\n\t"
"ds_read_b64_tr_b16 a[42:43], v225 offset:10240\n\t"
"v_mfma_f32_16x16x32_bf16 v[72:75], a[20:23], a[200:203], v[72:75]\n\t"
"ds_read_b64_tr_b16 a[44:45], v228 offset:8192\n\t"
"v_mfma_f32_16x16x32_bf16 v[76:79], a[20:23], a[204:207], v[76:79]\n\t"
"s_waitcnt lgkmcnt(7)\n\t"
"ds_read_b64_tr_b16 a[46:47], v228 offset:10240\n\t"
"v_mfma_f32_16x16x32_bf16 v[80:83], a[24:27], a[200:203], v[80:83]\n\t"
"buffer_load_dwordx4 v208, %[w2], s68 offen offset:2048 lds\n\t"
"v_mfma_f32_16x16x32_bf16 v[84:87], a[24:27], a[204:207], v[84:87]\n\t"
"ds_read_b64_tr_b16 a[0:1], v231 offset:8192\n\t"
"v_mfma_f32_16x16x32_bf16 v[88:91], a[28:31], a[200:203], v[88:91]\n\t"
"ds_read_b64_tr_b16 a[2:3], v231 offset:10240\n\t"
"v_mfma_f32_16x16x32_bf16 v[92:95], a[28:31], a[204:207], v[92:95]\n\t"
"s_waitcnt lgkmcnt(6)\n\t"
"ds_read_b64_tr_b16 a[4:5], v234 offset:8192\n\t"
"ds_read_b64_tr_b16 a[6:7], v234 offset:10240\n\t"
"s_waitcnt vmcnt(20)\n\t"
"v_mfma_f32_16x16x32_bf16 v[64:67], a[32:35], a[208:211], v[64:67]\n\t"
"ds_read_b64_tr_b16 a[8:9], v237 offset:8192\n\t"
"s_waitcnt vmcnt(16)\n\t"
"v_mfma_f32_16x16x32_bf16 v[68:71], a[32:35], a[212:215], v[68:71]\n\t"
"ds_read_b64_tr_b16 a[10:11], v237 offset:10240\n\t"
"v_mfma_f32_16x16x32_bf16 v[72:75], a[36:39], a[208:211], v[72:75]\n\t"
"ds_read_b64_tr_b16 a[12:13], v240 offset:8192\n\t"
"v_mfma_f32_16x16x32_bf16 v[76:79], a[36:39], a[212:215], v[76:79]\n\t"
"s_waitcnt lgkmcnt(7)\n\t"
"ds_read_b64_tr_b16 a[14:15], v240 offset:10240\n\t"
"v_mfma_f32_16x16x32_bf16 v[80:83], a[40:43], a[208:211], v[80:83]\n\t"
"buffer_load_dwordx4 v209, %[w2], s68 offen offset:3072 lds\n\t"
"v_mfma_f32_16x16x32_bf16 v[84:87], a[40:43], a[212:215], v[84:87]\n\t"
"ds_read_b64_tr_b16 a[16:17], v219 offset:16384\n\t"
"v_mfma_f32_16x16x32_bf16 v[88:91], a[44:47], a[208:211], v[88:91]\n\t"
"ds_read_b64_tr_b16 a[18:19], v219 offset:18432\n\t"
"v_mfma_f32_16x16x32_bf16 v[92:95], a[44:47], a[212:215], v[92:95]\n\t"
"s_waitcnt lgkmcnt(6)\n\t"
"ds_read_b64_tr_b16 a[20:21], v222 offset:16384\n\t"
"ds_read_b64_tr_b16 a[22:23], v222 offset:18432\n\t"
"v_mfma_f32_16x16x32_bf16 v[64:67], a[0:3], a[216:219], v[64:67]\n\t"
"ds_read_b64_tr_b16 a[24:25], v225 offset:16384\n\t"
"s_waitcnt vmcnt(16)\n\t"
"v_mfma_f32_16x16x32_bf16 v[68:71], a[0:3], a[220:223], v[68:71]\n\t"
"ds_read_b64_tr_b16 a[26:27], v225 offset:18432\n\t"
"v_mfma_f32_16x16x32_bf16 v[72:75], a[4:7], a[216:219], v[72:75]\n\t"
"s_add_u32 m0, s63, 4096\n\t"
"ds_read_b64_tr_b16 a[28:29], v228 offset:16384\n\t"
"v_mfma_f32_16x16x32_bf16 v[76:79], a[4:7], a[220:223], v[76:79]\n\t"
"s_waitcnt lgkmcnt(7)\n\t"
"ds_read_b64_tr_b16 a[30:31], v228 offset:18432\n\t"
"v_mfma_f32_16x16x32_bf16 v[80:83], a[8:11], a[216:219], v[80:83]\n\t"
"buffer_load_dwordx4 v210, %[w2], s68 offen offset:0 lds\n\t"
"v_mfma_f32_16x16x32_bf16 v[84:87], a[8:11], a[220:223], v[84:87]\n\t"
"ds_read_b64_tr_b16 a[32:33], v231 offset:16384\n\t"
"v_mfma_f32_16x16x32_bf16 v[88:91], a[12:15], a[216:219], v[88:91]\n\t"
"ds_read_b64_tr_b16 a[34:35], v231 offset:18432\n\t"
"v_mfma_f32_16x16x32_bf16 v[92:95], a[12:15], a[220:223], v[92:95]\n\t"
"s_waitcnt lgkmcnt(6)\n\t"
"ds_read_b64_tr_b16 a[36:37], v234 offset:16384\n\t"
"ds_read_b64_tr_b16 a[38:39], v234 offset:18432\n\t"
"s_waitcnt vmcnt(14)\n\t"
"v_mfma_f32_16x16x32_bf16 v[64:67], a[16:19], a[224:227], v[64:67]\n\t"
"ds_read_b64_tr_b16 a[40:41], v237 offset:16384\n\t"
"s_waitcnt vmcnt(11)\n\t"
"v_mfma_f32_16x16x32_bf16 v[68:71], a[16:19], a[228:231], v[68:71]\n\t"
"ds_read_b64_tr_b16 a[42:43], v237 offset:18432\n\t"
"v_mfma_f32_16x16x32_bf16 v[72:75], a[20:23], a[224:227], v[72:75]\n\t"
"ds_read_b64_tr_b16 a[44:45], v240 offset:16384\n\t"
"v_mfma_f32_16x16x32_bf16 v[76:79], a[20:23], a[228:231], v[76:79]\n\t"
"s_waitcnt lgkmcnt(7)\n\t"
"ds_read_b64_tr_b16 a[46:47], v240 offset:18432\n\t"
"v_mfma_f32_16x16x32_bf16 v[80:83], a[24:27], a[224:227], v[80:83]\n\t"
"buffer_load_dwordx4 v211, %[w2], s68 offen offset:1024 lds\n\t"
"v_mfma_f32_16x16x32_bf16 v[84:87], a[24:27], a[228:231], v[84:87]\n\t"
"ds_read_b64_tr_b16 a[0:1], v219 offset:24576\n\t"
"v_mfma_f32_16x16x32_bf16 v[88:91], a[28:31], a[224:227], v[88:91]\n\t"
"ds_read_b64_tr_b16 a[2:3], v219 offset:26624\n\t"
"v_mfma_f32_16x16x32_bf16 v[92:95], a[28:31], a[228:231], v[92:95]\n\t"
"s_waitcnt lgkmcnt(6)\n\t"
"ds_read_b64_tr_b16 a[4:5], v222 offset:24576\n\t"
"ds_read_b64_tr_b16 a[6:7], v222 offset:26624\n\t"
"v_mfma_f32_16x16x32_bf16 v[64:67], a[32:35], a[232:235], v[64:67]\n\t"
"s_mul_i32 s68, s95, 442368\n\t"
"ds_read_b64_tr_b16 a[8:9], v225 offset:24576\n\t"
"s_waitcnt vmcnt(11)\n\t"
"v_mfma_f32_16x16x32_bf16 v[68:71], a[32:35], a[236:239], v[68:71]\n\t"
"s_add_u32 s68, s68, s55\n\t"
"ds_read_b64_tr_b16 a[10:11], v225 offset:26624\n\t"
"v_mfma_f32_16x16x32_bf16 v[72:75], a[36:39], a[232:235], v[72:75]\n\t"
"s_add_u32 s68, s68, s50\n\t"
"ds_read_b64_tr_b16 a[12:13], v228 offset:24576\n\t"
"v_mfma_f32_16x16x32_bf16 v[76:79], a[36:39], a[236:239], v[76:79]\n\t"
"s_waitcnt lgkmcnt(7)\n\t"
"ds_read_b64_tr_b16 a[14:15], v228 offset:26624\n\t"
"v_mfma_f32_16x16x32_bf16 v[80:83], a[40:43], a[232:235], v[80:83]\n\t"
"s_add_u32 m0, s63, 24576\n\t"
"ds_read_b64_tr_b16 a[16:17], v231 offset:24576\n\t"
"v_mfma_f32_16x16x32_bf16 v[84:87], a[40:43], a[236:239], v[84:87]\n\t"
"buffer_load_dwordx4 v206, %[w2], s68 offen offset:0 lds\n\t"
"v_mfma_f32_16x16x32_bf16 v[88:91], a[44:47], a[232:235], v[88:91]\n\t"
"ds_read_b64_tr_b16 a[18:19], v231 offset:26624\n\t"
"v_mfma_f32_16x16x32_bf16 v[92:95], a[44:47], a[236:239], v[92:95]\n\t"
"s_waitcnt lgkmcnt(6)\n\t"
"ds_read_b64_tr_b16 a[20:21], v234 offset:24576\n\t"
"ds_read_b64_tr_b16 a[22:23], v234 offset:26624\n\t"
"v_mfma_f32_16x16x32_bf16 v[168:171], a[0:3], a[96:99], v[168:171]\n\t"
"buffer_load_dwordx4 v207, %[w2], s68 offen offset:1024 lds\n\t"
"v_mfma_f32_16x16x32_bf16 v[172:175], a[0:3], a[100:103], v[172:175]\n\t"
"ds_read_b64_tr_b16 a[24:25], v237 offset:24576\n\t"
"v_mfma_f32_16x16x32_bf16 v[176:179], a[4:7], a[96:99], v[176:179]\n\t"
"s_waitcnt lgkmcnt(5)\n\t"
"ds_read_b64_tr_b16 a[26:27], v237 offset:26624\n\t"
"v_mfma_f32_16x16x32_bf16 v[180:183], a[4:7], a[100:103], v[180:183]\n\t"
"s_mul_i32 s62, s53, 512\n\t"
"ds_read_b64_tr_b16 a[28:29], v240 offset:24576\n\t"
"ds_read_b64_tr_b16 a[30:31], v240 offset:26624\n\t"
"v_mfma_f32_16x16x32_bf16 v[184:187], a[8:11], a[96:99], v[184:187]\n\t"
"s_add_u32 s62, s62, s52\n\t"
"ds_read_b64_tr_b16 a[32:33], v219 offset:32768\n\t"
"v_cvt_pk_bf16_f32 v160, v64, v65\n\t"
"v_mfma_f32_16x16x32_bf16 v[188:191], a[8:11], a[100:103], v[188:191]\n\t"
"ds_read_b64_tr_b16 a[34:35], v219 offset:34816\n\t"
"v_cvt_pk_bf16_f32 v161, v66, v67\n\t"
"v_mfma_f32_16x16x32_bf16 v[200:203], a[12:15], a[96:99], v[200:203]\n\t"
"s_waitcnt lgkmcnt(6)\n\t"
"ds_read_b64_tr_b16 a[36:37], v222 offset:32768\n\t"
"v_cvt_pk_bf16_f32 v162, v72, v73\n\t"
"v_mfma_f32_16x16x32_bf16 v[212:215], a[12:15], a[100:103], v[212:215]\n\t"
"ds_read_b64_tr_b16 a[38:39], v222 offset:34816\n\t"
"v_mfma_f32_16x16x32_bf16 v[168:171], a[16:19], a[104:107], v[168:171]\n\t"
"buffer_load_dwordx4 v208, %[w2], s68 offen offset:2048 lds\n\t"
"v_mfma_f32_16x16x32_bf16 v[172:175], a[16:19], a[108:111], v[172:175]\n\t"
"ds_read_b64_tr_b16 a[40:41], v225 offset:32768\n\t"
"v_cvt_pk_bf16_f32 v163, v74, v75\n\t"
"v_mfma_f32_16x16x32_bf16 v[176:179], a[20:23], a[104:107], v[176:179]\n\t"
"buffer_store_dwordx4 v[160:163], v244, %[ws], s62 offen sc1\n\t"
"ds_read_b64_tr_b16 a[42:43], v225 offset:34816\n\t"
"ds_read_b64_tr_b16 a[44:45], v228 offset:32768\n\t"
"ds_read_b64_tr_b16 a[46:47], v228 offset:34816\n\t"
"v_mfma_f32_16x16x32_bf16 v[180:183], a[20:23], a[108:111], v[180:183]\n\t"
"s_waitcnt lgkmcnt(8)\n\t"
"ds_read_b64_tr_b16 a[0:1], v231 offset:32768\n\t"
"v_mfma_f32_16x16x32_bf16 v[184:187], a[24:27], a[104:107], v[184:187]\n\t"
"buffer_load_dwordx4 v209, %[w2], s68 offen offset:3072 lds\n\t"
"v_mfma_f32_16x16x32_bf16 v[188:191], a[24:27], a[108:111], v[188:191]\n\t"
"s_add_u32 s69, s62, 4096\n\t"
"ds_read_b64_tr_b16 a[2:3], v231 offset:34816\n\t"
"v_mfma_f32_16x16x32_bf16 v[200:203], a[28:31], a[104:107], v[200:203]\n\t"
"s_waitcnt lgkmcnt(6)\n\t"
"ds_read_b64_tr_b16 a[4:5], v234 offset:32768\n\t"
"v_mfma_f32_16x16x32_bf16 v[212:215], a[28:31], a[108:111], v[212:215]\n\t"
"ds_read_b64_tr_b16 a[6:7], v234 offset:34816\n\t"
"v_mfma_f32_16x16x32_bf16 v[168:171], a[32:35], a[112:115], v[168:171]\n\t"
"ds_read_b64_tr_b16 a[8:9], v237 offset:32768\n\t"
"v_cvt_pk_bf16_f32 v164, v80, v81\n\t"
"v_mfma_f32_16x16x32_bf16 v[172:175], a[32:35], a[116:119], v[172:175]\n\t"
"s_add_u32 m0, s63, 28672\n\t"
"ds_read_b64_tr_b16 a[10:11], v237 offset:34816\n\t"
"v_cvt_pk_bf16_f32 v165, v82, v83\n\t"
"v_mfma_f32_16x16x32_bf16 v[176:179], a[36:39], a[112:115], v[176:179]\n\t"
"s_waitcnt lgkmcnt(6)\n\t"
"ds_read_b64_tr_b16 a[12:13], v240 offset:32768\n\t"
"v_mfma_f32_16x16x32_bf16 v[180:183], a[36:39], a[116:119], v[180:183]\n\t"
"ds_read_b64_tr_b16 a[14:15], v240 offset:34816\n\t"
"v_mfma_f32_16x16x32_bf16 v[184:187], a[40:43], a[112:115], v[184:187]\n\t"
"ds_read_b64_tr_b16 a[16:17], v219 offset:40960\n\t"
"v_cvt_pk_bf16_f32 v166, v88, v89\n\t"
"v_mfma_f32_16x16x32_bf16 v[188:191], a[40:43], a[116:119], v[188:191]\n\t"
"ds_read_b64_tr_b16 a[18:19], v219 offset:43008\n\t"
"v_mfma_f32_16x16x32_bf16 v[200:203], a[44:47], a[112:115], v[200:203]\n\t"
"s_waitcnt lgkmcnt(6)\n\t"
"ds_read_b64_tr_b16 a[20:21], v222 offset:40960\n\t"
"v_cvt_pk_bf16_f32 v167, v90, v91\n\t"
"v_mfma_f32_16x16x32_bf16 v[212:215], a[44:47], a[116:119], v[212:215]\n\t"
"ds_read_b64_tr_b16 a[22:23], v222 offset:43008\n\t"
"v_mfma_f32_16x16x32_bf16 v[168:171], a[0:3], a[120:123], v[168:171]\n\t"
"buffer_store_dwordx4 v[164:167], v244, %[ws], s69 offen sc1\n\t"
"ds_read_b64_tr_b16 a[24:25], v225 offset:40960\n\t"
"ds_read_b64_tr_b16 a[26:27], v225 offset:43008\n\t"
"ds_read_b64_tr_b16 a[28:29], v228 offset:40960\n\t"
"v_mfma_f32_16x16x32_bf16 v[172:175], a[0:3], a[124:127], v[172:175]\n\t"
"buffer_load_dwordx4 v210, %[w2], s68 offen offset:0 lds\n\t"
"v_mfma_f32_16x16x32_bf16 v[176:179], a[4:7], a[120:123], v[176:179]\n\t"
"s_add_u32 s69, s62, 8192\n\t"
"ds_read_b64_tr_b16 a[30:31], v228 offset:43008\n\t"
"v_mfma_f32_16x16x32_bf16 v[180:183], a[4:7], a[124:127], v[180:183]\n\t"
"s_waitcnt lgkmcnt(8)\n\t"
"ds_read_b64_tr_b16 a[32:33], v231 offset:40960\n\t"
"v_mfma_f32_16x16x32_bf16 v[184:187], a[8:11], a[120:123], v[184:187]\n\t"
"ds_read_b64_tr_b16 a[34:35], v231 offset:43008\n\t"
"v_mfma_f32_16x16x32_bf16 v[188:191], a[8:11], a[124:127], v[188:191]\n\t"
"s_waitcnt vmcnt(17)\n\t"
"v_cmp_eq_u64 vcc, v[216:217], v[254:255]\n\t"
"s_cmp_eq_u64 vcc, -1\n\t"
"s_cbranch_scc1 109f\n\t"
"s_mov_b32 s98, 65536\n\t"
"108:\n\t"
"s_sleep 3\n\t"
"buffer_load_dwordx2 v[216:217], v31, %[ws], s78 offen offset:256 sc1\n\t"
"s_waitcnt vmcnt(0)\n\t"
"v_cmp_eq_u64 vcc, v[216:217], v[254:255]\n\t"
"s_cmp_eq_u64 vcc, -1\n\t"
"s_cbranch_scc1 109f\n\t"
"s_sub_u32 s98, s98, 1\n\t"
"s_cmp_lg_u32 s98, 0\n\t"
"s_cbranch_scc1 108b\n\t"
"109:\n\t"
"v_mfma_f32_16x16x32_bf16 v[200:203], a[12:15], a[120:123], v[200:203]\n\t"
"s_waitcnt lgkmcnt(6)\n\t"
"ds_read_b64_tr_b16 a[36:37], v234 offset:40960\n\t"
"v_cvt_pk_bf16_f32 v192, v68, v69\n\t"
"v_mfma_f32_16x16x32_bf16 v[212:215], a[12:15], a[124:127], v[212:215]\n\t"
"ds_read_b64_tr_b16 a[38:39], v234 offset:43008\n\t"
"ds_read_b64_tr_b16 a[40:41], v237 offset:40960\n\t"
"ds_read_b64_tr_b16 a[42:43], v237 offset:43008\n\t"
"ds_read_b64_tr_b16 a[44:45], v240 offset:40960\n\t"
"ds_read_b64_tr_b16 a[46:47], v240 offset:43008\n\t"
"v_mfma_f32_16x16x32_bf16 v[168:171], a[16:19], a[128:131], v[168:171]\n\t"
"buffer_load_dwordx4 v211, %[w2], s68 offen offset:1024 lds\n\t"
"v_mfma_f32_16x16x32_bf16 v[172:175], a[16:19], a[132:135], v[172:175]\n\t"
"v_mfma_f32_16x16x32_bf16 v[176:179], a[20:23], a[128:131], v[176:179]\n\t"
"v_cvt_pk_bf16_f32 v193, v70, v71\n\t"
"v_mfma_f32_16x16x32_bf16 v[180:183], a[20:23], a[132:135], v[180:183]\n\t"
"s_waitcnt lgkmcnt(8)\n\t"
"s_waitcnt vmcnt(14)\n\t"
"s_waitcnt lgkmcnt(0)\n\t"
"v_mfma_f32_16x16x32_bf16 v[184:187], a[24:27], a[128:131], v[184:187]\n\t"
"buffer_load_dwordx2 v[204:205], v29, %[ws], s78 offen offset:384 sc1\n\t"
"v_mfma_f32_16x16x32_bf16 v[188:191], a[24:27], a[132:135], v[188:191]\n\t"
"s_barrier\n\t"
"v_cvt_pk_bf16_f32 v194, v76, v77\n\t"
"v_cvt_pk_bf16_f32 v195, v78, v79\n\t"
"v_mfma_f32_16x16x32_bf16 v[200:203], a[28:31], a[128:131], v[200:203]\n\t"
"buffer_store_dwordx4 v[192:195], v244, %[ws], s69 offen sc1\n\t"
"ds_read_b64_tr_b16 a[0:1], v220 offset:0\n\t"
"ds_read_b64_tr_b16 a[2:3], v220 offset:2048\n\t"
"ds_read_b64_tr_b16 a[4:5], v223 offset:0\n\t"
"v_mfma_f32_16x16x32_bf16 v[212:215], a[28:31], a[132:135], v[212:215]\n\t"
"ds_read_b64_tr_b16 a[6:7], v223 offset:2048\n\t"
"v_mfma_f32_16x16x32_bf16 v[168:171], a[32:35], a[136:139], v[168:171]\n\t"
"s_add_u32 s69, s62, 12288\n\t"
"ds_read_b64_tr_b16 a[8:9], v226 offset:0\n\t"
"v_mfma_f32_16x16x32_bf16 v[172:175], a[32:35], a[140:143], v[172:175]\n\t"
"s_mul_i32 s68, s95, 442368\n\t"
"ds_read_b64_tr_b16 a[10:11], v226 offset:2048\n\t"
"v_mfma_f32_16x16x32_bf16 v[176:179], a[36:39], a[136:139], v[176:179]\n\t"
"s_add_u32 s68, s68, s55\n\t"
"ds_read_b64_tr_b16 a[12:13], v229 offset:0\n\t"
"v_cvt_pk_bf16_f32 v196, v84, v85\n\t"
"v_mfma_f32_16x16x32_bf16 v[180:183], a[36:39], a[140:143], v[180:183]\n\t"
"ds_read_b64_tr_b16 a[14:15], v229 offset:2048\n\t"
"v_mfma_f32_16x16x32_bf16 v[184:187], a[40:43], a[136:139], v[184:187]\n\t"
"s_add_u32 s68, s68, s51\n\t"
"ds_read_b64_tr_b16 a[16:17], v232 offset:0\n\t"
"v_mfma_f32_16x16x32_bf16 v[188:191], a[40:43], a[140:143], v[188:191]\n\t"
"s_add_u32 m0, s63, 49152\n\t"
"ds_read_b64_tr_b16 a[18:19], v232 offset:2048\n\t"
"v_cvt_pk_bf16_f32 v197, v86, v87\n\t"
"v_mfma_f32_16x16x32_bf16 v[200:203], a[44:47], a[136:139], v[200:203]\n\t"
"s_waitcnt lgkmcnt(6)\n\t"
"ds_read_b64_tr_b16 a[20:21], v235 offset:0\n\t"
"v_mfma_f32_16x16x32_bf16 v[212:215], a[44:47], a[140:143], v[212:215]\n\t"
"ds_read_b64_tr_b16 a[22:23], v235 offset:2048\n\t"
"v_mfma_f32_16x16x32_bf16 v[168:171], a[0:3], a[192:195], v[168:171]\n\t"
"buffer_load_dwordx4 v206, %[w2], s68 offen offset:0 lds\n\t"
"v_mfma_f32_16x16x32_bf16 v[172:175], a[0:3], a[196:199], v[172:175]\n\t"
"ds_read_b64_tr_b16 a[24:25], v238 offset:0\n\t"
"v_cvt_pk_bf16_f32 v198, v92, v93\n\t"
"v_mfma_f32_16x16x32_bf16 v[176:179], a[4:7], a[192:195], v[176:179]\n\t"
"s_waitcnt lgkmcnt(5)\n\t"
"ds_read_b64_tr_b16 a[26:27], v238 offset:2048\n\t"
"v_cvt_pk_bf16_f32 v199, v94, v95\n\t"
"v_mfma_f32_16x16x32_bf16 v[180:183], a[4:7], a[196:199], v[180:183]\n\t"
"ds_read_b64_tr_b16 a[28:29], v241 offset:0\n\t"
"ds_read_b64_tr_b16 a[30:31], v241 offset:2048\n\t"
"v_mfma_f32_16x16x32_bf16 v[184:187], a[8:11], a[192:195], v[184:187]\n\t"
"buffer_load_dwordx4 v207, %[w2], s68 offen offset:1024 lds\n\t"
"v_mfma_f32_16x16x32_bf16 v[188:191], a[8:11], a[196:199], v[188:191]\n\t"
"buffer_store_dwordx4 v[196:199], v244, %[ws], s69 offen sc1\n\t"
"ds_read_b64_tr_b16 a[32:33], v220 offset:8192\n\t"
"ds_read_b64_tr_b16 a[34:35], v220 offset:10240\n\t"
"ds_read_b64_tr_b16 a[36:37], v223 offset:8192\n\t"
"v_mfma_f32_16x16x32_bf16 v[200:203], a[12:15], a[192:195], v[200:203]\n\t"
"buffer_load_dwordx4 v208, %[w2], s68 offen offset:2048 lds\n\t"
"v_mfma_f32_16x16x32_bf16 v[212:215], a[12:15], a[196:199], v[212:215]\n\t"
"s_waitcnt lgkmcnt(7)\n\t"
"ds_read_b64_tr_b16 a[38:39], v223 offset:10240\n\t"
"v_mfma_f32_16x16x32_bf16 v[168:171], a[16:19], a[200:203], v[168:171]\n\t"
"ds_read_b64_tr_b16 a[40:41], v226 offset:8192\n\t"
"v_mfma_f32_16x16x32_bf16 v[172:175], a[16:19], a[204:207], v[172:175]\n\t"
"ds_read_b64_tr_b16 a[42:43], v226 offset:10240\n\t"
"v_mfma_f32_16x16x32_bf16 v[176:179], a[20:23], a[200:203], v[176:179]\n\t"
"s_waitcnt lgkmcnt(6)\n\t"
"ds_read_b64_tr_b16 a[44:45], v229 offset:8192\n\t"
"v_mfma_f32_16x16x32_bf16 v[180:183], a[20:23], a[204:207], v[180:183]\n\t"
"ds_read_b64_tr_b16 a[46:47], v229 offset:10240\n\t"
"v_mfma_f32_16x16x32_bf16 v[184:187], a[24:27], a[200:203], v[184:187]\n\t"
"ds_read_b64_tr_b16 a[0:1], v232 offset:8192\n\t"
"v_mfma_f32_16x16x32_bf16 v[188:191], a[24:27], a[204:207], v[188:191]\n\t"
"ds_read_b64_tr_b16 a[2:3], v232 offset:10240\n\t"
"v_mfma_f32_16x16x32_bf16 v[200:203], a[28:31], a[200:203], v[200:203]\n\t"
"ds_read_b64_tr_b16 a[4:5], v235 offset:8192\n\t"
"v_mfma_f32_16x16x32_bf16 v[212:215], a[28:31], a[204:207], v[212:215]\n\t"
"s_waitcnt lgkmcnt(7)\n\t"
"ds_read_b64_tr_b16 a[6:7], v235 offset:10240\n\t"
"v_mfma_f32_16x16x32_bf16 v[168:171], a[32:35], a[208:211], v[168:171]\n\t"
"ds_read_b64_tr_b16 a[8:9], v238 offset:8192\n\t"
"v_mfma_f32_16x16x32_bf16 v[172:175], a[32:35], a[212:215], v[172:175]\n\t"
"ds_read_b64_tr_b16 a[10:11], v238 offset:10240\n\t"
"v_mfma_f32_16x16x32_bf16 v[176:179], a[36:39], a[208:211], v[176:179]\n\t"
"ds_read_b64_tr_b16 a[12:13], v241 offset:8192\n\t"
"v_mfma_f32_16x16x32_bf16 v[180:183], a[36:39], a[212:215], v[180:183]\n\t"
"s_waitcnt lgkmcnt(7)\n\t"
"ds_read_b64_tr_b16 a[14:15], v241 offset:10240\n\t"
"v_mfma_f32_16x16x32_bf16 v[184:187], a[40:43], a[208:211], v[184:187]\n\t"
"buffer_load_dwordx4 v209, %[w2], s68 offen offset:3072 lds\n\t"
"v_mfma_f32_16x16x32_bf16 v[188:191], a[40:43], a[212:215], v[188:191]\n\t"
"ds_read_b64_tr_b16 a[16:17], v220 offset:16384\n\t"
"v_mfma_f32_16x16x32_bf16 v[200:203], a[44:47], a[208:211], v[200:203]\n\t"
"ds_read_b64_tr_b16 a[18:19], v220 offset:18432\n\t"
"v_mfma_f32_16x16x32_bf16 v[212:215], a[44:47], a[212:215], v[212:215]\n\t"
"s_waitcnt lgkmcnt(6)\n\t"
"ds_read_b64_tr_b16 a[20:21], v223 offset:16384\n\t"
"ds_read_b64_tr_b16 a[22:23], v223 offset:18432\n\t"
"v_mfma_f32_16x16x32_bf16 v[168:171], a[0:3], a[216:219], v[168:171]\n\t"
"ds_read_b64_tr_b16 a[24:25], v226 offset:16384\n\t"
"v_mfma_f32_16x16x32_bf16 v[172:175], a[0:3], a[220:223], v[172:175]\n\t"
"ds_read_b64_tr_b16 a[26:27], v226 offset:18432\n\t"
"v_mfma_f32_16x16x32_bf16 v[176:179], a[4:7], a[216:219], v[176:179]\n\t"
"s_add_u32 m0, s63, 53248\n\t"
"ds_read_b64_tr_b16 a[28:29], v229 offset:16384\n\t"
"v_mfma_f32_16x16x32_bf16 v[180:183], a[4:7], a[220:223], v[180:183]\n\t"
"s_waitcnt lgkmcnt(7)\n\t"
"ds_read_b64_tr_b16 a[30:31], v229 offset:18432\n\t"
"v_mfma_f32_16x16x32_bf16 v[184:187], a[8:11], a[216:219], v[184:187]\n\t"
"buffer_load_dwordx4 v210, %[w2], s68 offen offset:0 lds\n\t"
"v_mfma_f32_16x16x32_bf16 v[188:191], a[8:11], a[220:223], v[188:191]\n\t"
"ds_read_b64_tr_b16 a[32:33], v232 offset:16384\n\t"
"v_mfma_f32_16x16x32_bf16 v[200:203], a[12:15], a[216:219], v[200:203]\n\t"
"ds_read_b64_tr_b16 a[34:35], v232 offset:18432\n\t"
"v_mfma_f32_16x16x32_bf16 v[212:215], a[12:15], a[220:223], v[212:215]\n\t"
"s_waitcnt lgkmcnt(6)\n\t"
"ds_read_b64_tr_b16 a[36:37], v235 offset:16384\n\t"
"ds_read_b64_tr_b16 a[38:39], v235 offset:18432\n\t"
"v_mfma_f32_16x16x32_bf16 v[168:171], a[16:19], a[224:227], v[168:171]\n\t"
"ds_read_b64_tr_b16 a[40:41], v238 offset:16384\n\t"
"v_mfma_f32_16x16x32_bf16 v[172:175], a[16:19], a[228:231], v[172:175]\n\t"
"ds_read_b64_tr_b16 a[42:43], v238 offset:18432\n\t"
"v_mfma_f32_16x16x32_bf16 v[176:179], a[20:23], a[224:227], v[176:179]\n\t"
"ds_read_b64_tr_b16 a[44:45], v241 offset:16384\n\t"
"v_mfma_f32_16x16x32_bf16 v[180:183], a[20:23], a[228:231], v[180:183]\n\t"
"s_waitcnt lgkmcnt(7)\n\t"
"ds_read_b64_tr_b16 a[46:47], v241 offset:18432\n\t"
"v_mfma_f32_16x16x32_bf16 v[184:187], a[24:27], a[224:227], v[184:187]\n\t"
"buffer_load_dwordx4 v211, %[w2], s68 offen offset:1024 lds\n\t"
"v_mfma_f32_16x16x32_bf16 v[188:191], a[24:27], a[228:231], v[188:191]\n\t"
"ds_read_b64_tr_b16 a[0:1], v220 offset:24576\n\t"
"v_mfma_f32_16x16x32_bf16 v[200:203], a[28:31], a[224:227], v[200:203]\n\t"
"ds_read_b64_tr_b16 a[2:3], v220 offset:26624\n\t"
"v_mfma_f32_16x16x32_bf16 v[212:215], a[28:31], a[228:231], v[212:215]\n\t"
"s_waitcnt lgkmcnt(6)\n\t"
"ds_read_b64_tr_b16 a[4:5], v223 offset:24576\n\t"
"ds_read_b64_tr_b16 a[6:7], v223 offset:26624\n\t"
"v_mfma_f32_16x16x32_bf16 v[168:171], a[32:35], a[232:235], v[168:171]\n\t"
"ds_read_b64_tr_b16 a[8:9], v226 offset:24576\n\t"
"v_mfma_f32_16x16x32_bf16 v[172:175], a[32:35], a[236:239], v[172:175]\n\t"
"s_add_u32 s68, s75, 0\n\t"
"ds_read_b64_tr_b16 a[10:11], v226 offset:26624\n\t"
"v_mfma_f32_16x16x32_bf16 v[176:179], a[36:39], a[232:235], v[176:179]\n\t"
"s_add_u32 m0, s41, 73728\n\t"
"ds_read_b64_tr_b16 a[12:13], v229 offset:24576\n\t"
"v_mfma_f32_16x16x32_bf16 v[180:183], a[36:39], a[236:239], v[180:183]\n\t"
"s_waitcnt lgkmcnt(7)\n\t"
"ds_read_b64_tr_b16 a[14:15], v229 offset:26624\n\t"
"v_mfma_f32_16x16x32_bf16 v[184:187], a[40:43], a[232:235], v[184:187]\n\t"
"buffer_load_dwordx4 v244, %[ws], s68 offen sc1 lds\n\t"
"v_mfma_f32_16x16x32_bf16 v[188:191], a[40:43], a[236:239], v[188:191]\n\t"
"s_add_u32 s68, s75, 4096\n\t"
"ds_read_b64_tr_b16 a[16:17], v232 offset:24576\n\t"
"v_mfma_f32_16x16x32_bf16 v[200:203], a[44:47], a[232:235], v[200:203]\n\t"
"s_add_u32 m0, s41, 77824\n\t"
"ds_read_b64_tr_b16 a[18:19], v232 offset:26624\n\t"
"v_mfma_f32_16x16x32_bf16 v[212:215], a[44:47], a[236:239], v[212:215]\n\t"
"s_waitcnt lgkmcnt(6)\n\t"
"ds_read_b64_tr_b16 a[20:21], v235 offset:24576\n\t"
"ds_read_b64_tr_b16 a[22:23], v235 offset:26624\n\t"
"v_mfma_f32_16x16x32_bf16 v[32:35], a[0:3], a[48:51], v[32:35]\n\t"
"buffer_load_dwordx4 v244, %[ws], s68 offen sc1 lds\n\t"
"v_mfma_f32_16x16x32_bf16 v[36:39], a[0:3], a[52:55], v[36:39]\n\t"
"ds_read_b64_tr_b16 a[24:25], v238 offset:24576\n\t"
"v_mfma_f32_16x16x32_bf16 v[40:43], a[4:7], a[48:51], v[40:43]\n\t"
"s_waitcnt lgkmcnt(5)\n\t"
"ds_read_b64_tr_b16 a[26:27], v238 offset:26624\n\t"
"v_mfma_f32_16x16x32_bf16 v[44:47], a[4:7], a[52:55], v[44:47]\n\t"
"s_mul_i32 s62, s54, 512\n\t"
"ds_read_b64_tr_b16 a[28:29], v241 offset:24576\n\t"
"ds_read_b64_tr_b16 a[30:31], v241 offset:26624\n\t"
"v_mfma_f32_16x16x32_bf16 v[48:51], a[8:11], a[48:51], v[48:51]\n\t"
"s_add_u32 s62, s62, s52\n\t"
"ds_read_b64_tr_b16 a[32:33], v220 offset:32768\n\t"
"v_cvt_pk_bf16_f32 v160, v168, v169\n\t"
"v_mfma_f32_16x16x32_bf16 v[52:55], a[8:11], a[52:55], v[52:55]\n\t"
"s_add_u32 s68, s75, 8192\n\t"
"ds_read_b64_tr_b16 a[34:35], v220 offset:34816\n\t"
"v_cvt_pk_bf16_f32 v161, v170, v171\n\t"
"v_mfma_f32_16x16x32_bf16 v[56:59], a[12:15], a[48:51], v[56:59]\n\t"
"s_add_u32 m0, s41, 81920\n\t"
"ds_read_b64_tr_b16 a[36:37], v223 offset:32768\n\t"
"v_cvt_pk_bf16_f32 v162, v176, v177\n\t"
"v_mfma_f32_16x16x32_bf16 v[60:63], a[12:15], a[52:55], v[60:63]\n\t"
"s_waitcnt lgkmcnt(7)\n\t"
"ds_read_b64_tr_b16 a[38:39], v223 offset:34816\n\t"
"v_mfma_f32_16x16x32_bf16 v[32:35], a[16:19], a[56:59], v[32:35]\n\t"
"buffer_load_dwordx4 v244, %[ws], s68 offen sc1 lds\n\t"
"v_mfma_f32_16x16x32_bf16 v[36:39], a[16:19], a[60:63], v[36:39]\n\t"
"ds_read_b64_tr_b16 a[40:41], v226 offset:32768\n\t"
"v_cvt_pk_bf16_f32 v163, v178, v179\n\t"
"v_mfma_f32_16x16x32_bf16 v[40:43], a[20:23], a[56:59], v[40:43]\n\t"
"buffer_store_dwordx4 v[160:163], v244, %[ws], s62 offen sc1\n\t"
"ds_read_b64_tr_b16 a[42:43], v226 offset:34816\n\t"
"ds_read_b64_tr_b16 a[44:45], v229 offset:32768\n\t"
"ds_read_b64_tr_b16 a[46:47], v229 offset:34816\n\t"
"v_mfma_f32_16x16x32_bf16 v[44:47], a[20:23], a[60:63], v[44:47]\n\t"
"s_waitcnt lgkmcnt(8)\n\t"
"ds_read_b64_tr_b16 a[0:1], v232 offset:32768\n\t"
"v_mfma_f32_16x16x32_bf16 v[48:51], a[24:27], a[56:59], v[48:51]\n\t"
"s_add_u32 s68, s75, 12288\n\t"
"s_waitcnt vmcnt(12)\n\t"
"v_cmp_eq_u64 vcc, v[204:205], v[254:255]\n\t"
"s_cmp_eq_u64 vcc, -1\n\t"
"s_cselect_b32 s98, 0, 2130706432\n\t"
"s_cselect_b32 s43, 1, 0\n\t"
"v_add_u32 v30, s98, v244\n\t"
"v_mfma_f32_16x16x32_bf16 v[52:55], a[24:27], a[60:63], v[52:55]\n\t"
"s_add_u32 m0, s41, 86016\n\t"
"ds_read_b64_tr_b16 a[2:3], v232 offset:34816\n\t"
"buffer_load_dwordx4 v[96:99], v30, %[ws], s40 offen offset:0 sc1\n\t"
"v_mfma_f32_16x16x32_bf16 v[56:59], a[28:31], a[56:59], v[56:59]\n\t"
"s_waitcnt lgkmcnt(6)\n\t"
"ds_read_b64_tr_b16 a[4:5], v235 offset:32768\n\t"
"v_mfma_f32_16x16x32_bf16 v[60:63], a[28:31], a[60:63], v[60:63]\n\t"
"s_add_u32 s69, s62, 4096\n\t"
"ds_read_b64_tr_b16 a[6:7], v235 offset:34816\n\t"
"v_cvt_pk_bf16_f32 v164, v184, v185\n\t"
"v_mfma_f32_16x16x32_bf16 v[32:35], a[32:35], a[64:67], v[32:35]\n\t"
"buffer_load_dwordx4 v244, %[ws], s68 offen sc1 lds\n\t"
"v_mfma_f32_16x16x32_bf16 v[36:39], a[32:35], a[68:71], v[36:39]\n\t"
"s_add_u32 s68, s75, 16384\n\t"
"ds_read_b64_tr_b16 a[8:9], v238 offset:32768\n\t"
"v_cvt_pk_bf16_f32 v165, v186, v187\n\t"
"v_mfma_f32_16x16x32_bf16 v[40:43], a[36:39], a[64:67], v[40:43]\n\t"
"s_add_u32 m0, s41, 90112\n\t"
"ds_read_b64_tr_b16 a[10:11], v238 offset:34816\n\t"
"buffer_load_dwordx4 v[100:103], v30, %[ws], s40 offen offset:1024 sc1\n\t"
"v_mfma_f32_16x16x32_bf16 v[44:47], a[36:39], a[68:71], v[44:47]\n\t"
"s_waitcnt lgkmcnt(6)\n\t"
"ds_read_b64_tr_b16 a[12:13], v241 offset:32768\n\t"
"ds_read_b64_tr_b16 a[14:15], v241 offset:34816\n\t"
"v_mfma_f32_16x16x32_bf16 v[48:51], a[40:43], a[64:67], v[48:51]\n\t"
"buffer_load_dwordx4 v244, %[ws], s68 offen sc1 lds\n\t"
"v_mfma_f32_16x16x32_bf16 v[52:55], a[40:43], a[68:71], v[52:55]\n\t"
"ds_read_b64_tr_b16 a[16:17], v220 offset:40960\n\t"
"v_cvt_pk_bf16_f32 v166, v200, v201\n\t"
"v_mfma_f32_16x16x32_bf16 v[56:59], a[44:47], a[64:67], v[56:59]\n\t"
"s_waitcnt lgkmcnt(5)\n\t"
"ds_read_b64_tr_b16 a[18:19], v220 offset:43008\n\t"
"v_cvt_pk_bf16_f32 v167, v202, v203\n\t"
"v_mfma_f32_16x16x32_bf16 v[60:63], a[44:47], a[68:71], v[60:63]\n\t"
"ds_read_b64_tr_b16 a[20:21], v223 offset:40960\n\t"
"ds_read_b64_tr_b16 a[22:23], v223 offset:43008\n\t"
"v_mfma_f32_16x16x32_bf16 v[32:35], a[0:3], a[72:75], v[32:35]\n\t"
"buffer_store_dwordx4 v[164:167], v244, %[ws], s69 offen sc1\n\t"
"ds_read_b64_tr_b16 a[24:25], v226 offset:40960\n\t"
"ds_read_b64_tr_b16 a[26:27], v226 offset:43008\n\t"
"ds_read_b64_tr_b16 a[28:29], v229 offset:40960\n\t"
"v_mfma_f32_16x16x32_bf16 v[36:39], a[0:3], a[76:79], v[36:39]\n\t"
"buffer_load_dwordx4 v[104:107], v30, %[ws], s40 offen offset:2048 sc1\n\t"
"v_mfma_f32_16x16x32_bf16 v[40:43], a[4:7], a[72:75], v[40:43]\n\t"
"s_add_u32 s68, s75, 20480\n\t"
"ds_read_b64_tr_b16 a[30:31], v229 offset:43008\n\t"
"v_mfma_f32_16x16x32_bf16 v[44:47], a[4:7], a[76:79], v[44:47]\n\t"
"s_waitcnt lgkmcnt(8)\n\t"
"ds_read_b64_tr_b16 a[32:33], v232 offset:40960\n\t"
"v_mfma_f32_16x16x32_bf16 v[48:51], a[8:11], a[72:75], v[48:51]\n\t"
"s_add_u32 s69, s62, 8192\n\t"
"ds_read_b64_tr_b16 a[34:35], v232 offset:43008\n\t"
"v_mfma_f32_16x16x32_bf16 v[52:55], a[8:11], a[76:79], v[52:55]\n\t"
"s_add_u32 m0, s41, 94208\n\t"
"ds_read_b64_tr_b16 a[36:37], v235 offset:40960\n\t"
"v_cvt_pk_bf16_f32 v192, v172, v173\n\t"
"v_mfma_f32_16x16x32_bf16 v[56:59], a[12:15], a[72:75], v[56:59]\n\t"
"s_waitcnt lgkmcnt(7)\n\t"
"ds_read_b64_tr_b16 a[38:39], v235 offset:43008\n\t"
"v_mfma_f32_16x16x32_bf16 v[60:63], a[12:15], a[76:79], v[60:63]\n\t"
"ds_read_b64_tr_b16 a[40:41], v238 offset:40960\n\t"
"ds_read_b64_tr_b16 a[42:43], v238 offset:43008\n\t"
"ds_read_b64_tr_b16 a[44:45], v241 offset:40960\n\t"
"ds_read_b64_tr_b16 a[46:47], v241 offset:43008\n\t"
"v_mfma_f32_16x16x32_bf16 v[32:35], a[16:19], a[80:83], v[32:35]\n\t"
"buffer_load_dwordx4 v244, %[ws], s68 offen sc1 lds\n\t"
"v_mfma_f32_16x16x32_bf16 v[36:39], a[16:19], a[84:87], v[36:39]\n\t"
"buffer_load_dwordx4 v[108:111], v30, %[ws], s40 offen offset:3072 sc1\n\t"
"v_mfma_f32_16x16x32_bf16 v[40:43], a[20:23], a[80:83], v[40:43]\n\t"
"v_mfma_f32_16x16x32_bf16 v[44:47], a[20:23], a[84:87], v[44:47]\n\t"
"s_waitcnt lgkmcnt(8)\n\t"
"v_cvt_pk_bf16_f32 v193, v174, v175\n\t"
"v_mfma_f32_16x16x32_bf16 v[48:51], a[24:27], a[80:83], v[48:51]\n\t"
"s_waitcnt vmcnt(16)\n\t"
"s_waitcnt lgkmcnt(0)\n\t"
"v_cvt_pk_bf16_f32 v194, v180, v181\n\t"
"v_mfma_f32_16x16x32_bf16 v[52:55], a[24:27], a[84:87], v[52:55]\n\t"
"s_barrier\n\t"
"s_add_u32 s42, s40, 4096\n\t"
"v_cvt_pk_bf16_f32 v195, v182, v183\n\t"
"v_mfma_f32_16x16x32_bf16 v[56:59], a[28:31], a[80:83], v[56:59]\n\t"
"buffer_store_dwordx2 v[254:255], v252, %[ws], s79 offen offset:896 sc1\n\t"
"ds_read_b64_tr_b16 a[0:1], v218 offset:0\n\t"
"buffer_load_dwordx4 v[112:115], v30, %[ws], s42 offen offset:0 sc1\n\t"
"v_mfma_f32_16x16x32_bf16 v[60:63], a[28:31], a[84:87], v[60:63]\n\t"
"buffer_store_dwordx4 v[192:195], v244, %[ws], s69 offen sc1\n\t"
"ds_read_b64_tr_b16 a[2:3], v218 offset:2048\n\t"
"ds_read_b64_tr_b16 a[4:5], v221 offset:0\n\t"
"ds_read_b64_tr_b16 a[6:7], v221 offset:2048\n\t"
"v_mfma_f32_16x16x32_bf16 v[32:35], a[32:35], a[88:91], v[32:35]\n\t"
"s_add_u32 s69, s62, 12288\n\t"
"ds_read_b64_tr_b16 a[8:9], v224 offset:0\n\t"
"v_mfma_f32_16x16x32_bf16 v[36:39], a[32:35], a[92:95], v[36:39]\n\t"
"s_add_u32 s68, s75, 24576\n\t"
"ds_read_b64_tr_b16 a[10:11], v224 offset:2048\n\t"
"v_mfma_f32_16x16x32_bf16 v[40:43], a[36:39], a[88:91], v[40:43]\n\t"
"ds_read_b64_tr_b16 a[12:13], v227 offset:0\n\t"
"v_cvt_pk_bf16_f32 v196, v188, v189\n\t"
"v_mfma_f32_16x16x32_bf16 v[44:47], a[36:39], a[92:95], v[44:47]\n\t"
"s_add_u32 m0, s41, 98304\n\t"
"ds_read_b64_tr_b16 a[14:15], v227 offset:2048\n\t"
"v_mfma_f32_16x16x32_bf16 v[48:51], a[40:43], a[88:91], v[48:51]\n\t"
"buffer_load_dwordx4 v244, %[ws], s68 offen sc1 lds\n\t"
"v_mfma_f32_16x16x32_bf16 v[52:55], a[40:43], a[92:95], v[52:55]\n\t"
"s_add_u32 s68, s75, 28672\n\t"
"ds_read_b64_tr_b16 a[16:17], v230 offset:0\n\t"
"v_cvt_pk_bf16_f32 v197, v190, v191\n\t"
"v_mfma_f32_16x16x32_bf16 v[56:59], a[44:47], a[88:91], v[56:59]\n\t"
"s_add_u32 m0, s41, 102400\n\t"
"ds_read_b64_tr_b16 a[18:19], v230 offset:2048\n\t"
"v_mfma_f32_16x16x32_bf16 v[60:63], a[44:47], a[92:95], v[60:63]\n\t"
"s_waitcnt lgkmcnt(6)\n\t"
"ds_read_b64_tr_b16 a[20:21], v233 offset:0\n\t"
"ds_read_b64_tr_b16 a[22:23], v233 offset:2048\n\t"
"v_cvt_pk_bf16_f32 v198, v212, v213\n\t"
"v_mfma_f32_16x16x32_bf16 v[32:35], a[0:3], a[96:99], v[32:35]\n\t"
"buffer_load_dwordx4 v[116:119], v30, %[ws], s42 offen offset:1024 sc1\n\t"
"v_mfma_f32_16x16x32_bf16 v[36:39], a[0:3], a[100:103], v[36:39]\n\t"
"buffer_load_dwordx4 v244, %[ws], s68 offen sc1 lds\n\t"
"v_mfma_f32_16x16x32_bf16 v[40:43], a[4:7], a[96:99], v[40:43]\n\t"
"s_waitcnt lgkmcnt(4)\n\t"
"ds_read_b64_tr_b16 a[24:25], v236 offset:0\n\t"
"v_cvt_pk_bf16_f32 v199, v214, v215\n\t"
"v_mfma_f32_16x16x32_bf16 v[44:47], a[4:7], a[100:103], v[44:47]\n\t"
"ds_read_b64_tr_b16 a[26:27], v236 offset:2048\n\t"
"ds_read_b64_tr_b16 a[28:29], v239 offset:0\n\t"
"ds_read_b64_tr_b16 a[30:31], v239 offset:2048\n\t"
"v_mfma_f32_16x16x32_bf16 v[48:51], a[8:11], a[96:99], v[48:51]\n\t"
"buffer_store_dwordx4 v[196:199], v244, %[ws], s69 offen sc1\n\t"
"ds_read_b64_tr_b16 a[32:33], v218 offset:8192\n\t"
"ds_read_b64_tr_b16 a[34:35], v218 offset:10240\n\t"
"ds_read_b64_tr_b16 a[36:37], v221 offset:8192\n\t"
"v_mfma_f32_16x16x32_bf16 v[52:55], a[8:11], a[100:103], v[52:55]\n\t"
"buffer_load_dwordx4 v[120:123], v30, %[ws], s42 offen offset:2048 sc1\n\t"
"v_mfma_f32_16x16x32_bf16 v[56:59], a[12:15], a[96:99], v[56:59]\n\t"
"buffer_load_dwordx4 v[124:127], v30, %[ws], s42 offen offset:3072 sc1\n\t"
"v_mfma_f32_16x16x32_bf16 v[60:63], a[12:15], a[100:103], v[60:63]\n\t"
"s_waitcnt lgkmcnt(7)\n\t"
"ds_read_b64_tr_b16 a[38:39], v221 offset:10240\n\t"
"v_mfma_f32_16x16x32_bf16 v[32:35], a[16:19], a[104:107], v[32:35]\n\t"
"s_add_u32 s68, s75, 32768\n\t"
"ds_read_b64_tr_b16 a[40:41], v224 offset:8192\n\t"
"v_mfma_f32_16x16x32_bf16 v[36:39], a[16:19], a[108:111], v[36:39]\n\t"
"s_add_u32 m0, s41, 106496\n\t"
"ds_read_b64_tr_b16 a[42:43], v224 offset:10240\n\t"
"v_mfma_f32_16x16x32_bf16 v[40:43], a[20:23], a[104:107], v[40:43]\n\t"
"s_add_u32 s42, s40, 8192\n\t"
"ds_read_b64_tr_b16 a[44:45], v227 offset:8192\n\t"
"v_mfma_f32_16x16x32_bf16 v[44:47], a[20:23], a[108:111], v[44:47]\n\t"
"s_waitcnt lgkmcnt(7)\n\t"
"ds_read_b64_tr_b16 a[46:47], v227 offset:10240\n\t"
"v_mfma_f32_16x16x32_bf16 v[48:51], a[24:27], a[104:107], v[48:51]\n\t"
"buffer_load_dwordx4 v244, %[ws], s68 offen sc1 lds\n\t"
"v_mfma_f32_16x16x32_bf16 v[52:55], a[24:27], a[108:111], v[52:55]\n\t"
"s_add_u32 s68, s75, 36864\n\t"
"ds_read_b64_tr_b16 a[0:1], v230 offset:8192\n\t"
"v_mfma_f32_16x16x32_bf16 v[56:59], a[28:31], a[104:107], v[56:59]\n\t"
"s_add_u32 m0, s41, 110592\n\t"
"ds_read_b64_tr_b16 a[2:3], v230 offset:10240\n\t"
"v_mfma_f32_16x16x32_bf16 v[60:63], a[28:31], a[108:111], v[60:63]\n\t"
"s_waitcnt lgkmcnt(6)\n\t"
"ds_read_b64_tr_b16 a[4:5], v233 offset:8192\n\t"
"ds_read_b64_tr_b16 a[6:7], v233 offset:10240\n\t"
"v_mfma_f32_16x16x32_bf16 v[32:35], a[32:35], a[112:115], v[32:35]\n\t"
"buffer_load_dwordx4 v244, %[ws], s68 offen sc1 lds\n\t"
"v_mfma_f32_16x16x32_bf16 v[36:39], a[32:35], a[116:119], v[36:39]\n\t"
"buffer_load_dwordx4 v[128:131], v30, %[ws], s42 offen offset:0 sc1\n\t"
"v_mfma_f32_16x16x32_bf16 v[40:43], a[36:39], a[112:115], v[40:43]\n\t"
"s_waitcnt lgkmcnt(4)\n\t"
"ds_read_b64_tr_b16 a[8:9], v236 offset:8192\n\t"
"v_mfma_f32_16x16x32_bf16 v[44:47], a[36:39], a[116:119], v[44:47]\n\t"
"ds_read_b64_tr_b16 a[10:11], v236 offset:10240\n\t"
"ds_read_b64_tr_b16 a[12:13], v239 offset:8192\n\t"
"ds_read_b64_tr_b16 a[14:15], v239 offset:10240\n\t"
"v_mfma_f32_16x16x32_bf16 v[48:51], a[40:43], a[112:115], v[48:51]\n\t"
"s_add_u32 s68, s76, 0\n\t"
"ds_read_b64_tr_b16 a[16:17], v218 offset:16384\n\t"
"v_mfma_f32_16x16x32_bf16 v[52:55], a[40:43], a[116:119], v[52:55]\n\t"
"s_add_u32 m0, s41, 114688\n\t"
"ds_read_b64_tr_b16 a[18:19], v218 offset:18432\n\t"
"v_mfma_f32_16x16x32_bf16 v[56:59], a[44:47], a[112:115], v[56:59]\n\t"
"s_waitcnt lgkmcnt(6)\n\t"
"ds_read_b64_tr_b16 a[20:21], v221 offset:16384\n\t"
"v_mfma_f32_16x16x32_bf16 v[60:63], a[44:47], a[116:119], v[60:63]\n\t"
"ds_read_b64_tr_b16 a[22:23], v221 offset:18432\n\t"
"v_mfma_f32_16x16x32_bf16 v[32:35], a[0:3], a[120:123], v[32:35]\n\t"
"buffer_load_dwordx4 v[132:135], v30, %[ws], s42 offen offset:1024 sc1\n\t"
"v_mfma_f32_16x16x32_bf16 v[36:39], a[0:3], a[124:127], v[36:39]\n\t"
"buffer_load_dwordx4 v244, %[ws], s68 offen sc1 lds\n\t"
"v_mfma_f32_16x16x32_bf16 v[40:43], a[4:7], a[120:123], v[40:43]\n\t"
"s_waitcnt lgkmcnt(4)\n\t"
"ds_read_b64_tr_b16 a[24:25], v224 offset:16384\n\t"
"v_mfma_f32_16x16x32_bf16 v[44:47], a[4:7], a[124:127], v[44:47]\n\t"
"s_add_u32 s68, s76, 4096\n\t"
"ds_read_b64_tr_b16 a[26:27], v224 offset:18432\n\t"
"ds_read_b64_tr_b16 a[28:29], v227 offset:16384\n\t"
"ds_read_b64_tr_b16 a[30:31], v227 offset:18432\n\t"
"v_mfma_f32_16x16x32_bf16 v[48:51], a[8:11], a[120:123], v[48:51]\n\t"
"buffer_load_dwordx4 v[136:139], v30, %[ws], s42 offen offset:2048 sc1\n\t"
"v_mfma_f32_16x16x32_bf16 v[52:55], a[8:11], a[124:127], v[52:55]\n\t"
"s_add_u32 m0, s41, 118784\n\t"
"ds_read_b64_tr_b16 a[32:33], v230 offset:16384\n\t"
"v_mfma_f32_16x16x32_bf16 v[56:59], a[12:15], a[120:123], v[56:59]\n\t"
"s_waitcnt lgkmcnt(5)\n\t"
"ds_read_b64_tr_b16 a[34:35], v230 offset:18432\n\t"
"v_mfma_f32_16x16x32_bf16 v[60:63], a[12:15], a[124:127], v[60:63]\n\t"
"ds_read_b64_tr_b16 a[36:37], v233 offset:16384\n\t"
"ds_read_b64_tr_b16 a[38:39], v233 offset:18432\n\t"
"v_mfma_f32_16x16x32_bf16 v[32:35], a[16:19], a[128:131], v[32:35]\n\t"
"buffer_load_dwordx4 v[140:143], v30, %[ws], s42 offen offset:3072 sc1\n\t"
"v_mfma_f32_16x16x32_bf16 v[36:39], a[16:19], a[132:135], v[36:39]\n\t"
"s_add_u32 s42, s40, 12288\n\t"
"ds_read_b64_tr_b16 a[40:41], v236 offset:16384\n\t"
"v_mfma_f32_16x16x32_bf16 v[40:43], a[20:23], a[128:131], v[40:43]\n\t"
"s_waitcnt lgkmcnt(5)\n\t"
"ds_read_b64_tr_b16 a[42:43], v236 offset:18432\n\t"
"v_mfma_f32_16x16x32_bf16 v[44:47], a[20:23], a[132:135], v[44:47]\n\t"
"ds_read_b64_tr_b16 a[44:45], v239 offset:16384\n\t"
"ds_read_b64_tr_b16 a[46:47], v239 offset:18432\n\t"
"v_mfma_f32_16x16x32_bf16 v[48:51], a[24:27], a[128:131], v[48:51]\n\t"
"buffer_load_dwordx4 v244, %[ws], s68 offen sc1 lds\n\t"
"v_mfma_f32_16x16x32_bf16 v[52:55], a[24:27], a[132:135], v[52:55]\n\t"
"s_add_u32 s68, s76, 8192\n\t"
"ds_read_b64_tr_b16 a[0:1], v218 offset:24576\n\t"
"v_mfma_f32_16x16x32_bf16 v[56:59], a[28:31], a[128:131], v[56:59]\n\t"
"s_waitcnt lgkmcnt(5)\n\t"
"ds_read_b64_tr_b16 a[2:3], v218 offset:26624\n\t"
"v_mfma_f32_16x16x32_bf16 v[60:63], a[28:31], a[132:135], v[60:63]\n\t"
"s_add_u32 m0, s41, 122880\n\t"
"ds_read_b64_tr_b16 a[4:5], v221 offset:24576\n\t"
"ds_read_b64_tr_b16 a[6:7], v221 offset:26624\n\t"
"v_mfma_f32_16x16x32_bf16 v[32:35], a[32:35], a[136:139], v[32:35]\n\t"
"buffer_load_dwordx4 v244, %[ws], s68 offen sc1 lds\n\t"
"v_mfma_f32_16x16x32_bf16 v[36:39], a[32:35], a[140:143], v[36:39]\n\t"
"ds_read_b64_tr_b16 a[8:9], v224 offset:24576\n\t"
"v_mfma_f32_16x16x32_bf16 v[40:43], a[36:39], a[136:139], v[40:43]\n\t"
"s_waitcnt lgkmcnt(5)\n\t"
"ds_read_b64_tr_b16 a[10:11], v224 offset:26624\n\t"
"v_mfma_f32_16x16x32_bf16 v[44:47], a[36:39], a[140:143], v[44:47]\n\t"
"s_add_u32 s68, s76, 12288\n\t"
"ds_read_b64_tr_b16 a[12:13], v227 offset:24576\n\t"
"ds_read_b64_tr_b16 a[14:15], v227 offset:26624\n\t"
"v_mfma_f32_16x16x32_bf16 v[48:51], a[40:43], a[136:139], v[48:51]\n\t"
"buffer_load_dwordx4 v[144:147], v30, %[ws], s42 offen offset:0 sc1\n\t"
"v_mfma_f32_16x16x32_bf16 v[52:55], a[40:43], a[140:143], v[52:55]\n\t"
"s_add_u32 m0, s41, 126976\n\t"
"ds_read_b64_tr_b16 a[16:17], v230 offset:24576\n\t"
"v_mfma_f32_16x16x32_bf16 v[56:59], a[44:47], a[136:139], v[56:59]\n\t"
"s_waitcnt lgkmcnt(5)\n\t"
"ds_read_b64_tr_b16 a[18:19], v230 offset:26624\n\t"
"v_mfma_f32_16x16x32_bf16 v[60:63], a[44:47], a[140:143], v[60:63]\n\t"
"ds_read_b64_tr_b16 a[20:21], v233 offset:24576\n\t"
"ds_read_b64_tr_b16 a[22:23], v233 offset:26624\n\t"
"v_mfma_f32_16x16x32_bf16 v[32:35], a[0:3], a[144:147], v[32:35]\n\t"
"buffer_load_dwordx4 v[148:151], v30, %[ws], s42 offen offset:1024 sc1\n\t"
"v_mfma_f32_16x16x32_bf16 v[36:39], a[0:3], a[148:151], v[36:39]\n\t"
"buffer_load_dwordx4 v244, %[ws], s68 offen sc1 lds\n\t"
"v_mfma_f32_16x16x32_bf16 v[40:43], a[4:7], a[144:147], v[40:43]\n\t"
"s_add_u32 s68, s76, 16384\n\t"
"ds_read_b64_tr_b16 a[24:25], v236 offset:24576\n\t"
"v_mfma_f32_16x16x32_bf16 v[44:47], a[4:7], a[148:151], v[44:47]\n\t"
"s_waitcnt lgkmcnt(5)\n\t"
"ds_read_b64_tr_b16 a[26:27], v236 offset:26624\n\t"
"ds_read_b64_tr_b16 a[28:29], v239 offset:24576\n\t"
"ds_read_b64_tr_b16 a[30:31], v239 offset:26624\n\t"
"v_mfma_f32_16x16x32_bf16 v[48:51], a[8:11], a[144:147], v[48:51]\n\t"
"buffer_load_dwordx4 v[152:155], v30, %[ws], s42 offen offset:2048 sc1\n\t"
"v_mfma_f32_16x16x32_bf16 v[52:55], a[8:11], a[148:151], v[52:55]\n\t"
"s_add_u32 m0, s41, 131072\n\t"
"ds_read_b64_tr_b16 a[32:33], v218 offset:32768\n\t"
"v_mfma_f32_16x16x32_bf16 v[56:59], a[12:15], a[144:147], v[56:59]\n\t"
"s_waitcnt lgkmcnt(5)\n\t"
"ds_read_b64_tr_b16 a[34:35], v218 offset:34816\n\t"
"v_mfma_f32_16x16x32_bf16 v[60:63], a[12:15], a[148:151], v[60:63]\n\t"
"ds_read_b64_tr_b16 a[36:37], v221 offset:32768\n\t"
"ds_read_b64_tr_b16 a[38:39], v221 offset:34816\n\t"
"v_mfma_f32_16x16x32_bf16 v[32:35], a[16:19], a[152:155], v[32:35]\n\t"
"buffer_load_dwordx4 v[156:159], v30, %[ws], s42 offen offset:3072 sc1\n\t"
"v_mfma_f32_16x16x32_bf16 v[36:39], a[16:19], a[156:159], v[36:39]\n\t"
"s_add_u32 s42, s40, 16384\n\t"
"ds_read_b64_tr_b16 a[40:41], v224 offset:32768\n\t"
"v_mfma_f32_16x16x32_bf16 v[40:43], a[20:23], a[152:155], v[40:43]\n\t"
"s_waitcnt lgkmcnt(5)\n\t"
"ds_read_b64_tr_b16 a[42:43], v224 offset:34816\n\t"
"v_mfma_f32_16x16x32_bf16 v[44:47], a[20:23], a[156:159], v[44:47]\n\t"
"ds_read_b64_tr_b16 a[44:45], v227 offset:32768\n\t"
"ds_read_b64_tr_b16 a[46:47], v227 offset:34816\n\t"
"v_mfma_f32_16x16x32_bf16 v[48:51], a[24:27], a[152:155], v[48:51]\n\t"
"buffer_load_dwordx4 v244, %[ws], s68 offen sc1 lds\n\t"
"v_mfma_f32_16x16x32_bf16 v[52:55], a[24:27], a[156:159], v[52:55]\n\t"
"s_add_u32 s68, s76, 20480\n\t"
"ds_read_b64_tr_b16 a[0:1], v230 offset:32768\n\t"
"v_mfma_f32_16x16x32_bf16 v[56:59], a[28:31], a[152:155], v[56:59]\n\t"
"s_add_u32 m0, s41, 135168\n\t"
"ds_read_b64_tr_b16 a[2:3], v230 offset:34816\n\t"
"v_mfma_f32_16x16x32_bf16 v[60:63], a[28:31], a[156:159], v[60:63]\n\t"
"s_waitcnt lgkmcnt(6)\n\t"
"ds_read_b64_tr_b16 a[4:5], v233 offset:32768\n\t"
"ds_read_b64_tr_b16 a[6:7], v233 offset:34816\n\t"
"v_mfma_f32_16x16x32_bf16 v[32:35], a[32:35], a[160:163], v[32:35]\n\t"
"buffer_load_dwordx4 v244, %[ws], s68 offen sc1 lds\n\t"
"v_mfma_f32_16x16x32_bf16 v[36:39], a[32:35], a[164:167], v[36:39]\n\t"
"s_add_u32 s68, s76, 24576\n\t"
"ds_read_b64_tr_b16 a[8:9], v236 offset:32768\n\t"
"v_mfma_f32_16x16x32_bf16 v[40:43], a[36:39], a[160:163], v[40:43]\n\t"
"s_add_u32 m0, s41, 139264\n\t"
"ds_read_b64_tr_b16 a[10:11], v236 offset:34816\n\t"
"v_mfma_f32_16x16x32_bf16 v[44:47], a[36:39], a[164:167], v[44:47]\n\t"
"s_waitcnt lgkmcnt(6)\n\t"
"ds_read_b64_tr_b16 a[12:13], v239 offset:32768\n\t"
"ds_read_b64_tr_b16 a[14:15], v239 offset:34816\n\t"
"v_mfma_f32_16x16x32_bf16 v[48:51], a[40:43], a[160:163], v[48:51]\n\t"
"buffer_load_dwordx4 v[0:3], v30, %[ws], s42 offen offset:0 sc1\n\t"
"v_mfma_f32_16x16x32_bf16 v[52:55], a[40:43], a[164:167], v[52:55]\n\t"
"buffer_load_dwordx4 v244, %[ws], s68 offen sc1 lds\n\t"
"v_mfma_f32_16x16x32_bf16 v[56:59], a[44:47], a[160:163], v[56:59]\n\t"
"s_waitcnt lgkmcnt(4)\n\t"
"ds_read_b64_tr_b16 a[16:17], v218 offset:40960\n\t"
"v_mfma_f32_16x16x32_bf16 v[60:63], a[44:47], a[164:167], v[60:63]\n\t"
"ds_read_b64_tr_b16 a[18:19], v218 offset:43008\n\t"
"ds_read_b64_tr_b16 a[20:21], v221 offset:40960\n\t"
"ds_read_b64_tr_b16 a[22:23], v221 offset:43008\n\t"
"v_mfma_f32_16x16x32_bf16 v[32:35], a[0:3], a[168:171], v[32:35]\n\t"
"buffer_load_dwordx4 v[4:7], v30, %[ws], s42 offen offset:1024 sc1\n\t"
"v_mfma_f32_16x16x32_bf16 v[36:39], a[0:3], a[172:175], v[36:39]\n\t"
"s_add_u32 s68, s76, 28672\n\t"
"ds_read_b64_tr_b16 a[24:25], v224 offset:40960\n\t"
"v_mfma_f32_16x16x32_bf16 v[40:43], a[4:7], a[168:171], v[40:43]\n\t"
"s_add_u32 m0, s41, 143360\n\t"
"ds_read_b64_tr_b16 a[26:27], v224 offset:43008\n\t"
"v_mfma_f32_16x16x32_bf16 v[44:47], a[4:7], a[172:175], v[44:47]\n\t"
"s_waitcnt lgkmcnt(6)\n\t"
"ds_read_b64_tr_b16 a[28:29], v227 offset:40960\n\t"
"ds_read_b64_tr_b16 a[30:31], v227 offset:43008\n\t"
"v_mfma_f32_16x16x32_bf16 v[48:51], a[8:11], a[168:171], v[48:51]\n\t"
"buffer_load_dwordx4 v[8:11], v30, %[ws], s42 offen offset:2048 sc1\n\t"
"v_mfma_f32_16x16x32_bf16 v[52:55], a[8:11], a[172:175], v[52:55]\n\t"
"ds_read_b64_tr_b16 a[32:33], v230 offset:40960\n\t"
"v_mfma_f32_16x16x32_bf16 v[56:59], a[12:15], a[168:171], v[56:59]\n\t"
"ds_read_b64_tr_b16 a[34:35], v230 offset:43008\n\t"
"v_mfma_f32_16x16x32_bf16 v[60:63], a[12:15], a[172:175], v[60:63]\n\t"
"s_waitcnt lgkmcnt(6)\n\t"
"ds_read_b64_tr_b16 a[36:37], v233 offset:40960\n\t"
"ds_read_b64_tr_b16 a[38:39], v233 offset:43008\n\t"
"ds_read_b64_tr_b16 a[40:41], v236 offset:40960\n\t"
"ds_read_b64_tr_b16 a[42:43], v236 offset:43008\n\t"
"ds_read_b64_tr_b16 a[44:45], v239 offset:40960\n\t"
"ds_read_b64_tr_b16 a[46:47], v239 offset:43008\n\t"
"v_mfma_f32_16x16x32_bf16 v[32:35], a[16:19], a[176:179], v[32:35]\n\t"
"buffer_load_dwordx4 v244, %[ws], s68 offen sc1 lds\n\t"
"v_mfma_f32_16x16x32_bf16 v[36:39], a[16:19], a[180:183], v[36:39]\n\t"
"buffer_load_dwordx4 v[12:15], v30, %[ws], s42 offen offset:3072 sc1\n\t"
"v_mfma_f32_16x16x32_bf16 v[40:43], a[20:23], a[176:179], v[40:43]\n\t"
"v_mfma_f32_16x16x32_bf16 v[44:47], a[20:23], a[180:183], v[44:47]\n\t"
"s_waitcnt lgkmcnt(8)\n\t"
"v_mfma_f32_16x16x32_bf16 v[48:51], a[24:27], a[176:179], v[48:51]\n\t"
"s_waitcnt vmcnt(24)\n\t"
"s_waitcnt lgkmcnt(0)\n\t"
"v_mfma_f32_16x16x32_bf16 v[52:55], a[24:27], a[180:183], v[52:55]\n\t"
"s_barrier\n\t"
"v_mfma_f32_16x16x32_bf16 v[56:59], a[28:31], a[176:179], v[56:59]\n\t"
"s_add_u32 s68, s76, 32768\n\t"
"ds_read_b64_tr_b16 a[0:1], v219 offset:0\n\t"
"v_mfma_f32_16x16x32_bf16 v[60:63], a[28:31], a[180:183], v[60:63]\n\t"
"ds_read_b64_tr_b16 a[2:3], v219 offset:2048\n\t"
"v_mfma_f32_16x16x32_bf16 v[32:35], a[32:35], a[184:187], v[32:35]\n\t"
"s_add_u32 m0, s41, 0\n\t"
"ds_read_b64_tr_b16 a[4:5], v222 offset:0\n\t"
"buffer_load_dwordx4 v244, %[ws], s68 offen sc1 lds\n\t"
"v_mfma_f32_16x16x32_bf16 v[36:39], a[32:35], a[188:191], v[36:39]\n\t"
"s_add_u32 s68, s76, 36864\n\t"
"ds_read_b64_tr_b16 a[6:7], v222 offset:2048\n\t"
"v_mfma_f32_16x16x32_bf16 v[40:43], a[36:39], a[184:187], v[40:43]\n\t"
"s_add_u32 m0, s41, 4096\n\t"
"buffer_store_dwordx2 v[254:255], v252, %[ws], s79 offen offset:1024 sc1\n\t"
"ds_read_b64_tr_b16 a[8:9], v225 offset:0\n\t"
"buffer_load_dwordx4 v244, %[ws], s68 offen sc1 lds\n\t"
"v_mfma_f32_16x16x32_bf16 v[44:47], a[36:39], a[188:191], v[44:47]\n\t"
"ds_read_b64_tr_b16 a[10:11], v225 offset:2048\n\t"
"v_mfma_f32_16x16x32_bf16 v[48:51], a[40:43], a[184:187], v[48:51]\n\t"
"ds_read_b64_tr_b16 a[12:13], v228 offset:0\n\t"
"ds_read_b64_tr_b16 a[14:15], v228 offset:2048\n\t"
"v_mfma_f32_16x16x32_bf16 v[52:55], a[40:43], a[188:191], v[52:55]\n\t"
"s_add_u32 s68, s77, 0\n\t"
"ds_read_b64_tr_b16 a[16:17], v231 offset:0\n\t"
"v_mfma_f32_16x16x32_bf16 v[56:59], a[44:47], a[184:187], v[56:59]\n\t"
"s_add_u32 m0, s41, 8192\n\t"
"ds_read_b64_tr_b16 a[18:19], v231 offset:2048\n\t"
"v_mfma_f32_16x16x32_bf16 v[60:63], a[44:47], a[188:191], v[60:63]\n\t"
"s_waitcnt lgkmcnt(6)\n\t"
"ds_read_b64_tr_b16 a[20:21], v234 offset:0\n\t"
"ds_read_b64_tr_b16 a[22:23], v234 offset:2048\n\t"
"v_mfma_f32_16x16x32_bf16 v[32:35], a[0:3], a[192:195], v[32:35]\n\t"
"buffer_load_dwordx4 v244, %[ws], s68 offen sc1 lds\n\t"
"v_mfma_f32_16x16x32_bf16 v[36:39], a[0:3], a[196:199], v[36:39]\n\t"
"s_add_u32 s68, s77, 4096\n\t"
"ds_read_b64_tr_b16 a[24:25], v237 offset:0\n\t"
"v_mfma_f32_16x16x32_bf16 v[40:43], a[4:7], a[192:195], v[40:43]\n\t"
"s_add_u32 m0, s41, 12288\n\t"
"ds_read_b64_tr_b16 a[26:27], v237 offset:2048\n\t"
"buffer_load_dwordx4 v244, %[ws], s68 offen sc1 lds\n\t"
"v_mfma_f32_16x16x32_bf16 v[44:47], a[4:7], a[196:199], v[44:47]\n\t"
"s_waitcnt lgkmcnt(6)\n\t"
"ds_read_b64_tr_b16 a[28:29], v240 offset:0\n\t"
"ds_read_b64_tr_b16 a[30:31], v240 offset:2048\n\t"
"v_mfma_f32_16x16x32_bf16 v[48:51], a[8:11], a[192:195], v[48:51]\n\t"
"s_add_u32 s68, s77, 8192\n\t"
"ds_read_b64_tr_b16 a[32:33], v219 offset:8192\n\t"
"v_mfma_f32_16x16x32_bf16 v[52:55], a[8:11], a[196:199], v[52:55]\n\t"
"s_add_u32 m0, s41, 16384\n\t"
"ds_read_b64_tr_b16 a[34:35], v219 offset:10240\n\t"
"v_mfma_f32_16x16x32_bf16 v[56:59], a[12:15], a[192:195], v[56:59]\n\t"
"s_waitcnt lgkmcnt(6)\n\t"
"ds_read_b64_tr_b16 a[36:37], v222 offset:8192\n\t"
"buffer_load_dwordx4 v244, %[ws], s68 offen sc1 lds\n\t"
"v_mfma_f32_16x16x32_bf16 v[60:63], a[12:15], a[196:199], v[60:63]\n\t"
"ds_read_b64_tr_b16 a[38:39], v222 offset:10240\n\t"
"v_mfma_f32_16x16x32_bf16 v[32:35], a[16:19], a[200:203], v[32:35]\n\t"
"s_add_u32 s68, s77, 12288\n\t"
"ds_read_b64_tr_b16 a[40:41], v225 offset:8192\n\t"
"v_mfma_f32_16x16x32_bf16 v[36:39], a[16:19], a[204:207], v[36:39]\n\t"
"s_add_u32 m0, s41, 20480\n\t"
"ds_read_b64_tr_b16 a[42:43], v225 offset:10240\n\t"
"v_mfma_f32_16x16x32_bf16 v[40:43], a[20:23], a[200:203], v[40:43]\n\t"
"s_waitcnt lgkmcnt(6)\n\t"
"ds_read_b64_tr_b16 a[44:45], v228 offset:8192\n\t"
"buffer_load_dwordx2 v[216:217], v28, %[ws], s78 offen offset:896 sc1\n\t"
"v_mfma_f32_16x16x32_bf16 v[44:47], a[20:23], a[204:207], v[44:47]\n\t"
"ds_read_b64_tr_b16 a[46:47], v228 offset:10240\n\t"
"v_mfma_f32_16x16x32_bf16 v[48:51], a[24:27], a[200:203], v[48:51]\n\t"
"buffer_load_dwordx4 v244, %[ws], s68 offen sc1 lds\n\t"
"v_mfma_f32_16x16x32_bf16 v[52:55], a[24:27], a[204:207], v[52:55]\n\t"
"s_add_u32 s68, s77, 16384\n\t"
"ds_read_b64_tr_b16 a[0:1], v231 offset:8192\n\t"
"v_mfma_f32_16x16x32_bf16 v[56:59], a[28:31], a[200:203], v[56:59]\n\t"
"s_add_u32 m0, s41, 24576\n\t"
"ds_read_b64_tr_b16 a[2:3], v231 offset:10240\n\t"
"v_mfma_f32_16x16x32_bf16 v[60:63], a[28:31], a[204:207], v[60:63]\n\t"
"s_waitcnt lgkmcnt(6)\n\t"
"ds_read_b64_tr_b16 a[4:5], v234 offset:8192\n\t"
"ds_read_b64_tr_b16 a[6:7], v234 offset:10240\n\t"
"v_mfma_f32_16x16x32_bf16 v[32:35], a[32:35], a[208:211], v[32:35]\n\t"
"buffer_load_dwordx4 v244, %[ws], s68 offen sc1 lds\n\t"
"v_mfma_f32_16x16x32_bf16 v[36:39], a[32:35], a[212:215], v[36:39]\n\t"
"s_add_u32 s68, s77, 20480\n\t"
"ds_read_b64_tr_b16 a[8:9], v237 offset:8192\n\t"
"v_mfma_f32_16x16x32_bf16 v[40:43], a[36:39], a[208:211], v[40:43]\n\t"
"s_add_u32 m0, s41, 28672\n\t"
"ds_read_b64_tr_b16 a[10:11], v237 offset:10240\n\t"
"v_mfma_f32_16x16x32_bf16 v[44:47], a[36:39], a[212:215], v[44:47]\n\t"
"s_waitcnt lgkmcnt(6)\n\t"
"ds_read_b64_tr_b16 a[12:13], v240 offset:8192\n\t"
"ds_read_b64_tr_b16 a[14:15], v240 offset:10240\n\t"
"v_mfma_f32_16x16x32_bf16 v[48:51], a[40:43], a[208:211], v[48:51]\n\t"
"buffer_load_dwordx4 v244, %[ws], s68 offen sc1 lds\n\t"
"v_mfma_f32_16x16x32_bf16 v[52:55], a[40:43], a[212:215], v[52:55]\n\t"
"s_add_u32 s68, s77, 24576\n\t"
"ds_read_b64_tr_b16 a[16:17], v219 offset:16384\n\t"
"v_mfma_f32_16x16x32_bf16 v[56:59], a[44:47], a[208:211], v[56:59]\n\t"
"s_add_u32 m0, s41, 32768\n\t"
"ds_read_b64_tr_b16 a[18:19], v219 offset:18432\n\t"
"v_mfma_f32_16x16x32_bf16 v[60:63], a[44:47], a[212:215], v[60:63]\n\t"
"s_waitcnt lgkmcnt(6)\n\t"
"ds_read_b64_tr_b16 a[20:21], v222 offset:16384\n\t"
"ds_read_b64_tr_b16 a[22:23], v222 offset:18432\n\t"
"v_mfma_f32_16x16x32_bf16 v[32:35], a[0:3], a[216:219], v[32:35]\n\t"
"buffer_load_dwordx4 v244, %[ws], s68 offen sc1 lds\n\t"
"v_mfma_f32_16x16x32_bf16 v[36:39], a[0:3], a[220:223], v[36:39]\n\t"
"s_add_u32 s68, s77, 28672\n\t"
"ds_read_b64_tr_b16 a[24:25], v225 offset:16384\n\t"
"v_mfma_f32_16x16x32_bf16 v[40:43], a[4:7], a[216:219], v[40:43]\n\t"
"s_add_u32 m0, s41, 36864\n\t"
"ds_read_b64_tr_b16 a[26:27], v225 offset:18432\n\t"
"v_mfma_f32_16x16x32_bf16 v[44:47], a[4:7], a[220:223], v[44:47]\n\t"
"s_waitcnt lgkmcnt(6)\n\t"
"ds_read_b64_tr_b16 a[28:29], v228 offset:16384\n\t"
"ds_read_b64_tr_b16 a[30:31], v228 offset:18432\n\t"
"v_mfma_f32_16x16x32_bf16 v[48:51], a[8:11], a[216:219], v[48:51]\n\t"
"buffer_load_dwordx4 v244, %[ws], s68 offen sc1 lds\n\t"
"v_mfma_f32_16x16x32_bf16 v[52:55], a[8:11], a[220:223], v[52:55]\n\t"
"s_add_u32 s68, s77, 32768\n\t"
"ds_read_b64_tr_b16 a[32:33], v231 offset:16384\n\t"
"v_mfma_f32_16x16x32_bf16 v[56:59], a[12:15], a[216:219], v[56:59]\n\t"
"s_add_u32 m0, s41, 40960\n\t"
"ds_read_b64_tr_b16 a[34:35], v231 offset:18432\n\t"
"v_mfma_f32_16x16x32_bf16 v[60:63], a[12:15], a[220:223], v[60:63]\n\t"
"s_waitcnt lgkmcnt(6)\n\t"
"ds_read_b64_tr_b16 a[36:37], v234 offset:16384\n\t"
"ds_read_b64_tr_b16 a[38:39], v234 offset:18432\n\t"
"v_mfma_f32_16x16x32_bf16 v[32:35], a[16:19], a[224:227], v[32:35]\n\t"
"buffer_load_dwordx4 v244, %[ws], s68 offen sc1 lds\n\t"
"v_mfma_f32_16x16x32_bf16 v[36:39], a[16:19], a[228:231], v[36:39]\n\t"
"s_add_u32 s68, s77, 36864\n\t"
"ds_read_b64_tr_b16 a[40:41], v237 offset:16384\n\t"
"v_mfma_f32_16x16x32_bf16 v[40:43], a[20:23], a[224:227], v[40:43]\n\t"
"s_add_u32 m0, s41, 45056\n\t"
"ds_read_b64_tr_b16 a[42:43], v237 offset:18432\n\t"
"buffer_load_dwordx4 v244, %[ws], s68 offen sc1 lds\n\t"
"v_mfma_f32_16x16x32_bf16 v[44:47], a[20:23], a[228:231], v[44:47]\n\t"
"s_waitcnt lgkmcnt(6)\n\t"
"ds_read_b64_tr_b16 a[44:45], v240 offset:16384\n\t"
"ds_read_b64_tr_b16 a[46:47], v240 offset:18432\n\t"
"v_mfma_f32_16x16x32_bf16 v[48:51], a[24:27], a[224:227], v[48:51]\n\t"
"v_mfma_f32_16x16x32_bf16 v[52:55], a[24:27], a[228:231], v[52:55]\n\t"
"v_mfma_f32_16x16x32_bf16 v[56:59], a[28:31], a[224:227], v[56:59]\n\t"
"v_mfma_f32_16x16x32_bf16 v[60:63], a[28:31], a[228:231], v[60:63]\n\t"
"s_waitcnt lgkmcnt(4)\n\t"
"v_mfma_f32_16x16x32_bf16 v[32:35], a[32:35], a[232:235], v[32:35]\n\t"
"v_mfma_f32_16x16x32_bf16 v[36:39], a[32:35], a[236:239], v[36:39]\n\t"
"v_mfma_f32_16x16x32_bf16 v[40:43], a[36:39], a[232:235], v[40:43]\n\t"
"v_mfma_f32_16x16x32_bf16 v[44:47], a[36:39], a[236:239], v[44:47]\n\t"
"s_waitcnt lgkmcnt(0)\n\t"
"v_mfma_f32_16x16x32_bf16 v[48:51], a[40:43], a[232:235], v[48:51]\n\t"
"v_mfma_f32_16x16x32_bf16 v[52:55], a[40:43], a[236:239], v[52:55]\n\t"
"v_mfma_f32_16x16x32_bf16 v[56:59], a[44:47], a[232:235], v[56:59]\n\t"
"v_mfma_f32_16x16x32_bf16 v[60:63], a[44:47], a[236:239], v[60:63]\n\t"
"s_mul_i32 s62, s55, 512\n\t"
"s_add_u32 s62, s62, s52\n\t"
"v_cvt_pk_bf16_f32 v160, v32, v33\n\t"
"v_cvt_pk_bf16_f32 v161, v34, v35\n\t"
"v_cvt_pk_bf16_f32 v162, v40, v41\n\t"
"v_cvt_pk_bf16_f32 v163, v42, v43\n\t"
"buffer_store_dwordx4 v[160:163], v244, %[ws], s62 offen sc1\n\t"
"s_add_u32 s69, s62, 4096\n\t"
"v_cvt_pk_bf16_f32 v164, v48, v49\n\t"
"v_cvt_pk_bf16_f32 v165, v50, v51\n\t"
"v_cvt_pk_bf16_f32 v166, v56, v57\n\t"
"v_cvt_pk_bf16_f32 v167, v58, v59\n\t"
"buffer_store_dwordx4 v[164:167], v244, %[ws], s69 offen sc1\n\t"
"s_add_u32 s69, s62, 8192\n\t"
"v_cvt_pk_bf16_f32 v192, v36, v37\n\t"
"v_cvt_pk_bf16_f32 v193, v38, v39\n\t"
"v_cvt_pk_bf16_f32 v194, v44, v45\n\t"
"v_cvt_pk_bf16_f32 v195, v46, v47\n\t"
"buffer_store_dwordx4 v[192:195], v244, %[ws], s69 offen sc1\n\t"
"s_add_u32 s69, s62, 12288\n\t"
"v_cvt_pk_bf16_f32 v196, v52, v53\n\t"
"v_cvt_pk_bf16_f32 v197, v54, v55\n\t"
"v_cvt_pk_bf16_f32 v198, v60, v61\n\t"
"v_cvt_pk_bf16_f32 v199, v62, v63\n\t"
"buffer_store_dwordx4 v[196:199], v244, %[ws], s69 offen sc1\n\t"
"s_waitcnt vmcnt(11)\n\t"
"v_cmp_eq_u64 vcc, v[216:217], v[254:255]\n\t"
"s_cmp_eq_u64 vcc, -1\n\t"
"s_cbranch_scc1 111f\n\t"
"s_mov_b32 s98, 65536\n\t"
"110:\n\t"
"s_sleep 3\n\t"
"buffer_load_dwordx2 v[216:217], v28, %[ws], s78 offen offset:896 sc1\n\t"
"s_waitcnt vmcnt(0)\n\t"
"v_cmp_eq_u64 vcc, v[216:217], v[254:255]\n\t"
"s_cmp_eq_u64 vcc, -1\n\t"
"s_cbranch_scc1 111f\n\t"
"s_sub_u32 s98, s98, 1\n\t"
"s_cmp_lg_u32 s98, 0\n\t"
"s_cbranch_scc1 110b\n\t"
"111:\n\t"
"buffer_load_dwordx4 a[192:195], v244, %[ws], s72 offen offset:0 sc1\n\t"
"buffer_load_dwordx4 a[196:199], v244, %[ws], s72 offen offset:1024 sc1\n\t"
"buffer_load_dwordx4 a[200:203], v244, %[ws], s72 offen offset:2048 sc1\n\t"
"buffer_load_dwordx4 a[204:207], v244, %[ws], s72 offen offset:3072 sc1\n\t"
"buffer_load_dwordx2 v[216:217], v28, %[ws], s78 offen offset:1024 sc1\n\t"
"s_waitcnt vmcnt(9)\n\t"
"s_barrier\n\t"
"s_cmp_lg_u32 s43, 0\n\t"
"s_cbranch_scc1 113f\n\t"
"s_mov_b32 s98, 65536\n\t"
"112:\n\t"
"buffer_load_dwordx2 v[204:205], v29, %[ws], s78 offen offset:384 sc1\n\t"
"s_waitcnt vmcnt(0)\n\t"
"v_cmp_eq_u64 vcc, v[204:205], v[254:255]\n\t"
"s_cmp_eq_u64 vcc, -1\n\t"
"s_cbranch_scc1 114f\n\t"
"s_sleep 3\n\t"
"s_sub_u32 s98, s98, 1\n\t"
"s_cmp_lg_u32 s98, 0\n\t"
"s_cbranch_scc1 112b\n\t"
"114:\n\t"
"buffer_load_dwordx4 v[96:99], v244, %[ws], s40 offen offset:0 sc1\n\t"
"buffer_load_dwordx4 v[100:103], v244, %[ws], s40 offen offset:1024 sc1\n\t"
"buffer_load_dwordx4 v[104:107], v244, %[ws], s40 offen offset:2048 sc1\n\t"
"buffer_load_dwordx4 v[108:111], v244, %[ws], s40 offen offset:3072 sc1\n\t"
"s_add_u32 s42, s40, 4096\n\t"
"buffer_load_dwordx4 v[112:115], v244, %[ws], s42 offen offset:0 sc1\n\t"
"buffer_load_dwordx4 v[116:119], v244, %[ws], s42 offen offset:1024 sc1\n\t"
"buffer_load_dwordx4 v[120:123], v244, %[ws], s42 offen offset:2048 sc1\n\t"
"buffer_load_dwordx4 v[124:127], v244, %[ws], s42 offen offset:3072 sc1\n\t"
"s_add_u32 s42, s40, 8192\n\t"
"buffer_load_dwordx4 v[128:131], v244, %[ws], s42 offen offset:0 sc1\n\t"
"buffer_load_dwordx4 v[132:135], v244, %[ws], s42 offen offset:1024 sc1\n\t"
"buffer_load_dwordx4 v[136:139], v244, %[ws], s42 offen offset:2048 sc1\n\t"
"buffer_load_dwordx4 v[140:143], v244, %[ws], s42 offen offset:3072 sc1\n\t"
"s_add_u32 s42, s40, 12288\n\t"
"buffer_load_dwordx4 v[144:147], v244, %[ws], s42 offen offset:0 sc1\n\t"
"buffer_load_dwordx4 v[148:151], v244, %[ws], s42 offen offset:1024 sc1\n\t"
"buffer_load_dwordx4 v[152:155], v244, %[ws], s42 offen offset:2048 sc1\n\t"
"buffer_load_dwordx4 v[156:159], v244, %[ws], s42 offen offset:3072 sc1\n\t"
"s_add_u32 s42, s40, 16384\n\t"
"buffer_load_dwordx4 v[0:3], v244, %[ws], s42 offen offset:0 sc1\n\t"
"buffer_load_dwordx4 v[4:7], v244, %[ws], s42 offen offset:1024 sc1\n\t"
"buffer_load_dwordx4 v[8:11], v244, %[ws], s42 offen offset:2048 sc1\n\t"
"buffer_load_dwordx4 v[12:15], v244, %[ws], s42 offen offset:3072 sc1\n\t"
"s_waitcnt vmcnt(0)\n\t"
"113:\n\t"
"ds_read_b128 a[0:3], v25 offset:24576\n\t"
"ds_read_b128 a[4:7], v25 offset:25600\n\t"
"ds_read_b128 a[8:11], v25 offset:26624\n\t"
"ds_read_b128 a[12:15], v25 offset:27648\n\t"
"ds_read_b128 a[16:19], v25 offset:28672\n\t"
"ds_read_b128 a[20:23], v25 offset:29696\n\t"
"ds_read_b128 a[24:27], v25 offset:30720\n\t"
"ds_read_b128 a[28:31], v25 offset:31744\n\t"
"s_waitcnt lgkmcnt(6)\n\t"
"v_mfma_f32_16x16x32_bf16 v[64:67], a[0:3], v[96:99], 0\n\t"
"v_mfma_f32_16x16x32_bf16 v[68:71], a[4:7], v[96:99], 0\n\t"
"s_waitcnt lgkmcnt(4)\n\t"
"ds_read_b128 a[32:35], v25 offset:32768\n\t"
"v_mfma_f32_16x16x32_bf16 v[64:67], a[8:11], v[116:119], v[64:67]\n\t"
"ds_read_b128 a[36:39], v25 offset:33792\n\t"
"v_mfma_f32_16x16x32_bf16 v[68:71], a[12:15], v[116:119], v[68:71]\n\t"
"s_waitcnt lgkmcnt(4)\n\t"
"ds_read_b128 a[40:43], v25 offset:34816\n\t"
"v_mfma_f32_16x16x32_bf16 v[64:67], a[16:19], v[100:103], v[64:67]\n\t"
"ds_read_b128 a[44:47], v25 offset:35840\n\t"
"v_mfma_f32_16x16x32_bf16 v[68:71], a[20:23], v[100:103], v[68:71]\n\t"
"s_waitcnt lgkmcnt(4)\n\t"
"ds_read_b128 a[48:51], v25 offset:36864\n\t"
"v_mfma_f32_16x16x32_bf16 v[64:67], a[24:27], v[120:123], v[64:67]\n\t"
"ds_read_b128 a[52:55], v25 offset:37888\n\t"
"v_mfma_f32_16x16x32_bf16 v[68:71], a[28:31], v[120:123], v[68:71]\n\t"
"s_waitcnt lgkmcnt(4)\n\t"
"ds_read_b128 a[56:59], v25 offset:38912\n\t"
"v_mfma_f32_16x16x32_bf16 v[64:67], a[32:35], v[104:107], v[64:67]\n\t"
"ds_read_b128 a[60:63], v25 offset:39936\n\t"
"v_mfma_f32_16x16x32_bf16 v[68:71], a[36:39], v[104:107], v[68:71]\n\t"
"s_waitcnt lgkmcnt(4)\n\t"
"ds_read_b128 a[0:3], v25 offset:40960\n\t"
"v_mfma_f32_16x16x32_bf16 v[64:67], a[40:43], v[124:127], v[64:67]\n\t"
"ds_read_b128 a[4:7], v25 offset:41984\n\t"
"v_mfma_f32_16x16x32_bf16 v[68:71], a[44:47], v[124:127], v[68:71]\n\t"
"s_waitcnt lgkmcnt(4)\n\t"
"ds_read_b128 a[8:11], v25 offset:43008\n\t"
"v_mfma_f32_16x16x32_bf16 v[64:67], a[48:51], v[108:111], v[64:67]\n\t"
"ds_read_b128 a[12:15], v25 offset:44032\n\t"
"v_mfma_f32_16x16x32_bf16 v[68:71], a[52:55], v[108:111], v[68:71]\n\t"
"s_waitcnt lgkmcnt(4)\n\t"
"ds_read_b128 a[16:19], v25 offset:45056\n\t"
"v_mfma_f32_16x16x32_bf16 v[64:67], a[56:59], v[128:131], v[64:67]\n\t"
"ds_read_b128 a[20:23], v25 offset:46080\n\t"
"v_mfma_f32_16x16x32_bf16 v[68:71], a[60:63], v[128:131], v[68:71]\n\t"
"s_waitcnt lgkmcnt(4)\n\t"
"ds_read_b128 a[24:27], v25 offset:47104\n\t"
"v_mfma_f32_16x16x32_bf16 v[64:67], a[0:3], v[112:115], v[64:67]\n\t"
"ds_read_b128 a[28:31], v25 offset:48128\n\t"
"v_mfma_f32_16x16x32_bf16 v[68:71], a[4:7], v[112:115], v[68:71]\n\t"
"s_waitcnt lgkmcnt(4)\n\t"
"ds_read_b128 a[32:35], v26 offset:0\n\t"
"v_mfma_f32_16x16x32_bf16 v[64:67], a[8:11], v[132:135], v[64:67]\n\t"
"ds_read_b128 a[36:39], v26 offset:1024\n\t"
"v_mfma_f32_16x16x32_bf16 v[68:71], a[12:15], v[132:135], v[68:71]\n\t"
"s_waitcnt lgkmcnt(4)\n\t"
"ds_read_b128 a[40:43], v26 offset:2048\n\t"
"v_mfma_f32_16x16x32_bf16 v[64:67], a[16:19], v[136:139], v[64:67]\n\t"
"ds_read_b128 a[44:47], v26 offset:3072\n\t"
"v_mfma_f32_16x16x32_bf16 v[68:71], a[20:23], v[136:139], v[68:71]\n\t"
"s_waitcnt lgkmcnt(4)\n\t"
"ds_read_b128 a[48:51], v26 offset:4096\n\t"
"v_mfma_f32_16x16x32_bf16 v[64:67], a[24:27], v[156:159], v[64:67]\n\t"
"ds_read_b128 a[52:55], v26 offset:5120\n\t"
"v_mfma_f32_16x16x32_bf16 v[68:71], a[28:31], v[156:159], v[68:71]\n\t"
"s_waitcnt lgkmcnt(4)\n\t"
"ds_read_b128 a[56:59], v26 offset:6144\n\t"
"v_mfma_f32_16x16x32_bf16 v[64:67], a[32:35], v[140:143], v[64:67]\n\t"
"ds_read_b128 a[60:63], v26 offset:7168\n\t"
"v_mfma_f32_16x16x32_bf16 v[68:71], a[36:39], v[140:143], v[68:71]\n\t"
"s_waitcnt lgkmcnt(4)\n\t"
"ds_read_b128 a[0:3], v26 offset:8192\n\t"
"v_mfma_f32_16x16x32_bf16 v[64:67], a[40:43], v[0:3], v[64:67]\n\t"
"ds_read_b128 a[4:7], v26 offset:9216\n\t"
"v_mfma_f32_16x16x32_bf16 v[68:71], a[44:47], v[0:3], v[68:71]\n\t"
"s_waitcnt lgkmcnt(4)\n\t"
"ds_read_b128 a[8:11], v26 offset:10240\n\t"
"v_mfma_f32_16x16x32_bf16 v[64:67], a[48:51], v[144:147], v[64:67]\n\t"
"ds_read_b128 a[12:15], v26 offset:11264\n\t"
"v_mfma_f32_16x16x32_bf16 v[68:71], a[52:55], v[144:147], v[68:71]\n\t"
"s_waitcnt lgkmcnt(4)\n\t"
"ds_read_b128 a[16:19], v26 offset:12288\n\t"
"v_mfma_f32_16x16x32_bf16 v[64:67], a[56:59], v[4:7], v[64:67]\n\t"
"ds_read_b128 a[20:23], v26 offset:13312\n\t"
"v_mfma_f32_16x16x32_bf16 v[68:71], a[60:63], v[4:7], v[68:71]\n\t"
"s_waitcnt lgkmcnt(4)\n\t"
"ds_read_b128 a[24:27], v26 offset:14336\n\t"
"v_mfma_f32_16x16x32_bf16 v[64:67], a[0:3], v[148:151], v[64:67]\n\t"
"ds_read_b128 a[28:31], v26 offset:15360\n\t"
"v_mfma_f32_16x16x32_bf16 v[68:71], a[4:7], v[148:151], v[68:71]\n\t"
"s_waitcnt lgkmcnt(4)\n\t"
"ds_read_b128 a[32:35], v26 offset:16384\n\t"
"v_mfma_f32_16x16x32_bf16 v[64:67], a[8:11], v[8:11], v[64:67]\n\t"
"ds_read_b128 a[36:39], v26 offset:17408\n\t"
"v_mfma_f32_16x16x32_bf16 v[68:71], a[12:15], v[8:11], v[68:71]\n\t"
"s_waitcnt lgkmcnt(4)\n\t"
"ds_read_b128 a[40:43], v26 offset:18432\n\t"
"v_mfma_f32_16x16x32_bf16 v[64:67], a[16:19], v[152:155], v[64:67]\n\t"
"ds_read_b128 a[44:47], v26 offset:19456\n\t"
"v_mfma_f32_16x16x32_bf16 v[68:71], a[20:23], v[152:155], v[68:71]\n\t"
"s_waitcnt lgkmcnt(4)\n\t"
"ds_read_b128 a[48:51], v26 offset:20480\n\t"
"v_mfma_f32_16x16x32_bf16 v[64:67], a[24:27], v[12:15], v[64:67]\n\t"
"ds_read_b128 a[52:55], v26 offset:21504\n\t"
"v_mfma_f32_16x16x32_bf16 v[68:71], a[28:31], v[12:15], v[68:71]\n\t"
"s_waitcnt lgkmcnt(4)\n\t"
"ds_read_b128 a[56:59], v26 offset:22528\n\t"
"v_mfma_f32_16x16x32_bf16 v[72:75], a[32:35], v[96:99], 0\n\t"
"ds_read_b128 a[60:63], v26 offset:23552\n\t"
"v_mfma_f32_16x16x32_bf16 v[76:79], a[36:39], v[96:99], 0\n\t"
"s_waitcnt lgkmcnt(4)\n\t"
"v_mfma_f32_16x16x32_bf16 v[72:75], a[40:43], v[116:119], v[72:75]\n\t"
"v_mfma_f32_16x16x32_bf16 v[76:79], a[44:47], v[116:119], v[76:79]\n\t"
"s_waitcnt lgkmcnt(2)\n\t"
"v_mfma_f32_16x16x32_bf16 v[72:75], a[48:51], v[100:103], v[72:75]\n\t"
"v_mfma_f32_16x16x32_bf16 v[76:79], a[52:55], v[100:103], v[76:79]\n\t"
"s_waitcnt lgkmcnt(0)\n\t"
"v_mfma_f32_16x16x32_bf16 v[72:75], a[56:59], v[120:123], v[72:75]\n\t"
"v_mfma_f32_16x16x32_bf16 v[76:79], a[60:63], v[120:123], v[76:79]\n\t"
"s_waitcnt vmcnt(5)\n\t"
"s_barrier\n\t"
"buffer_store_dwordx2 v[254:255], v252, %[ws], s79 offen offset:1152 sc1\n\t"
"s_waitcnt vmcnt(1)\n\t"
"v_cmp_eq_u64 vcc, v[216:217], v[254:255]\n\t"
"s_cmp_eq_u64 vcc, -1\n\t"
"s_cbranch_scc1 117f\n\t"
"s_mov_b32 s98, 65536\n\t"
"116:\n\t"
"s_sleep 3\n\t"
"buffer_load_dwordx2 v[216:217], v28, %[ws], s78 offen offset:1024 sc1\n\t"
"s_waitcnt vmcnt(0)\n\t"
"v_cmp_eq_u64 vcc, v[216:217], v[254:255]\n\t"
"s_cmp_eq_u64 vcc, -1\n\t"
"s_cbranch_scc1 117f\n\t"
"s_sub_u32 s98, s98, 1\n\t"
"s_cmp_lg_u32 s98, 0\n\t"
"s_cbranch_scc1 116b\n\t"
"117:\n\t"
"buffer_load_dwordx4 a[208:211], v244, %[ws], s73 offen offset:0 sc1\n\t"
"buffer_load_dwordx4 a[212:215], v244, %[ws], s73 offen offset:1024 sc1\n\t"
"buffer_load_dwordx4 a[216:219], v244, %[ws], s73 offen offset:2048 sc1\n\t"
"buffer_load_dwordx4 a[220:223], v244, %[ws], s73 offen offset:3072 sc1\n\t"
"buffer_load_dwordx2 v[246:247], v27, %[x], s59 offen offset:0\n\t"
"buffer_load_dwordx2 v[248:249], v27, %[x], s59 offen offset:32\n\t"
"buffer_load_dwordx2 v[206:207], v27, %[x], s60 offen offset:0\n\t"
"buffer_load_dwordx2 v[208:209], v27, %[x], s60 offen offset:32\n\t"
"buffer_load_dwordx2 v[168:169], v27, %[x], s61 offen offset:0\n\t"
"buffer_load_dwordx2 v[170:171], v27, %[x], s61 offen offset:32\n\t"
"ds_read_b128 a[0:3], v26 offset:24576\n\t"
"ds_read_b128 a[4:7], v26 offset:25600\n\t"
"ds_read_b128 a[8:11], v26 offset:26624\n\t"
"ds_read_b128 a[12:15], v26 offset:27648\n\t"
"ds_read_b128 a[16:19], v26 offset:28672\n\t"
"ds_read_b128 a[20:23], v26 offset:29696\n\t"
"ds_read_b128 a[24:27], v26 offset:30720\n\t"
"ds_read_b128 a[28:31], v26 offset:31744\n\t"
"s_waitcnt lgkmcnt(6)\n\t"
"v_mfma_f32_16x16x32_bf16 v[72:75], a[0:3], v[104:107], v[72:75]\n\t"
"v_mfma_f32_16x16x32_bf16 v[76:79], a[4:7], v[104:107], v[76:79]\n\t"
"s_waitcnt lgkmcnt(4)\n\t"
"ds_read_b128 a[32:35], v26 offset:32768\n\t"
"v_mfma_f32_16x16x32_bf16 v[72:75], a[8:11], v[124:127], v[72:75]\n\t"
"ds_read_b128 a[36:39], v26 offset:33792\n\t"
"v_mfma_f32_16x16x32_bf16 v[76:79], a[12:15], v[124:127], v[76:79]\n\t"
"s_waitcnt lgkmcnt(4)\n\t"
"ds_read_b128 a[40:43], v26 offset:34816\n\t"
"v_mfma_f32_16x16x32_bf16 v[72:75], a[16:19], v[108:111], v[72:75]\n\t"
"ds_read_b128 a[44:47], v26 offset:35840\n\t"
"v_mfma_f32_16x16x32_bf16 v[76:79], a[20:23], v[108:111], v[76:79]\n\t"
"s_waitcnt lgkmcnt(4)\n\t"
"ds_read_b128 a[48:51], v26 offset:36864\n\t"
"v_mfma_f32_16x16x32_bf16 v[72:75], a[24:27], v[128:131], v[72:75]\n\t"
"ds_read_b128 a[52:55], v26 offset:37888\n\t"
"v_mfma_f32_16x16x32_bf16 v[76:79], a[28:31], v[128:131], v[76:79]\n\t"
"s_waitcnt lgkmcnt(4)\n\t"
"ds_read_b128 a[56:59], v26 offset:38912\n\t"
"v_mfma_f32_16x16x32_bf16 v[72:75], a[32:35], v[112:115], v[72:75]\n\t"
"ds_read_b128 a[60:63], v26 offset:39936\n\t"
"v_mfma_f32_16x16x32_bf16 v[76:79], a[36:39], v[112:115], v[76:79]\n\t"
"s_waitcnt lgkmcnt(4)\n\t"
"ds_read_b128 a[0:3], v26 offset:40960\n\t"
"v_mfma_f32_16x16x32_bf16 v[72:75], a[40:43], v[132:135], v[72:75]\n\t"
"ds_read_b128 a[4:7], v26 offset:41984\n\t"
"v_mfma_f32_16x16x32_bf16 v[76:79], a[44:47], v[132:135], v[76:79]\n\t"
"s_waitcnt lgkmcnt(4)\n\t"
"ds_read_b128 a[8:11], v26 offset:43008\n\t"
"v_mfma_f32_16x16x32_bf16 v[72:75], a[48:51], v[136:139], v[72:75]\n\t"
"ds_read_b128 a[12:15], v26 offset:44032\n\t"
"v_mfma_f32_16x16x32_bf16 v[76:79], a[52:55], v[136:139], v[76:79]\n\t"
"s_waitcnt lgkmcnt(4)\n\t"
"ds_read_b128 a[16:19], v26 offset:45056\n\t"
"v_mfma_f32_16x16x32_bf16 v[72:75], a[56:59], v[156:159], v[72:75]\n\t"
"ds_read_b128 a[20:23], v26 offset:46080\n\t"
"v_mfma_f32_16x16x32_bf16 v[76:79], a[60:63], v[156:159], v[76:79]\n\t"
"s_waitcnt lgkmcnt(4)\n\t"
"ds_read_b128 a[24:27], v26 offset:47104\n\t"
"v_mfma_f32_16x16x32_bf16 v[72:75], a[0:3], v[140:143], v[72:75]\n\t"
"ds_read_b128 a[28:31], v26 offset:48128\n\t"
"v_mfma_f32_16x16x32_bf16 v[76:79], a[4:7], v[140:143], v[76:79]\n\t"
"s_waitcnt lgkmcnt(4)\n\t"
"ds_read_b128 a[32:35], v24 offset:0\n\t"
"v_mfma_f32_16x16x32_bf16 v[72:75], a[8:11], v[0:3], v[72:75]\n\t"
"ds_read_b128 a[36:39], v24 offset:1024\n\t"
"v_mfma_f32_16x16x32_bf16 v[76:79], a[12:15], v[0:3], v[76:79]\n\t"
"s_waitcnt lgkmcnt(4)\n\t"
"ds_read_b128 a[40:43], v24 offset:2048\n\t"
"v_mfma_f32_16x16x32_bf16 v[72:75], a[16:19], v[144:147], v[72:75]\n\t"
"ds_read_b128 a[44:47], v24 offset:3072\n\t"
"v_mfma_f32_16x16x32_bf16 v[76:79], a[20:23], v[144:147], v[76:79]\n\t"
"s_waitcnt lgkmcnt(4)\n\t"
"ds_read_b128 a[48:51], v24 offset:4096\n\t"
"v_mfma_f32_16x16x32_bf16 v[72:75], a[24:27], v[4:7], v[72:75]\n\t"
"ds_read_b128 a[52:55], v24 offset:5120\n\t"
"v_mfma_f32_16x16x32_bf16 v[76:79], a[28:31], v[4:7], v[76:79]\n\t"
"s_waitcnt lgkmcnt(4)\n\t"
"ds_read_b128 a[56:59], v24 offset:6144\n\t"
"v_mfma_f32_16x16x32_bf16 v[72:75], a[32:35], v[148:151], v[72:75]\n\t"
"ds_read_b128 a[60:63], v24 offset:7168\n\t"
"v_mfma_f32_16x16x32_bf16 v[76:79], a[36:39], v[148:151], v[76:79]\n\t"
"s_waitcnt lgkmcnt(4)\n\t"
"ds_read_b128 a[0:3], v24 offset:8192\n\t"
"v_mfma_f32_16x16x32_bf16 v[72:75], a[40:43], v[8:11], v[72:75]\n\t"
"ds_read_b128 a[4:7], v24 offset:9216\n\t"
"v_mfma_f32_16x16x32_bf16 v[76:79], a[44:47], v[8:11], v[76:79]\n\t"
"s_waitcnt lgkmcnt(4)\n\t"
"ds_read_b128 a[8:11], v24 offset:10240\n\t"
"v_mfma_f32_16x16x32_bf16 v[72:75], a[48:51], v[152:155], v[72:75]\n\t"
"ds_read_b128 a[12:15], v24 offset:11264\n\t"
"v_mfma_f32_16x16x32_bf16 v[76:79], a[52:55], v[152:155], v[76:79]\n\t"
"s_waitcnt lgkmcnt(4)\n\t"
"ds_read_b128 a[16:19], v24 offset:12288\n\t"
"v_mfma_f32_16x16x32_bf16 v[72:75], a[56:59], v[12:15], v[72:75]\n\t"
"ds_read_b128 a[20:23], v24 offset:13312\n\t"
"v_mfma_f32_16x16x32_bf16 v[76:79], a[60:63], v[12:15], v[76:79]\n\t"
"s_waitcnt lgkmcnt(4)\n\t"
"ds_read_b128 a[24:27], v24 offset:14336\n\t"
"v_mfma_f32_16x16x32_bf16 v[80:83], a[0:3], v[96:99], 0\n\t"
"ds_read_b128 a[28:31], v24 offset:15360\n\t"
"v_mfma_f32_16x16x32_bf16 v[84:87], a[4:7], v[96:99], 0\n\t"
"s_waitcnt lgkmcnt(4)\n\t"
"ds_read_b128 a[32:35], v24 offset:16384\n\t"
"v_mfma_f32_16x16x32_bf16 v[80:83], a[8:11], v[116:119], v[80:83]\n\t"
"ds_read_b128 a[36:39], v24 offset:17408\n\t"
"v_mfma_f32_16x16x32_bf16 v[84:87], a[12:15], v[116:119], v[84:87]\n\t"
"s_waitcnt lgkmcnt(4)\n\t"
"ds_read_b128 a[40:43], v24 offset:18432\n\t"
"v_mfma_f32_16x16x32_bf16 v[80:83], a[16:19], v[100:103], v[80:83]\n\t"
"ds_read_b128 a[44:47], v24 offset:19456\n\t"
"v_mfma_f32_16x16x32_bf16 v[84:87], a[20:23], v[100:103], v[84:87]\n\t"
"s_waitcnt lgkmcnt(4)\n\t"
"ds_read_b128 a[48:51], v24 offset:20480\n\t"
"v_mfma_f32_16x16x32_bf16 v[80:83], a[24:27], v[120:123], v[80:83]\n\t"
"ds_read_b128 a[52:55], v24 offset:21504\n\t"
"v_mfma_f32_16x16x32_bf16 v[84:87], a[28:31], v[120:123], v[84:87]\n\t"
"s_waitcnt lgkmcnt(4)\n\t"
"ds_read_b128 a[56:59], v24 offset:22528\n\t"
"v_mfma_f32_16x16x32_bf16 v[80:83], a[32:35], v[104:107], v[80:83]\n\t"
"ds_read_b128 a[60:63], v24 offset:23552\n\t"
"v_mfma_f32_16x16x32_bf16 v[84:87], a[36:39], v[104:107], v[84:87]\n\t"
"s_waitcnt lgkmcnt(4)\n\t"
"ds_read_b128 a[0:3], v24 offset:24576\n\t"
"v_mfma_f32_16x16x32_bf16 v[80:83], a[40:43], v[124:127], v[80:83]\n\t"
"ds_read_b128 a[4:7], v24 offset:25600\n\t"
"v_mfma_f32_16x16x32_bf16 v[84:87], a[44:47], v[124:127], v[84:87]\n\t"
"s_waitcnt lgkmcnt(4)\n\t"
"ds_read_b128 a[8:11], v24 offset:26624\n\t"
"v_mfma_f32_16x16x32_bf16 v[80:83], a[48:51], v[108:111], v[80:83]\n\t"
"ds_read_b128 a[12:15], v24 offset:27648\n\t"
"v_mfma_f32_16x16x32_bf16 v[84:87], a[52:55], v[108:111], v[84:87]\n\t"
"s_waitcnt lgkmcnt(4)\n\t"
"ds_read_b128 a[16:19], v24 offset:28672\n\t"
"v_mfma_f32_16x16x32_bf16 v[80:83], a[56:59], v[128:131], v[80:83]\n\t"
"ds_read_b128 a[20:23], v24 offset:29696\n\t"
"v_mfma_f32_16x16x32_bf16 v[84:87], a[60:63], v[128:131], v[84:87]\n\t"
"s_waitcnt lgkmcnt(4)\n\t"
"ds_read_b128 a[24:27], v24 offset:30720\n\t"
"v_mfma_f32_16x16x32_bf16 v[80:83], a[0:3], v[112:115], v[80:83]\n\t"
"ds_read_b128 a[28:31], v24 offset:31744\n\t"
"v_mfma_f32_16x16x32_bf16 v[84:87], a[4:7], v[112:115], v[84:87]\n\t"
"s_waitcnt lgkmcnt(4)\n\t"
"v_mfma_f32_16x16x32_bf16 v[80:83], a[8:11], v[132:135], v[80:83]\n\t"
"v_mfma_f32_16x16x32_bf16 v[84:87], a[12:15], v[132:135], v[84:87]\n\t"
"s_waitcnt lgkmcnt(2)\n\t"
"v_mfma_f32_16x16x32_bf16 v[80:83], a[16:19], v[136:139], v[80:83]\n\t"
"v_mfma_f32_16x16x32_bf16 v[84:87], a[20:23], v[136:139], v[84:87]\n\t"
"s_waitcnt lgkmcnt(0)\n\t"
"v_mfma_f32_16x16x32_bf16 v[80:83], a[24:27], v[156:159], v[80:83]\n\t"
"v_mfma_f32_16x16x32_bf16 v[84:87], a[28:31], v[156:159], v[84:87]\n\t"
"buffer_load_dwordx2 v[216:217], v28, %[ws], s78 offen offset:1152 sc1\n\t"
"ds_read_b128 a[0:3], v24 offset:32768\n\t"
"ds_read_b128 a[4:7], v24 offset:33792\n\t"
"ds_read_b128 a[8:11], v24 offset:34816\n\t"
"ds_read_b128 a[12:15], v24 offset:35840\n\t"
"ds_read_b128 a[16:19], v24 offset:36864\n\t"
"ds_read_b128 a[20:23], v24 offset:37888\n\t"
"ds_read_b128 a[24:27], v24 offset:38912\n\t"
"ds_read_b128 a[28:31], v24 offset:39936\n\t"
"s_waitcnt lgkmcnt(6)\n\t"
"v_mfma_f32_16x16x32_bf16 v[80:83], a[0:3], v[140:143], v[80:83]\n\t"
"v_mfma_f32_16x16x32_bf16 v[84:87], a[4:7], v[140:143], v[84:87]\n\t"
"s_waitcnt lgkmcnt(4)\n\t"
"ds_read_b128 a[32:35], v24 offset:40960\n\t"
"v_mfma_f32_16x16x32_bf16 v[80:83], a[8:11], v[0:3], v[80:83]\n\t"
"ds_read_b128 a[36:39], v24 offset:41984\n\t"
"v_mfma_f32_16x16x32_bf16 v[84:87], a[12:15], v[0:3], v[84:87]\n\t"
"s_waitcnt lgkmcnt(4)\n\t"
"ds_read_b128 a[40:43], v24 offset:43008\n\t"
"v_mfma_f32_16x16x32_bf16 v[80:83], a[16:19], v[144:147], v[80:83]\n\t"
"ds_read_b128 a[44:47], v24 offset:44032\n\t"
"v_mfma_f32_16x16x32_bf16 v[84:87], a[20:23], v[144:147], v[84:87]\n\t"
"s_waitcnt lgkmcnt(4)\n\t"
"ds_read_b128 a[48:51], v24 offset:45056\n\t"
"v_mfma_f32_16x16x32_bf16 v[80:83], a[24:27], v[4:7], v[80:83]\n\t"
"ds_read_b128 a[52:55], v24 offset:46080\n\t"
"v_mfma_f32_16x16x32_bf16 v[84:87], a[28:31], v[4:7], v[84:87]\n\t"
"s_waitcnt lgkmcnt(4)\n\t"
"ds_read_b128 a[56:59], v24 offset:47104\n\t"
"v_mfma_f32_16x16x32_bf16 v[80:83], a[32:35], v[148:151], v[80:83]\n\t"
"ds_read_b128 a[60:63], v24 offset:48128\n\t"
"v_mfma_f32_16x16x32_bf16 v[84:87], a[36:39], v[148:151], v[84:87]\n\t"
"s_waitcnt lgkmcnt(4)\n\t"
"v_mfma_f32_16x16x32_bf16 v[80:83], a[40:43], v[8:11], v[80:83]\n\t"
"v_mfma_f32_16x16x32_bf16 v[84:87], a[44:47], v[8:11], v[84:87]\n\t"
"s_waitcnt lgkmcnt(2)\n\t"
"v_mfma_f32_16x16x32_bf16 v[80:83], a[48:51], v[152:155], v[80:83]\n\t"
"v_mfma_f32_16x16x32_bf16 v[84:87], a[52:55], v[152:155], v[84:87]\n\t"
"s_waitcnt lgkmcnt(0)\n\t"
"v_mfma_f32_16x16x32_bf16 v[80:83], a[56:59], v[12:15], v[80:83]\n\t"
"v_mfma_f32_16x16x32_bf16 v[84:87], a[60:63], v[12:15], v[84:87]\n\t"
"v_mfma_f32_16x16x32_bf16 v[172:175], v[16:19], a[192:195], 0\n\t"
"v_mfma_f32_16x16x32_bf16 v[176:179], v[20:23], a[192:195], 0\n\t"
"v_mfma_f32_16x16x32_bf16 v[172:175], v[16:19], a[196:199], v[172:175]\n\t"
"v_mfma_f32_16x16x32_bf16 v[176:179], v[20:23], a[196:199], v[176:179]\n\t"
"v_mfma_f32_16x16x32_bf16 v[172:175], v[16:19], a[200:203], v[172:175]\n\t"
"v_mfma_f32_16x16x32_bf16 v[176:179], v[20:23], a[200:203], v[176:179]\n\t"
"v_mfma_f32_16x16x32_bf16 v[172:175], v[16:19], a[204:207], v[172:175]\n\t"
"v_mfma_f32_16x16x32_bf16 v[176:179], v[20:23], a[204:207], v[176:179]\n\t"
"s_waitcnt vmcnt(10)\n\t"
"v_mfma_f32_16x16x32_bf16 v[180:183], v[16:19], a[208:211], 0\n\t"
"v_mfma_f32_16x16x32_bf16 v[184:187], v[20:23], a[208:211], 0\n\t"
"s_waitcnt vmcnt(9)\n\t"
"v_mfma_f32_16x16x32_bf16 v[180:183], v[16:19], a[212:215], v[180:183]\n\t"
"v_mfma_f32_16x16x32_bf16 v[184:187], v[20:23], a[212:215], v[184:187]\n\t"
"s_waitcnt vmcnt(8)\n\t"
"v_mfma_f32_16x16x32_bf16 v[180:183], v[16:19], a[216:219], v[180:183]\n\t"
"v_mfma_f32_16x16x32_bf16 v[184:187], v[20:23], a[216:219], v[184:187]\n\t"
"s_waitcnt vmcnt(7)\n\t"
"v_mfma_f32_16x16x32_bf16 v[180:183], v[16:19], a[220:223], v[180:183]\n\t"
"v_mfma_f32_16x16x32_bf16 v[184:187], v[20:23], a[220:223], v[184:187]\n\t"
"v_exp_f32 v88, v172\n\t"
"v_exp_f32 v89, v173\n\t"
"v_exp_f32 v90, v174\n\t"
"v_exp_f32 v91, v175\n\t"
"v_exp_f32 v92, v176\n\t"
"v_exp_f32 v93, v177\n\t"
"v_exp_f32 v94, v178\n\t"
"v_exp_f32 v95, v179\n\t"
"v_mul_f32 v172, v172, v64\n\t"
"v_mul_f32 v173, v173, v65\n\t"
"v_mul_f32 v174, v174, v66\n\t"
"v_mul_f32 v175, v175, v67\n\t"
"v_mul_f32 v176, v176, v68\n\t"
"v_mul_f32 v177, v177, v69\n\t"
"v_mul_f32 v178, v178, v70\n\t"
"v_mul_f32 v179, v179, v71\n\t"
"v_add_f32 v88, 1.0, v88\n\t"
"v_add_f32 v89, 1.0, v89\n\t"
"v_add_f32 v90, 1.0, v90\n\t"
"v_add_f32 v91, 1.0, v91\n\t"
"v_add_f32 v92, 1.0, v92\n\t"
"v_add_f32 v93, 1.0, v93\n\t"
"v_add_f32 v94, 1.0, v94\n\t"
"v_add_f32 v95, 1.0, v95\n\t"
"v_rcp_f32 v88, v88\n\t"
"v_rcp_f32 v89, v89\n\t"
"v_rcp_f32 v90, v90\n\t"
"v_rcp_f32 v91, v91\n\t"
"v_rcp_f32 v92, v92\n\t"
"v_rcp_f32 v93, v93\n\t"
"v_rcp_f32 v94, v94\n\t"
"v_rcp_f32 v95, v95\n\t"
"s_waitcnt vmcnt(6)\n\t"
"v_lshlrev_b32 v64, 16, v246\n\t"
"v_and_b32 v65, s71, v246\n\t"
"v_lshlrev_b32 v66, 16, v247\n\t"
"v_and_b32 v67, s71, v247\n\t"
"s_waitcnt vmcnt(5)\n\t"
"v_lshlrev_b32 v68, 16, v248\n\t"
"v_and_b32 v69, s71, v248\n\t"
"v_lshlrev_b32 v70, 16, v249\n\t"
"v_and_b32 v71, s71, v249\n\t"
"v_mul_f32 v172, v172, v88\n\t"
"v_mul_f32 v173, v173, v89\n\t"
"v_mul_f32 v174, v174, v90\n\t"
"v_mul_f32 v175, v175, v91\n\t"
"v_mul_f32 v176, v176, v92\n\t"
"v_mul_f32 v177, v177, v93\n\t"
"v_mul_f32 v178, v178, v94\n\t"
"v_mul_f32 v179, v179, v95\n\t"
"v_fmamk_f32 v172, v172, 0xbf317218, v64\n\t"
"v_fmamk_f32 v173, v173, 0xbf317218, v65\n\t"
"v_fmamk_f32 v174, v174, 0xbf317218, v66\n\t"
"v_fmamk_f32 v175, v175, 0xbf317218, v67\n\t"
"v_fmamk_f32 v176, v176, 0xbf317218, v68\n\t"
"v_fmamk_f32 v177, v177, 0xbf317218, v69\n\t"
"v_fmamk_f32 v178, v178, 0xbf317218, v70\n\t"
"v_fmamk_f32 v179, v179, 0xbf317218, v71\n\t"
"v_cvt_pk_bf16_f32 v246, v172, v173\n\t"
"v_cvt_pk_bf16_f32 v247, v174, v175\n\t"
"v_cvt_pk_bf16_f32 v248, v176, v177\n\t"
"v_cvt_pk_bf16_f32 v249, v178, v179\n\t"
"buffer_store_dwordx2 v[246:247], v27, %[out], s59 offen offset:0 \n\t"
"buffer_store_dwordx2 v[248:249], v27, %[out], s59 offen offset:32 \n\t"
"s_waitcnt vmcnt(2)\n\t"
"v_cmp_eq_u64 vcc, v[216:217], v[254:255]\n\t"
"s_cmp_eq_u64 vcc, -1\n\t"
"s_cbranch_scc1 119f\n\t"
"s_mov_b32 s98, 65536\n\t"
"118:\n\t"
"s_sleep 3\n\t"
"buffer_load_dwordx2 v[216:217], v28, %[ws], s78 offen offset:1152 sc1\n\t"
"s_waitcnt vmcnt(0)\n\t"
"v_cmp_eq_u64 vcc, v[216:217], v[254:255]\n\t"
"s_cmp_eq_u64 vcc, -1\n\t"
"s_cbranch_scc1 119f\n\t"
"s_sub_u32 s98, s98, 1\n\t"
"s_cmp_lg_u32 s98, 0\n\t"
"s_cbranch_scc1 118b\n\t"
"119:\n\t"
"buffer_load_dwordx4 a[224:227], v244, %[ws], s74 offen offset:0 sc1\n\t"
"buffer_load_dwordx4 a[228:231], v244, %[ws], s74 offen offset:1024 sc1\n\t"
"buffer_load_dwordx4 a[232:235], v244, %[ws], s74 offen offset:2048 sc1\n\t"
"buffer_load_dwordx4 a[236:239], v244, %[ws], s74 offen offset:3072 sc1\n\t"
"v_exp_f32 v88, v180\n\t"
"v_exp_f32 v89, v181\n\t"
"v_exp_f32 v90, v182\n\t"
"v_exp_f32 v91, v183\n\t"
"v_exp_f32 v92, v184\n\t"
"v_exp_f32 v93, v185\n\t"
"v_exp_f32 v94, v186\n\t"
"v_exp_f32 v95, v187\n\t"
"v_mul_f32 v180, v180, v72\n\t"
"v_mul_f32 v181, v181, v73\n\t"
"v_mul_f32 v182, v182, v74\n\t"
"v_mul_f32 v183, v183, v75\n\t"
"v_mul_f32 v184, v184, v76\n\t"
"v_mul_f32 v185, v185, v77\n\t"
"v_mul_f32 v186, v186, v78\n\t"
"v_mul_f32 v187, v187, v79\n\t"
"v_add_f32 v88, 1.0, v88\n\t"
"v_add_f32 v89, 1.0, v89\n\t"
"v_add_f32 v90, 1.0, v90\n\t"
"v_add_f32 v91, 1.0, v91\n\t"
"v_add_f32 v92, 1.0, v92\n\t"
"v_add_f32 v93, 1.0, v93\n\t"
"v_add_f32 v94, 1.0, v94\n\t"
"v_add_f32 v95, 1.0, v95\n\t"
"v_rcp_f32 v88, v88\n\t"
"v_rcp_f32 v89, v89\n\t"
"v_rcp_f32 v90, v90\n\t"
"v_rcp_f32 v91, v91\n\t"
"v_rcp_f32 v92, v92\n\t"
"v_rcp_f32 v93, v93\n\t"
"v_rcp_f32 v94, v94\n\t"
"v_rcp_f32 v95, v95\n\t"
"v_lshlrev_b32 v72, 16, v206\n\t"
"v_and_b32 v73, s71, v206\n\t"
"v_lshlrev_b32 v74, 16, v207\n\t"
"v_and_b32 v75, s71, v207\n\t"
"v_lshlrev_b32 v76, 16, v208\n\t"
"v_and_b32 v77, s71, v208\n\t"
"v_lshlrev_b32 v78, 16, v209\n\t"
"v_and_b32 v79, s71, v209\n\t"
"v_mul_f32 v180, v180, v88\n\t"
"v_mul_f32 v181, v181, v89\n\t"
"v_mul_f32 v182, v182, v90\n\t"
"v_mul_f32 v183, v183, v91\n\t"
"v_mul_f32 v184, v184, v92\n\t"
"v_mul_f32 v185, v185, v93\n\t"
"v_mul_f32 v186, v186, v94\n\t"
"v_mul_f32 v187, v187, v95\n\t"
"v_fmamk_f32 v180, v180, 0xbf317218, v72\n\t"
"v_fmamk_f32 v181, v181, 0xbf317218, v73\n\t"
"v_fmamk_f32 v182, v182, 0xbf317218, v74\n\t"
"v_fmamk_f32 v183, v183, 0xbf317218, v75\n\t"
"v_fmamk_f32 v184, v184, 0xbf317218, v76\n\t"
"v_fmamk_f32 v185, v185, 0xbf317218, v77\n\t"
"v_fmamk_f32 v186, v186, 0xbf317218, v78\n\t"
"v_fmamk_f32 v187, v187, 0xbf317218, v79\n\t"
"v_cvt_pk_bf16_f32 v206, v180, v181\n\t"
"v_cvt_pk_bf16_f32 v207, v182, v183\n\t"
"v_cvt_pk_bf16_f32 v208, v184, v185\n\t"
"v_cvt_pk_bf16_f32 v209, v186, v187\n\t"
"buffer_store_dwordx2 v[206:207], v27, %[out], s60 offen offset:0 \n\t"
"buffer_store_dwordx2 v[208:209], v27, %[out], s60 offen offset:32 \n\t"
"v_mul_f32 v80, 0xbf317218, v80\n\t"
"v_mul_f32 v81, 0xbf317218, v81\n\t"
"v_mul_f32 v82, 0xbf317218, v82\n\t"
"v_mul_f32 v83, 0xbf317218, v83\n\t"
"v_mul_f32 v84, 0xbf317218, v84\n\t"
"v_mul_f32 v85, 0xbf317218, v85\n\t"
"v_mul_f32 v86, 0xbf317218, v86\n\t"
"v_mul_f32 v87, 0xbf317218, v87\n\t"
"v_lshlrev_b32 v218, 16, v168\n\t"
"v_and_b32 v219, s71, v168\n\t"
"v_lshlrev_b32 v220, 16, v169\n\t"
"v_and_b32 v221, s71, v169\n\t"
"v_lshlrev_b32 v222, 16, v170\n\t"
"v_and_b32 v223, s71, v170\n\t"
"v_lshlrev_b32 v224, 16, v171\n\t"
"v_and_b32 v225, s71, v171\n\t"
"s_waitcnt vmcnt(5)\n\t"
"v_mfma_f32_16x16x32_bf16 v[188:191], v[16:19], a[224:227], 0\n\t"
"v_mfma_f32_16x16x32_bf16 v[192:195], v[20:23], a[224:227], 0\n\t"
"s_waitcnt vmcnt(4)\n\t"
"v_mfma_f32_16x16x32_bf16 v[188:191], v[16:19], a[228:231], v[188:191]\n\t"
"v_mfma_f32_16x16x32_bf16 v[192:195], v[20:23], a[228:231], v[192:195]\n\t"
"s_waitcnt vmcnt(3)\n\t"
"v_mfma_f32_16x16x32_bf16 v[188:191], v[16:19], a[232:235], v[188:191]\n\t"
"v_mfma_f32_16x16x32_bf16 v[192:195], v[20:23], a[232:235], v[192:195]\n\t"
"s_waitcnt vmcnt(2)\n\t"
"v_mfma_f32_16x16x32_bf16 v[188:191], v[16:19], a[236:239], v[188:191]\n\t"
"v_mfma_f32_16x16x32_bf16 v[192:195], v[20:23], a[236:239], v[192:195]\n\t"
"s_nop 6\n\t"
"v_exp_f32 v88, v188\n\t"
"v_exp_f32 v89, v189\n\t"
"v_exp_f32 v90, v190\n\t"
"v_exp_f32 v91, v191\n\t"
"v_exp_f32 v92, v192\n\t"
"v_exp_f32 v93, v193\n\t"
"v_exp_f32 v94, v194\n\t"
"v_exp_f32 v95, v195\n\t"
"v_mul_f32 v188, v188, v80\n\t"
"v_mul_f32 v189, v189, v81\n\t"
"v_mul_f32 v190, v190, v82\n\t"
"v_mul_f32 v191, v191, v83\n\t"
"v_mul_f32 v192, v192, v84\n\t"
"v_mul_f32 v193, v193, v85\n\t"
"v_mul_f32 v194, v194, v86\n\t"
"v_mul_f32 v195, v195, v87\n\t"
"v_add_f32 v88, 1.0, v88\n\t"
"v_add_f32 v89, 1.0, v89\n\t"
"v_add_f32 v90, 1.0, v90\n\t"
"v_add_f32 v91, 1.0, v91\n\t"
"v_add_f32 v92, 1.0, v92\n\t"
"v_add_f32 v93, 1.0, v93\n\t"
"v_add_f32 v94, 1.0, v94\n\t"
"v_add_f32 v95, 1.0, v95\n\t"
"v_rcp_f32 v88, v88\n\t"
"v_rcp_f32 v89, v89\n\t"
"v_rcp_f32 v90, v90\n\t"
"v_rcp_f32 v91, v91\n\t"
"v_rcp_f32 v92, v92\n\t"
"v_rcp_f32 v93, v93\n\t"
"v_rcp_f32 v94, v94\n\t"
"v_rcp_f32 v95, v95\n\t"
"v_fmac_f32 v218, v188, v88\n\t"
"v_fmac_f32 v219, v189, v89\n\t"
"v_fmac_f32 v220, v190, v90\n\t"
"v_fmac_f32 v221, v191, v91\n\t"
"v_fmac_f32 v222, v192, v92\n\t"
"v_fmac_f32 v223, v193, v93\n\t"
"v_fmac_f32 v224, v194, v94\n\t"
"v_fmac_f32 v225, v195, v95\n\t"
"v_cvt_pk_bf16_f32 v168, v218, v219\n\t"
"v_cvt_pk_bf16_f32 v169, v220, v221\n\t"
"v_cvt_pk_bf16_f32 v170, v222, v223\n\t"
"v_cvt_pk_bf16_f32 v171, v224, v225\n\t"
"buffer_store_dwordx2 v[168:169], v27, %[out], s61 offen offset:0 \n\t"
"buffer_store_dwordx2 v[170:171], v27, %[out], s61 offen offset:32 \n\t"
"s_or_b32 s64, %[j], %[wv]\n\t"
"s_cmp_lg_u32 s64, 0\n\t"
"s_cbranch_scc1 99f\n\t"
"s_lshl_b32 s64, %[mi], 7\n\t"
"s_add_u32 s64, s64, 49829888\n\t"
"v_mov_b32 v152, 0\n\t"
"buffer_store_dwordx2 v[254:255], v152, %[ws], s64 offen offset:0 sc1\n\t"
"99:\n\t"
"s_waitcnt vmcnt(0)\n\t"
"97:\n\t"
"s_nop 0\n\t"
".Lfused_kernel_c:\n\ts_endpgm\n"
    :: [x]"s"(rx), [w1]"s"(r1), [w2]"s"(r2), [wq]"s"(rq), [wo]"s"(ro), [out]"s"(rout), [ws]"s"(rw), [wv]"s"(wv), [lds]"s"(ldsb), [mi]"s"(mi), [j]"s"(j), [rb]"s"(rb), [nc0]"s"(nc0), [slow]"s"(slow)
    : CLOBBERS);
    __builtin_unreachable();
  } else {
    const unsigned a = xcd & 3, nc0 = mi % 9;
    const __amdgpu_buffer_rsrc_t rq = SRD(wqkv - 4096, 576 * 1728 * 2 + 4096), rp = SRD(wphi, 32 * 18 * 18 * 2), ro = SRD(wo, 576 * 576 * 2), r2 = SRD(w2, 576 * 4608 * 2);
    asm volatile(
"s_nop 4\n\t"
"v_mbcnt_lo_u32_b32 v245, -1, 0\n\t"
"s_lshl_b32 s40, %[mi], 2\n\t"
"v_mbcnt_hi_u32_b32 v245, -1, v245\n\t"
"s_add_u32 s40, s40, %[wv]\n\t"
"s_mul_i32 s40, s40, 36864\n\t"
"v_lshrrev_b32 v0, 3, v245\n\t"
"v_and_b32 v1, 7, v245\n\t"
"v_mul_u32_u24 v2, 1152, v0\n\t"
"v_lshl_add_u32 v2, v1, 4, v2\n\t"
"s_add_u32 s41, s40, 9216\n\t"
"s_add_u32 s42, s40, 18432\n\t"
"s_add_u32 s43, s40, 27648\n\t"
"buffer_load_dwordx4 v[32:35], v2, %[x], s40 offen offset:0\n\t"
"buffer_load_dwordx4 v[36:39], v2, %[x], s40 offen offset:128\n\t"
"buffer_load_dwordx4 v[40:43], v2, %[x], s40 offen offset:256\n\t"
"buffer_load_dwordx4 v[44:47], v2, %[x], s40 offen offset:384\n\t"
"buffer_load_dwordx4 v[48:51], v2, %[x], s40 offen offset:512\n\t"
"buffer_load_dwordx4 v[52:55], v2, %[x], s40 offen offset:640\n\t"
"buffer_load_dwordx4 v[56:59], v2, %[x], s40 offen offset:768\n\t"
"buffer_load_dwordx4 v[60:63], v2, %[x], s40 offen offset:896\n\t"
"buffer_load_dwordx4 v[64:67], v2, %[x], s40 offen offset:1024\n\t"
"s_mul_i32 s63, %[wv], 9216\n\t"
"s_add_u32 s63, s63, %[lds]\n\t"
"s_mul_i32 s64, %[a], 288\n\t"
"s_mul_i32 s65, %[wv], 110592\n\t"
"s_add_u32 s44, s64, s65\n\t"
"s_movk_i32 s66, 3456\n\t"
"v_add_u32 v3, 0, v245\n\t"
"buffer_load_dwordx4 v[68:71], v2, %[x], s41 offen offset:0\n\t"
"v_mul_u32_u24 v4, 3641, v3\n\t"
"v_lshrrev_b32 v4, 16, v4\n\t"
"v_mul_u32_u24 v5, 18, v4\n\t"
"v_sub_u32 v3, v3, v5\n\t"
"v_and_b32 v5, 12, v4\n\t"
"v_add_u32 v5, v5, v4\n\t"
"v_lshrrev_b32 v4, 4, v4\n\t"
"buffer_load_dwordx4 v[72:75], v2, %[x], s41 offen offset:128\n\t"
"v_mul_u32_u24 v4, 12, v4\n\t"
"v_sub_u32 v5, v5, v4\n\t"
"v_lshlrev_b32 v3, 4, v3\n\t"
"v_mad_u32_u24 v3, v5, s66, v3\n\t"
"v_add_u32 v200, 4096, v3\n\t"
"v_add_u32 v3, 64, v245\n\t"
"v_mul_u32_u24 v4, 3641, v3\n\t"
"buffer_load_dwordx4 v[76:79], v2, %[x], s41 offen offset:256\n\t"
"v_lshrrev_b32 v4, 16, v4\n\t"
"v_mul_u32_u24 v5, 18, v4\n\t"
"v_sub_u32 v3, v3, v5\n\t"
"v_and_b32 v5, 12, v4\n\t"
"v_add_u32 v5, v5, v4\n\t"
"v_lshrrev_b32 v4, 4, v4\n\t"
"v_mul_u32_u24 v4, 12, v4\n\t"
"buffer_load_dwordx4 v[80:83], v2, %[x], s41 offen offset:384\n\t"
"v_sub_u32 v5, v5, v4\n\t"
"v_lshlrev_b32 v3, 4, v3\n\t"
"v_mad_u32_u24 v3, v5, s66, v3\n\t"
"v_add_u32 v201, 3072, v3\n\t"
"v_add_u32 v3, 128, v245\n\t"
"v_mul_u32_u24 v4, 3641, v3\n\t"
"v_lshrrev_b32 v4, 16, v4\n\t"
"buffer_load_dwordx4 v[84:87], v2, %[x], s41 offen offset:512\n\t"
"v_mul_u32_u24 v5, 18, v4\n\t"
"v_sub_u32 v3, v3, v5\n\t"
"v_and_b32 v5, 12, v4\n\t"
"v_add_u32 v5, v5, v4\n\t"
"v_lshrrev_b32 v4, 4, v4\n\t"
"v_mul_u32_u24 v4, 12, v4\n\t"
"v_sub_u32 v5, v5, v4\n\t"
"buffer_load_dwordx4 v[88:91], v2, %[x], s41 offen offset:640\n\t"
"v_lshlrev_b32 v3, 4, v3\n\t"
"v_mad_u32_u24 v3, v5, s66, v3\n\t"
"v_add_u32 v202, 2048, v3\n\t"
"v_add_u32 v3, 192, v245\n\t"
"v_mul_u32_u24 v4, 3641, v3\n\t"
"v_lshrrev_b32 v4, 16, v4\n\t"
"v_mul_u32_u24 v5, 18, v4\n\t"
"buffer_load_dwordx4 v[92:95], v2, %[x], s41 offen offset:768\n\t"
"v_sub_u32 v3, v3, v5\n\t"
"v_and_b32 v5, 12, v4\n\t"
"v_add_u32 v5, v5, v4\n\t"
"v_lshrrev_b32 v4, 4, v4\n\t"
"v_mul_u32_u24 v4, 12, v4\n\t"
"v_sub_u32 v5, v5, v4\n\t"
"v_lshlrev_b32 v3, 4, v3\n\t"
"buffer_load_dwordx4 v[96:99], v2, %[x], s41 offen offset:896\n\t"
"v_mad_u32_u24 v3, v5, s66, v3\n\t"
"v_add_u32 v203, 1024, v3\n\t"
"v_add_u32 v3, 256, v245\n\t"
"v_mul_u32_u24 v4, 3641, v3\n\t"
"v_lshrrev_b32 v4, 16, v4\n\t"
"v_mul_u32_u24 v5, 18, v4\n\t"
"v_sub_u32 v3, v3, v5\n\t"
"buffer_load_dwordx4 v[100:103], v2, %[x], s41 offen offset:1024\n\t"
"v_and_b32 v5, 12, v4\n\t"
"v_add_u32 v5, v5, v4\n\t"
"v_lshrrev_b32 v4, 4, v4\n\t"
"v_mul_u32_u24 v4, 12, v4\n\t"
"v_sub_u32 v5, v5, v4\n\t"
"v_lshlrev_b32 v3, 4, v3\n\t"
"v_mad_u32_u24 v3, v5, s66, v3\n\t"
"buffer_load_dwordx4 v[104:107], v2, %[x], s42 offen offset:0\n\t"
"v_add_u32 v204, 4096, v3\n\t"
"v_add_u32 v3, 320, v245\n\t"
"v_mul_u32_u24 v4, 3641, v3\n\t"
"v_lshrrev_b32 v4, 16, v4\n\t"
"v_mul_u32_u24 v5, 18, v4\n\t"
"v_sub_u32 v3, v3, v5\n\t"
"v_and_b32 v5, 12, v4\n\t"
"buffer_load_dwordx4 v[108:111], v2, %[x], s42 offen offset:128\n\t"
"v_add_u32 v5, v5, v4\n\t"
"v_lshrrev_b32 v4, 4, v4\n\t"
"v_mul_u32_u24 v4, 12, v4\n\t"
"v_sub_u32 v5, v5, v4\n\t"
"v_lshlrev_b32 v3, 4, v3\n\t"
"v_mad_u32_u24 v3, v5, s66, v3\n\t"
"v_add_u32 v205, 3072, v3\n\t"
"buffer_load_dwordx4 v[112:115], v2, %[x], s42 offen offset:256\n\t"
"v_add_u32 v3, 384, v245\n\t"
"v_mul_u32_u24 v4, 3641, v3\n\t"
"v_lshrrev_b32 v4, 16, v4\n\t"
"v_mul_u32_u24 v5, 18, v4\n\t"
"v_sub_u32 v3, v3, v5\n\t"
"v_and_b32 v5, 12, v4\n\t"
"v_add_u32 v5, v5, v4\n\t"
"buffer_load_dwordx4 v[116:119], v2, %[x], s42 offen offset:384\n\t"
"v_lshrrev_b32 v4, 4, v4\n\t"
"v_mul_u32_u24 v4, 12, v4\n\t"
"v_sub_u32 v5, v5, v4\n\t"
"v_lshlrev_b32 v3, 4, v3\n\t"
"v_mad_u32_u24 v3, v5, s66, v3\n\t"
"v_add_u32 v206, 2048, v3\n\t"
"v_add_u32 v3, 448, v245\n\t"
"buffer_load_dwordx4 v[120:123], v2, %[x], s42 offen offset:512\n\t"
"v_mul_u32_u24 v4, 3641, v3\n\t"
"v_lshrrev_b32 v4, 16, v4\n\t"
"v_mul_u32_u24 v5, 18, v4\n\t"
"v_sub_u32 v3, v3, v5\n\t"
"v_and_b32 v5, 12, v4\n\t"
"v_add_u32 v5, v5, v4\n\t"
"v_lshrrev_b32 v4, 4, v4\n\t"
"buffer_load_dwordx4 v[124:127], v2, %[x], s42 offen offset:640\n\t"
"v_mul_u32_u24 v4, 12, v4\n\t"
"v_sub_u32 v5, v5, v4\n\t"
"v_lshlrev_b32 v3, 4, v3\n\t"
"v_mad_u32_u24 v3, v5, s66, v3\n\t"
"v_add_u32 v207, 1024, v3\n\t"
"v_add_u32 v3, 512, v245\n\t"
"v_mul_u32_u24 v4, 3641, v3\n\t"
"buffer_load_dwordx4 v[128:131], v2, %[x], s42 offen offset:768\n\t"
"v_lshrrev_b32 v4, 16, v4\n\t"
"v_mul_u32_u24 v5, 18, v4\n\t"
"v_sub_u32 v3, v3, v5\n\t"
"v_and_b32 v5, 12, v4\n\t"
"v_add_u32 v5, v5, v4\n\t"
"v_lshrrev_b32 v4, 4, v4\n\t"
"v_mul_u32_u24 v4, 12, v4\n\t"
"buffer_load_dwordx4 v[132:135], v2, %[x], s42 offen offset:896\n\t"
"v_sub_u32 v5, v5, v4\n\t"
"v_lshlrev_b32 v3, 4, v3\n\t"
"v_mad_u32_u24 v3, v5, s66, v3\n\t"
"v_add_u32 v208, 4096, v3\n\t"
"s_add_u32 s64, s44, 1770624\n\t"
"s_add_u32 s65, s44, 4294748416\n\t"
"s_cmp_lt_u32 %[wv], 2\n\t"
"s_cselect_b32 s45, s64, s65\n\t"
"buffer_load_dwordx4 v[136:139], v2, %[x], s42 offen offset:1024\n\t"
"v_mov_b32 v9, 0\n\t"
"s_lshl_b32 s64, %[mi], 7\n\t"
"s_add_u32 s64, s64, 49829888\n\t"
"buffer_load_dwordx2 v[254:255], v9, %[ws], s64 offen sc1\n\t"
"s_lshl_b32 s64, %[a], 7\n\t"
"s_add_u32 s64, s64, 49831936\n\t"
"buffer_load_dwordx2 v[232:233], v9, %[ws], s64 offen sc1\n\t"
"buffer_load_dwordx4 v[140:143], v2, %[x], s43 offen offset:0\n\t"
"s_lshl_b32 s64, %[wv], 6\n\t"
"v_add_u32 v10, s64, v245\n\t"
"v_lshlrev_b32 v11, 2, v10\n\t"
"s_mul_i32 s64, %[a], 5184\n\t"
"s_add_u32 s65, s64, 4096\n\t"
"buffer_load_dword v16, v11, %[wp], s64 offen offset:0\n\t"
"buffer_load_dword v17, v11, %[wp], s64 offen offset:1024\n\t"
"buffer_load_dword v18, v11, %[wp], s64 offen offset:2048\n\t"
"buffer_load_dword v19, v11, %[wp], s64 offen offset:3072\n\t"
"buffer_load_dword v20, v11, %[wp], s65 offen offset:0\n\t"
"buffer_load_dword v21, v11, %[wp], s65 offen offset:1024\n\t"
"v_xor_b32 v5, v0, v1\n\t"
"v_lshlrev_b32 v5, 4, v5\n\t"
"v_and_b32 v6, 3, v245\n\t"
"v_lshl_add_u32 v5, v6, 8, v5\n\t"
"v_and_b32 v6, 4, v245\n\t"
"v_lshl_add_u32 v5, v6, 10, v5\n\t"
"s_lshl_b32 s65, %[wv], 10\n\t"
"s_add_u32 s66, %[lds], 78848\n\t"
"s_add_u32 s65, s65, s66\n\t"
"v_add_u32 v176, s65, v5\n\t"
"v_add_u32 v177, 32768, v176\n\t"
"v_lshrrev_b32 v5, 4, v245\n\t"
"v_add_u32 v6, 0, v5\n\t"
"v_xor_b32 v6, v6, v245\n\t"
"v_lshlrev_b32 v6, 4, v6\n\t"
"v_add_u32 v246, s65, v6\n\t"
"v_add_u32 v250, 32768, v246\n\t"
"v_add_u32 v6, 4, v5\n\t"
"v_xor_b32 v6, v6, v245\n\t"
"v_lshlrev_b32 v6, 4, v6\n\t"
"v_add_u32 v248, s65, v6\n\t"
"v_add_u32 v252, 32768, v248\n\t"
"s_mov_b32 s70, 0x3f803f80\n\t"
"s_mov_b32 s71, 0xffff0000\n\t"
"v_lshrrev_b32 v5, 2, v245\n\t"
"v_and_b32 v7, 3, v245\n\t"
"v_mul_u32_u24 v6, 288, v5\n\t"
"v_add_u32 v6, %[lds], v6\n\t"
"v_lshl_add_u32 v209, v7, 3, v6\n\t"
"v_add_u32 v210, 53248, v209\n\t"
"v_lshrrev_b32 v8, 1, v7\n\t"
"v_mul_u32_u24 v8, 72, v8\n\t"
"v_add3_u32 v211, v6, v8, 32\n\t"
"v_add_u32 v212, 53248, v211\n\t"
"buffer_load_dwordx4 v[144:147], v2, %[x], s43 offen offset:128\n\t"
"v_mov_b32 v188, 0\n\t"
"v_mov_b32 v189, 0\n\t"
"v_mov_b32 v190, 0\n\t"
"v_mov_b32 v191, 0\n\t"
"s_waitcnt vmcnt(36)\n\t"
"v_dot2c_f32_bf16 v188, s70, v32\n\t"
"v_dot2c_f32_bf16 v189, v32, v32\n\t"
"v_dot2c_f32_bf16 v190, s70, v33\n\t"
"v_dot2c_f32_bf16 v191, v33, v33\n\t"
"v_dot2c_f32_bf16 v188, s70, v34\n\t"
"v_dot2c_f32_bf16 v189, v34, v34\n\t"
"v_dot2c_f32_bf16 v190, s70, v35\n\t"
"v_dot2c_f32_bf16 v191, v35, v35\n\t"
"s_waitcnt vmcnt(35)\n\t"
"v_dot2c_f32_bf16 v188, s70, v36\n\t"
"v_dot2c_f32_bf16 v189, v36, v36\n\t"
"v_dot2c_f32_bf16 v190, s70, v37\n\t"
"v_dot2c_f32_bf16 v191, v37, v37\n\t"
"v_dot2c_f32_bf16 v188, s70, v38\n\t"
"v_dot2c_f32_bf16 v189, v38, v38\n\t"
"buffer_load_dwordx4 v[148:151], v2, %[x], s43 offen offset:256\n\t"
"v_dot2c_f32_bf16 v190, s70, v39\n\t"
"v_dot2c_f32_bf16 v191, v39, v39\n\t"
"s_waitcnt vmcnt(35)\n\t"
"v_dot2c_f32_bf16 v188, s70, v40\n\t"
"v_dot2c_f32_bf16 v189, v40, v40\n\t"
"v_dot2c_f32_bf16 v190, s70, v41\n\t"
"v_dot2c_f32_bf16 v191, v41, v41\n\t"
"v_dot2c_f32_bf16 v188, s70, v42\n\t"
"v_dot2c_f32_bf16 v189, v42, v42\n\t"
"v_dot2c_f32_bf16 v190, s70, v43\n\t"
"v_dot2c_f32_bf16 v191, v43, v43\n\t"
"s_waitcnt vmcnt(34)\n\t"
"v_dot2c_f32_bf16 v188, s70, v44\n\t"
"v_dot2c_f32_bf16 v189, v44, v44\n\t"
"v_dot2c_f32_bf16 v190, s70, v45\n\t"
"v_dot2c_f32_bf16 v191, v45, v45\n\t"
"v_dot2c_f32_bf16 v188, s70, v46\n\t"
"v_dot2c_f32_bf16 v189, v46, v46\n\t"
"v_dot2c_f32_bf16 v190, s70, v47\n\t"
"v_dot2c_f32_bf16 v191, v47, v47\n\t"
"buffer_load_dwordx4 v[152:155], v2, %[x], s43 offen offset:384\n\t"
"s_waitcnt vmcnt(34)\n\t"
"v_dot2c_f32_bf16 v188, s70, v48\n\t"
"v_dot2c_f32_bf16 v189, v48, v48\n\t"
"v_dot2c_f32_bf16 v190, s70, v49\n\t"
"v_dot2c_f32_bf16 v191, v49, v49\n\t"
"v_dot2c_f32_bf16 v188, s70, v50\n\t"
"v_dot2c_f32_bf16 v189, v50, v50\n\t"
"v_dot2c_f32_bf16 v190, s70, v51\n\t"
"v_dot2c_f32_bf16 v191, v51, v51\n\t"
"s_waitcnt vmcnt(33)\n\t"
"v_dot2c_f32_bf16 v188, s70, v52\n\t"
"v_dot2c_f32_bf16 v189, v52, v52\n\t"
"v_dot2c_f32_bf16 v190, s70, v53\n\t"
"v_dot2c_f32_bf16 v191, v53, v53\n\t"
"v_dot2c_f32_bf16 v188, s70, v54\n\t"
"v_dot2c_f32_bf16 v189, v54, v54\n\t"
"v_dot2c_f32_bf16 v190, s70, v55\n\t"
"v_dot2c_f32_bf16 v191, v55, v55\n\t"
"s_waitcnt vmcnt(32)\n\t"
"v_dot2c_f32_bf16 v188, s70, v56\n\t"
"v_dot2c_f32_bf16 v189, v56, v56\n\t"
"buffer_load_dwordx4 v[156:159], v2, %[x], s43 offen offset:512\n\t"
"v_dot2c_f32_bf16 v190, s70, v57\n\t"
"v_dot2c_f32_bf16 v191, v57, v57\n\t"
"v_dot2c_f32_bf16 v188, s70, v58\n\t"
"v_dot2c_f32_bf16 v189, v58, v58\n\t"
"v_dot2c_f32_bf16 v190, s70, v59\n\t"
"v_dot2c_f32_bf16 v191, v59, v59\n\t"
"s_waitcnt vmcnt(32)\n\t"
"v_dot2c_f32_bf16 v188, s70, v60\n\t"
"v_dot2c_f32_bf16 v189, v60, v60\n\t"
"v_dot2c_f32_bf16 v190, s70, v61\n\t"
"v_dot2c_f32_bf16 v191, v61, v61\n\t"
"v_dot2c_f32_bf16 v188, s70, v62\n\t"
"v_dot2c_f32_bf16 v189, v62, v62\n\t"
"v_dot2c_f32_bf16 v190, s70, v63\n\t"
"v_dot2c_f32_bf16 v191, v63, v63\n\t"
"s_waitcnt vmcnt(31)\n\t"
"v_dot2c_f32_bf16 v188, s70, v64\n\t"
"v_dot2c_f32_bf16 v189, v64, v64\n\t"
"v_dot2c_f32_bf16 v190, s70, v65\n\t"
"v_dot2c_f32_bf16 v191, v65, v65\n\t"
"buffer_load_dwordx4 v[160:163], v2, %[x], s43 offen offset:640\n\t"
"v_dot2c_f32_bf16 v188, s70, v66\n\t"
"v_dot2c_f32_bf16 v189, v66, v66\n\t"
"v_dot2c_f32_bf16 v190, s70, v67\n\t"
"v_dot2c_f32_bf16 v191, v67, v67\n\t"
"s_nop 1\n\t"
"v_add_f32 v188, v188, v190\n\t"
"v_add_f32 v189, v189, v191\n\t"
"s_nop 0\n\t"
"v_add_f32_dpp v188, v188, v188 quad_perm:[1,0,3,2] row_mask:0xf bank_mask:0xf\n\t"
"v_add_f32_dpp v189, v189, v189 quad_perm:[1,0,3,2] row_mask:0xf bank_mask:0xf\n\t"
"s_nop 0\n\t"
"v_add_f32_dpp v188, v188, v188 quad_perm:[2,3,0,1] row_mask:0xf bank_mask:0xf\n\t"
"v_add_f32_dpp v189, v189, v189 quad_perm:[2,3,0,1] row_mask:0xf bank_mask:0xf\n\t"
"s_nop 0\n\t"
"v_add_f32_dpp v188, v188, v188 row_half_mirror row_mask:0xf bank_mask:0xf\n\t"
"v_add_f32_dpp v189, v189, v189 row_half_mirror row_mask:0xf bank_mask:0xf\n\t"
"v_mul_f32 v188, 0x3ae38e39, v188\n\t"
"v_mul_f32 v189, 0x3ae38e39, v189\n\t"
"v_fma_f32 v189, -v188, v188, v189\n\t"
"v_max_f32 v189, 0, v189\n\t"
"v_cvt_pk_bf16_f32 v188, v188, v189\n\t"
"v_and_b32 v189, s71, v188\n\t"
"buffer_load_dwordx4 v[164:167], v2, %[x], s43 offen offset:768\n\t"
"v_lshlrev_b32 v188, 16, v188\n\t"
"v_add_f32 v189, 0x3727c5ac, v189\n\t"
"v_cvt_pk_bf16_f32 v189, v189, v189\n\t"
"v_lshlrev_b32 v189, 16, v189\n\t"
"v_sqrt_f32 v189, v189\n\t"
"s_nop 0\n\t"
"v_cvt_pk_bf16_f32 v189, v189, v189\n\t"
"v_lshlrev_b32 v189, 16, v189\n\t"
"v_rcp_f32 v189, v189\n\t"
"s_nop 0\n\t"
"v_mul_f32 v192, 0x3f800000, v189\n\t"
"v_mul_f32 v193, -v188, v192\n\t"
"v_lshlrev_b32 v180, 16, v32\n\t"
"v_and_b32 v181, s71, v32\n\t"
"v_lshlrev_b32 v182, 16, v33\n\t"
"v_and_b32 v183, s71, v33\n\t"
"v_lshlrev_b32 v184, 16, v34\n\t"
"v_and_b32 v185, s71, v34\n\t"
"v_lshlrev_b32 v186, 16, v35\n\t"
"v_and_b32 v187, s71, v35\n\t"
"buffer_load_dwordx4 v[168:171], v2, %[x], s43 offen offset:896\n\t"
"v_pk_fma_f32 v[180:181], v[180:181], v[192:193], v[192:193] op_sel:[0,0,1] op_sel_hi:[1,0,1]\n\t"
"v_pk_fma_f32 v[182:183], v[182:183], v[192:193], v[192:193] op_sel:[0,0,1] op_sel_hi:[1,0,1]\n\t"
"v_pk_fma_f32 v[184:185], v[184:185], v[192:193], v[192:193] op_sel:[0,0,1] op_sel_hi:[1,0,1]\n\t"
"v_pk_fma_f32 v[186:187], v[186:187], v[192:193], v[192:193] op_sel:[0,0,1] op_sel_hi:[1,0,1]\n\t"
"v_cvt_pk_bf16_f32 v32, v180, v181\n\t"
"v_cvt_pk_bf16_f32 v33, v182, v183\n\t"
"v_cvt_pk_bf16_f32 v34, v184, v185\n\t"
"v_cvt_pk_bf16_f32 v35, v186, v187\n\t"
"ds_write_b128 v176, v[32:35] offset:0\n\t"
"v_lshlrev_b32 v180, 16, v36\n\t"
"v_and_b32 v181, s71, v36\n\t"
"v_lshlrev_b32 v182, 16, v37\n\t"
"v_and_b32 v183, s71, v37\n\t"
"v_lshlrev_b32 v184, 16, v38\n\t"
"v_and_b32 v185, s71, v38\n\t"
"v_lshlrev_b32 v186, 16, v39\n\t"
"v_and_b32 v187, s71, v39\n\t"
"v_pk_fma_f32 v[180:181], v[180:181], v[192:193], v[192:193] op_sel:[0,0,1] op_sel_hi:[1,0,1]\n\t"
"buffer_load_dwordx4 v[172:175], v2, %[x], s43 offen offset:1024\n\t"
"v_pk_fma_f32 v[182:183], v[182:183], v[192:193], v[192:193] op_sel:[0,0,1] op_sel_hi:[1,0,1]\n\t"
"v_pk_fma_f32 v[184:185], v[184:185], v[192:193], v[192:193] op_sel:[0,0,1] op_sel_hi:[1,0,1]\n\t"
"v_pk_fma_f32 v[186:187], v[186:187], v[192:193], v[192:193] op_sel:[0,0,1] op_sel_hi:[1,0,1]\n\t"
"v_cvt_pk_bf16_f32 v36, v180, v181\n\t"
"v_cvt_pk_bf16_f32 v37, v182, v183\n\t"
"v_cvt_pk_bf16_f32 v38, v184, v185\n\t"
"v_cvt_pk_bf16_f32 v39, v186, v187\n\t"
"ds_write_b128 v176, v[36:39] offset:8192\n\t"
"v_lshlrev_b32 v180, 16, v40\n\t"
"v_and_b32 v181, s71, v40\n\t"
"v_lshlrev_b32 v182, 16, v41\n\t"
"v_and_b32 v183, s71, v41\n\t"
"v_lshlrev_b32 v184, 16, v42\n\t"
"v_and_b32 v185, s71, v42\n\t"
"v_lshlrev_b32 v186, 16, v43\n\t"
"v_and_b32 v187, s71, v43\n\t"
"v_pk_fma_f32 v[180:181], v[180:181], v[192:193], v[192:193] op_sel:[0,0,1] op_sel_hi:[1,0,1]\n\t"
"v_pk_fma_f32 v[182:183], v[182:183], v[192:193], v[192:193] op_sel:[0,0,1] op_sel_hi:[1,0,1]\n\t"
"s_add_u32 s68, s44, 1152\n\t"
"s_add_u32 m0, s63, 0\n\t"
"s_nop 0\n\t"
"buffer_load_dwordx4 v200, %[wq], s68 offen offset:0 lds\n\t"
"v_pk_fma_f32 v[184:185], v[184:185], v[192:193], v[192:193] op_sel:[0,0,1] op_sel_hi:[1,0,1]\n\t"
"v_pk_fma_f32 v[186:187], v[186:187], v[192:193], v[192:193] op_sel:[0,0,1] op_sel_hi:[1,0,1]\n\t"
"v_cvt_pk_bf16_f32 v40, v180, v181\n\t"
"v_cvt_pk_bf16_f32 v41, v182, v183\n\t"
"v_cvt_pk_bf16_f32 v42, v184, v185\n\t"
"v_cvt_pk_bf16_f32 v43, v186, v187\n\t"
"ds_write_b128 v176, v[40:43] offset:16384\n\t"
"v_lshlrev_b32 v180, 16, v44\n\t"
"v_and_b32 v181, s71, v44\n\t"
"v_lshlrev_b32 v182, 16, v45\n\t"
"v_and_b32 v183, s71, v45\n\t"
"v_lshlrev_b32 v184, 16, v46\n\t"
"v_and_b32 v185, s71, v46\n\t"
"v_lshlrev_b32 v186, 16, v47\n\t"
"v_and_b32 v187, s71, v47\n\t"
"v_pk_fma_f32 v[180:181], v[180:181], v[192:193], v[192:193] op_sel:[0,0,1] op_sel_hi:[1,0,1]\n\t"
"v_pk_fma_f32 v[182:183], v[182:183], v[192:193], v[192:193] op_sel:[0,0,1] op_sel_hi:[1,0,1]\n\t"
"v_pk_fma_f32 v[184:185], v[184:185], v[192:193], v[192:193] op_sel:[0,0,1] op_sel_hi:[1,0,1]\n\t"
"buffer_load_dwordx4 v201, %[wq], s68 offen offset:1024 lds\n\t"
"v_pk_fma_f32 v[186:187], v[186:187], v[192:193], v[192:193] op_sel:[0,0,1] op_sel_hi:[1,0,1]\n\t"
"v_cvt_pk_bf16_f32 v44, v180, v181\n\t"
"v_cvt_pk_bf16_f32 v45, v182, v183\n\t"
"v_cvt_pk_bf16_f32 v46, v184, v185\n\t"
"v_cvt_pk_bf16_f32 v47, v186, v187\n\t"
"ds_write_b128 v176, v[44:47] offset:24576\n\t"
"v_lshlrev_b32 v180, 16, v48\n\t"
"v_and_b32 v181, s71, v48\n\t"
"v_lshlrev_b32 v182, 16, v49\n\t"
"v_and_b32 v183, s71, v49\n\t"
"v_lshlrev_b32 v184, 16, v50\n\t"
"v_and_b32 v185, s71, v50\n\t"
"v_lshlrev_b32 v186, 16, v51\n\t"
"v_and_b32 v187, s71, v51\n\t"
"v_mov_b32 v188, 0\n\t"
"v_mov_b32 v189, 0\n\t"
"v_mov_b32 v190, 0\n\t"
"v_mov_b32 v191, 0\n\t"
"buffer_load_dwordx4 v202, %[wq], s68 offen offset:2048 lds\n\t"
"s_waitcnt vmcnt(37)\n\t"
"v_dot2c_f32_bf16 v188, s70, v68\n\t"
"v_dot2c_f32_bf16 v189, v68, v68\n\t"
"v_dot2c_f32_bf16 v190, s70, v69\n\t"
"v_dot2c_f32_bf16 v191, v69, v69\n\t"
"v_dot2c_f32_bf16 v188, s70, v70\n\t"
"v_dot2c_f32_bf16 v189, v70, v70\n\t"
"v_dot2c_f32_bf16 v190, s70, v71\n\t"
"v_dot2c_f32_bf16 v191, v71, v71\n\t"
"s_waitcnt vmcnt(36)\n\t"
"v_dot2c_f32_bf16 v188, s70, v72\n\t"
"v_dot2c_f32_bf16 v189, v72, v72\n\t"
"v_dot2c_f32_bf16 v190, s70, v73\n\t"
"v_dot2c_f32_bf16 v191, v73, v73\n\t"
"v_dot2c_f32_bf16 v188, s70, v74\n\t"
"v_dot2c_f32_bf16 v189, v74, v74\n\t"
"v_dot2c_f32_bf16 v190, s70, v75\n\t"
"v_dot2c_f32_bf16 v191, v75, v75\n\t"
"s_waitcnt vmcnt(35)\n\t"
"v_dot2c_f32_bf16 v188, s70, v76\n\t"
"v_dot2c_f32_bf16 v189, v76, v76\n\t"
"buffer_load_dwordx4 v203, %[wq], s68 offen offset:3072 lds\n\t"
"v_dot2c_f32_bf16 v190, s70, v77\n\t"
"v_dot2c_f32_bf16 v191, v77, v77\n\t"
"v_dot2c_f32_bf16 v188, s70, v78\n\t"
"v_dot2c_f32_bf16 v189, v78, v78\n\t"
"v_dot2c_f32_bf16 v190, s70, v79\n\t"
"v_dot2c_f32_bf16 v191, v79, v79\n\t"
"s_waitcnt vmcnt(35)\n\t"
"v_dot2c_f32_bf16 v188, s70, v80\n\t"
"v_dot2c_f32_bf16 v189, v80, v80\n\t"
"v_dot2c_f32_bf16 v190, s70, v81\n\t"
"v_dot2c_f32_bf16 v191, v81, v81\n\t"
"v_dot2c_f32_bf16 v188, s70, v82\n\t"
"v_dot2c_f32_bf16 v189, v82, v82\n\t"
"v_dot2c_f32_bf16 v190, s70, v83\n\t"
"v_dot2c_f32_bf16 v191, v83, v83\n\t"
"s_waitcnt vmcnt(34)\n\t"
"v_dot2c_f32_bf16 v188, s70, v84\n\t"
"v_dot2c_f32_bf16 v189, v84, v84\n\t"
"v_dot2c_f32_bf16 v190, s70, v85\n\t"
"v_dot2c_f32_bf16 v191, v85, v85\n\t"
"s_add_u32 m0, s63, 4096\n\t"
"s_nop 0\n\t"
"buffer_load_dwordx4 v204, %[wq], s68 offen offset:0 lds\n\t"
"v_dot2c_f32_bf16 v188, s70, v86\n\t"
"v_dot2c_f32_bf16 v189, v86, v86\n\t"
"v_dot2c_f32_bf16 v190, s70, v87\n\t"
"v_dot2c_f32_bf16 v191, v87, v87\n\t"
"s_waitcnt vmcnt(34)\n\t"
"v_dot2c_f32_bf16 v188, s70, v88\n\t"
"v_dot2c_f32_bf16 v189, v88, v88\n\t"
"v_dot2c_f32_bf16 v190, s70, v89\n\t"
"v_dot2c_f32_bf16 v191, v89, v89\n\t"
"v_dot2c_f32_bf16 v188, s70, v90\n\t"
"v_dot2c_f32_bf16 v189, v90, v90\n\t"
"v_dot2c_f32_bf16 v190, s70, v91\n\t"
"v_dot2c_f32_bf16 v191, v91, v91\n\t"
"s_waitcnt vmcnt(33)\n\t"
"v_dot2c_f32_bf16 v188, s70, v92\n\t"
"v_dot2c_f32_bf16 v189, v92, v92\n\t"
"v_dot2c_f32_bf16 v190, s70, v93\n\t"
"v_dot2c_f32_bf16 v191, v93, v93\n\t"
"v_dot2c_f32_bf16 v188, s70, v94\n\t"
"v_dot2c_f32_bf16 v189, v94, v94\n\t"
"buffer_load_dwordx4 v205, %[wq], s68 offen offset:1024 lds\n\t"
"v_dot2c_f32_bf16 v190, s70, v95\n\t"
"v_dot2c_f32_bf16 v191, v95, v95\n\t"
"s_waitcnt vmcnt(33)\n\t"
"v_dot2c_f32_bf16 v188, s70, v96\n\t"
"v_dot2c_f32_bf16 v189, v96, v96\n\t"
"v_dot2c_f32_bf16 v190, s70, v97\n\t"
"v_dot2c_f32_bf16 v191, v97, v97\n\t"
"v_dot2c_f32_bf16 v188, s70, v98\n\t"
"v_dot2c_f32_bf16 v189, v98, v98\n\t"
"v_dot2c_f32_bf16 v190, s70, v99\n\t"
"v_dot2c_f32_bf16 v191, v99, v99\n\t"
"s_waitcnt vmcnt(32)\n\t"
"v_dot2c_f32_bf16 v188, s70, v100\n\t"
"v_dot2c_f32_bf16 v189, v100, v100\n\t"
"v_dot2c_f32_bf16 v190, s70, v101\n\t"
"v_dot2c_f32_bf16 v191, v101, v101\n\t"
"v_dot2c_f32_bf16 v188, s70, v102\n\t"
"v_dot2c_f32_bf16 v189, v102, v102\n\t"
"v_dot2c_f32_bf16 v190, s70, v103\n\t"
"v_dot2c_f32_bf16 v191, v103, v103\n\t"
"buffer_load_dwordx4 v206, %[wq], s68 offen offset:2048 lds\n\t"
"v_pk_fma_f32 v[180:181], v[180:181], v[192:193], v[192:193] op_sel:[0,0,1] op_sel_hi:[1,0,1]\n\t"
"v_add_f32 v188, v188, v190\n\t"
"v_pk_fma_f32 v[182:183], v[182:183], v[192:193], v[192:193] op_sel:[0,0,1] op_sel_hi:[1,0,1]\n\t"
"v_pk_fma_f32 v[184:185], v[184:185], v[192:193], v[192:193] op_sel:[0,0,1] op_sel_hi:[1,0,1]\n\t"
"v_pk_fma_f32 v[186:187], v[186:187], v[192:193], v[192:193] op_sel:[0,0,1] op_sel_hi:[1,0,1]\n\t"
"v_add_f32 v189, v189, v191\n\t"
"v_cvt_pk_bf16_f32 v48, v180, v181\n\t"
"v_cvt_pk_bf16_f32 v49, v182, v183\n\t"
"v_cvt_pk_bf16_f32 v50, v184, v185\n\t"
"v_add_f32_dpp v188, v188, v188 quad_perm:[1,0,3,2] row_mask:0xf bank_mask:0xf\n\t"
"v_cvt_pk_bf16_f32 v51, v186, v187\n\t"
"ds_write_b128 v177, v[48:51] offset:0\n\t"
"v_lshlrev_b32 v180, 16, v52\n\t"
"v_add_f32_dpp v189, v189, v189 quad_perm:[1,0,3,2] row_mask:0xf bank_mask:0xf\n\t"
"v_and_b32 v181, s71, v52\n\t"
"v_lshlrev_b32 v182, 16, v53\n\t"
"v_and_b32 v183, s71, v53\n\t"
"v_add_f32_dpp v188, v188, v188 quad_perm:[2,3,0,1] row_mask:0xf bank_mask:0xf\n\t"
"buffer_load_dwordx4 v207, %[wq], s68 offen offset:3072 lds\n\t"
"v_lshlrev_b32 v184, 16, v54\n\t"
"v_and_b32 v185, s71, v54\n\t"
"v_lshlrev_b32 v186, 16, v55\n\t"
"v_and_b32 v187, s71, v55\n\t"
"v_add_f32_dpp v189, v189, v189 quad_perm:[2,3,0,1] row_mask:0xf bank_mask:0xf\n\t"
"v_pk_fma_f32 v[180:181], v[180:181], v[192:193], v[192:193] op_sel:[0,0,1] op_sel_hi:[1,0,1]\n\t"
"v_pk_fma_f32 v[182:183], v[182:183], v[192:193], v[192:193] op_sel:[0,0,1] op_sel_hi:[1,0,1]\n\t"
"v_pk_fma_f32 v[184:185], v[184:185], v[192:193], v[192:193] op_sel:[0,0,1] op_sel_hi:[1,0,1]\n\t"
"v_add_f32_dpp v188, v188, v188 row_half_mirror row_mask:0xf bank_mask:0xf\n\t"
"v_pk_fma_f32 v[186:187], v[186:187], v[192:193], v[192:193] op_sel:[0,0,1] op_sel_hi:[1,0,1]\n\t"
"v_cvt_pk_bf16_f32 v52, v180, v181\n\t"
"v_cvt_pk_bf16_f32 v53, v182, v183\n\t"
"v_add_f32_dpp v189, v189, v189 row_half_mirror row_mask:0xf bank_mask:0xf\n\t"
"v_cvt_pk_bf16_f32 v54, v184, v185\n\t"
"v_cvt_pk_bf16_f32 v55, v186, v187\n\t"
"ds_write_b128 v177, v[52:55] offset:8192\n\t"
"v_mul_f32 v188, 0x3ae38e39, v188\n\t"
"v_lshlrev_b32 v180, 16, v56\n\t"
"s_add_u32 m0, s63, 8192\n\t"
"s_nop 0\n\t"
"buffer_load_dwordx4 v208, %[wq], s68 offen offset:0 lds\n\t"
"v_and_b32 v181, s71, v56\n\t"
"v_lshlrev_b32 v182, 16, v57\n\t"
"v_mul_f32 v189, 0x3ae38e39, v189\n\t"
"v_and_b32 v183, s71, v57\n\t"
"v_lshlrev_b32 v184, 16, v58\n\t"
"v_and_b32 v185, s71, v58\n\t"
"v_lshlrev_b32 v186, 16, v59\n\t"
"v_fma_f32 v189, -v188, v188, v189\n\t"
"v_and_b32 v187, s71, v59\n\t"
"v_pk_fma_f32 v[180:181], v[180:181], v[192:193], v[192:193] op_sel:[0,0,1] op_sel_hi:[1,0,1]\n\t"
"v_pk_fma_f32 v[182:183], v[182:183], v[192:193], v[192:193] op_sel:[0,0,1] op_sel_hi:[1,0,1]\n\t"
"v_max_f32 v189, 0, v189\n\t"
"v_pk_fma_f32 v[184:185], v[184:185], v[192:193], v[192:193] op_sel:[0,0,1] op_sel_hi:[1,0,1]\n\t"
"v_pk_fma_f32 v[186:187], v[186:187], v[192:193], v[192:193] op_sel:[0,0,1] op_sel_hi:[1,0,1]\n\t"
"v_cvt_pk_bf16_f32 v56, v180, v181\n\t"
"v_cvt_pk_bf16_f32 v188, v188, v189\n\t"
"v_cvt_pk_bf16_f32 v57, v182, v183\n\t"
"v_cvt_pk_bf16_f32 v58, v184, v185\n\t"
"s_add_u32 s68, s44, 443520\n\t"
"s_add_u32 m0, s63, 37888\n\t"
"s_nop 0\n\t"
"buffer_load_dwordx4 v200, %[wq], s68 offen offset:0 lds\n\t"
"v_cvt_pk_bf16_f32 v59, v186, v187\n\t"
"v_and_b32 v189, s71, v188\n\t"
"ds_write_b128 v177, v[56:59] offset:16384\n\t"
"v_lshlrev_b32 v180, 16, v60\n\t"
"v_and_b32 v181, s71, v60\n\t"
"v_lshlrev_b32 v188, 16, v188\n\t"
"v_lshlrev_b32 v182, 16, v61\n\t"
"v_and_b32 v183, s71, v61\n\t"
"v_lshlrev_b32 v184, 16, v62\n\t"
"v_and_b32 v185, s71, v62\n\t"
"v_add_f32 v189, 0x3727c5ac, v189\n\t"
"v_lshlrev_b32 v186, 16, v63\n\t"
"v_and_b32 v187, s71, v63\n\t"
"v_pk_fma_f32 v[180:181], v[180:181], v[192:193], v[192:193] op_sel:[0,0,1] op_sel_hi:[1,0,1]\n\t"
"v_cvt_pk_bf16_f32 v189, v189, v189\n\t"
"v_pk_fma_f32 v[182:183], v[182:183], v[192:193], v[192:193] op_sel:[0,0,1] op_sel_hi:[1,0,1]\n\t"
"v_pk_fma_f32 v[184:185], v[184:185], v[192:193], v[192:193] op_sel:[0,0,1] op_sel_hi:[1,0,1]\n\t"
"v_pk_fma_f32 v[186:187], v[186:187], v[192:193], v[192:193] op_sel:[0,0,1] op_sel_hi:[1,0,1]\n\t"
"buffer_load_dwordx4 v201, %[wq], s68 offen offset:1024 lds\n\t"
"v_lshlrev_b32 v189, 16, v189\n\t"
"v_cvt_pk_bf16_f32 v60, v180, v181\n\t"
"v_cvt_pk_bf16_f32 v61, v182, v183\n\t"
"v_cvt_pk_bf16_f32 v62, v184, v185\n\t"
"v_sqrt_f32 v189, v189\n\t"
"v_cvt_pk_bf16_f32 v63, v186, v187\n\t"
"ds_write_b128 v177, v[60:63] offset:24576\n\t"
"v_lshlrev_b32 v180, 16, v64\n\t"
"v_cvt_pk_bf16_f32 v189, v189, v189\n\t"
"v_and_b32 v181, s71, v64\n\t"
"v_lshlrev_b32 v182, 16, v65\n\t"
"v_and_b32 v183, s71, v65\n\t"
"v_lshlrev_b32 v184, 16, v66\n\t"
"v_lshlrev_b32 v189, 16, v189\n\t"
"v_and_b32 v185, s71, v66\n\t"
"v_lshlrev_b32 v186, 16, v67\n\t"
"v_and_b32 v187, s71, v67\n\t"
"v_rcp_f32 v189, v189\n\t"
"buffer_load_dwordx4 v202, %[wq], s68 offen offset:2048 lds\n\t"
"v_pk_fma_f32 v[180:181], v[180:181], v[192:193], v[192:193] op_sel:[0,0,1] op_sel_hi:[1,0,1]\n\t"
"v_pk_fma_f32 v[182:183], v[182:183], v[192:193], v[192:193] op_sel:[0,0,1] op_sel_hi:[1,0,1]\n\t"
"v_pk_fma_f32 v[184:185], v[184:185], v[192:193], v[192:193] op_sel:[0,0,1] op_sel_hi:[1,0,1]\n\t"
"v_mul_f32 v194, 0x3f800000, v189\n\t"
"v_pk_fma_f32 v[186:187], v[186:187], v[192:193], v[192:193] op_sel:[0,0,1] op_sel_hi:[1,0,1]\n\t"
"v_cvt_pk_bf16_f32 v64, v180, v181\n\t"
"v_cvt_pk_bf16_f32 v65, v182, v183\n\t"
"v_mul_f32 v195, -v188, v194\n\t"
"v_cvt_pk_bf16_f32 v66, v184, v185\n\t"
"v_cvt_pk_bf16_f32 v67, v186, v187\n\t"
"ds_write_b128 v177, v[64:67] offset:32768\n\t"
"v_lshlrev_b32 v180, 16, v68\n\t"
"v_and_b32 v181, s71, v68\n\t"
"v_lshlrev_b32 v182, 16, v69\n\t"
"v_and_b32 v183, s71, v69\n\t"
"v_lshlrev_b32 v184, 16, v70\n\t"
"v_and_b32 v185, s71, v70\n\t"
"v_lshlrev_b32 v186, 16, v71\n\t"
"buffer_load_dwordx4 v203, %[wq], s68 offen offset:3072 lds\n\t"
"v_and_b32 v187, s71, v71\n\t"
"v_pk_fma_f32 v[180:181], v[180:181], v[194:195], v[194:195] op_sel:[0,0,1] op_sel_hi:[1,0,1]\n\t"
"v_pk_fma_f32 v[182:183], v[182:183], v[194:195], v[194:195] op_sel:[0,0,1] op_sel_hi:[1,0,1]\n\t"
"v_pk_fma_f32 v[184:185], v[184:185], v[194:195], v[194:195] op_sel:[0,0,1] op_sel_hi:[1,0,1]\n\t"
"v_pk_fma_f32 v[186:187], v[186:187], v[194:195], v[194:195] op_sel:[0,0,1] op_sel_hi:[1,0,1]\n\t"
"v_cvt_pk_bf16_f32 v68, v180, v181\n\t"
"v_cvt_pk_bf16_f32 v69, v182, v183\n\t"
"v_cvt_pk_bf16_f32 v70, v184, v185\n\t"
"v_cvt_pk_bf16_f32 v71, v186, v187\n\t"
"ds_write_b128 v176, v[68:71] offset:128\n\t"
"v_lshlrev_b32 v180, 16, v72\n\t"
"v_and_b32 v181, s71, v72\n\t"
"v_lshlrev_b32 v182, 16, v73\n\t"
"v_and_b32 v183, s71, v73\n\t"
"v_lshlrev_b32 v184, 16, v74\n\t"
"v_and_b32 v185, s71, v74\n\t"
"v_lshlrev_b32 v186, 16, v75\n\t"
"v_and_b32 v187, s71, v75\n\t"
"s_add_u32 m0, s63, 41984\n\t"
"s_nop 0\n\t"
"buffer_load_dwordx4 v204, %[wq], s68 offen offset:0 lds\n\t"
"v_pk_fma_f32 v[180:181], v[180:181], v[194:195], v[194:195] op_sel:[0,0,1] op_sel_hi:[1,0,1]\n\t"
"v_pk_fma_f32 v[182:183], v[182:183], v[194:195], v[194:195] op_sel:[0,0,1] op_sel_hi:[1,0,1]\n\t"
"v_pk_fma_f32 v[184:185], v[184:185], v[194:195], v[194:195] op_sel:[0,0,1] op_sel_hi:[1,0,1]\n\t"
"v_pk_fma_f32 v[186:187], v[186:187], v[194:195], v[194:195] op_sel:[0,0,1] op_sel_hi:[1,0,1]\n\t"
"v_cvt_pk_bf16_f32 v72, v180, v181\n\t"
"v_cvt_pk_bf16_f32 v73, v182, v183\n\t"
"v_cvt_pk_bf16_f32 v74, v184, v185\n\t"
"v_cvt_pk_bf16_f32 v75, v186, v187\n\t"
"ds_write_b128 v176, v[72:75] offset:8320\n\t"
"v_lshlrev_b32 v180, 16, v76\n\t"
"v_and_b32 v181, s71, v76\n\t"
"v_lshlrev_b32 v182, 16, v77\n\t"
"v_and_b32 v183, s71, v77\n\t"
"v_lshlrev_b32 v184, 16, v78\n\t"
"v_and_b32 v185, s71, v78\n\t"
"v_lshlrev_b32 v186, 16, v79\n\t"
"v_and_b32 v187, s71, v79\n\t"
"v_pk_fma_f32 v[180:181], v[180:181], v[194:195], v[194:195] op_sel:[0,0,1] op_sel_hi:[1,0,1]\n\t"
"buffer_load_dwordx4 v205, %[wq], s68 offen offset:1024 lds\n\t"
"v_pk_fma_f32 v[182:183], v[182:183], v[194:195], v[194:195] op_sel:[0,0,1] op_sel_hi:[1,0,1]\n\t"
"v_pk_fma_f32 v[184:185], v[184:185], v[194:195], v[194:195] op_sel:[0,0,1] op_sel_hi:[1,0,1]\n\t"
"v_pk_fma_f32 v[186:187], v[186:187], v[194:195], v[194:195] op_sel:[0,0,1] op_sel_hi:[1,0,1]\n\t"
"v_cvt_pk_bf16_f32 v76, v180, v181\n\t"
"v_cvt_pk_bf16_f32 v77, v182, v183\n\t"
"v_cvt_pk_bf16_f32 v78, v184, v185\n\t"
"v_cvt_pk_bf16_f32 v79, v186, v187\n\t"
"ds_write_b128 v176, v[76:79] offset:16512\n\t"
"v_lshlrev_b32 v180, 16, v80\n\t"
"v_and_b32 v181, s71, v80\n\t"
"v_lshlrev_b32 v182, 16, v81\n\t"
"v_and_b32 v183, s71, v81\n\t"
"v_lshlrev_b32 v184, 16, v82\n\t"
"v_and_b32 v185, s71, v82\n\t"
"v_lshlrev_b32 v186, 16, v83\n\t"
"v_and_b32 v187, s71, v83\n\t"
"v_pk_fma_f32 v[180:181], v[180:181], v[194:195], v[194:195] op_sel:[0,0,1] op_sel_hi:[1,0,1]\n\t"
"v_pk_fma_f32 v[182:183], v[182:183], v[194:195], v[194:195] op_sel:[0,0,1] op_sel_hi:[1,0,1]\n\t"
"buffer_load_dwordx4 v206, %[wq], s68 offen offset:2048 lds\n\t"
"v_pk_fma_f32 v[184:185], v[184:185], v[194:195], v[194:195] op_sel:[0,0,1] op_sel_hi:[1,0,1]\n\t"
"v_pk_fma_f32 v[186:187], v[186:187], v[194:195], v[194:195] op_sel:[0,0,1] op_sel_hi:[1,0,1]\n\t"
"v_cvt_pk_bf16_f32 v80, v180, v181\n\t"
"v_cvt_pk_bf16_f32 v81, v182, v183\n\t"
"v_cvt_pk_bf16_f32 v82, v184, v185\n\t"
"v_cvt_pk_bf16_f32 v83, v186, v187\n\t"
"ds_write_b128 v176, v[80:83] offset:24704\n\t"
"v_lshlrev_b32 v180, 16, v84\n\t"
"v_and_b32 v181, s71, v84\n\t"
"v_lshlrev_b32 v182, 16, v85\n\t"
"v_and_b32 v183, s71, v85\n\t"
"v_lshlrev_b32 v184, 16, v86\n\t"
"v_and_b32 v185, s71, v86\n\t"
"v_lshlrev_b32 v186, 16, v87\n\t"
"v_and_b32 v187, s71, v87\n\t"
"v_mov_b32 v188, 0\n\t"
"v_mov_b32 v189, 0\n\t"
"v_mov_b32 v190, 0\n\t"
"buffer_load_dwordx4 v207, %[wq], s68 offen offset:3072 lds\n\t"
"v_mov_b32 v191, 0\n\t"
"s_waitcnt vmcnt(42)\n\t"
"v_dot2c_f32_bf16 v188, s70, v104\n\t"
"v_dot2c_f32_bf16 v189, v104, v104\n\t"
"v_dot2c_f32_bf16 v190, s70, v105\n\t"
"v_dot2c_f32_bf16 v191, v105, v105\n\t"
"v_dot2c_f32_bf16 v188, s70, v106\n\t"
"v_dot2c_f32_bf16 v189, v106, v106\n\t"
"v_dot2c_f32_bf16 v190, s70, v107\n\t"
"v_dot2c_f32_bf16 v191, v107, v107\n\t"
"s_waitcnt vmcnt(41)\n\t"
"v_dot2c_f32_bf16 v188, s70, v108\n\t"
"v_dot2c_f32_bf16 v189, v108, v108\n\t"
"v_dot2c_f32_bf16 v190, s70, v109\n\t"
"v_dot2c_f32_bf16 v191, v109, v109\n\t"
"v_dot2c_f32_bf16 v188, s70, v110\n\t"
"v_dot2c_f32_bf16 v189, v110, v110\n\t"
"v_dot2c_f32_bf16 v190, s70, v111\n\t"
"v_dot2c_f32_bf16 v191, v111, v111\n\t"
"s_waitcnt vmcnt(40)\n\t"
"v_dot2c_f32_bf16 v188, s70, v112\n\t"
"s_add_u32 m0, s63, 46080\n\t"
"s_nop 0\n\t"
"buffer_load_dwordx4 v208, %[wq], s68 offen offset:0 lds\n\t"
"v_dot2c_f32_bf16 v189, v112, v112\n\t"
"v_dot2c_f32_bf16 v190, s70, v113\n\t"
"v_dot2c_f32_bf16 v191, v113, v113\n\t"
"v_dot2c_f32_bf16 v188, s70, v114\n\t"
"v_dot2c_f32_bf16 v189, v114, v114\n\t"
"v_dot2c_f32_bf16 v190, s70, v115\n\t"
"v_dot2c_f32_bf16 v191, v115, v115\n\t"
"s_waitcnt vmcnt(40)\n\t"
"v_dot2c_f32_bf16 v188, s70, v116\n\t"
"v_dot2c_f32_bf16 v189, v116, v116\n\t"
"v_dot2c_f32_bf16 v190, s70, v117\n\t"
"v_dot2c_f32_bf16 v191, v117, v117\n\t"
"v_dot2c_f32_bf16 v188, s70, v118\n\t"
"v_dot2c_f32_bf16 v189, v118, v118\n\t"
"v_dot2c_f32_bf16 v190, s70, v119\n\t"
"v_dot2c_f32_bf16 v191, v119, v119\n\t"
"s_waitcnt vmcnt(39)\n\t"
"v_dot2c_f32_bf16 v188, s70, v120\n\t"
"v_dot2c_f32_bf16 v189, v120, v120\n\t"
"v_dot2c_f32_bf16 v190, s70, v121\n\t"
"s_and_b32 s73, %[mi], 7\n\t"
"s_lshl_b32 s73, s73, 2\n\t"
"s_add_u32 s73, s73, %[wv]\n\t"
"s_mul_i32 s62, s73, 62208\n\t"
"s_mul_i32 s75, %[a], 288\n\t"
"s_and_b32 s75, s75, 0xffffff80\n\t"
"s_add_u32 s62, s62, s75\n\t"
"s_add_u32 s62, s62, 4096\n\t"
"v_mul_u32_u24 v28, 43, v245\n\t"
"v_lshrrev_b32 v28, 7, v28\n\t"
"v_mul_u32_u24 v28, 3072, v28\n\t"
"v_lshl_add_u32 v28, v245, 7, v28\n\t"
"buffer_load_dword v29, v28, %[wq], s62 offen offset:1152\n\t"
"v_dot2c_f32_bf16 v191, v121, v121\n\t"
"v_dot2c_f32_bf16 v188, s70, v122\n\t"
"v_dot2c_f32_bf16 v189, v122, v122\n\t"
"v_dot2c_f32_bf16 v190, s70, v123\n\t"
"v_dot2c_f32_bf16 v191, v123, v123\n\t"
"s_waitcnt vmcnt(39)\n\t"
"v_dot2c_f32_bf16 v188, s70, v124\n\t"
"v_dot2c_f32_bf16 v189, v124, v124\n\t"
"v_dot2c_f32_bf16 v190, s70, v125\n\t"
"v_dot2c_f32_bf16 v191, v125, v125\n\t"
"v_dot2c_f32_bf16 v188, s70, v126\n\t"
"v_dot2c_f32_bf16 v189, v126, v126\n\t"
"v_dot2c_f32_bf16 v190, s70, v127\n\t"
"v_dot2c_f32_bf16 v191, v127, v127\n\t"
"s_waitcnt vmcnt(38)\n\t"
"v_dot2c_f32_bf16 v188, s70, v128\n\t"
"v_dot2c_f32_bf16 v189, v128, v128\n\t"
"v_dot2c_f32_bf16 v190, s70, v129\n\t"
"v_dot2c_f32_bf16 v191, v129, v129\n\t"
"v_dot2c_f32_bf16 v188, s70, v130\n\t"
"v_dot2c_f32_bf16 v189, v130, v130\n\t"
"v_dot2c_f32_bf16 v190, s70, v131\n\t"
"v_dot2c_f32_bf16 v191, v131, v131\n\t"
"s_waitcnt vmcnt(37)\n\t"
"v_dot2c_f32_bf16 v188, s70, v132\n\t"
"v_dot2c_f32_bf16 v189, v132, v132\n\t"
"v_dot2c_f32_bf16 v190, s70, v133\n\t"
"v_dot2c_f32_bf16 v191, v133, v133\n\t"
"v_dot2c_f32_bf16 v188, s70, v134\n\t"
"v_dot2c_f32_bf16 v189, v134, v134\n\t"
"v_dot2c_f32_bf16 v190, s70, v135\n\t"
"v_dot2c_f32_bf16 v191, v135, v135\n\t"
"s_waitcnt vmcnt(36)\n\t"
"v_dot2c_f32_bf16 v188, s70, v136\n\t"
"v_dot2c_f32_bf16 v189, v136, v136\n\t"
"v_dot2c_f32_bf16 v190, s70, v137\n\t"
"v_dot2c_f32_bf16 v191, v137, v137\n\t"
"v_dot2c_f32_bf16 v188, s70, v138\n\t"
"v_dot2c_f32_bf16 v189, v138, v138\n\t"
"v_dot2c_f32_bf16 v190, s70, v139\n\t"
"v_dot2c_f32_bf16 v191, v139, v139\n\t"
"v_pk_fma_f32 v[180:181], v[180:181], v[194:195], v[194:195] op_sel:[0,0,1] op_sel_hi:[1,0,1]\n\t"
"s_nop 0\n\t"
"v_add_f32 v188, v188, v190\n\t"
"v_pk_fma_f32 v[182:183], v[182:183], v[194:195], v[194:195] op_sel:[0,0,1] op_sel_hi:[1,0,1]\n\t"
"v_pk_fma_f32 v[184:185], v[184:185], v[194:195], v[194:195] op_sel:[0,0,1] op_sel_hi:[1,0,1]\n\t"
"v_pk_fma_f32 v[186:187], v[186:187], v[194:195], v[194:195] op_sel:[0,0,1] op_sel_hi:[1,0,1]\n\t"
"v_add_f32 v189, v189, v191\n\t"
"v_cvt_pk_bf16_f32 v84, v180, v181\n\t"
"v_cvt_pk_bf16_f32 v85, v182, v183\n\t"
"v_cvt_pk_bf16_f32 v86, v184, v185\n\t"
"v_add_f32_dpp v188, v188, v188 quad_perm:[1,0,3,2] row_mask:0xf bank_mask:0xf\n\t"
"v_cvt_pk_bf16_f32 v87, v186, v187\n\t"
"ds_write_b128 v177, v[84:87] offset:128\n\t"
"v_lshlrev_b32 v180, 16, v88\n\t"
"v_add_f32_dpp v189, v189, v189 quad_perm:[1,0,3,2] row_mask:0xf bank_mask:0xf\n\t"
"v_and_b32 v181, s71, v88\n\t"
"v_lshlrev_b32 v182, 16, v89\n\t"
"v_and_b32 v183, s71, v89\n\t"
"v_add_f32_dpp v188, v188, v188 quad_perm:[2,3,0,1] row_mask:0xf bank_mask:0xf\n\t"
"v_lshlrev_b32 v184, 16, v90\n\t"
"v_and_b32 v185, s71, v90\n\t"
"v_lshlrev_b32 v186, 16, v91\n\t"
"v_and_b32 v187, s71, v91\n\t"
"v_add_f32_dpp v189, v189, v189 quad_perm:[2,3,0,1] row_mask:0xf bank_mask:0xf\n\t"
"v_pk_fma_f32 v[180:181], v[180:181], v[194:195], v[194:195] op_sel:[0,0,1] op_sel_hi:[1,0,1]\n\t"
"v_pk_fma_f32 v[182:183], v[182:183], v[194:195], v[194:195] op_sel:[0,0,1] op_sel_hi:[1,0,1]\n\t"
"v_pk_fma_f32 v[184:185], v[184:185], v[194:195], v[194:195] op_sel:[0,0,1] op_sel_hi:[1,0,1]\n\t"
"v_add_f32_dpp v188, v188, v188 row_half_mirror row_mask:0xf bank_mask:0xf\n\t"
"v_pk_fma_f32 v[186:187], v[186:187], v[194:195], v[194:195] op_sel:[0,0,1] op_sel_hi:[1,0,1]\n\t"
"v_cvt_pk_bf16_f32 v88, v180, v181\n\t"
"v_cvt_pk_bf16_f32 v89, v182, v183\n\t"
"v_add_f32_dpp v189, v189, v189 row_half_mirror row_mask:0xf bank_mask:0xf\n\t"
"v_cvt_pk_bf16_f32 v90, v184, v185\n\t"
"v_cvt_pk_bf16_f32 v91, v186, v187\n\t"
"ds_write_b128 v177, v[88:91] offset:8320\n\t"
"v_mul_f32 v188, 0x3ae38e39, v188\n\t"
"v_lshlrev_b32 v180, 16, v92\n\t"
"v_and_b32 v181, s71, v92\n\t"
"v_lshlrev_b32 v182, 16, v93\n\t"
"v_mul_f32 v189, 0x3ae38e39, v189\n\t"
"v_and_b32 v183, s71, v93\n\t"
"v_lshlrev_b32 v184, 16, v94\n\t"
"v_and_b32 v185, s71, v94\n\t"
"v_lshlrev_b32 v186, 16, v95\n\t"
"v_fma_f32 v189, -v188, v188, v189\n\t"
"v_and_b32 v187, s71, v95\n\t"
"v_pk_fma_f32 v[180:181], v[180:181], v[194:195], v[194:195] op_sel:[0,0,1] op_sel_hi:[1,0,1]\n\t"
"v_pk_fma_f32 v[182:183], v[182:183], v[194:195], v[194:195] op_sel:[0,0,1] op_sel_hi:[1,0,1]\n\t"
"v_max_f32 v189, 0, v189\n\t"
"v_pk_fma_f32 v[184:185], v[184:185], v[194:195], v[194:195] op_sel:[0,0,1] op_sel_hi:[1,0,1]\n\t"
"v_pk_fma_f32 v[186:187], v[186:187], v[194:195], v[194:195] op_sel:[0,0,1] op_sel_hi:[1,0,1]\n\t"
"v_cvt_pk_bf16_f32 v92, v180, v181\n\t"
"v_cvt_pk_bf16_f32 v188, v188, v189\n\t"
"v_cvt_pk_bf16_f32 v93, v182, v183\n\t"
"v_cvt_pk_bf16_f32 v94, v184, v185\n\t"
"v_cvt_pk_bf16_f32 v95, v186, v187\n\t"
"v_and_b32 v189, s71, v188\n\t"
"ds_write_b128 v177, v[92:95] offset:16512\n\t"
"v_lshlrev_b32 v180, 16, v96\n\t"
"v_and_b32 v181, s71, v96\n\t"
"v_lshlrev_b32 v188, 16, v188\n\t"
"v_lshlrev_b32 v182, 16, v97\n\t"
"v_and_b32 v183, s71, v97\n\t"
"v_lshlrev_b32 v184, 16, v98\n\t"
"v_and_b32 v185, s71, v98\n\t"
"v_add_f32 v189, 0x3727c5ac, v189\n\t"
"v_lshlrev_b32 v186, 16, v99\n\t"
"v_and_b32 v187, s71, v99\n\t"
"v_pk_fma_f32 v[180:181], v[180:181], v[194:195], v[194:195] op_sel:[0,0,1] op_sel_hi:[1,0,1]\n\t"
"v_cvt_pk_bf16_f32 v189, v189, v189\n\t"
"v_pk_fma_f32 v[182:183], v[182:183], v[194:195], v[194:195] op_sel:[0,0,1] op_sel_hi:[1,0,1]\n\t"
"v_pk_fma_f32 v[184:185], v[184:185], v[194:195], v[194:195] op_sel:[0,0,1] op_sel_hi:[1,0,1]\n\t"
"v_pk_fma_f32 v[186:187], v[186:187], v[194:195], v[194:195] op_sel:[0,0,1] op_sel_hi:[1,0,1]\n\t"
"v_lshlrev_b32 v189, 16, v189\n\t"
"v_cvt_pk_bf16_f32 v96, v180, v181\n\t"
"v_cvt_pk_bf16_f32 v97, v182, v183\n\t"
"v_cvt_pk_bf16_f32 v98, v184, v185\n\t"
"v_sqrt_f32 v189, v189\n\t"
"v_cvt_pk_bf16_f32 v99, v186, v187\n\t"
"ds_write_b128 v177, v[96:99] offset:24704\n\t"
"v_lshlrev_b32 v180, 16, v100\n\t"
"v_cvt_pk_bf16_f32 v189, v189, v189\n\t"
"v_and_b32 v181, s71, v100\n\t"
"v_lshlrev_b32 v182, 16, v101\n\t"
"v_and_b32 v183, s71, v101\n\t"
"v_lshlrev_b32 v184, 16, v102\n\t"
"v_lshlrev_b32 v189, 16, v189\n\t"
"v_and_b32 v185, s71, v102\n\t"
"v_lshlrev_b32 v186, 16, v103\n\t"
"v_and_b32 v187, s71, v103\n\t"
"v_rcp_f32 v189, v189\n\t"
"v_pk_fma_f32 v[180:181], v[180:181], v[194:195], v[194:195] op_sel:[0,0,1] op_sel_hi:[1,0,1]\n\t"
"v_pk_fma_f32 v[182:183], v[182:183], v[194:195], v[194:195] op_sel:[0,0,1] op_sel_hi:[1,0,1]\n\t"
"v_pk_fma_f32 v[184:185], v[184:185], v[194:195], v[194:195] op_sel:[0,0,1] op_sel_hi:[1,0,1]\n\t"
"v_mul_f32 v196, 0x3f800000, v189\n\t"
"v_pk_fma_f32 v[186:187], v[186:187], v[194:195], v[194:195] op_sel:[0,0,1] op_sel_hi:[1,0,1]\n\t"
"v_cvt_pk_bf16_f32 v100, v180, v181\n\t"
"v_cvt_pk_bf16_f32 v101, v182, v183\n\t"
"v_mul_f32 v197, -v188, v196\n\t"
"v_cvt_pk_bf16_f32 v102, v184, v185\n\t"
"v_cvt_pk_bf16_f32 v103, v186, v187\n\t"
"ds_write_b128 v177, v[100:103] offset:32896\n\t"
"s_waitcnt lgkmcnt(0)\n\t"
"ds_read_b128 a[0:3], v246 offset:0\n\t"
"v_lshlrev_b32 v180, 16, v104\n\t"
"v_and_b32 v181, s71, v104\n\t"
"v_lshlrev_b32 v182, 16, v105\n\t"
"v_and_b32 v183, s71, v105\n\t"
"v_lshlrev_b32 v184, 16, v106\n\t"
"ds_read_b128 a[8:11], v248 offset:4096\n\t"
"v_and_b32 v185, s71, v106\n\t"
"v_lshlrev_b32 v186, 16, v107\n\t"
"v_and_b32 v187, s71, v107\n\t"
"v_pk_fma_f32 v[180:181], v[180:181], v[196:197], v[196:197] op_sel:[0,0,1] op_sel_hi:[1,0,1]\n\t"
"v_pk_fma_f32 v[182:183], v[182:183], v[196:197], v[196:197] op_sel:[0,0,1] op_sel_hi:[1,0,1]\n\t"
"ds_read_b128 a[16:19], v246 offset:8192\n\t"
"v_pk_fma_f32 v[184:185], v[184:185], v[196:197], v[196:197] op_sel:[0,0,1] op_sel_hi:[1,0,1]\n\t"
"v_pk_fma_f32 v[186:187], v[186:187], v[196:197], v[196:197] op_sel:[0,0,1] op_sel_hi:[1,0,1]\n\t"
"v_cvt_pk_bf16_f32 v104, v180, v181\n\t"
"v_cvt_pk_bf16_f32 v105, v182, v183\n\t"
"v_cvt_pk_bf16_f32 v106, v184, v185\n\t"
"ds_read_b128 a[24:27], v248 offset:12288\n\t"
"v_cvt_pk_bf16_f32 v107, v186, v187\n\t"
"v_lshlrev_b32 v180, 16, v108\n\t"
"v_and_b32 v181, s71, v108\n\t"
"v_lshlrev_b32 v182, 16, v109\n\t"
"v_and_b32 v183, s71, v109\n\t"
"ds_read_b128 a[32:35], v246 offset:16384\n\t"
"v_lshlrev_b32 v184, 16, v110\n\t"
"v_and_b32 v185, s71, v110\n\t"
"v_lshlrev_b32 v186, 16, v111\n\t"
"v_and_b32 v187, s71, v111\n\t"
"v_pk_fma_f32 v[180:181], v[180:181], v[196:197], v[196:197] op_sel:[0,0,1] op_sel_hi:[1,0,1]\n\t"
"ds_read_b128 a[40:43], v248 offset:20480\n\t"
"v_pk_fma_f32 v[182:183], v[182:183], v[196:197], v[196:197] op_sel:[0,0,1] op_sel_hi:[1,0,1]\n\t"
"v_pk_fma_f32 v[184:185], v[184:185], v[196:197], v[196:197] op_sel:[0,0,1] op_sel_hi:[1,0,1]\n\t"
"v_pk_fma_f32 v[186:187], v[186:187], v[196:197], v[196:197] op_sel:[0,0,1] op_sel_hi:[1,0,1]\n\t"
"v_cvt_pk_bf16_f32 v108, v180, v181\n\t"
"v_cvt_pk_bf16_f32 v109, v182, v183\n\t"
"ds_read_b128 a[48:51], v246 offset:24576\n\t"
"v_cvt_pk_bf16_f32 v110, v184, v185\n\t"
"v_cvt_pk_bf16_f32 v111, v186, v187\n\t"
"v_lshlrev_b32 v180, 16, v112\n\t"
"v_and_b32 v181, s71, v112\n\t"
"v_lshlrev_b32 v182, 16, v113\n\t"
"ds_read_b128 a[56:59], v248 offset:28672\n\t"
"v_and_b32 v183, s71, v113\n\t"
"v_lshlrev_b32 v184, 16, v114\n\t"
"v_and_b32 v185, s71, v114\n\t"
"v_lshlrev_b32 v186, 16, v115\n\t"
"v_and_b32 v187, s71, v115\n\t"
"ds_read_b128 a[64:67], v250 offset:0\n\t"
"v_pk_fma_f32 v[180:181], v[180:181], v[196:197], v[196:197] op_sel:[0,0,1] op_sel_hi:[1,0,1]\n\t"
"v_pk_fma_f32 v[182:183], v[182:183], v[196:197], v[196:197] op_sel:[0,0,1] op_sel_hi:[1,0,1]\n\t"
"v_pk_fma_f32 v[184:185], v[184:185], v[196:197], v[196:197] op_sel:[0,0,1] op_sel_hi:[1,0,1]\n\t"
"v_pk_fma_f32 v[186:187], v[186:187], v[196:197], v[196:197] op_sel:[0,0,1] op_sel_hi:[1,0,1]\n\t"
"v_cvt_pk_bf16_f32 v112, v180, v181\n\t"
"ds_read_b128 a[72:75], v252 offset:4096\n\t"
"v_cvt_pk_bf16_f32 v113, v182, v183\n\t"
"v_cvt_pk_bf16_f32 v114, v184, v185\n\t"
"v_cvt_pk_bf16_f32 v115, v186, v187\n\t"
"v_lshlrev_b32 v180, 16, v116\n\t"
"v_and_b32 v181, s71, v116\n\t"
"ds_read_b128 a[80:83], v250 offset:8192\n\t"
"v_lshlrev_b32 v182, 16, v117\n\t"
"v_and_b32 v183, s71, v117\n\t"
"v_lshlrev_b32 v184, 16, v118\n\t"
"v_and_b32 v185, s71, v118\n\t"
"v_lshlrev_b32 v186, 16, v119\n\t"
"ds_read_b128 a[88:91], v252 offset:12288\n\t"
"v_and_b32 v187, s71, v119\n\t"
"v_pk_fma_f32 v[180:181], v[180:181], v[196:197], v[196:197] op_sel:[0,0,1] op_sel_hi:[1,0,1]\n\t"
"v_pk_fma_f32 v[182:183], v[182:183], v[196:197], v[196:197] op_sel:[0,0,1] op_sel_hi:[1,0,1]\n\t"
"v_pk_fma_f32 v[184:185], v[184:185], v[196:197], v[196:197] op_sel:[0,0,1] op_sel_hi:[1,0,1]\n\t"
"v_pk_fma_f32 v[186:187], v[186:187], v[196:197], v[196:197] op_sel:[0,0,1] op_sel_hi:[1,0,1]\n\t"
"ds_read_b128 a[96:99], v250 offset:16384\n\t"
"v_cvt_pk_bf16_f32 v116, v180, v181\n\t"
"v_cvt_pk_bf16_f32 v117, v182, v183\n\t"
"v_cvt_pk_bf16_f32 v118, v184, v185\n\t"
"v_cvt_pk_bf16_f32 v119, v186, v187\n\t"
"v_lshlrev_b32 v180, 16, v120\n\t"
"ds_read_b128 a[104:107], v252 offset:20480\n\t"
"v_and_b32 v181, s71, v120\n\t"
"v_lshlrev_b32 v182, 16, v121\n\t"
"v_and_b32 v183, s71, v121\n\t"
"v_lshlrev_b32 v184, 16, v122\n\t"
"v_and_b32 v185, s71, v122\n\t"
"ds_read_b128 a[112:115], v250 offset:24576\n\t"
"v_lshlrev_b32 v186, 16, v123\n\t"
"v_and_b32 v187, s71, v123\n\t"
"v_mov_b32 v188, 0\n\t"
"v_mov_b32 v189, 0\n\t"
"v_mov_b32 v190, 0\n\t"
"ds_read_b128 a[120:123], v252 offset:28672\n\t"
"v_mov_b32 v191, 0\n\t"
"s_waitcnt vmcnt(33)\n\t"
"v_dot2c_f32_bf16 v188, s70, v140\n\t"
"v_dot2c_f32_bf16 v189, v140, v140\n\t"
"v_dot2c_f32_bf16 v190, s70, v141\n\t"
"v_dot2c_f32_bf16 v191, v141, v141\n\t"
"ds_read_b128 a[128:131], v250 offset:32768\n\t"
"v_dot2c_f32_bf16 v188, s70, v142\n\t"
"v_dot2c_f32_bf16 v189, v142, v142\n\t"
"v_dot2c_f32_bf16 v190, s70, v143\n\t"
"v_dot2c_f32_bf16 v191, v143, v143\n\t"
"s_waitcnt vmcnt(26)\n\t"
"v_dot2c_f32_bf16 v188, s70, v144\n\t"
"ds_read_b128 a[136:139], v252 offset:36864\n\t"
"v_dot2c_f32_bf16 v189, v144, v144\n\t"
"v_dot2c_f32_bf16 v190, s70, v145\n\t"
"v_dot2c_f32_bf16 v191, v145, v145\n\t"
"v_dot2c_f32_bf16 v188, s70, v146\n\t"
"v_dot2c_f32_bf16 v189, v146, v146\n\t"
"v_dot2c_f32_bf16 v190, s70, v147\n\t"
"v_dot2c_f32_bf16 v191, v147, v147\n\t"
"s_waitcnt vmcnt(25)\n\t"
"v_dot2c_f32_bf16 v188, s70, v148\n\t"
"v_dot2c_f32_bf16 v189, v148, v148\n\t"
"v_dot2c_f32_bf16 v190, s70, v149\n\t"
"v_dot2c_f32_bf16 v191, v149, v149\n\t"
"v_dot2c_f32_bf16 v188, s70, v150\n\t"
"v_dot2c_f32_bf16 v189, v150, v150\n\t"
"v_dot2c_f32_bf16 v190, s70, v151\n\t"
"v_dot2c_f32_bf16 v191, v151, v151\n\t"
"s_waitcnt vmcnt(24)\n\t"
"v_dot2c_f32_bf16 v188, s70, v152\n\t"
"v_dot2c_f32_bf16 v189, v152, v152\n\t"
"v_dot2c_f32_bf16 v190, s70, v153\n\t"
"v_dot2c_f32_bf16 v191, v153, v153\n\t"
"v_dot2c_f32_bf16 v188, s70, v154\n\t"
"v_dot2c_f32_bf16 v189, v154, v154\n\t"
"v_dot2c_f32_bf16 v190, s70, v155\n\t"
"v_dot2c_f32_bf16 v191, v155, v155\n\t"
"s_waitcnt vmcnt(23)\n\t"
"v_dot2c_f32_bf16 v188, s70, v156\n\t"
"v_dot2c_f32_bf16 v189, v156, v156\n\t"
"v_dot2c_f32_bf16 v190, s70, v157\n\t"
"v_dot2c_f32_bf16 v191, v157, v157\n\t"
"v_dot2c_f32_bf16 v188, s70, v158\n\t"
"v_dot2c_f32_bf16 v189, v158, v158\n\t"
"v_dot2c_f32_bf16 v190, s70, v159\n\t"
"v_dot2c_f32_bf16 v191, v159, v159\n\t"
"s_waitcnt vmcnt(22)\n\t"
"v_dot2c_f32_bf16 v188, s70, v160\n\t"
"v_dot2c_f32_bf16 v189, v160, v160\n\t"
"v_dot2c_f32_bf16 v190, s70, v161\n\t"
"v_dot2c_f32_bf16 v191, v161, v161\n\t"
"v_dot2c_f32_bf16 v188, s70, v162\n\t"
"v_dot2c_f32_bf16 v189, v162, v162\n\t"
"v_dot2c_f32_bf16 v190, s70, v163\n\t"
"v_dot2c_f32_bf16 v191, v163, v163\n\t"
"s_waitcnt vmcnt(21)\n\t"
"v_dot2c_f32_bf16 v188, s70, v164\n\t"
"v_dot2c_f32_bf16 v189, v164, v164\n\t"
"v_dot2c_f32_bf16 v190, s70, v165\n\t"
"v_dot2c_f32_bf16 v191, v165, v165\n\t"
"v_dot2c_f32_bf16 v188, s70, v166\n\t"
"v_dot2c_f32_bf16 v189, v166, v166\n\t"
"v_dot2c_f32_bf16 v190, s70, v167\n\t"
"v_dot2c_f32_bf16 v191, v167, v167\n\t"
"s_waitcnt vmcnt(20)\n\t"
"v_dot2c_f32_bf16 v188, s70, v168\n\t"
"v_dot2c_f32_bf16 v189, v168, v168\n\t"
"v_dot2c_f32_bf16 v190, s70, v169\n\t"
"v_dot2c_f32_bf16 v191, v169, v169\n\t"
"v_dot2c_f32_bf16 v188, s70, v170\n\t"
"v_dot2c_f32_bf16 v189, v170, v170\n\t"
"v_dot2c_f32_bf16 v190, s70, v171\n\t"
"v_dot2c_f32_bf16 v191, v171, v171\n\t"
"s_waitcnt vmcnt(19)\n\t"
"v_dot2c_f32_bf16 v188, s70, v172\n\t"
"v_dot2c_f32_bf16 v189, v172, v172\n\t"
"v_dot2c_f32_bf16 v190, s70, v173\n\t"
"v_dot2c_f32_bf16 v191, v173, v173\n\t"
"v_dot2c_f32_bf16 v188, s70, v174\n\t"
"v_dot2c_f32_bf16 v189, v174, v174\n\t"
"v_dot2c_f32_bf16 v190, s70, v175\n\t"
"v_dot2c_f32_bf16 v191, v175, v175\n\t"
"v_pk_fma_f32 v[180:181], v[180:181], v[196:197], v[196:197] op_sel:[0,0,1] op_sel_hi:[1,0,1]\n\t"
"s_nop 0\n\t"
"v_add_f32 v188, v188, v190\n\t"
"v_pk_fma_f32 v[182:183], v[182:183], v[196:197], v[196:197] op_sel:[0,0,1] op_sel_hi:[1,0,1]\n\t"
"v_pk_fma_f32 v[184:185], v[184:185], v[196:197], v[196:197] op_sel:[0,0,1] op_sel_hi:[1,0,1]\n\t"
"v_pk_fma_f32 v[186:187], v[186:187], v[196:197], v[196:197] op_sel:[0,0,1] op_sel_hi:[1,0,1]\n\t"
"v_add_f32 v189, v189, v191\n\t"
"v_cvt_pk_bf16_f32 v120, v180, v181\n\t"
"v_cvt_pk_bf16_f32 v121, v182, v183\n\t"
"v_cvt_pk_bf16_f32 v122, v184, v185\n\t"
"v_add_f32_dpp v188, v188, v188 quad_perm:[1,0,3,2] row_mask:0xf bank_mask:0xf\n\t"
"v_cvt_pk_bf16_f32 v123, v186, v187\n\t"
"v_lshlrev_b32 v180, 16, v124\n\t"
"v_and_b32 v181, s71, v124\n\t"
"v_add_f32_dpp v189, v189, v189 quad_perm:[1,0,3,2] row_mask:0xf bank_mask:0xf\n\t"
"v_lshlrev_b32 v182, 16, v125\n\t"
"v_and_b32 v183, s71, v125\n\t"
"v_lshlrev_b32 v184, 16, v126\n\t"
"v_add_f32_dpp v188, v188, v188 quad_perm:[2,3,0,1] row_mask:0xf bank_mask:0xf\n\t"
"v_and_b32 v185, s71, v126\n\t"
"v_lshlrev_b32 v186, 16, v127\n\t"
"v_and_b32 v187, s71, v127\n\t"
"v_add_f32_dpp v189, v189, v189 quad_perm:[2,3,0,1] row_mask:0xf bank_mask:0xf\n\t"
"v_pk_fma_f32 v[180:181], v[180:181], v[196:197], v[196:197] op_sel:[0,0,1] op_sel_hi:[1,0,1]\n\t"
"v_pk_fma_f32 v[182:183], v[182:183], v[196:197], v[196:197] op_sel:[0,0,1] op_sel_hi:[1,0,1]\n\t"
"v_pk_fma_f32 v[184:185], v[184:185], v[196:197], v[196:197] op_sel:[0,0,1] op_sel_hi:[1,0,1]\n\t"
"v_add_f32_dpp v188, v188, v188 row_half_mirror row_mask:0xf bank_mask:0xf\n\t"
"v_pk_fma_f32 v[186:187], v[186:187], v[196:197], v[196:197] op_sel:[0,0,1] op_sel_hi:[1,0,1]\n\t"
"v_cvt_pk_bf16_f32 v124, v180, v181\n\t"
"v_cvt_pk_bf16_f32 v125, v182, v183\n\t"
"v_add_f32_dpp v189, v189, v189 row_half_mirror row_mask:0xf bank_mask:0xf\n\t"
"v_cvt_pk_bf16_f32 v126, v184, v185\n\t"
"v_cvt_pk_bf16_f32 v127, v186, v187\n\t"
"v_lshlrev_b32 v180, 16, v128\n\t"
"v_mul_f32 v188, 0x3ae38e39, v188\n\t"
"v_and_b32 v181, s71, v128\n\t"
"v_lshlrev_b32 v182, 16, v129\n\t"
"v_and_b32 v183, s71, v129\n\t"
"v_mul_f32 v189, 0x3ae38e39, v189\n\t"
"v_lshlrev_b32 v184, 16, v130\n\t"
"v_and_b32 v185, s71, v130\n\t"
"v_lshlrev_b32 v186, 16, v131\n\t"
"v_fma_f32 v189, -v188, v188, v189\n\t"
"v_and_b32 v187, s71, v131\n\t"
"v_pk_fma_f32 v[180:181], v[180:181], v[196:197], v[196:197] op_sel:[0,0,1] op_sel_hi:[1,0,1]\n\t"
"v_pk_fma_f32 v[182:183], v[182:183], v[196:197], v[196:197] op_sel:[0,0,1] op_sel_hi:[1,0,1]\n\t"
"v_max_f32 v189, 0, v189\n\t"
"v_pk_fma_f32 v[184:185], v[184:185], v[196:197], v[196:197] op_sel:[0,0,1] op_sel_hi:[1,0,1]\n\t"
"v_pk_fma_f32 v[186:187], v[186:187], v[196:197], v[196:197] op_sel:[0,0,1] op_sel_hi:[1,0,1]\n\t"
"v_cvt_pk_bf16_f32 v128, v180, v181\n\t"
"v_cvt_pk_bf16_f32 v188, v188, v189\n\t"
"v_cvt_pk_bf16_f32 v129, v182, v183\n\t"
"v_cvt_pk_bf16_f32 v130, v184, v185\n\t"
"v_cvt_pk_bf16_f32 v131, v186, v187\n\t"
"v_and_b32 v189, s71, v188\n\t"
"v_lshlrev_b32 v180, 16, v132\n\t"
"v_and_b32 v181, s71, v132\n\t"
"v_lshlrev_b32 v182, 16, v133\n\t"
"v_lshlrev_b32 v188, 16, v188\n\t"
"v_and_b32 v183, s71, v133\n\t"
"v_lshlrev_b32 v184, 16, v134\n\t"
"v_and_b32 v185, s71, v134\n\t"
"v_add_f32 v189, 0x3727c5ac, v189\n\t"
"v_lshlrev_b32 v186, 16, v135\n\t"
"v_and_b32 v187, s71, v135\n\t"
"v_pk_fma_f32 v[180:181], v[180:181], v[196:197], v[196:197] op_sel:[0,0,1] op_sel_hi:[1,0,1]\n\t"
"v_cvt_pk_bf16_f32 v189, v189, v189\n\t"
"v_pk_fma_f32 v[182:183], v[182:183], v[196:197], v[196:197] op_sel:[0,0,1] op_sel_hi:[1,0,1]\n\t"
"v_pk_fma_f32 v[184:185], v[184:185], v[196:197], v[196:197] op_sel:[0,0,1] op_sel_hi:[1,0,1]\n\t"
"v_pk_fma_f32 v[186:187], v[186:187], v[196:197], v[196:197] op_sel:[0,0,1] op_sel_hi:[1,0,1]\n\t"
"v_lshlrev_b32 v189, 16, v189\n\t"
"v_cvt_pk_bf16_f32 v132, v180, v181\n\t"
"v_cvt_pk_bf16_f32 v133, v182, v183\n\t"
"v_cvt_pk_bf16_f32 v134, v184, v185\n\t"
"v_sqrt_f32 v189, v189\n\t"
"v_cvt_pk_bf16_f32 v135, v186, v187\n\t"
"v_lshlrev_b32 v180, 16, v136\n\t"
"v_and_b32 v181, s71, v136\n\t"
"v_cvt_pk_bf16_f32 v189, v189, v189\n\t"
"v_lshlrev_b32 v182, 16, v137\n\t"
"v_and_b32 v183, s71, v137\n\t"
"v_lshlrev_b32 v184, 16, v138\n\t"
"v_lshlrev_b32 v189, 16, v189\n\t"
"v_and_b32 v185, s71, v138\n\t"
"v_lshlrev_b32 v186, 16, v139\n\t"
"v_and_b32 v187, s71, v139\n\t"
"v_rcp_f32 v189, v189\n\t"
"v_pk_fma_f32 v[180:181], v[180:181], v[196:197], v[196:197] op_sel:[0,0,1] op_sel_hi:[1,0,1]\n\t"
"v_pk_fma_f32 v[182:183], v[182:183], v[196:197], v[196:197] op_sel:[0,0,1] op_sel_hi:[1,0,1]\n\t"
"v_pk_fma_f32 v[184:185], v[184:185], v[196:197], v[196:197] op_sel:[0,0,1] op_sel_hi:[1,0,1]\n\t"
"v_mul_f32 v198, 0x3f800000, v189\n\t"
"v_pk_fma_f32 v[186:187], v[186:187], v[196:197], v[196:197] op_sel:[0,0,1] op_sel_hi:[1,0,1]\n\t"
"v_cvt_pk_bf16_f32 v136, v180, v181\n\t"
"v_cvt_pk_bf16_f32 v137, v182, v183\n\t"
"v_mul_f32 v199, -v188, v198\n\t"
"v_cvt_pk_bf16_f32 v138, v184, v185\n\t"
"v_cvt_pk_bf16_f32 v139, v186, v187\n\t"
"s_waitcnt lgkmcnt(0)\n\t"
"v_lshlrev_b32 v180, 16, v140\n\t"
"v_and_b32 v181, s71, v140\n\t"
"v_lshlrev_b32 v182, 16, v141\n\t"
"v_and_b32 v183, s71, v141\n\t"
"v_lshlrev_b32 v184, 16, v142\n\t"
"v_and_b32 v185, s71, v142\n\t"
"v_lshlrev_b32 v186, 16, v143\n\t"
"v_and_b32 v187, s71, v143\n\t"
"ds_write_b128 v176, v[104:107] offset:0\n\t"
"v_pk_fma_f32 v[180:181], v[180:181], v[198:199], v[198:199] op_sel:[0,0,1] op_sel_hi:[1,0,1]\n\t"
"v_pk_fma_f32 v[182:183], v[182:183], v[198:199], v[198:199] op_sel:[0,0,1] op_sel_hi:[1,0,1]\n\t"
"v_pk_fma_f32 v[184:185], v[184:185], v[198:199], v[198:199] op_sel:[0,0,1] op_sel_hi:[1,0,1]\n\t"
"v_pk_fma_f32 v[186:187], v[186:187], v[198:199], v[198:199] op_sel:[0,0,1] op_sel_hi:[1,0,1]\n\t"
"v_cvt_pk_bf16_f32 v140, v180, v181\n\t"
"v_cvt_pk_bf16_f32 v141, v182, v183\n\t"
"v_cvt_pk_bf16_f32 v142, v184, v185\n\t"
"v_cvt_pk_bf16_f32 v143, v186, v187\n\t"
"ds_write_b128 v176, v[140:143] offset:128\n\t"
"v_lshlrev_b32 v180, 16, v144\n\t"
"v_and_b32 v181, s71, v144\n\t"
"v_lshlrev_b32 v182, 16, v145\n\t"
"v_and_b32 v183, s71, v145\n\t"
"v_lshlrev_b32 v184, 16, v146\n\t"
"v_and_b32 v185, s71, v146\n\t"
"v_lshlrev_b32 v186, 16, v147\n\t"
"v_and_b32 v187, s71, v147\n\t"
"ds_write_b128 v176, v[108:111] offset:8192\n\t"
"v_pk_fma_f32 v[180:181], v[180:181], v[198:199], v[198:199] op_sel:[0,0,1] op_sel_hi:[1,0,1]\n\t"
"v_pk_fma_f32 v[182:183], v[182:183], v[198:199], v[198:199] op_sel:[0,0,1] op_sel_hi:[1,0,1]\n\t"
"v_pk_fma_f32 v[184:185], v[184:185], v[198:199], v[198:199] op_sel:[0,0,1] op_sel_hi:[1,0,1]\n\t"
"v_pk_fma_f32 v[186:187], v[186:187], v[198:199], v[198:199] op_sel:[0,0,1] op_sel_hi:[1,0,1]\n\t"
"v_cvt_pk_bf16_f32 v144, v180, v181\n\t"
"v_cvt_pk_bf16_f32 v145, v182, v183\n\t"
"v_cvt_pk_bf16_f32 v146, v184, v185\n\t"
"v_cvt_pk_bf16_f32 v147, v186, v187\n\t"
"ds_write_b128 v176, v[144:147] offset:8320\n\t"
"v_lshlrev_b32 v180, 16, v148\n\t"
"v_and_b32 v181, s71, v148\n\t"
"v_lshlrev_b32 v182, 16, v149\n\t"
"v_and_b32 v183, s71, v149\n\t"
"v_lshlrev_b32 v184, 16, v150\n\t"
"v_and_b32 v185, s71, v150\n\t"
"v_lshlrev_b32 v186, 16, v151\n\t"
"v_and_b32 v187, s71, v151\n\t"
"ds_write_b128 v176, v[112:115] offset:16384\n\t"
"v_pk_fma_f32 v[180:181], v[180:181], v[198:199], v[198:199] op_sel:[0,0,1] op_sel_hi:[1,0,1]\n\t"
"v_pk_fma_f32 v[182:183], v[182:183], v[198:199], v[198:199] op_sel:[0,0,1] op_sel_hi:[1,0,1]\n\t"
"v_pk_fma_f32 v[184:185], v[184:185], v[198:199], v[198:199] op_sel:[0,0,1] op_sel_hi:[1,0,1]\n\t"
"v_pk_fma_f32 v[186:187], v[186:187], v[198:199], v[198:199] op_sel:[0,0,1] op_sel_hi:[1,0,1]\n\t"
"v_cvt_pk_bf16_f32 v148, v180, v181\n\t"
"v_cvt_pk_bf16_f32 v149, v182, v183\n\t"
"v_cvt_pk_bf16_f32 v150, v184, v185\n\t"
"v_cvt_pk_bf16_f32 v151, v186, v187\n\t"
"ds_write_b128 v176, v[148:151] offset:16512\n\t"
"v_lshlrev_b32 v180, 16, v152\n\t"
"v_and_b32 v181, s71, v152\n\t"
"v_lshlrev_b32 v182, 16, v153\n\t"
"v_and_b32 v183, s71, v153\n\t"
"v_lshlrev_b32 v184, 16, v154\n\t"
"v_and_b32 v185, s71, v154\n\t"
"v_lshlrev_b32 v186, 16, v155\n\t"
"v_and_b32 v187, s71, v155\n\t"
"ds_write_b128 v176, v[116:119] offset:24576\n\t"
"v_pk_fma_f32 v[180:181], v[180:181], v[198:199], v[198:199] op_sel:[0,0,1] op_sel_hi:[1,0,1]\n\t"
"v_pk_fma_f32 v[182:183], v[182:183], v[198:199], v[198:199] op_sel:[0,0,1] op_sel_hi:[1,0,1]\n\t"
"v_pk_fma_f32 v[184:185], v[184:185], v[198:199], v[198:199] op_sel:[0,0,1] op_sel_hi:[1,0,1]\n\t"
"v_pk_fma_f32 v[186:187], v[186:187], v[198:199], v[198:199] op_sel:[0,0,1] op_sel_hi:[1,0,1]\n\t"
"v_cvt_pk_bf16_f32 v152, v180, v181\n\t"
"v_cvt_pk_bf16_f32 v153, v182, v183\n\t"
"v_cvt_pk_bf16_f32 v154, v184, v185\n\t"
"v_cvt_pk_bf16_f32 v155, v186, v187\n\t"
"ds_write_b128 v176, v[152:155] offset:24704\n\t"
"v_lshlrev_b32 v180, 16, v156\n\t"
"v_and_b32 v181, s71, v156\n\t"
"v_lshlrev_b32 v182, 16, v157\n\t"
"v_and_b32 v183, s71, v157\n\t"
"v_lshlrev_b32 v184, 16, v158\n\t"
"v_and_b32 v185, s71, v158\n\t"
"v_lshlrev_b32 v186, 16, v159\n\t"
"v_and_b32 v187, s71, v159\n\t"
"ds_write_b128 v177, v[120:123] offset:0\n\t"
"v_pk_fma_f32 v[180:181], v[180:181], v[198:199], v[198:199] op_sel:[0,0,1] op_sel_hi:[1,0,1]\n\t"
"v_pk_fma_f32 v[182:183], v[182:183], v[198:199], v[198:199] op_sel:[0,0,1] op_sel_hi:[1,0,1]\n\t"
"v_pk_fma_f32 v[184:185], v[184:185], v[198:199], v[198:199] op_sel:[0,0,1] op_sel_hi:[1,0,1]\n\t"
"v_pk_fma_f32 v[186:187], v[186:187], v[198:199], v[198:199] op_sel:[0,0,1] op_sel_hi:[1,0,1]\n\t"
"v_cvt_pk_bf16_f32 v156, v180, v181\n\t"
"v_cvt_pk_bf16_f32 v157, v182, v183\n\t"
"v_cvt_pk_bf16_f32 v158, v184, v185\n\t"
"v_cvt_pk_bf16_f32 v159, v186, v187\n\t"
"ds_write_b128 v177, v[156:159] offset:128\n\t"
"v_lshlrev_b32 v180, 16, v160\n\t"
"v_and_b32 v181, s71, v160\n\t"
"v_lshlrev_b32 v182, 16, v161\n\t"
"v_and_b32 v183, s71, v161\n\t"
"v_lshlrev_b32 v184, 16, v162\n\t"
"v_and_b32 v185, s71, v162\n\t"
"v_lshlrev_b32 v186, 16, v163\n\t"
"v_and_b32 v187, s71, v163\n\t"
"ds_write_b128 v177, v[124:127] offset:8192\n\t"
"v_pk_fma_f32 v[180:181], v[180:181], v[198:199], v[198:199] op_sel:[0,0,1] op_sel_hi:[1,0,1]\n\t"
"v_pk_fma_f32 v[182:183], v[182:183], v[198:199], v[198:199] op_sel:[0,0,1] op_sel_hi:[1,0,1]\n\t"
"v_pk_fma_f32 v[184:185], v[184:185], v[198:199], v[198:199] op_sel:[0,0,1] op_sel_hi:[1,0,1]\n\t"
"v_pk_fma_f32 v[186:187], v[186:187], v[198:199], v[198:199] op_sel:[0,0,1] op_sel_hi:[1,0,1]\n\t"
"v_cvt_pk_bf16_f32 v160, v180, v181\n\t"
"v_cvt_pk_bf16_f32 v161, v182, v183\n\t"
"v_cvt_pk_bf16_f32 v162, v184, v185\n\t"
"v_cvt_pk_bf16_f32 v163, v186, v187\n\t"
"ds_write_b128 v177, v[160:163] offset:8320\n\t"
"v_lshlrev_b32 v180, 16, v164\n\t"
"v_and_b32 v181, s71, v164\n\t"
"v_lshlrev_b32 v182, 16, v165\n\t"
"v_and_b32 v183, s71, v165\n\t"
"v_lshlrev_b32 v184, 16, v166\n\t"
"v_and_b32 v185, s71, v166\n\t"
"v_lshlrev_b32 v186, 16, v167\n\t"
"v_and_b32 v187, s71, v167\n\t"
"ds_write_b128 v177, v[128:131] offset:16384\n\t"
"v_pk_fma_f32 v[180:181], v[180:181], v[198:199], v[198:199] op_sel:[0,0,1] op_sel_hi:[1,0,1]\n\t"
"v_pk_fma_f32 v[182:183], v[182:183], v[198:199], v[198:199] op_sel:[0,0,1] op_sel_hi:[1,0,1]\n\t"
"v_pk_fma_f32 v[184:185], v[184:185], v[198:199], v[198:199] op_sel:[0,0,1] op_sel_hi:[1,0,1]\n\t"
"v_pk_fma_f32 v[186:187], v[186:187], v[198:199], v[198:199] op_sel:[0,0,1] op_sel_hi:[1,0,1]\n\t"
"v_cvt_pk_bf16_f32 v164, v180, v181\n\t"
"v_cvt_pk_bf16_f32 v165, v182, v183\n\t"
"v_cvt_pk_bf16_f32 v166, v184, v185\n\t"
"v_cvt_pk_bf16_f32 v167, v186, v187\n\t"
"ds_write_b128 v177, v[164:167] offset:16512\n\t"
"v_lshlrev_b32 v180, 16, v168\n\t"
"v_and_b32 v181, s71, v168\n\t"
"v_lshlrev_b32 v182, 16, v169\n\t"
"v_and_b32 v183, s71, v169\n\t"
"v_lshlrev_b32 v184, 16, v170\n\t"
"v_and_b32 v185, s71, v170\n\t"
"v_lshlrev_b32 v186, 16, v171\n\t"
"v_and_b32 v187, s71, v171\n\t"
"ds_write_b128 v177, v[132:135] offset:24576\n\t"
"v_pk_fma_f32 v[180:181], v[180:181], v[198:199], v[198:199] op_sel:[0,0,1] op_sel_hi:[1,0,1]\n\t"
"v_pk_fma_f32 v[182:183], v[182:183], v[198:199], v[198:199] op_sel:[0,0,1] op_sel_hi:[1,0,1]\n\t"
"v_pk_fma_f32 v[184:185], v[184:185], v[198:199], v[198:199] op_sel:[0,0,1] op_sel_hi:[1,0,1]\n\t"
"v_pk_fma_f32 v[186:187], v[186:187], v[198:199], v[198:199] op_sel:[0,0,1] op_sel_hi:[1,0,1]\n\t"
"v_cvt_pk_bf16_f32 v168, v180, v181\n\t"
"v_cvt_pk_bf16_f32 v169, v182, v183\n\t"
"v_cvt_pk_bf16_f32 v170, v184, v185\n\t"
"v_cvt_pk_bf16_f32 v171, v186, v187\n\t"
"ds_write_b128 v177, v[168:171] offset:24704\n\t"
"v_lshlrev_b32 v180, 16, v172\n\t"
"v_and_b32 v181, s71, v172\n\t"
"v_lshlrev_b32 v182, 16, v173\n\t"
"v_and_b32 v183, s71, v173\n\t"
"v_lshlrev_b32 v184, 16, v174\n\t"
"v_and_b32 v185, s71, v174\n\t"
"v_lshlrev_b32 v186, 16, v175\n\t"
"v_and_b32 v187, s71, v175\n\t"
"ds_write_b128 v177, v[136:139] offset:32768\n\t"
"v_pk_fma_f32 v[180:181], v[180:181], v[198:199], v[198:199] op_sel:[0,0,1] op_sel_hi:[1,0,1]\n\t"
"v_pk_fma_f32 v[182:183], v[182:183], v[198:199], v[198:199] op_sel:[0,0,1] op_sel_hi:[1,0,1]\n\t"
"v_pk_fma_f32 v[184:185], v[184:185], v[198:199], v[198:199] op_sel:[0,0,1] op_sel_hi:[1,0,1]\n\t"
"v_pk_fma_f32 v[186:187], v[186:187], v[198:199], v[198:199] op_sel:[0,0,1] op_sel_hi:[1,0,1]\n\t"
"v_cvt_pk_bf16_f32 v172, v180, v181\n\t"
"v_cvt_pk_bf16_f32 v173, v182, v183\n\t"
"v_cvt_pk_bf16_f32 v174, v184, v185\n\t"
"v_cvt_pk_bf16_f32 v175, v186, v187\n\t"
"ds_write_b128 v177, v[172:175] offset:32896\n\t"
"s_waitcnt lgkmcnt(0)\n\t"
"s_waitcnt vmcnt(10)\n\t"
"s_barrier\n\t"
"ds_read_b64_tr_b16 a[144:145], v209 offset:0\n\t"
"ds_read_b64_tr_b16 a[146:147], v209 offset:4608\n\t"
"ds_read_b64_tr_b16 a[148:149], v209 offset:32\n\t"
"ds_read_b64_tr_b16 a[150:151], v209 offset:4640\n\t"
"ds_read_b64_tr_b16 a[152:153], v209 offset:64\n\t"
"ds_read_b64_tr_b16 a[154:155], v209 offset:4672\n\t"
"ds_read_b64_tr_b16 a[156:157], v209 offset:96\n\t"
"ds_read_b64_tr_b16 a[158:159], v209 offset:4704\n\t"
"ds_read_b128 a[4:7], v246 offset:0\n\t"
"ds_read_b128 a[12:15], v248 offset:4096\n\t"
"s_waitcnt lgkmcnt(1)\n\t"
"v_mfma_f32_16x16x32_bf16 v[40:43], a[144:147], a[0:3], 0\n\t"
"ds_read_b64_tr_b16 a[160:161], v209 offset:128\n\t"
"v_mfma_f32_16x16x32_bf16 v[44:47], a[144:147], a[4:7], 0\n\t"
"ds_read_b64_tr_b16 a[162:163], v209 offset:4736\n\t"
"v_mfma_f32_16x16x32_bf16 v[48:51], a[148:151], a[0:3], 0\n\t"
"ds_read_b64_tr_b16 a[164:165], v209 offset:160\n\t"
"v_mfma_f32_16x16x32_bf16 v[52:55], a[148:151], a[4:7], 0\n\t"
"ds_read_b64_tr_b16 a[166:167], v209 offset:4768\n\t"
"ds_read_b64_tr_b16 a[168:169], v209 offset:192\n\t"
"ds_read_b64_tr_b16 a[170:171], v209 offset:4800\n\t"
"ds_read_b64_tr_b16 a[172:173], v209 offset:224\n\t"
"ds_read_b64_tr_b16 a[174:175], v209 offset:4832\n\t"
"v_mfma_f32_16x16x32_bf16 v[56:59], a[152:155], a[0:3], 0\n\t"
"ds_read_b64_tr_b16 a[176:177], v209 offset:256\n\t"
"v_mfma_f32_16x16x32_bf16 v[60:63], a[152:155], a[4:7], 0\n\t"
"ds_read_b64_tr_b16 a[178:179], v209 offset:4864\n\t"
"v_mfma_f32_16x16x32_bf16 v[64:67], a[156:159], a[0:3], 0\n\t"
"s_waitcnt lgkmcnt(6)\n\t"
"ds_read_b64_tr_b16 a[180:181], v209 offset:9216\n\t"
"v_mfma_f32_16x16x32_bf16 v[68:71], a[156:159], a[4:7], 0\n\t"
"ds_read_b64_tr_b16 a[182:183], v209 offset:13824\n\t"
"v_mfma_f32_16x16x32_bf16 v[72:75], a[160:163], a[0:3], 0\n\t"
"ds_read_b64_tr_b16 a[184:185], v209 offset:9248\n\t"
"v_mfma_f32_16x16x32_bf16 v[76:79], a[160:163], a[4:7], 0\n\t"
"ds_read_b64_tr_b16 a[186:187], v209 offset:13856\n\t"
"v_mfma_f32_16x16x32_bf16 v[80:83], a[164:167], a[0:3], 0\n\t"
"s_waitcnt lgkmcnt(6)\n\t"
"ds_read_b64_tr_b16 a[188:189], v209 offset:9280\n\t"
"v_mfma_f32_16x16x32_bf16 v[84:87], a[164:167], a[4:7], 0\n\t"
"ds_read_b64_tr_b16 a[190:191], v209 offset:13888\n\t"
"v_mfma_f32_16x16x32_bf16 v[88:91], a[168:171], a[0:3], 0\n\t"
"ds_read_b64_tr_b16 a[192:193], v209 offset:9312\n\t"
"v_mfma_f32_16x16x32_bf16 v[92:95], a[168:171], a[4:7], 0\n\t"
"ds_read_b64_tr_b16 a[194:195], v209 offset:13920\n\t"
"v_mfma_f32_16x16x32_bf16 v[96:99], a[172:175], a[0:3], 0\n\t"
"s_waitcnt lgkmcnt(6)\n\t"
"ds_read_b64_tr_b16 a[196:197], v209 offset:9344\n\t"
"v_mfma_f32_16x16x32_bf16 v[100:103], a[172:175], a[4:7], 0\n\t"
"ds_read_b64_tr_b16 a[198:199], v209 offset:13952\n\t"
"v_mfma_f32_16x16x32_bf16 v[104:107], a[176:179], a[0:3], 0\n\t"
"ds_read_b64_tr_b16 a[200:201], v209 offset:9376\n\t"
"v_mfma_f32_16x16x32_bf16 v[108:111], a[176:179], a[4:7], 0\n\t"
"ds_read_b64_tr_b16 a[202:203], v209 offset:13984\n\t"
"v_mfma_f32_16x16x32_bf16 v[40:43], a[180:183], a[8:11], v[40:43]\n\t"
"s_waitcnt lgkmcnt(6)\n\t"
"ds_read_b64_tr_b16 a[204:205], v209 offset:9408\n\t"
"v_mfma_f32_16x16x32_bf16 v[44:47], a[180:183], a[12:15], v[44:47]\n\t"
"ds_read_b64_tr_b16 a[206:207], v209 offset:14016\n\t"
"v_mfma_f32_16x16x32_bf16 v[48:51], a[184:187], a[8:11], v[48:51]\n\t"
"ds_read_b64_tr_b16 a[144:145], v209 offset:9440\n\t"
"v_mfma_f32_16x16x32_bf16 v[52:55], a[184:187], a[12:15], v[52:55]\n\t"
"ds_read_b64_tr_b16 a[146:147], v209 offset:14048\n\t"
"v_mfma_f32_16x16x32_bf16 v[56:59], a[188:191], a[8:11], v[56:59]\n\t"
"ds_read_b128 a[20:23], v246 offset:8192\n\t"
"ds_read_b128 a[28:31], v248 offset:12288\n\t"
"ds_read_b128 a[36:39], v246 offset:16384\n\t"
"ds_read_b128 a[44:47], v248 offset:20480\n\t"
"ds_read_b128 a[52:55], v246 offset:24576\n\t"
"ds_read_b128 a[60:63], v248 offset:28672\n\t"
"ds_read_b128 a[68:71], v250 offset:0\n\t"
"ds_read_b128 a[76:79], v252 offset:4096\n\t"
"ds_read_b128 a[84:87], v250 offset:8192\n\t"
"ds_read_b128 a[92:95], v252 offset:12288\n\t"
"ds_read_b128 a[100:103], v250 offset:16384\n\t"
"ds_read_b128 a[108:111], v252 offset:20480\n\t"
"ds_read_b128 a[116:119], v250 offset:24576\n\t"
"ds_read_b128 a[124:127], v252 offset:28672\n\t"
"ds_read_b128 a[132:135], v250 offset:32768\n\t"
"ds_read_b128 a[140:143], v252 offset:36864\n\t"
"v_mfma_f32_16x16x32_bf16 v[60:63], a[188:191], a[12:15], v[60:63]\n\t"
"s_waitcnt lgkmcnt(15)\n\t"
"ds_read_b64_tr_b16 a[148:149], v209 offset:9472\n\t"
"ds_read_b64_tr_b16 a[150:151], v209 offset:14080\n\t"
"v_mfma_f32_16x16x32_bf16 v[64:67], a[192:195], a[8:11], v[64:67]\n\t"
"ds_read_b64_tr_b16 a[152:153], v209 offset:18432\n\t"
"v_mfma_f32_16x16x32_bf16 v[68:71], a[192:195], a[12:15], v[68:71]\n\t"
"ds_read_b64_tr_b16 a[154:155], v209 offset:23040\n\t"
"v_mfma_f32_16x16x32_bf16 v[72:75], a[196:199], a[8:11], v[72:75]\n\t"
"ds_read_b64_tr_b16 a[156:157], v209 offset:18464\n\t"
"v_mfma_f32_16x16x32_bf16 v[76:79], a[196:199], a[12:15], v[76:79]\n\t"
"ds_read_b64_tr_b16 a[158:159], v209 offset:23072\n\t"
"v_mfma_f32_16x16x32_bf16 v[80:83], a[200:203], a[8:11], v[80:83]\n\t"
"s_waitcnt lgkmcnt(6)\n\t"
"ds_read_b64_tr_b16 a[160:161], v209 offset:18496\n\t"
"v_mfma_f32_16x16x32_bf16 v[84:87], a[200:203], a[12:15], v[84:87]\n\t"
"s_barrier\n\t"
"ds_read_b64_tr_b16 a[162:163], v209 offset:23104\n\t"
"v_mfma_f32_16x16x32_bf16 v[88:91], a[204:207], a[8:11], v[88:91]\n\t"
"s_waitcnt lgkmcnt(6)\n\t"
"ds_read_b64_tr_b16 a[164:165], v209 offset:18528\n\t"
"v_mfma_f32_16x16x32_bf16 v[92:95], a[204:207], a[12:15], v[92:95]\n\t"
"s_add_u32 s68, s44, 885888\n\t"
"ds_read_b64_tr_b16 a[166:167], v209 offset:23136\n\t"
"v_mfma_f32_16x16x32_bf16 v[96:99], a[144:147], a[8:11], v[96:99]\n\t"
"s_lshl_b32 s64, %[wv], 6\n\t"
"ds_read_b64_tr_b16 a[168:169], v209 offset:18560\n\t"
"v_add_u32 v120, s64, v245\n\t"
"v_mfma_f32_16x16x32_bf16 v[100:103], a[144:147], a[12:15], v[100:103]\n\t"
"s_add_u32 s66, %[lds], 153600\n\t"
"ds_read_b64_tr_b16 a[170:171], v209 offset:23168\n\t"
"v_add_u32 v121, 0, v120\n\t"
"v_mfma_f32_16x16x32_bf16 v[104:107], a[148:151], a[8:11], v[104:107]\n\t"
"s_add_u32 m0, s63, 78848\n\t"
"ds_read_b64_tr_b16 a[172:173], v209 offset:18592\n\t"
"v_mul_u32_u24 v122, 7282, v121\n\t"
"v_mfma_f32_16x16x32_bf16 v[108:111], a[148:151], a[12:15], v[108:111]\n\t"
"s_waitcnt lgkmcnt(7)\n\t"
"ds_read_b64_tr_b16 a[174:175], v209 offset:23200\n\t"
"v_lshrrev_b32 v122, 16, v122\n\t"
"v_mfma_f32_16x16x32_bf16 v[40:43], a[152:155], a[16:19], v[40:43]\n\t"
"buffer_load_dwordx4 v200, %[wq], s68 offen offset:0 lds\n\t"
"v_mfma_f32_16x16x32_bf16 v[44:47], a[152:155], a[20:23], v[44:47]\n\t"
"buffer_load_dwordx4 v201, %[wq], s68 offen offset:1024 lds\n\t"
"v_mfma_f32_16x16x32_bf16 v[48:51], a[156:159], a[16:19], v[48:51]\n\t"
"s_waitcnt lgkmcnt(4)\n\t"
"ds_read_b64_tr_b16 a[176:177], v209 offset:18624\n\t"
"v_add_lshl_u32 v122, v122, v121, 2\n\t"
"v_mfma_f32_16x16x32_bf16 v[52:55], a[156:159], a[20:23], v[52:55]\n\t"
"ds_read_b64_tr_b16 a[178:179], v209 offset:23232\n\t"
"ds_read_b64_tr_b16 a[180:181], v209 offset:18656\n\t"
"ds_read_b64_tr_b16 a[182:183], v209 offset:23264\n\t"
"v_add_u32 v122, s66, v122\n\t"
"v_mfma_f32_16x16x32_bf16 v[56:59], a[160:163], a[16:19], v[56:59]\n\t"
"ds_write_b32 v122, v16\n\t"
"v_mfma_f32_16x16x32_bf16 v[60:63], a[160:163], a[20:23], v[60:63]\n\t"
"buffer_load_dwordx4 v202, %[wq], s68 offen offset:2048 lds\n\t"
"v_mfma_f32_16x16x32_bf16 v[64:67], a[164:167], a[16:19], v[64:67]\n\t"
"s_waitcnt lgkmcnt(5)\n\t"
"ds_read_b64_tr_b16 a[184:185], v209 offset:18688\n\t"
"v_add_u32 v121, 256, v120\n\t"
"v_mfma_f32_16x16x32_bf16 v[68:71], a[164:167], a[20:23], v[68:71]\n\t"
"ds_read_b64_tr_b16 a[186:187], v209 offset:23296\n\t"
"ds_read_b64_tr_b16 a[188:189], v209 offset:27648\n\t"
"ds_read_b64_tr_b16 a[190:191], v209 offset:32256\n\t"
"v_mul_u32_u24 v122, 7282, v121\n\t"
"v_mfma_f32_16x16x32_bf16 v[72:75], a[168:171], a[16:19], v[72:75]\n\t"
"buffer_load_dwordx4 v203, %[wq], s68 offen offset:3072 lds\n\t"
"v_mfma_f32_16x16x32_bf16 v[76:79], a[168:171], a[20:23], v[76:79]\n\t"
"s_add_u32 m0, s63, 82944\n\t"
"ds_read_b64_tr_b16 a[192:193], v209 offset:27680\n\t"
"v_lshrrev_b32 v122, 16, v122\n\t"
"v_mfma_f32_16x16x32_bf16 v[80:83], a[172:175], a[16:19], v[80:83]\n\t"
"s_waitcnt lgkmcnt(6)\n\t"
"ds_read_b64_tr_b16 a[194:195], v209 offset:32288\n\t"
"v_add_lshl_u32 v122, v122, v121, 2\n\t"
"v_mfma_f32_16x16x32_bf16 v[84:87], a[172:175], a[20:23], v[84:87]\n\t"
"ds_read_b64_tr_b16 a[196:197], v209 offset:27712\n\t"
"ds_read_b64_tr_b16 a[198:199], v209 offset:32320\n\t"
"buffer_load_dwordx4 v204, %[wq], s68 offen offset:0 lds\n\t"
"v_mfma_f32_16x16x32_bf16 v[88:91], a[176:179], a[16:19], v[88:91]\n\t"
"buffer_load_dwordx4 v205, %[wq], s68 offen offset:1024 lds\n\t"
"v_mfma_f32_16x16x32_bf16 v[92:95], a[176:179], a[20:23], v[92:95]\n\t"
"ds_read_b64_tr_b16 a[200:201], v209 offset:27744\n\t"
"v_add_u32 v122, s66, v122\n\t"
"v_mfma_f32_16x16x32_bf16 v[96:99], a[180:183], a[16:19], v[96:99]\n\t"
"s_waitcnt lgkmcnt(5)\n\t"
"ds_write_b32 v122, v17\n\t"
"v_mfma_f32_16x16x32_bf16 v[100:103], a[180:183], a[20:23], v[100:103]\n\t"
"ds_read_b64_tr_b16 a[202:203], v209 offset:32352\n\t"
"ds_read_b64_tr_b16 a[204:205], v209 offset:27776\n\t"
"ds_read_b64_tr_b16 a[206:207], v209 offset:32384\n\t"
"v_add_u32 v121, 512, v120\n\t"
"v_mfma_f32_16x16x32_bf16 v[104:107], a[184:187], a[16:19], v[104:107]\n\t"
"buffer_load_dwordx4 v206, %[wq], s68 offen offset:2048 lds\n\t"
"v_mfma_f32_16x16x32_bf16 v[108:111], a[184:187], a[20:23], v[108:111]\n\t"
"buffer_load_dwordx4 v207, %[wq], s68 offen offset:3072 lds\n\t"
"v_mfma_f32_16x16x32_bf16 v[40:43], a[188:191], a[24:27], v[40:43]\n\t"
"s_add_u32 m0, s63, 87040\n\t"
"ds_read_b64_tr_b16 a[144:145], v209 offset:27808\n\t"
"v_mul_u32_u24 v122, 7282, v121\n\t"
"v_mfma_f32_16x16x32_bf16 v[44:47], a[188:191], a[28:31], v[44:47]\n\t"
"s_waitcnt lgkmcnt(6)\n\t"
"ds_read_b64_tr_b16 a[146:147], v209 offset:32416\n\t"
"ds_read_b64_tr_b16 a[148:149], v209 offset:27840\n\t"
"ds_read_b64_tr_b16 a[150:151], v209 offset:32448\n\t"
"ds_read_b64_tr_b16 a[152:153], v209 offset:27872\n\t"
"ds_read_b64_tr_b16 a[154:155], v209 offset:32480\n\t"
"ds_read_b64_tr_b16 a[156:157], v209 offset:27904\n\t"
"ds_read_b64_tr_b16 a[158:159], v209 offset:32512\n\t"
"v_lshrrev_b32 v122, 16, v122\n\t"
"v_mfma_f32_16x16x32_bf16 v[48:51], a[192:195], a[24:27], v[48:51]\n\t"
"buffer_load_dwordx4 v208, %[wq], s68 offen offset:0 lds\n\t"
"v_mfma_f32_16x16x32_bf16 v[52:55], a[192:195], a[28:31], v[52:55]\n\t"
"v_add_lshl_u32 v122, v122, v121, 2\n\t"
"v_add_u32 v122, s66, v122\n\t"
"v_mfma_f32_16x16x32_bf16 v[56:59], a[196:199], a[24:27], v[56:59]\n\t"
"s_waitcnt vmcnt(10)\n\t"
"ds_write_b32 v122, v18\n\t"
"v_mfma_f32_16x16x32_bf16 v[60:63], a[196:199], a[28:31], v[60:63]\n\t"
"s_waitcnt lgkmcnt(9)\n\t"
"v_add_u32 v121, 768, v120\n\t"
"v_mul_u32_u24 v122, 7282, v121\n\t"
"v_mfma_f32_16x16x32_bf16 v[64:67], a[200:203], a[24:27], v[64:67]\n\t"
"s_waitcnt lgkmcnt(1)\n\t"
"v_lshrrev_b32 v122, 16, v122\n\t"
"v_add_lshl_u32 v122, v122, v121, 2\n\t"
"v_add_u32 v122, s66, v122\n\t"
"v_mfma_f32_16x16x32_bf16 v[68:71], a[200:203], a[28:31], v[68:71]\n\t"
"s_barrier\n\t"
"ds_write_b32 v122, v19\n\t"
"v_mfma_f32_16x16x32_bf16 v[72:75], a[204:207], a[24:27], v[72:75]\n\t"
"s_add_u32 s68, s44, 1328256\n\t"
"ds_read_b64_tr_b16 a[160:161], v209 offset:37888\n\t"
"v_add_u32 v121, 1024, v120\n\t"
"v_mfma_f32_16x16x32_bf16 v[76:79], a[204:207], a[28:31], v[76:79]\n\t"
"ds_read_b64_tr_b16 a[162:163], v209 offset:42496\n\t"
"ds_read_b64_tr_b16 a[164:165], v209 offset:37920\n\t"
"ds_read_b64_tr_b16 a[166:167], v209 offset:42528\n\t"
"v_mul_u32_u24 v122, 7282, v121\n\t"
"v_mfma_f32_16x16x32_bf16 v[80:83], a[144:147], a[24:27], v[80:83]\n\t"
"s_add_u32 m0, s63, 0\n\t"
"ds_read_b64_tr_b16 a[168:169], v209 offset:37952\n\t"
"v_lshrrev_b32 v122, 16, v122\n\t"
"v_mfma_f32_16x16x32_bf16 v[84:87], a[144:147], a[28:31], v[84:87]\n\t"
"v_add_lshl_u32 v122, v122, v121, 2\n\t"
"v_add_u32 v122, s66, v122\n\t"
"v_mfma_f32_16x16x32_bf16 v[88:91], a[148:151], a[24:27], v[88:91]\n\t"
"ds_write_b32 v122, v20\n\t"
"v_mfma_f32_16x16x32_bf16 v[92:95], a[148:151], a[28:31], v[92:95]\n\t"
"ds_read_b64_tr_b16 a[170:171], v209 offset:42560\n\t"
"ds_read_b64_tr_b16 a[172:173], v209 offset:37984\n\t"
"ds_read_b64_tr_b16 a[174:175], v209 offset:42592\n\t"
"v_add_u32 v121, 1280, v120\n\t"
"v_mfma_f32_16x16x32_bf16 v[96:99], a[152:155], a[24:27], v[96:99]\n\t"
"buffer_load_dword v29, v28, %[wq], s62 offen offset:2304\n\t"
"v_mfma_f32_16x16x32_bf16 v[100:103], a[152:155], a[28:31], v[100:103]\n\t"
"buffer_load_dwordx4 v200, %[wq], s68 offen offset:0 lds\n\t"
"v_mfma_f32_16x16x32_bf16 v[104:107], a[156:159], a[24:27], v[104:107]\n\t"
"s_waitcnt lgkmcnt(5)\n\t"
"ds_read_b64_tr_b16 a[176:177], v209 offset:38016\n\t"
"v_mul_u32_u24 v122, 7282, v121\n\t"
"v_mfma_f32_16x16x32_bf16 v[108:111], a[156:159], a[28:31], v[108:111]\n\t"
"ds_read_b64_tr_b16 a[178:179], v209 offset:42624\n\t"
"ds_read_b64_tr_b16 a[180:181], v209 offset:38048\n\t"
"ds_read_b64_tr_b16 a[182:183], v209 offset:42656\n\t"
"v_lshrrev_b32 v122, 16, v122\n\t"
"v_add_lshl_u32 v122, v122, v121, 2\n\t"
"v_add_u32 v122, s66, v122\n\t"
"v_mfma_f32_16x16x32_bf16 v[40:43], a[160:163], a[32:35], v[40:43]\n\t"
"ds_write_b32 v122, v21\n\t"
"v_mfma_f32_16x16x32_bf16 v[44:47], a[160:163], a[36:39], v[44:47]\n\t"
"v_lshrrev_b32 v160, 3, v245\n\t"
"v_bfe_u32 v162, v245, 2, 1\n\t"
"v_mfma_f32_16x16x32_bf16 v[48:51], a[164:167], a[32:35], v[48:51]\n\t"
"s_waitcnt lgkmcnt(5)\n\t"
"ds_read_b64_tr_b16 a[184:185], v209 offset:38080\n\t"
"v_and_b32 v161, 3, v245\n\t"
"v_mul_u32_u24 v162, 165888, v162\n\t"
"v_lshl_add_u32 v161, v161, 4, v162\n\t"
"v_mfma_f32_16x16x32_bf16 v[52:55], a[164:167], a[36:39], v[52:55]\n\t"
"s_lshr_b32 s40, %[wv], 1\n\t"
"ds_read_b64_tr_b16 a[186:187], v209 offset:42688\n\t"
"ds_read_b64_tr_b16 a[188:189], v209 offset:38112\n\t"
"ds_read_b64_tr_b16 a[190:191], v209 offset:42720\n\t"
"v_lshrrev_b32 v162, 2, v160\n\t"
"v_lshl_add_u32 v162, v162, 4, v160\n\t"
"v_mfma_f32_16x16x32_bf16 v[56:59], a[168:171], a[32:35], v[56:59]\n\t"
"s_and_b32 s41, %[wv], 1\n\t"
"ds_read_b64_tr_b16 a[192:193], v209 offset:38144\n\t"
"v_lshrrev_b32 v123, 2, v245\n\t"
"v_mfma_f32_16x16x32_bf16 v[60:63], a[168:171], a[36:39], v[60:63]\n\t"
"s_mul_i32 s41, s41, 36\n\t"
"ds_read_b64_tr_b16 a[194:195], v209 offset:42752\n\t"
"v_and_b32 v124, 3, v245\n\t"
"v_mfma_f32_16x16x32_bf16 v[64:67], a[172:175], a[32:35], v[64:67]\n\t"
"s_lshl_b32 s42, s40, 2\n\t"
"ds_read_b64_tr_b16 a[196:197], v209 offset:47104\n\t"
"v_and_b32 v125, 15, v245\n\t"
"v_mfma_f32_16x16x32_bf16 v[68:71], a[172:175], a[36:39], v[68:71]\n\t"
"s_waitcnt lgkmcnt(8)\n\t"
"ds_read_b64_tr_b16 a[198:199], v209 offset:51712\n\t"
"buffer_load_dwordx4 v201, %[wq], s68 offen offset:1024 lds\n\t"
"v_mfma_f32_16x16x32_bf16 v[72:75], a[176:179], a[32:35], v[72:75]\n\t"
"s_add_u32 s67, %[lds], 160448\n\t"
"v_lshrrev_b32 v126, 4, v245\n\t"
"v_mov_b32 v130, s67\n\t"
"v_mfma_f32_16x16x32_bf16 v[76:79], a[176:179], a[36:39], v[76:79]\n\t"
"s_add_u32 s42, s42, s41\n\t"
"v_mov_b32 v133, 0\n\t"
"v_lshl_add_u32 v131, v125, 2, v130\n\t"
"v_mfma_f32_16x16x32_bf16 v[80:83], a[180:183], a[32:35], v[80:83]\n\t"
"s_mul_i32 s40, s40, 72\n\t"
"ds_write_b32 v131, v133\n\t"
"v_mfma_f32_16x16x32_bf16 v[84:87], a[180:183], a[36:39], v[84:87]\n\t"
"s_waitcnt lgkmcnt(5)\n\t"
"ds_read_b64_tr_b16 a[200:201], v209 offset:47136\n\t"
"ds_read_b64_tr_b16 a[202:203], v209 offset:51744\n\t"
"ds_read_b64_tr_b16 a[204:205], v209 offset:47168\n\t"
"ds_read_b64_tr_b16 a[206:207], v209 offset:51776\n\t"
"v_lshrrev_b32 v141, 2, v245\n\t"
"v_mfma_f32_16x16x32_bf16 v[88:91], a[184:187], a[32:35], v[88:91]\n\t"
"s_add_u32 s40, s40, s41\n\t"
"ds_read_b64_tr_b16 a[144:145], v209 offset:47200\n\t"
"v_and_b32 v136, 3, v245\n\t"
"v_mfma_f32_16x16x32_bf16 v[92:95], a[184:187], a[36:39], v[92:95]\n\t"
"s_add_u32 s67, %[lds], 160448\n\t"
"ds_read_b64_tr_b16 a[146:147], v209 offset:51808\n\t"
"buffer_load_dwordx4 v202, %[wq], s68 offen offset:2048 lds\n\t"
"v_mfma_f32_16x16x32_bf16 v[96:99], a[188:191], a[32:35], v[96:99]\n\t"
"s_waitcnt lgkmcnt(7)\n\t"
"ds_read_b64_tr_b16 a[148:149], v209 offset:47232\n\t"
"v_mov_b32 v138, s67\n\t"
"v_mfma_f32_16x16x32_bf16 v[100:103], a[188:191], a[36:39], v[100:103]\n\t"
"s_add_u32 s40, s40, 16\n\t"
"ds_read_b64_tr_b16 a[150:151], v209 offset:51840\n\t"
"v_add_u32 v162, s42, v162\n\t"
"v_mfma_f32_16x16x32_bf16 v[104:107], a[192:195], a[32:35], v[104:107]\n\t"
"ds_read_b64_tr_b16 a[152:153], v209 offset:47264\n\t"
"v_mul_u32_u24 v162, 1152, v162\n\t"
"v_mfma_f32_16x16x32_bf16 v[108:111], a[192:195], a[36:39], v[108:111]\n\t"
"ds_read_b64_tr_b16 a[154:155], v209 offset:51872\n\t"
"v_mul_u32_u24 v136, 18, v136\n\t"
"v_mfma_f32_16x16x32_bf16 v[40:43], a[196:199], a[40:43], v[40:43]\n\t"
"s_add_u32 s67, %[lds], 153600\n\t"
"ds_read_b64_tr_b16 a[156:157], v209 offset:47296\n\t"
"v_sub_u32 v136, v141, v136\n\t"
"v_mfma_f32_16x16x32_bf16 v[44:47], a[196:199], a[44:47], v[44:47]\n\t"
"s_waitcnt lgkmcnt(7)\n\t"
"ds_read_b64_tr_b16 a[158:159], v209 offset:51904\n\t"
"v_add_u32 v165, v162, v161\n\t"
"v_mfma_f32_16x16x32_bf16 v[48:51], a[200:203], a[40:43], v[48:51]\n\t"
"buffer_load_dwordx4 v203, %[wq], s68 offen offset:3072 lds\n\t"
"v_mfma_f32_16x16x32_bf16 v[52:55], a[200:203], a[44:47], v[52:55]\n\t"
"s_add_u32 m0, s63, 4096\n\t"
"ds_read_b64_tr_b16 a[160:161], v209 offset:47328\n\t"
"v_and_b32 v162, 3, v160\n\t"
"v_mfma_f32_16x16x32_bf16 v[56:59], a[204:207], a[40:43], v[56:59]\n\t"
"s_waitcnt lgkmcnt(5)\n\t"
"ds_read_b64_tr_b16 a[162:163], v209 offset:51936\n\t"
"v_add_u32 v162, s40, v162\n\t"
"v_mfma_f32_16x16x32_bf16 v[60:63], a[204:207], a[44:47], v[60:63]\n\t"
"ds_read_b64_tr_b16 a[164:165], v209 offset:47360\n\t"
"ds_read_b64_tr_b16 a[166:167], v209 offset:51968\n\t"
"v_mul_u32_u24 v162, 1152, v162\n\t"
"v_mfma_f32_16x16x32_bf16 v[64:67], a[144:147], a[40:43], v[64:67]\n\t"
"ds_read_b64_tr_b16 a[168:169], v210 offset:3072\n\t"
"v_add_u32 v162, v162, v161\n\t"
"v_mfma_f32_16x16x32_bf16 v[68:71], a[144:147], a[44:47], v[68:71]\n\t"
"ds_read_b64_tr_b16 a[170:171], v210 offset:7680\n\t"
"v_add_u32 v163, 2, v160\n\t"
"v_mfma_f32_16x16x32_bf16 v[72:75], a[148:151], a[40:43], v[72:75]\n\t"
"s_waitcnt lgkmcnt(6)\n\t"
"ds_read_b64_tr_b16 a[172:173], v210 offset:3104\n\t"
"v_and_b32 v163, 4, v163\n\t"
"v_mfma_f32_16x16x32_bf16 v[76:79], a[148:151], a[44:47], v[76:79]\n\t"
"ds_read_b64_tr_b16 a[174:175], v210 offset:7712\n\t"
"v_lshlrev_b32 v163, 28, v163\n\t"
"v_mfma_f32_16x16x32_bf16 v[80:83], a[152:155], a[40:43], v[80:83]\n\t"
"buffer_load_dwordx4 v204, %[wq], s68 offen offset:0 lds\n\t"
"v_mfma_f32_16x16x32_bf16 v[84:87], a[152:155], a[44:47], v[84:87]\n\t"
"ds_read_b64_tr_b16 a[176:177], v210 offset:3136\n\t"
"v_or_b32 v166, v162, v163\n\t"
"v_mfma_f32_16x16x32_bf16 v[88:91], a[156:159], a[40:43], v[88:91]\n\t"
"s_waitcnt lgkmcnt(5)\n\t"
"ds_read_b64_tr_b16 a[178:179], v210 offset:7744\n\t"
"v_lshrrev_b32 v162, 2, v245\n\t"
"v_mfma_f32_16x16x32_bf16 v[92:95], a[156:159], a[44:47], v[92:95]\n\t"
"ds_read_b64_tr_b16 a[180:181], v210 offset:3168\n\t"
"ds_read_b64_tr_b16 a[182:183], v210 offset:7776\n\t"
"v_mul_i32_i24 v137, 40, v141\n\t"
"v_mfma_f32_16x16x32_bf16 v[96:99], a[160:163], a[40:43], v[96:99]\n\t"
"v_add_u32 v137, s67, v137\n\t"
"v_and_b32 v163, 3, v245\n\t"
"v_mfma_f32_16x16x32_bf16 v[100:103], a[160:163], a[44:47], v[100:103]\n\t"
"v_add_u32 v139, 0, v136\n\t"
"v_lshlrev_b32 v163, 3, v163\n\t"
"v_mfma_f32_16x16x32_bf16 v[104:107], a[164:167], a[40:43], v[104:107]\n\t"
"s_lshl_b32 s40, %[wv], 5\n\t"
"v_cmp_gt_u32 vcc, 18, v139\n\t"
"v_lshl_add_u32 v162, v162, 7, v163\n\t"
"v_mfma_f32_16x16x32_bf16 v[108:111], a[164:167], a[44:47], v[108:111]\n\t"
"s_waitcnt lgkmcnt(4)\n\t"
"ds_read_b64_tr_b16 a[184:185], v210 offset:3200\n\t"
"ds_read_b64_tr_b16 a[186:187], v210 offset:7808\n\t"
"ds_read_b64_tr_b16 a[188:189], v210 offset:3232\n\t"
"ds_read_b64_tr_b16 a[190:191], v210 offset:7840\n\t"
"v_add_u32 v140, 0, v137\n\t"
"v_mfma_f32_16x16x32_bf16 v[40:43], a[168:171], a[48:51], v[40:43]\n\t"
"s_add_u32 s40, s40, %[lds]\n\t"
"ds_read_b64_tr_b16 a[192:193], v210 offset:3264\n\t"
"v_cndmask_b32 v213, v138, v140, vcc\n\t"
"v_mfma_f32_16x16x32_bf16 v[44:47], a[168:171], a[52:55], v[44:47]\n\t"
"s_add_u32 s40, s40, 115712\n\t"
"ds_read_b64_tr_b16 a[194:195], v210 offset:7872\n\t"
"buffer_load_dwordx4 v205, %[wq], s68 offen offset:1024 lds\n\t"
"v_mfma_f32_16x16x32_bf16 v[48:51], a[172:175], a[48:51], v[48:51]\n\t"
"s_waitcnt lgkmcnt(6)\n\t"
"ds_read_b64_tr_b16 a[196:197], v210 offset:3296\n\t"
"v_add_u32 v139, 16, v136\n\t"
"v_mfma_f32_16x16x32_bf16 v[52:55], a[172:175], a[52:55], v[52:55]\n\t"
"ds_read_b64_tr_b16 a[198:199], v210 offset:7904\n\t"
"v_cmp_gt_u32 vcc, 18, v139\n\t"
"v_mfma_f32_16x16x32_bf16 v[56:59], a[176:179], a[48:51], v[56:59]\n\t"
"ds_read_b64_tr_b16 a[200:201], v210 offset:3328\n\t"
"v_add_u32 v140, 640, v137\n\t"
"v_mfma_f32_16x16x32_bf16 v[60:63], a[176:179], a[52:55], v[60:63]\n\t"
"ds_read_b64_tr_b16 a[202:203], v210 offset:7936\n\t"
"v_cndmask_b32 v214, v138, v140, vcc\n\t"
"v_mfma_f32_16x16x32_bf16 v[64:67], a[180:183], a[48:51], v[64:67]\n\t"
"s_waitcnt lgkmcnt(6)\n\t"
"ds_read_b64_tr_b16 a[204:205], v210 offset:12288\n\t"
"v_add_u32 v139, 32, v136\n\t"
"v_mfma_f32_16x16x32_bf16 v[68:71], a[180:183], a[52:55], v[68:71]\n\t"
"ds_read_b64_tr_b16 a[206:207], v210 offset:16896\n\t"
"v_cmp_gt_u32 vcc, 18, v139\n\t"
"v_mfma_f32_16x16x32_bf16 v[72:75], a[184:187], a[48:51], v[72:75]\n\t"
"buffer_load_dwordx4 v206, %[wq], s68 offen offset:2048 lds\n\t"
"v_mfma_f32_16x16x32_bf16 v[76:79], a[184:187], a[52:55], v[76:79]\n\t"
"v_add_u32 v140, 1280, v137\n\t"
"v_add_u32 v167, s40, v162\n\t"
"v_mfma_f32_16x16x32_bf16 v[80:83], a[188:191], a[48:51], v[80:83]\n\t"
"s_waitcnt lgkmcnt(4)\n\t"
"ds_read_b64_tr_b16 a[144:145], v210 offset:12320\n\t"
"v_cndmask_b32 v215, v138, v140, vcc\n\t"
"v_mfma_f32_16x16x32_bf16 v[84:87], a[188:191], a[52:55], v[84:87]\n\t"
"ds_read_b64_tr_b16 a[146:147], v210 offset:16928\n\t"
"ds_read_b64_tr_b16 a[148:149], v210 offset:12352\n\t"
"ds_read_b64_tr_b16 a[150:151], v210 offset:16960\n\t"
"v_add_u32 v139, 48, v136\n\t"
"v_mfma_f32_16x16x32_bf16 v[88:91], a[192:195], a[48:51], v[88:91]\n\t"
"s_lshl_b32 s43, %[a], 4\n\t"
"v_cmp_gt_u32 vcc, 18, v139\n\t"
"v_lshlrev_b32 v168, 4, v245\n\t"
"v_mfma_f32_16x16x32_bf16 v[92:95], a[192:195], a[52:55], v[92:95]\n\t"
"s_add_u32 s43, s43, %[mi]\n\t"
"ds_read_b64_tr_b16 a[152:153], v210 offset:12384\n\t"
"v_add_u32 v140, 1920, v137\n\t"
"v_mfma_f32_16x16x32_bf16 v[96:99], a[196:199], a[48:51], v[96:99]\n\t"
"s_waitcnt lgkmcnt(5)\n\t"
"ds_read_b64_tr_b16 a[154:155], v210 offset:16992\n\t"
"v_cndmask_b32 v216, v138, v140, vcc\n\t"
"v_mfma_f32_16x16x32_bf16 v[100:103], a[196:199], a[52:55], v[100:103]\n\t"
"ds_read_b64_tr_b16 a[156:157], v210 offset:12416\n\t"
"ds_read_b64_tr_b16 a[158:159], v210 offset:17024\n\t"
"v_add_u32 v139, 64, v136\n\t"
"v_mfma_f32_16x16x32_bf16 v[104:107], a[200:203], a[48:51], v[104:107]\n\t"
"buffer_load_dwordx4 v207, %[wq], s68 offen offset:3072 lds\n\t"
"v_mfma_f32_16x16x32_bf16 v[108:111], a[200:203], a[52:55], v[108:111]\n\t"
"s_add_u32 m0, s63, 8192\n\t"
"ds_read_b64_tr_b16 a[160:161], v210 offset:12448\n\t"
"v_cmp_gt_u32 vcc, 18, v139\n\t"
"v_mfma_f32_16x16x32_bf16 v[40:43], a[204:207], a[56:59], v[40:43]\n\t"
"s_waitcnt lgkmcnt(5)\n\t"
"ds_read_b64_tr_b16 a[162:163], v210 offset:17056\n\t"
"v_add_u32 v140, 2560, v137\n\t"
"v_mfma_f32_16x16x32_bf16 v[44:47], a[204:207], a[60:63], v[44:47]\n\t"
"ds_read_b64_tr_b16 a[164:165], v210 offset:12480\n\t"
"ds_read_b64_tr_b16 a[166:167], v210 offset:17088\n\t"
"ds_read_b64_tr_b16 a[168:169], v210 offset:12512\n\t"
"ds_read_b64_tr_b16 a[170:171], v210 offset:17120\n\t"
"ds_read_b64_tr_b16 a[172:173], v210 offset:12544\n\t"
"ds_read_b64_tr_b16 a[174:175], v210 offset:17152\n\t"
"v_cndmask_b32 v217, v138, v140, vcc\n\t"
"v_mfma_f32_16x16x32_bf16 v[48:51], a[144:147], a[56:59], v[48:51]\n\t"
"v_add_u32 v139, 80, v136\n\t"
"v_cmp_gt_u32 vcc, 18, v139\n\t"
"v_mfma_f32_16x16x32_bf16 v[52:55], a[144:147], a[60:63], v[52:55]\n\t"
"buffer_load_dwordx4 v208, %[wq], s68 offen offset:0 lds\n\t"
"v_mfma_f32_16x16x32_bf16 v[56:59], a[148:151], a[56:59], v[56:59]\n\t"
"s_waitcnt vmcnt(10)\n\t"
"v_add_u32 v140, 3200, v137\n\t"
"v_cndmask_b32 v218, v138, v140, vcc\n\t"
"v_mfma_f32_16x16x32_bf16 v[60:63], a[148:151], a[60:63], v[60:63]\n\t"
"s_waitcnt lgkmcnt(8)\n\t"
"v_add_u32 v139, -8, v136\n\t"
"v_cmp_gt_u32 vcc, 18, v139\n\t"
"v_mfma_f32_16x16x32_bf16 v[64:67], a[152:155], a[56:59], v[64:67]\n\t"
"s_waitcnt lgkmcnt(0)\n\t"
"v_add_u32 v140, 2560, v137\n\t"
"v_cndmask_b32 v219, v138, v140, vcc\n\t"
"v_mfma_f32_16x16x32_bf16 v[68:71], a[152:155], a[60:63], v[68:71]\n\t"
"s_barrier\n\t"
"v_add_u32 v139, 8, v136\n\t"
"v_cmp_gt_u32 vcc, 18, v139\n\t"
"v_mfma_f32_16x16x32_bf16 v[72:75], a[156:159], a[56:59], v[72:75]\n\t"
"s_add_u32 m0, s63, 37888\n\t"
"ds_read_b64_tr_b16 a[176:177], v210 offset:25600\n\t"
"v_add_u32 v140, 3200, v137\n\t"
"v_mfma_f32_16x16x32_bf16 v[76:79], a[156:159], a[60:63], v[76:79]\n\t"
"ds_read_b64_tr_b16 a[178:179], v210 offset:30208\n\t"
"ds_read_b64_tr_b16 a[180:181], v210 offset:25632\n\t"
"ds_read_b64_tr_b16 a[182:183], v210 offset:30240\n\t"
"s_add_u32 s40, s43, 0\n\t"
"s_mul_i32 s41, s40, 6554\n\t"
"s_lshr_b32 s41, s41, 16\n\t"
"s_mul_i32 s42, s41, 10\n\t"
"s_sub_u32 s40, s40, s42\n\t"
"s_lshl_b32 s41, s41, 6\n\t"
"s_cmp_ge_u32 s40, 5\n\t"
"s_cselect_b32 s42, 331776, 0\n\t"
"s_cselect_b32 s98, 5, 0\n\t"
"s_add_u32 s41, s41, s42\n\t"
"s_sub_u32 s40, s40, s98\n\t"
"s_and_b32 s42, s40, 2\n\t"
"s_mul_i32 s42, s42, 41472\n\t"
"s_add_u32 s41, s41, s42\n\t"
"s_and_b32 s42, s40, 1\n\t"
"s_mul_i32 s42, s42, 9216\n\t"
"s_add_u32 s41, s41, s42\n\t"
"s_cmp_eq_u32 s40, 4\n\t"
"s_cselect_b64 s[98:99], -1, 0\n\t"
"v_cndmask_b32 v164, v165, v166, s[98:99]\n\t"
"s_mov_b32 s98, m0\n\t"
"s_lshl_b32 s40, %[wv], 10\n\t"
"s_add_u32 s40, s40, %[lds]\n\t"
"s_add_u32 m0, s40, 115712\n\t"
"s_nop 0\n\t"
"buffer_load_dwordx4 v164, %[wo], s41 offen lds\n\t"
"s_mov_b32 m0, s98\n\t"
"v_mfma_f32_16x16x32_bf16 v[80:83], a[160:163], a[56:59], v[80:83]\n\t"
"ds_read_b64_tr_b16 a[184:185], v210 offset:25664\n\t"
"v_cndmask_b32 v220, v138, v140, vcc\n\t"
"v_mfma_f32_16x16x32_bf16 v[84:87], a[160:163], a[60:63], v[84:87]\n\t"
"ds_read_b64_tr_b16 a[186:187], v210 offset:30272\n\t"
"v_add_u32 v139, 24, v136\n\t"
"v_mfma_f32_16x16x32_bf16 v[88:91], a[164:167], a[56:59], v[88:91]\n\t"
"ds_read_b64_tr_b16 a[188:189], v210 offset:25696\n\t"
"v_cmp_gt_u32 vcc, 18, v139\n\t"
"v_mfma_f32_16x16x32_bf16 v[92:95], a[164:167], a[60:63], v[92:95]\n\t"
"ds_read_b64_tr_b16 a[190:191], v210 offset:30304\n\t"
"v_add_u32 v140, 3840, v137\n\t"
"v_mfma_f32_16x16x32_bf16 v[96:99], a[168:171], a[56:59], v[96:99]\n\t"
"ds_read_b64_tr_b16 a[192:193], v210 offset:25728\n\t"
"v_cndmask_b32 v221, v138, v140, vcc\n\t"
"v_mfma_f32_16x16x32_bf16 v[100:103], a[168:171], a[60:63], v[100:103]\n\t"
"ds_read_b64_tr_b16 a[194:195], v210 offset:30336\n\t"
"v_add_u32 v139, 40, v136\n\t"
"v_mfma_f32_16x16x32_bf16 v[104:107], a[172:175], a[56:59], v[104:107]\n\t"
"s_waitcnt lgkmcnt(6)\n\t"
"ds_read_b64_tr_b16 a[196:197], v210 offset:25760\n\t"
"v_cmp_gt_u32 vcc, 18, v139\n\t"
"v_mfma_f32_16x16x32_bf16 v[108:111], a[172:175], a[60:63], v[108:111]\n\t"
"ds_read_b64_tr_b16 a[198:199], v210 offset:30368\n\t"
"v_add_u32 v140, 4480, v137\n\t"
"v_mfma_f32_16x16x32_bf16 v[40:43], a[176:179], a[64:67], v[40:43]\n\t"
"buffer_load_dwordx4 v200, %[wq], s45 offen offset:0 lds\n\t"
"v_mfma_f32_16x16x32_bf16 v[44:47], a[176:179], a[68:71], v[44:47]\n\t"
"ds_read_b64_tr_b16 a[200:201], v210 offset:25792\n\t"
"v_cndmask_b32 v222, v138, v140, vcc\n\t"
"v_mfma_f32_16x16x32_bf16 v[48:51], a[180:183], a[64:67], v[48:51]\n\t"
"s_waitcnt lgkmcnt(5)\n\t"
"s_add_u32 s40, s43, 64\n\t"
"s_mul_i32 s41, s40, 6554\n\t"
"s_lshr_b32 s41, s41, 16\n\t"
"s_mul_i32 s42, s41, 10\n\t"
"s_sub_u32 s40, s40, s42\n\t"
"s_lshl_b32 s41, s41, 6\n\t"
"s_cmp_ge_u32 s40, 5\n\t"
"s_cselect_b32 s42, 331776, 0\n\t"
"s_cselect_b32 s98, 5, 0\n\t"
"s_add_u32 s41, s41, s42\n\t"
"s_sub_u32 s40, s40, s98\n\t"
"s_and_b32 s42, s40, 2\n\t"
"s_mul_i32 s42, s42, 41472\n\t"
"s_add_u32 s41, s41, s42\n\t"
"s_and_b32 s42, s40, 1\n\t"
"s_mul_i32 s42, s42, 9216\n\t"
"s_add_u32 s41, s41, s42\n\t"
"s_cmp_eq_u32 s40, 4\n\t"
"s_cselect_b64 s[98:99], -1, 0\n\t"
"v_cndmask_b32 v164, v165, v166, s[98:99]\n\t"
"s_mov_b32 s98, m0\n\t"
"s_lshl_b32 s40, %[wv], 10\n\t"
"s_add_u32 s40, s40, %[lds]\n\t"
"s_add_u32 m0, s40, 119808\n\t"
"s_nop 0\n\t"
"buffer_load_dwordx4 v164, %[wo], s41 offen lds\n\t"
"s_mov_b32 m0, s98\n\t"
"v_mfma_f32_16x16x32_bf16 v[52:55], a[180:183], a[68:71], v[52:55]\n\t"
"ds_read_b64_tr_b16 a[202:203], v210 offset:30400\n\t"
"ds_read_b64_tr_b16 a[204:205], v210 offset:25824\n\t"
"ds_read_b64_tr_b16 a[206:207], v210 offset:30432\n\t"
"v_add_u32 v139, 56, v136\n\t"
"v_mfma_f32_16x16x32_bf16 v[56:59], a[184:187], a[64:67], v[56:59]\n\t"
"buffer_load_dwordx4 v201, %[wq], s45 offen offset:1024 lds\n\t"
"v_mfma_f32_16x16x32_bf16 v[60:63], a[184:187], a[68:71], v[60:63]\n\t"
"ds_read_b64_tr_b16 a[144:145], v210 offset:25856\n\t"
"v_cmp_gt_u32 vcc, 18, v139\n\t"
"v_mfma_f32_16x16x32_bf16 v[64:67], a[188:191], a[64:67], v[64:67]\n\t"
"s_waitcnt lgkmcnt(5)\n\t"
"ds_read_b64_tr_b16 a[146:147], v210 offset:30464\n\t"
"v_add_u32 v140, 5120, v137\n\t"
"v_mfma_f32_16x16x32_bf16 v[68:71], a[188:191], a[68:71], v[68:71]\n\t"
"ds_read_b64_tr_b16 a[148:149], v210 offset:34816\n\t"
"ds_read_b64_tr_b16 a[150:151], v210 offset:39424\n\t"
"v_cndmask_b32 v223, v138, v140, vcc\n\t"
"v_mfma_f32_16x16x32_bf16 v[72:75], a[192:195], a[64:67], v[72:75]\n\t"
"ds_read_b64_tr_b16 a[152:153], v210 offset:34848\n\t"
"v_add_u32 v139, 72, v136\n\t"
"v_mfma_f32_16x16x32_bf16 v[76:79], a[192:195], a[68:71], v[76:79]\n\t"
"s_add_u32 s40, s43, 128\n\t"
"s_mul_i32 s41, s40, 6554\n\t"
"s_lshr_b32 s41, s41, 16\n\t"
"s_mul_i32 s42, s41, 10\n\t"
"s_sub_u32 s40, s40, s42\n\t"
"s_lshl_b32 s41, s41, 6\n\t"
"s_cmp_ge_u32 s40, 5\n\t"
"s_cselect_b32 s42, 331776, 0\n\t"
"s_cselect_b32 s98, 5, 0\n\t"
"s_add_u32 s41, s41, s42\n\t"
"s_sub_u32 s40, s40, s98\n\t"
"s_and_b32 s42, s40, 2\n\t"
"s_mul_i32 s42, s42, 41472\n\t"
"s_add_u32 s41, s41, s42\n\t"
"s_and_b32 s42, s40, 1\n\t"
"s_mul_i32 s42, s42, 9216\n\t"
"s_add_u32 s41, s41, s42\n\t"
"s_cmp_eq_u32 s40, 4\n\t"
"s_cselect_b64 s[98:99], -1, 0\n\t"
"v_cndmask_b32 v164, v165, v166, s[98:99]\n\t"
"s_mov_b32 s98, m0\n\t"
"s_lshl_b32 s40, %[wv], 10\n\t"
"s_add_u32 s40, s40, %[lds]\n\t"
"s_add_u32 m0, s40, 123904\n\t"
"s_nop 0\n\t"
"buffer_load_dwordx4 v164, %[wo], s41 offen lds\n\t"
"s_mov_b32 m0, s98\n\t"
"v_mfma_f32_16x16x32_bf16 v[80:83], a[196:199], a[64:67], v[80:83]\n\t"
"s_waitcnt lgkmcnt(5)\n\t"
"ds_read_b64_tr_b16 a[154:155], v210 offset:39456\n\t"
"v_cmp_gt_u32 vcc, 18, v139\n\t"
"v_mfma_f32_16x16x32_bf16 v[84:87], a[196:199], a[68:71], v[84:87]\n\t"
"ds_read_b64_tr_b16 a[156:157], v210 offset:34880\n\t"
"ds_read_b64_tr_b16 a[158:159], v210 offset:39488\n\t"
"v_add_u32 v140, 5760, v137\n\t"
"v_mfma_f32_16x16x32_bf16 v[88:91], a[200:203], a[64:67], v[88:91]\n\t"
"buffer_load_dwordx4 v202, %[wq], s45 offen offset:2048 lds\n\t"
"v_mfma_f32_16x16x32_bf16 v[92:95], a[200:203], a[68:71], v[92:95]\n\t"
"ds_read_b64_tr_b16 a[160:161], v210 offset:34912\n\t"
"v_cndmask_b32 v224, v138, v140, vcc\n\t"
"v_mfma_f32_16x16x32_bf16 v[96:99], a[204:207], a[64:67], v[96:99]\n\t"
"s_waitcnt lgkmcnt(5)\n\t"
"ds_read_b64_tr_b16 a[162:163], v210 offset:39520\n\t"
"v_lshlrev_b32 v230, 4, v245\n\t"
"v_mfma_f32_16x16x32_bf16 v[100:103], a[204:207], a[68:71], v[100:103]\n\t"
"ds_read_b64_tr_b16 a[164:165], v210 offset:34944\n\t"
"ds_read_b64_tr_b16 a[166:167], v210 offset:39552\n\t"
"v_mul_u32_u24 v128, 296, v125\n\t"
"v_mfma_f32_16x16x32_bf16 v[104:107], a[144:147], a[64:67], v[104:107]\n\t"
"ds_read_b64_tr_b16 a[168:169], v210 offset:34976\n\t"
"v_mul_u32_u24 v129, 72, v126\n\t"
"v_mfma_f32_16x16x32_bf16 v[108:111], a[144:147], a[68:71], v[108:111]\n\t"
"s_mul_i32 s64, %[wv], 9472\n\t"
"ds_read_b64_tr_b16 a[170:171], v210 offset:39584\n\t"
"v_add_u32 v128, v128, v129\n\t"
"v_mfma_f32_16x16x32_bf16 v[40:43], a[148:151], a[72:75], v[40:43]\n\t"
"s_add_u32 s65, %[lds], 115712\n\t"
"ds_read_b64_tr_b16 a[172:173], v210 offset:35008\n\t"
"buffer_load_dwordx4 v203, %[wq], s45 offen offset:3072 lds\n\t"
"v_mfma_f32_16x16x32_bf16 v[44:47], a[148:151], a[76:79], v[44:47]\n\t"
"s_waitcnt lgkmcnt(7)\n\t"
"ds_read_b64_tr_b16 a[174:175], v210 offset:39616\n\t"
"v_mfma_f32_16x16x32_bf16 v[48:51], a[152:155], a[72:75], v[48:51]\n\t"
"s_add_u32 s64, s64, s65\n\t"
"ds_read_b64_tr_b16 a[176:177], v210 offset:35040\n\t"
"v_add_u32 v225, s64, v128\n\t"
"v_mfma_f32_16x16x32_bf16 v[52:55], a[152:155], a[76:79], v[52:55]\n\t"
"s_add_u32 m0, s63, 41984\n\t"
"ds_read_b64_tr_b16 a[178:179], v210 offset:39648\n\t"
"v_mul_u32_u24 v128, 296, v123\n\t"
"v_mfma_f32_16x16x32_bf16 v[56:59], a[156:159], a[72:75], v[56:59]\n\t"
"s_mul_i32 s64, %[wv], 72\n\t"
"ds_read_b64_tr_b16 a[180:181], v210 offset:35072\n\t"
"v_lshl_add_u32 v128, v124, 3, v128\n\t"
"v_mfma_f32_16x16x32_bf16 v[60:63], a[156:159], a[76:79], v[60:63]\n\t"
"s_waitcnt lgkmcnt(7)\n\t"
"ds_read_b64_tr_b16 a[182:183], v210 offset:39680\n\t"
"buffer_load_dwordx4 v204, %[wq], s45 offen offset:0 lds\n\t"
"v_mfma_f32_16x16x32_bf16 v[64:67], a[160:163], a[72:75], v[64:67]\n\t"
"s_add_u32 s64, s64, s65\n\t"
"ds_read_b64_tr_b16 a[184:185], v210 offset:44032\n\t"
"v_add_u32 v226, s64, v128\n\t"
"v_mfma_f32_16x16x32_bf16 v[68:71], a[160:163], a[76:79], v[68:71]\n\t"
"ds_read_b64_tr_b16 a[186:187], v210 offset:48640\n\t"
"v_subrev_u32 v250, 115712, v225\n\t"
"v_mfma_f32_16x16x32_bf16 v[72:75], a[164:167], a[72:75], v[72:75]\n\t"
"s_mul_i32 s64, %[a], 9\n\t"
"ds_read_b64_tr_b16 a[188:189], v210 offset:44064\n\t"
"v_subrev_u32 v251, 115712, v226\n\t"
"v_mfma_f32_16x16x32_bf16 v[76:79], a[164:167], a[76:79], v[76:79]\n\t"
"s_waitcnt lgkmcnt(7)\n\t"
"ds_read_b64_tr_b16 a[190:191], v210 offset:48672\n\t"
"buffer_load_dwordx4 v205, %[wq], s45 offen offset:1024 lds\n\t"
"v_mfma_f32_16x16x32_bf16 v[80:83], a[168:171], a[72:75], v[80:83]\n\t"
"s_lshr_b32 s64, s64, 1\n\t"
"ds_read_b64_tr_b16 a[192:193], v210 offset:44096\n\t"
"v_mfma_f32_16x16x32_bf16 v[84:87], a[168:171], a[76:79], v[84:87]\n\t"
"s_lshl_b32 s65, %[mi], 2\n\t"
"ds_read_b64_tr_b16 a[194:195], v210 offset:48704\n\t"
"v_mfma_f32_16x16x32_bf16 v[88:91], a[172:175], a[72:75], v[88:91]\n\t"
"s_add_u32 s65, s65, %[wv]\n\t"
"ds_read_b64_tr_b16 a[196:197], v210 offset:44128\n\t"
"v_mfma_f32_16x16x32_bf16 v[92:95], a[172:175], a[76:79], v[92:95]\n\t"
"s_waitcnt lgkmcnt(7)\n\t"
"ds_read_b64_tr_b16 a[198:199], v210 offset:48736\n\t"
"v_mfma_f32_16x16x32_bf16 v[96:99], a[176:179], a[72:75], v[96:99]\n\t"
"s_mul_i32 s65, s65, 36\n\t"
"ds_read_b64_tr_b16 a[200:201], v210 offset:44160\n\t"
"v_mfma_f32_16x16x32_bf16 v[100:103], a[176:179], a[76:79], v[100:103]\n\t"
"s_add_u32 s65, s65, s64\n\t"
"ds_read_b64_tr_b16 a[202:203], v210 offset:48768\n\t"
"v_mfma_f32_16x16x32_bf16 v[104:107], a[180:183], a[72:75], v[104:107]\n\t"
"s_lshl_b32 s65, s65, 10\n\t"
"ds_read_b64_tr_b16 a[204:205], v210 offset:44192\n\t"
"buffer_load_dwordx4 v206, %[wq], s45 offen offset:2048 lds\n\t"
"v_mfma_f32_16x16x32_bf16 v[108:111], a[180:183], a[76:79], v[108:111]\n\t"
"s_waitcnt lgkmcnt(7)\n\t"
"ds_read_b64_tr_b16 a[206:207], v210 offset:48800\n\t"
"v_mfma_f32_16x16x32_bf16 v[40:43], a[184:187], a[80:83], v[40:43]\n\t"
"s_add_u32 s47, s65, 37748736\n\t"
"ds_read_b64_tr_b16 a[144:145], v210 offset:44224\n\t"
"v_mfma_f32_16x16x32_bf16 v[44:47], a[184:187], a[84:87], v[44:47]\n\t"
"s_add_u32 s65, %[lds], 37888\n\t"
"ds_read_b64_tr_b16 a[146:147], v210 offset:48832\n\t"
"v_mfma_f32_16x16x32_bf16 v[48:51], a[188:191], a[80:83], v[48:51]\n\t"
"s_waitcnt lgkmcnt(6)\n\t"
"ds_read_b64_tr_b16 a[148:149], v210 offset:44256\n\t"
"v_mfma_f32_16x16x32_bf16 v[52:55], a[188:191], a[84:87], v[52:55]\n\t"
"s_mul_i32 s64, %[wv], 10240\n\t"
"ds_read_b64_tr_b16 a[150:151], v210 offset:48864\n\t"
"v_mfma_f32_16x16x32_bf16 v[56:59], a[192:195], a[80:83], v[56:59]\n\t"
"s_add_u32 s64, s64, s65\n\t"
"ds_read_b64_tr_b16 a[152:153], v210 offset:44288\n\t"
"v_add_u32 v227, s64, v230\n\t"
"v_mfma_f32_16x16x32_bf16 v[60:63], a[192:195], a[84:87], v[60:63]\n\t"
"s_lshl_b32 s64, %[wv], 10\n\t"
"ds_read_b64_tr_b16 a[154:155], v210 offset:48896\n\t"
"v_mfma_f32_16x16x32_bf16 v[64:67], a[196:199], a[80:83], v[64:67]\n\t"
"s_add_u32 s64, s64, s65\n\t"
"ds_read_b64_tr_b16 a[156:157], v210 offset:53248\n\t"
"v_add_u32 v228, s64, v230\n\t"
"v_mfma_f32_16x16x32_bf16 v[68:71], a[196:199], a[84:87], v[68:71]\n\t"
"s_waitcnt lgkmcnt(7)\n\t"
"ds_read_b64_tr_b16 a[158:159], v210 offset:57856\n\t"
"v_add_u32 v229, s65, v230\n\t"
"v_mfma_f32_16x16x32_bf16 v[72:75], a[200:203], a[80:83], v[72:75]\n\t"
"s_mov_b32 s72, 0x3fb8aa3b\n\t"
"ds_read_b64_tr_b16 a[160:161], v210 offset:53280\n\t"
"buffer_load_dwordx4 v207, %[wq], s45 offen offset:3072 lds\n\t"
"v_mfma_f32_16x16x32_bf16 v[76:79], a[200:203], a[84:87], v[76:79]\n\t"
"s_mov_b32 s73, 0x3fb8aa3b\n\t"
"ds_read_b64_tr_b16 a[162:163], v210 offset:57888\n\t"
"v_mfma_f32_16x16x32_bf16 v[80:83], a[204:207], a[80:83], v[80:83]\n\t"
"s_mov_b32 s74, 0x3c3c3c3c\n\t"
"ds_read_b64_tr_b16 a[164:165], v210 offset:53312\n\t"
"v_mfma_f32_16x16x32_bf16 v[84:87], a[204:207], a[84:87], v[84:87]\n\t"
"s_waitcnt lgkmcnt(7)\n\t"
"ds_read_b64_tr_b16 a[166:167], v210 offset:57920\n\t"
"v_mfma_f32_16x16x32_bf16 v[88:91], a[144:147], a[80:83], v[88:91]\n\t"
"s_mov_b32 s75, 0x3c3c3c3c\n\t"
"ds_read_b64_tr_b16 a[168:169], v210 offset:53344\n\t"
"v_mfma_f32_16x16x32_bf16 v[92:95], a[144:147], a[84:87], v[92:95]\n\t"
"s_mov_b32 s54, 0xffff0000\n\t"
"ds_read_b64_tr_b16 a[170:171], v210 offset:57952\n\t"
"v_mfma_f32_16x16x32_bf16 v[96:99], a[148:151], a[80:83], v[96:99]\n\t"
"s_waitcnt lgkmcnt(6)\n\t"
"ds_read_b64_tr_b16 a[172:173], v210 offset:53376\n\t"
"v_mfma_f32_16x16x32_bf16 v[100:103], a[148:151], a[84:87], v[100:103]\n\t"
"s_mov_b32 s55, 0xffff0000\n\t"
"ds_read_b64_tr_b16 a[174:175], v210 offset:57984\n\t"
"v_mfma_f32_16x16x32_bf16 v[104:107], a[152:155], a[80:83], v[104:107]\n\t"
"s_lshl_b32 s64, %[a], 4\n\t"
"ds_read_b64_tr_b16 a[176:177], v210 offset:53408\n\t"
"v_mfma_f32_16x16x32_bf16 v[108:111], a[152:155], a[84:87], v[108:111]\n\t"
"s_add_u32 s64, s64, %[mi]\n\t"
"ds_read_b64_tr_b16 a[178:179], v210 offset:58016\n\t"
"v_mfma_f32_16x16x32_bf16 v[40:43], a[156:159], a[88:91], v[40:43]\n\t"
"s_waitcnt lgkmcnt(6)\n\t"
"ds_read_b64_tr_b16 a[180:181], v210 offset:53440\n\t"
"v_mfma_f32_16x16x32_bf16 v[44:47], a[156:159], a[92:95], v[44:47]\n\t"
"s_add_u32 m0, s63, 46080\n\t"
"ds_read_b64_tr_b16 a[182:183], v210 offset:58048\n\t"
"ds_read_b64_tr_b16 a[184:185], v210 offset:53472\n\t"
"ds_read_b64_tr_b16 a[186:187], v210 offset:58080\n\t"
"ds_read_b64_tr_b16 a[188:189], v210 offset:53504\n\t"
"ds_read_b64_tr_b16 a[190:191], v210 offset:58112\n\t"
"v_mfma_f32_16x16x32_bf16 v[48:51], a[160:163], a[88:91], v[48:51]\n\t"
"buffer_load_dwordx4 v208, %[wq], s45 offen offset:0 lds\n\t"
"v_mfma_f32_16x16x32_bf16 v[52:55], a[160:163], a[92:95], v[52:55]\n\t"
"s_mul_i32 s76, s64, 23040\n\t"
"s_add_u32 s76, s76, 48234496\n\t"
"v_mfma_f32_16x16x32_bf16 v[56:59], a[164:167], a[88:91], v[56:59]\n\t"
"s_waitcnt lgkmcnt(8)\n\t"
"s_waitcnt vmcnt(7)\n\t"
"s_mul_i32 s65, %[wv], 5760\n\t"
"v_mfma_f32_16x16x32_bf16 v[60:63], a[164:167], a[92:95], v[60:63]\n\t"
"s_waitcnt lgkmcnt(0)\n\t"
"s_add_u32 s79, s76, s65\n\t"
"s_mul_i32 s65, %[wv], 2880\n\t"
"v_mfma_f32_16x16x32_bf16 v[64:67], a[168:171], a[88:91], v[64:67]\n\t"
"s_barrier\n\t"
"s_add_u32 s76, s76, s65\n\t"
"s_lshl_b32 s56, %[a], 7\n\t"
"v_mfma_f32_16x16x32_bf16 v[68:71], a[168:171], a[92:95], v[68:71]\n\t"
"s_add_u32 s56, s56, 49823744\n\t"
"s_lshl_b32 s65, %[mi], 3\n\t"
"s_add_u32 s77, s56, s65\n\t"
"v_mfma_f32_16x16x32_bf16 v[72:75], a[172:175], a[88:91], v[72:75]\n\t"
"s_add_u32 s68, s44, 223488\n\t"
"ds_read_b64_tr_b16 a[192:193], v209 offset:0\n\t"
"v_mfma_f32_16x16x32_bf16 v[76:79], a[172:175], a[92:95], v[76:79]\n\t"
"ds_read_b64_tr_b16 a[194:195], v209 offset:4608\n\t"
"ds_read_b64_tr_b16 a[196:197], v209 offset:32\n\t"
"ds_read_b64_tr_b16 a[198:199], v209 offset:4640\n\t"
"v_mfma_f32_16x16x32_bf16 v[80:83], a[176:179], a[88:91], v[80:83]\n\t"
"s_mul_i32 s78, %[a], 368640\n\t"
"ds_read_b64_tr_b16 a[200:201], v209 offset:64\n\t"
"v_mfma_f32_16x16x32_bf16 v[84:87], a[176:179], a[92:95], v[84:87]\n\t"
"s_mul_i32 s65, %[mi], 1440\n\t"
"ds_read_b64_tr_b16 a[202:203], v209 offset:4672\n\t"
"v_mfma_f32_16x16x32_bf16 v[88:91], a[180:183], a[88:91], v[88:91]\n\t"
"s_add_u32 s78, s78, s65\n\t"
"ds_read_b64_tr_b16 a[204:205], v209 offset:96\n\t"
"v_mfma_f32_16x16x32_bf16 v[92:95], a[180:183], a[92:95], v[92:95]\n\t"
"ds_read_b64_tr_b16 a[206:207], v209 offset:4704\n\t"
"v_mfma_f32_16x16x32_bf16 v[96:99], a[184:187], a[88:91], v[96:99]\n\t"
"s_mul_i32 s65, %[wv], 360\n\t"
"ds_read_b64_tr_b16 v[170:171], v167 offset:0\n\t"
"v_mfma_f32_16x16x32_bf16 v[100:103], a[184:187], a[92:95], v[100:103]\n\t"
"s_add_u32 s78, s78, s65\n\t"
"ds_read_b64_tr_b16 a[144:145], v209 offset:128\n\t"
"v_mfma_f32_16x16x32_bf16 v[104:107], a[188:191], a[88:91], v[104:107]\n\t"
"s_add_u32 m0, s63, 78848\n\t"
"ds_read_b64_tr_b16 a[146:147], v209 offset:4736\n\t"
"v_mfma_f32_16x16x32_bf16 v[108:111], a[188:191], a[92:95], v[108:111]\n\t"
"s_waitcnt lgkmcnt(7)\n\t"
"ds_read_b64_tr_b16 a[148:149], v209 offset:160\n\t"
"ds_read_b64_tr_b16 a[150:151], v209 offset:4768\n\t"
"buffer_load_dwordx4 v200, %[wq], s68 offen offset:0 lds\n\t"
"v_mfma_f32_16x16x32_bf16 v[40:43], a[192:195], a[96:99], v[40:43]\n\t"
"s_add_u32 s78, s78, 48234496\n\t"
"ds_read_b64_tr_b16 v[172:173], v167 offset:2048\n\t"
"v_mfma_f32_16x16x32_bf16 v[44:47], a[192:195], a[100:103], v[44:47]\n\t"
"s_mul_i32 s61, %[a], 12288\n\t"
"ds_read_b64_tr_b16 a[152:153], v209 offset:192\n\t"
"v_mfma_f32_16x16x32_bf16 v[48:51], a[196:199], a[96:99], v[48:51]\n\t"
"s_add_u32 s61, s61, 49709056\n\t"
"ds_read_b64_tr_b16 a[154:155], v209 offset:4800\n\t"
"v_mfma_f32_16x16x32_bf16 v[52:55], a[196:199], a[100:103], v[52:55]\n\t"
"s_waitcnt lgkmcnt(8)\n\t"
"ds_read_b64_tr_b16 a[156:157], v209 offset:224\n\t"
"ds_read_b64_tr_b16 a[158:159], v209 offset:4832\n\t"
"v_mfma_f32_16x16x32_bf16 v[56:59], a[200:203], a[96:99], v[56:59]\n\t"
"s_mul_i32 s65, %[mi], 768\n\t"
"ds_read_b64_tr_b16 v[174:175], v167 offset:4096\n\t"
"v_mfma_f32_16x16x32_bf16 v[60:63], a[200:203], a[100:103], v[60:63]\n\t"
"s_add_u32 s57, s61, s65\n\t"
"ds_read_b64_tr_b16 a[160:161], v209 offset:256\n\t"
"v_mfma_f32_16x16x32_bf16 v[64:67], a[204:207], a[96:99], v[64:67]\n\t"
"s_mov_b32 s58, s57\n\t"
"ds_read_b64_tr_b16 a[162:163], v209 offset:4864\n\t"
"v_mfma_f32_16x16x32_bf16 v[68:71], a[204:207], a[100:103], v[68:71]\n\t"
"s_waitcnt lgkmcnt(8)\n\t"
"ds_read_b64_tr_b16 a[164:165], v209 offset:9216\n\t"
"ds_read_b64_tr_b16 a[166:167], v209 offset:13824\n\t"
"buffer_load_dwordx4 v201, %[wq], s68 offen offset:1024 lds\n\t"
"v_mfma_f32_16x16x32_bf16 v[72:75], a[144:147], a[96:99], v[72:75]\n\t"
"s_mul_i32 s65, %[wv], 180\n\t"
"ds_read_b64_tr_b16 v[176:177], v167 offset:6144\n\t"
"v_mfma_f32_16x16x32_bf16 v[76:79], a[144:147], a[100:103], v[76:79]\n\t"
"s_add_u32 s57, s57, s65\n\t"
"ds_read_b64_tr_b16 a[168:169], v209 offset:9248\n\t"
"v_mfma_f32_16x16x32_bf16 v[80:83], a[148:151], a[96:99], v[80:83]\n\t"
"s_lshl_b32 s64, %[mi], 2\n\t"
"ds_read_b64_tr_b16 a[170:171], v209 offset:13856\n\t"
"v_mfma_f32_16x16x32_bf16 v[84:87], a[148:151], a[100:103], v[84:87]\n\t"
"s_waitcnt lgkmcnt(8)\n\t"
"ds_read_b64_tr_b16 a[172:173], v209 offset:9280\n\t"
"ds_read_b64_tr_b16 a[174:175], v209 offset:13888\n\t"
"v_mfma_f32_16x16x32_bf16 v[88:91], a[152:155], a[96:99], v[88:91]\n\t"
"s_add_u32 s64, s64, %[wv]\n\t"
"ds_read_b64_tr_b16 v[178:179], v167 offset:8192\n\t"
"v_mfma_f32_16x16x32_bf16 v[92:95], a[152:155], a[100:103], v[92:95]\n\t"
"s_mul_i32 s59, s64, 40960\n\t"
"ds_read_b64_tr_b16 a[176:177], v209 offset:9312\n\t"
"buffer_load_dwordx4 v202, %[wq], s68 offen offset:2048 lds\n\t"
"v_mfma_f32_16x16x32_bf16 v[96:99], a[156:159], a[96:99], v[96:99]\n\t"
"s_mul_i32 s65, %[a], 5120\n\t"
"ds_read_b64_tr_b16 a[178:179], v209 offset:13920\n\t"
"v_mfma_f32_16x16x32_bf16 v[100:103], a[156:159], a[100:103], v[100:103]\n\t"
"s_waitcnt lgkmcnt(8)\n\t"
"ds_read_b64_tr_b16 a[180:181], v209 offset:9344\n\t"
"ds_read_b64_tr_b16 a[182:183], v209 offset:13952\n\t"
"v_mfma_f32_16x16x32_bf16 v[104:107], a[160:163], a[96:99], v[104:107]\n\t"
"s_lshl_b32 s40, s43, 2\n\t"
"ds_read_b64_tr_b16 v[180:181], v167 offset:10240\n\t"
"v_mfma_f32_16x16x32_bf16 v[108:111], a[160:163], a[100:103], v[108:111]\n\t"
"s_add_u32 s40, s40, %[wv]\n\t"
"ds_read_b64_tr_b16 a[184:185], v209 offset:9376\n\t"
"v_mfma_f32_16x16x32_bf16 v[40:43], a[164:167], a[104:107], v[40:43]\n\t"
"s_add_u32 s59, s59, s65\n\t"
"ds_read_b64_tr_b16 a[186:187], v209 offset:13984\n\t"
"v_mfma_f32_16x16x32_bf16 v[44:47], a[164:167], a[108:111], v[44:47]\n\t"
"s_waitcnt lgkmcnt(8)\n\t"
"ds_read_b64_tr_b16 a[188:189], v209 offset:9408\n\t"
"ds_read_b64_tr_b16 a[190:191], v209 offset:14016\n\t"
"v_mfma_f32_16x16x32_bf16 v[48:51], a[168:171], a[104:107], v[48:51]\n\t"
"buffer_load_dwordx4 v203, %[wq], s68 offen offset:3072 lds\n\t"
"v_mfma_f32_16x16x32_bf16 v[52:55], a[168:171], a[108:111], v[52:55]\n\t"
"s_add_u32 s59, s59, 40108032\n\t"
"ds_read_b64_tr_b16 a[192:193], v209 offset:9440\n\t"
"v_mfma_f32_16x16x32_bf16 v[56:59], a[172:175], a[104:107], v[56:59]\n\t"
"s_lshl_b32 s48, %[mi], 11\n\t"
"ds_read_b64_tr_b16 a[194:195], v209 offset:14048\n\t"
"v_mfma_f32_16x16x32_bf16 v[60:63], a[172:175], a[108:111], v[60:63]\n\t"
"s_waitcnt lgkmcnt(7)\n\t"
"ds_read_b64_tr_b16 a[196:197], v209 offset:9472\n\t"
"ds_read_b64_tr_b16 a[198:199], v209 offset:14080\n\t"
"v_mfma_f32_16x16x32_bf16 v[64:67], a[176:179], a[104:107], v[64:67]\n\t"
"s_add_u32 s48, s48, 49758208\n\t"
"s_lshl_b32 s65, %[a], 3\n\t"
"s_add_u32 s60, s48, s65\n\t"
"v_mfma_f32_16x16x32_bf16 v[68:71], a[176:179], a[108:111], v[68:71]\n\t"
"s_add_u32 s60, s60, 256\n\t"
"ds_read_b64_tr_b16 a[200:201], v209 offset:18432\n\t"
"v_lshlrev_b32 v128, 4, v125\n\t"
"v_mfma_f32_16x16x32_bf16 v[72:75], a[180:183], a[104:107], v[72:75]\n\t"
"s_lshl_b32 s40, s40, 10\n\t"
"ds_read_b64_tr_b16 a[202:203], v209 offset:23040\n\t"
"v_mov_b32 v132, 2130706432\n\t"
"v_mfma_f32_16x16x32_bf16 v[76:79], a[180:183], a[108:111], v[76:79]\n\t"
"s_waitcnt lgkmcnt(6)\n\t"
"ds_read_b64_tr_b16 a[204:205], v209 offset:18464\n\t"
"ds_read_b64_tr_b16 a[206:207], v209 offset:23072\n\t"
"v_lshl_add_u32 v129, v126, 6, v128\n\t"
"v_mfma_f32_16x16x32_bf16 v[80:83], a[184:187], a[104:107], v[80:83]\n\t"
"s_add_u32 m0, s63, 82944\n\t"
"ds_read_b64_tr_b16 a[144:145], v209 offset:18496\n\t"
"v_cmp_eq_u32 vcc, %[wv], v126\n\t"
"v_mfma_f32_16x16x32_bf16 v[84:87], a[184:187], a[108:111], v[84:87]\n\t"
"buffer_load_dwordx4 v204, %[wq], s68 offen offset:0 lds\n\t"
"v_mfma_f32_16x16x32_bf16 v[88:91], a[188:191], a[104:107], v[88:91]\n\t"
"s_add_u32 s40, s40, 42729472\n\t"
"ds_read_b64_tr_b16 a[146:147], v209 offset:23104\n\t"
"v_add_u32 v254, 1, v254\n\t"
"v_mfma_f32_16x16x32_bf16 v[92:95], a[188:191], a[108:111], v[92:95]\n\t"
"s_waitcnt lgkmcnt(6)\n\t"
"ds_read_b64_tr_b16 a[148:149], v209 offset:18528\n\t"
"ds_read_b64_tr_b16 a[150:151], v209 offset:23136\n\t"
"v_add_u32 v232, 1, v232\n\t"
"v_mfma_f32_16x16x32_bf16 v[96:99], a[192:195], a[104:107], v[96:99]\n\t"
"buffer_load_dwordx4 v205, %[wq], s68 offen offset:1024 lds\n\t"
"v_mfma_f32_16x16x32_bf16 v[100:103], a[192:195], a[108:111], v[100:103]\n\t"
"ds_read_b64_tr_b16 a[152:153], v209 offset:18560\n\t"
"v_cndmask_b32 v236, v132, v128, vcc\n\t"
"v_mfma_f32_16x16x32_bf16 v[104:107], a[196:199], a[104:107], v[104:107]\n\t"
"s_waitcnt lgkmcnt(5)\n\t"
"ds_read_b64_tr_b16 a[154:155], v209 offset:23168\n\t"
"v_cmp_gt_u32 vcc, 4, v125\n\t"
"v_mfma_f32_16x16x32_bf16 v[108:111], a[196:199], a[108:111], v[108:111]\n\t"
"buffer_store_dwordx4 v[170:173], v168, %[ws], s40 offen sc1\n\t"
"ds_read_b64_tr_b16 a[156:157], v209 offset:18592\n\t"
"ds_read_b64_tr_b16 a[158:159], v209 offset:23200\n\t"
"ds_read_b64_tr_b16 a[160:161], v209 offset:18624\n\t"
"v_mov_b32 v38, 0\n\t"
"v_mfma_f32_16x16x32_bf16 v[40:43], a[200:203], a[112:115], v[40:43]\n\t"
"s_add_u32 s40, s40, 262144\n\t"
"ds_read_b64_tr_b16 a[162:163], v209 offset:23232\n\t"
"v_mov_b32 v39, 0\n\t"
"v_mfma_f32_16x16x32_bf16 v[44:47], a[200:203], a[116:119], v[44:47]\n\t"
"buffer_load_dwordx4 v206, %[wq], s68 offen offset:2048 lds\n\t"
"v_mfma_f32_16x16x32_bf16 v[48:51], a[204:207], a[112:115], v[48:51]\n\t"
"s_waitcnt lgkmcnt(6)\n\t"
"ds_read_b64_tr_b16 a[164:165], v209 offset:18656\n\t"
"v_cndmask_b32 v235, v132, v129, vcc\n\t"
"v_mfma_f32_16x16x32_bf16 v[52:55], a[204:207], a[116:119], v[52:55]\n\t"
"ds_read_b64_tr_b16 a[166:167], v209 offset:23264\n\t"
"v_cndmask_b32 v237, v132, v236, vcc\n\t"
"v_mfma_f32_16x16x32_bf16 v[56:59], a[144:147], a[112:115], v[56:59]\n\t"
"ds_read_b64_tr_b16 a[168:169], v209 offset:18688\n\t"
"v_cmp_eq_u32 vcc, 0, v120\n\t"
"v_mfma_f32_16x16x32_bf16 v[60:63], a[144:147], a[116:119], v[60:63]\n\t"
"ds_read_b64_tr_b16 a[170:171], v209 offset:23296\n\t"
"v_mov_b32 v18, 0\n\t"
"v_mfma_f32_16x16x32_bf16 v[64:67], a[148:151], a[112:115], v[64:67]\n\t"
"s_waitcnt lgkmcnt(6)\n\t"
"ds_read_b64_tr_b16 a[172:173], v209 offset:27648\n\t"
"v_mov_b32 v19, 0\n\t"
"v_mfma_f32_16x16x32_bf16 v[68:71], a[148:151], a[116:119], v[68:71]\n\t"
"buffer_store_dwordx4 v[174:177], v168, %[ws], s40 offen sc1\n\t"
"ds_read_b64_tr_b16 a[174:175], v209 offset:32256\n\t"
"ds_read_b64_tr_b16 a[176:177], v209 offset:27680\n\t"
"ds_read_b64_tr_b16 a[178:179], v209 offset:32288\n\t"
"v_cndmask_b32 v231, v132, v133, vcc\n\t"
"v_mfma_f32_16x16x32_bf16 v[72:75], a[152:155], a[112:115], v[72:75]\n\t"
"s_add_u32 s40, s40, 262144\n\t"
"ds_read_b64_tr_b16 a[180:181], v209 offset:27712\n\t"
"v_cmp_gt_u32 vcc, 45, v245\n\t"
"v_mfma_f32_16x16x32_bf16 v[76:79], a[152:155], a[116:119], v[76:79]\n\t"
"buffer_store_dwordx4 v[178:181], v168, %[ws], s40 offen sc1\n\t"
"ds_read_b64_tr_b16 a[182:183], v209 offset:32320\n\t"
"ds_read_b64_tr_b16 a[184:185], v209 offset:27744\n\t"
"ds_read_b64_tr_b16 a[186:187], v209 offset:32352\n\t"
"v_lshlrev_b32 v238, 3, v125\n\t"
"v_mfma_f32_16x16x32_bf16 v[80:83], a[156:159], a[112:115], v[80:83]\n\t"
"s_waitcnt lgkmcnt(10)\n\t"
"ds_read_b64_tr_b16 a[188:189], v209 offset:27776\n\t"
"v_lshlrev_b32 v239, 3, v245\n\t"
"v_mfma_f32_16x16x32_bf16 v[84:87], a[156:159], a[116:119], v[84:87]\n\t"
"ds_read_b64_tr_b16 a[190:191], v209 offset:32384\n\t"
"v_lshlrev_b32 v128, 2, v245\n\t"
"v_mfma_f32_16x16x32_bf16 v[88:91], a[160:163], a[112:115], v[88:91]\n\t"
"ds_read_b64_tr_b16 a[192:193], v209 offset:27808\n\t"
"v_cndmask_b32 v240, v132, v239, vcc\n\t"
"v_mfma_f32_16x16x32_bf16 v[92:95], a[160:163], a[116:119], v[92:95]\n\t"
"ds_read_b64_tr_b16 a[194:195], v209 offset:32416\n\t"
"v_cndmask_b32 v241, v132, v128, vcc\n\t"
"v_mfma_f32_16x16x32_bf16 v[96:99], a[164:167], a[112:115], v[96:99]\n\t"
"buffer_load_dwordx4 v207, %[wq], s68 offen offset:3072 lds\n\t"
"v_mfma_f32_16x16x32_bf16 v[100:103], a[164:167], a[116:119], v[100:103]\n\t"
"s_waitcnt lgkmcnt(10)\n\t"
"ds_read_b64_tr_b16 a[196:197], v209 offset:27840\n\t"
"v_mfma_f32_16x16x32_bf16 v[104:107], a[168:171], a[112:115], v[104:107]\n\t"
"ds_read_b64_tr_b16 a[198:199], v209 offset:32448\n\t"
"v_mfma_f32_16x16x32_bf16 v[108:111], a[168:171], a[116:119], v[108:111]\n\t"
"ds_read_b64_tr_b16 a[200:201], v209 offset:27872\n\t"
"v_mfma_f32_16x16x32_bf16 v[40:43], a[172:175], a[120:123], v[40:43]\n\t"
"ds_read_b64_tr_b16 a[202:203], v209 offset:32480\n\t"
"v_mfma_f32_16x16x32_bf16 v[44:47], a[172:175], a[124:127], v[44:47]\n\t"
"s_waitcnt lgkmcnt(10)\n\t"
"ds_read_b64_tr_b16 a[204:205], v209 offset:27904\n\t"
"ds_read_b64_tr_b16 a[206:207], v209 offset:32512\n\t"
"v_mfma_f32_16x16x32_bf16 v[48:51], a[176:179], a[120:123], v[48:51]\n\t"
"s_add_u32 m0, s63, 87040\n\t"
"v_mfma_f32_16x16x32_bf16 v[52:55], a[176:179], a[124:127], v[52:55]\n\t"
"buffer_load_dwordx4 v208, %[wq], s68 offen offset:0 lds\n\t"
"v_mfma_f32_16x16x32_bf16 v[56:59], a[180:183], a[120:123], v[56:59]\n\t"
"v_mfma_f32_16x16x32_bf16 v[60:63], a[180:183], a[124:127], v[60:63]\n\t"
"s_waitcnt lgkmcnt(8)\n\t"
"v_mov_b32 v246, -1\n\t"
"v_mfma_f32_16x16x32_bf16 v[64:67], a[184:187], a[120:123], v[64:67]\n\t"
"s_waitcnt vmcnt(12)\n\t"
"s_waitcnt lgkmcnt(0)\n\t"
"v_mov_b32 v247, -1\n\t"
"v_mfma_f32_16x16x32_bf16 v[68:71], a[184:187], a[124:127], v[68:71]\n\t"
"s_barrier\n\t"
"v_mov_b32 v248, -1\n\t"
"v_mov_b32 v249, -1\n\t"
"v_mfma_f32_16x16x32_bf16 v[72:75], a[188:191], a[120:123], v[72:75]\n\t"
"s_add_u32 s68, s44, 665856\n\t"
"ds_read_b64_tr_b16 a[144:145], v209 offset:37888\n\t"
"v_mov_b32 v244, s70\n\t"
"v_mfma_f32_16x16x32_bf16 v[76:79], a[188:191], a[124:127], v[76:79]\n\t"
"ds_read_b64_tr_b16 a[146:147], v209 offset:42496\n\t"
"ds_read_b64_tr_b16 a[148:149], v209 offset:37920\n\t"
"ds_read_b64_tr_b16 a[150:151], v209 offset:42528\n\t"
"v_mfma_f32_16x16x32_bf16 v[80:83], a[192:195], a[120:123], v[80:83]\n\t"
"ds_read_b64_tr_b16 a[152:153], v209 offset:37952\n\t"
"v_mfma_f32_16x16x32_bf16 v[84:87], a[192:195], a[124:127], v[84:87]\n\t"
"s_add_u32 m0, s63, 0\n\t"
"ds_read_b64_tr_b16 a[154:155], v209 offset:42560\n\t"
"v_min_u32 v199, 752, v230\n\t"
"v_mfma_f32_16x16x32_bf16 v[88:91], a[196:199], a[120:123], v[88:91]\n\t"
"ds_read_b64_tr_b16 a[156:157], v209 offset:37984\n\t"
"v_mfma_f32_16x16x32_bf16 v[92:95], a[196:199], a[124:127], v[92:95]\n\t"
"ds_read_b64_tr_b16 a[158:159], v209 offset:42592\n\t"
"v_mfma_f32_16x16x32_bf16 v[96:99], a[200:203], a[120:123], v[96:99]\n\t"
"buffer_store_dwordx4 v[246:249], v199, %[ws], s58 offen sc1\n\t"
"ds_read_b64_tr_b16 a[160:161], v209 offset:38016\n\t"
"ds_read_b64_tr_b16 a[162:163], v209 offset:42624\n\t"
"ds_read_b64_tr_b16 a[164:165], v209 offset:38048\n\t"
"v_mfma_f32_16x16x32_bf16 v[100:103], a[200:203], a[124:127], v[100:103]\n\t"
"buffer_load_dwordx4 v200, %[wq], s68 offen offset:0 lds\n\t"
"v_mfma_f32_16x16x32_bf16 v[104:107], a[204:207], a[120:123], v[104:107]\n\t"
"s_waitcnt lgkmcnt(7)\n\t"
"ds_read_b64_tr_b16 a[166:167], v209 offset:42656\n\t"
"v_mfma_f32_16x16x32_bf16 v[108:111], a[204:207], a[124:127], v[108:111]\n\t"
"ds_read_b64_tr_b16 a[168:169], v209 offset:38080\n\t"
"v_mfma_f32_16x16x32_bf16 v[40:43], a[144:147], a[128:131], v[40:43]\n\t"
"buffer_store_dwordx4 v[246:249], v230, %[ws], s79 offen offset:0 sc1\n\t"
"ds_read_b64_tr_b16 a[170:171], v209 offset:42688\n\t"
"ds_read_b64_tr_b16 a[172:173], v209 offset:38112\n\t"
"ds_read_b64_tr_b16 a[174:175], v209 offset:42720\n\t"
"v_mfma_f32_16x16x32_bf16 v[44:47], a[144:147], a[132:135], v[44:47]\n\t"
"buffer_load_dwordx4 v201, %[wq], s68 offen offset:1024 lds\n\t"
"v_mfma_f32_16x16x32_bf16 v[48:51], a[148:151], a[128:131], v[48:51]\n\t"
"s_waitcnt lgkmcnt(8)\n\t"
"ds_read_b64_tr_b16 a[176:177], v209 offset:38144\n\t"
"v_mfma_f32_16x16x32_bf16 v[52:55], a[148:151], a[132:135], v[52:55]\n\t"
"buffer_store_dwordx4 v[246:249], v230, %[ws], s79 offen offset:1024 sc1\n\t"
"ds_read_b64_tr_b16 a[178:179], v209 offset:42752\n\t"
"ds_read_b64_tr_b16 a[180:181], v209 offset:47104\n\t"
"ds_read_b64_tr_b16 a[182:183], v209 offset:51712\n\t"
"v_mfma_f32_16x16x32_bf16 v[56:59], a[152:155], a[128:131], v[56:59]\n\t"
"ds_read_b64_tr_b16 a[184:185], v209 offset:47136\n\t"
"v_mfma_f32_16x16x32_bf16 v[60:63], a[152:155], a[132:135], v[60:63]\n\t"
"ds_read_b64_tr_b16 a[186:187], v209 offset:51744\n\t"
"v_mfma_f32_16x16x32_bf16 v[64:67], a[156:159], a[128:131], v[64:67]\n\t"
"s_waitcnt lgkmcnt(10)\n\t"
"ds_read_b64_tr_b16 a[188:189], v209 offset:47168\n\t"
"v_mfma_f32_16x16x32_bf16 v[68:71], a[156:159], a[132:135], v[68:71]\n\t"
"ds_read_b64_tr_b16 a[190:191], v209 offset:51776\n\t"
"v_mfma_f32_16x16x32_bf16 v[72:75], a[160:163], a[128:131], v[72:75]\n\t"
"ds_read_b64_tr_b16 a[192:193], v209 offset:47200\n\t"
"v_mfma_f32_16x16x32_bf16 v[76:79], a[160:163], a[132:135], v[76:79]\n\t"
"buffer_store_dwordx4 v[246:249], v230, %[ws], s79 offen offset:2048 sc1\n\t"
"ds_read_b64_tr_b16 a[194:195], v209 offset:51808\n\t"
"ds_read_b64_tr_b16 a[196:197], v209 offset:47232\n\t"
"ds_read_b64_tr_b16 a[198:199], v209 offset:51840\n\t"
"v_mfma_f32_16x16x32_bf16 v[80:83], a[164:167], a[128:131], v[80:83]\n\t"
"s_waitcnt lgkmcnt(12)\n\t"
"ds_read_b64_tr_b16 a[200:201], v209 offset:47264\n\t"
"v_mfma_f32_16x16x32_bf16 v[84:87], a[164:167], a[132:135], v[84:87]\n\t"
"buffer_store_dwordx4 v[246:249], v230, %[ws], s79 offen offset:3072 sc1\n\t"
"ds_read_b64_tr_b16 a[202:203], v209 offset:51872\n\t"
"ds_read_b64_tr_b16 a[204:205], v209 offset:47296\n\t"
"ds_read_b64_tr_b16 a[206:207], v209 offset:51904\n\t"
"v_mfma_f32_16x16x32_bf16 v[88:91], a[168:171], a[128:131], v[88:91]\n\t"
"buffer_load_dwordx4 v202, %[wq], s68 offen offset:2048 lds\n\t"
"v_mfma_f32_16x16x32_bf16 v[92:95], a[168:171], a[132:135], v[92:95]\n\t"
"ds_read_b64_tr_b16 a[144:145], v209 offset:47328\n\t"
"v_mfma_f32_16x16x32_bf16 v[96:99], a[172:175], a[128:131], v[96:99]\n\t"
"s_waitcnt lgkmcnt(13)\n\t"
"ds_read_b64_tr_b16 a[146:147], v209 offset:51936\n\t"
"v_mfma_f32_16x16x32_bf16 v[100:103], a[172:175], a[132:135], v[100:103]\n\t"
"ds_read_b64_tr_b16 a[148:149], v209 offset:47360\n\t"
"v_mfma_f32_16x16x32_bf16 v[104:107], a[176:179], a[128:131], v[104:107]\n\t"
"ds_read_b64_tr_b16 a[150:151], v209 offset:51968\n\t"
"v_mfma_f32_16x16x32_bf16 v[108:111], a[176:179], a[132:135], v[108:111]\n\t"
"s_add_u32 s79, s79, 4096\n\t"
"ds_read_b64_tr_b16 a[152:153], v210 offset:3072\n\t"
"v_mfma_f32_16x16x32_bf16 v[40:43], a[180:183], a[136:139], v[40:43]\n\t"
"s_waitcnt lgkmcnt(13)\n\t"
"ds_read_b64_tr_b16 a[154:155], v210 offset:7680\n\t"
"v_mfma_f32_16x16x32_bf16 v[44:47], a[180:183], a[140:143], v[44:47]\n\t"
"ds_read_b64_tr_b16 a[156:157], v210 offset:3112\n\t"
"v_mfma_f32_16x16x32_bf16 v[48:51], a[184:187], a[136:139], v[48:51]\n\t"
"ds_read_b64_tr_b16 a[158:159], v210 offset:7720\n\t"
"v_mfma_f32_16x16x32_bf16 v[52:55], a[184:187], a[140:143], v[52:55]\n\t"
"buffer_store_dwordx4 v[246:249], v230, %[ws], s79 offen offset:0 sc1\n\t"
"ds_read_b64_tr_b16 a[160:161], v210 offset:3144\n\t"
"ds_read_b64_tr_b16 a[162:163], v210 offset:7752\n\t"
"v_cvt_pk_bf16_f32 v0, v40, v41\n\t"
"v_cvt_pk_bf16_f32 v1, v42, v43\n\t"
"v_mfma_f32_16x16x32_bf16 v[56:59], a[188:191], a[136:139], v[56:59]\n\t"
"s_waitcnt lgkmcnt(14)\n\t"
"v_cvt_pk_bf16_f32 v20, v44, v45\n\t"
"v_cvt_pk_bf16_f32 v21, v46, v47\n\t"
"v_mfma_f32_16x16x32_bf16 v[60:63], a[188:191], a[140:143], v[60:63]\n\t"
"buffer_store_dwordx4 v[246:249], v230, %[ws], s79 offen offset:640 sc1\n\t"
"ds_read_b64_tr_b16 a[164:165], v210 offset:3184\n\t"
"ds_read_b64_tr_b16 a[166:167], v210 offset:7792\n\t"
"v_cvt_pk_bf16_f32 v2, v48, v49\n\t"
"v_cvt_pk_bf16_f32 v3, v50, v51\n\t"
"v_mfma_f32_16x16x32_bf16 v[64:67], a[192:195], a[136:139], v[64:67]\n\t"
"v_cvt_pk_bf16_f32 v22, v52, v53\n\t"
"v_cvt_pk_bf16_f32 v23, v54, v55\n\t"
"v_mfma_f32_16x16x32_bf16 v[68:71], a[192:195], a[140:143], v[68:71]\n\t"
"v_cvt_pk_bf16_f32 v4, v56, v57\n\t"
"v_cvt_pk_bf16_f32 v5, v58, v59\n\t"
"v_mfma_f32_16x16x32_bf16 v[72:75], a[196:199], a[136:139], v[72:75]\n\t"
"s_waitcnt lgkmcnt(12)\n\t"
"v_cvt_pk_bf16_f32 v24, v60, v61\n\t"
"v_cvt_pk_bf16_f32 v25, v62, v63\n\t"
"v_mfma_f32_16x16x32_bf16 v[76:79], a[196:199], a[140:143], v[76:79]\n\t"
"v_cvt_pk_bf16_f32 v6, v64, v65\n\t"
"v_cvt_pk_bf16_f32 v7, v66, v67\n\t"
"v_mfma_f32_16x16x32_bf16 v[80:83], a[200:203], a[136:139], v[80:83]\n\t"
"ds_read_b64_tr_b16 a[168:169], v212 offset:3072\n\t"
"v_cvt_pk_bf16_f32 v26, v68, v69\n\t"
"v_mfma_f32_16x16x32_bf16 v[84:87], a[200:203], a[140:143], v[84:87]\n\t"
"ds_read_b64_tr_b16 a[170:171], v212 offset:7680\n\t"
"v_cvt_pk_bf16_f32 v27, v70, v71\n\t"
"v_mfma_f32_16x16x32_bf16 v[88:91], a[204:207], a[136:139], v[88:91]\n\t"
"s_waitcnt lgkmcnt(10)\n\t"
"ds_read_b64_tr_b16 a[172:173], v210 offset:3216\n\t"
"v_cvt_pk_bf16_f32 v8, v72, v73\n\t"
"v_mfma_f32_16x16x32_bf16 v[92:95], a[204:207], a[140:143], v[92:95]\n\t"
"ds_read_b64_tr_b16 a[174:175], v210 offset:7824\n\t"
"v_cvt_pk_bf16_f32 v9, v74, v75\n\t"
"v_mfma_f32_16x16x32_bf16 v[96:99], a[144:147], a[136:139], v[96:99]\n\t"
"buffer_load_dwordx4 v203, %[wq], s68 offen offset:3072 lds\n\t"
"v_mfma_f32_16x16x32_bf16 v[100:103], a[144:147], a[140:143], v[100:103]\n\t"
"ds_read_b64_tr_b16 a[176:177], v210 offset:3256\n\t"
"v_cvt_pk_bf16_f32 v28, v76, v77\n\t"
"v_mfma_f32_16x16x32_bf16 v[104:107], a[148:151], a[136:139], v[104:107]\n\t"
"s_waitcnt lgkmcnt(9)\n\t"
"ds_read_b64_tr_b16 a[178:179], v210 offset:7864\n\t"
"v_cvt_pk_bf16_f32 v29, v78, v79\n\t"
"v_mfma_f32_16x16x32_bf16 v[108:111], a[148:151], a[140:143], v[108:111]\n\t"
"s_add_u32 m0, s63, 4096\n\t"
"ds_read_b64_tr_b16 a[180:181], v210 offset:3288\n\t"
"v_cvt_pk_bf16_f32 v10, v80, v81\n\t"
"v_mfma_f32_16x16x32_bf16 v[40:43], a[0:3], a[152:155], 0\n\t"
"ds_read_b64_tr_b16 a[182:183], v210 offset:7896\n\t"
"v_cvt_pk_bf16_f32 v11, v82, v83\n\t"
"v_mfma_f32_16x16x32_bf16 v[44:47], a[4:7], a[152:155], 0\n\t"
"ds_read_b64_tr_b16 a[184:185], v210 offset:3328\n\t"
"v_cvt_pk_bf16_f32 v30, v84, v85\n\t"
"v_mfma_f32_16x16x32_bf16 v[48:51], a[0:3], a[156:159], 0\n\t"
"s_waitcnt lgkmcnt(9)\n\t"
"ds_read_b64_tr_b16 a[186:187], v210 offset:7936\n\t"
"v_cvt_pk_bf16_f32 v31, v86, v87\n\t"
"v_mfma_f32_16x16x32_bf16 v[52:55], a[4:7], a[156:159], 0\n\t"
"ds_read_b64_tr_b16 a[188:189], v212 offset:3216\n\t"
"v_cvt_pk_bf16_f32 v12, v88, v89\n\t"
"v_mfma_f32_16x16x32_bf16 v[56:59], a[0:3], a[160:163], 0\n\t"
"ds_read_b64_tr_b16 a[190:191], v212 offset:7824\n\t"
"v_cvt_pk_bf16_f32 v13, v90, v91\n\t"
"v_mfma_f32_16x16x32_bf16 v[60:63], a[4:7], a[160:163], 0\n\t"
"ds_read_b64_tr_b16 a[192:193], v210 offset:12288\n\t"
"v_cvt_pk_bf16_f32 v32, v92, v93\n\t"
"v_mfma_f32_16x16x32_bf16 v[64:67], a[0:3], a[164:167], 0\n\t"
"s_waitcnt lgkmcnt(9)\n\t"
"ds_read_b64_tr_b16 a[194:195], v210 offset:16896\n\t"
"v_cvt_pk_bf16_f32 v33, v94, v95\n\t"
"v_mfma_f32_16x16x32_bf16 v[68:71], a[4:7], a[164:167], 0\n\t"
"ds_read_b64_tr_b16 a[196:197], v210 offset:12328\n\t"
"v_cvt_pk_bf16_f32 v14, v96, v97\n\t"
"v_mfma_f32_16x16x32_bf16 v[72:75], a[0:3], a[168:171], 0\n\t"
"ds_read_b64_tr_b16 a[198:199], v210 offset:16936\n\t"
"v_cvt_pk_bf16_f32 v15, v98, v99\n\t"
"v_mfma_f32_16x16x32_bf16 v[76:79], a[4:7], a[168:171], 0\n\t"
"ds_read_b64_tr_b16 a[200:201], v210 offset:12360\n\t"
"v_cvt_pk_bf16_f32 v34, v100, v101\n\t"
"v_mfma_f32_16x16x32_bf16 v[80:83], a[0:3], a[172:175], 0\n\t"
"s_waitcnt lgkmcnt(9)\n\t"
"ds_read_b64_tr_b16 a[202:203], v210 offset:16968\n\t"
"v_cvt_pk_bf16_f32 v35, v102, v103\n\t"
"v_mfma_f32_16x16x32_bf16 v[84:87], a[4:7], a[172:175], 0\n\t"
"buffer_load_dwordx4 v204, %[wq], s68 offen offset:0 lds\n\t"
"v_mfma_f32_16x16x32_bf16 v[88:91], a[0:3], a[176:179], 0\n\t"
"ds_read_b64_tr_b16 a[204:205], v210 offset:12400\n\t"
"v_cvt_pk_bf16_f32 v16, v104, v105\n\t"
"v_mfma_f32_16x16x32_bf16 v[92:95], a[4:7], a[176:179], 0\n\t"
"s_getpc_b64 s[96:97]\n\t"
"70:\n\t"
"s_and_b32 s98, %[mi], 3\n\t"
"s_lshl_b32 s98, s98, 2\n\t"
"s_add_u32 s98, s98, %[wv]\n\t"
"s_lshl_b32 s98, s98, 10\n\t"
"v_add_u32 v198, s98, v230\n\t"
"v_min_u32 v198, 97f-70b, v198\n\t"
"global_load_dword v198, v198, s[96:97]\n\t"
"v_mfma_f32_16x16x32_bf16 v[96:99], a[0:3], a[180:183], 0\n\t"
"s_waitcnt lgkmcnt(7)\n\t"
"ds_read_b64_tr_b16 a[206:207], v210 offset:17008\n\t"
"v_cvt_pk_bf16_f32 v17, v106, v107\n\t"
"v_mfma_f32_16x16x32_bf16 v[100:103], a[4:7], a[180:183], 0\n\t"
"ds_read_b64_tr_b16 a[144:145], v212 offset:12288\n\t"
"v_cvt_pk_bf16_f32 v36, v108, v109\n\t"
"v_mfma_f32_16x16x32_bf16 v[104:107], a[0:3], a[184:187], 0\n\t"
"ds_read_b64_tr_b16 a[146:147], v212 offset:16896\n\t"
"v_cvt_pk_bf16_f32 v37, v110, v111\n\t"
"v_mfma_f32_16x16x32_bf16 v[108:111], a[4:7], a[184:187], 0\n\t"
"buffer_load_dwordx4 v205, %[wq], s68 offen offset:1024 lds\n\t"
"v_mfma_f32_16x16x32_bf16 v[112:115], a[0:3], a[188:191], 0\n\t"
"ds_read_b64_tr_b16 a[148:149], v210 offset:12432\n\t"
"v_mfma_f32_16x16x32_bf16 v[116:119], a[4:7], a[188:191], 0\n\t"
"s_waitcnt lgkmcnt(7)\n\t"
"ds_read_b64_tr_b16 a[150:151], v210 offset:17040\n\t"
"v_mfma_f32_16x16x32_bf16 v[40:43], a[8:11], a[192:195], v[40:43]\n\t"
"ds_read_b64_tr_b16 a[152:153], v210 offset:12472\n\t"
"v_mfma_f32_16x16x32_bf16 v[44:47], a[12:15], a[192:195], v[44:47]\n\t"
"ds_read_b64_tr_b16 a[154:155], v210 offset:17080\n\t"
"v_mfma_f32_16x16x32_bf16 v[48:51], a[8:11], a[196:199], v[48:51]\n\t"
"ds_read_b64_tr_b16 a[156:157], v210 offset:12504\n\t"
"v_mfma_f32_16x16x32_bf16 v[52:55], a[12:15], a[196:199], v[52:55]\n\t"
"s_waitcnt lgkmcnt(7)\n\t"
"ds_read_b64_tr_b16 a[158:159], v210 offset:17112\n\t"
"v_mfma_f32_16x16x32_bf16 v[56:59], a[8:11], a[200:203], v[56:59]\n\t"
"ds_read_b64_tr_b16 a[160:161], v210 offset:12544\n\t"
"v_mfma_f32_16x16x32_bf16 v[60:63], a[12:15], a[200:203], v[60:63]\n\t"
"ds_read_b64_tr_b16 a[162:163], v210 offset:17152\n\t"
"v_mfma_f32_16x16x32_bf16 v[64:67], a[8:11], a[204:207], v[64:67]\n\t"
"ds_read_b64_tr_b16 a[164:165], v212 offset:12432\n\t"
"v_mfma_f32_16x16x32_bf16 v[68:71], a[12:15], a[204:207], v[68:71]\n\t"
"s_waitcnt lgkmcnt(7)\n\t"
"ds_read_b64_tr_b16 a[166:167], v212 offset:17040\n\t"
"v_mfma_f32_16x16x32_bf16 v[72:75], a[8:11], a[144:147], v[72:75]\n\t"
"ds_read_b64_tr_b16 a[168:169], v213 offset:0\n\t"
"v_mfma_f32_16x16x32_bf16 v[76:79], a[12:15], a[144:147], v[76:79]\n\t"
"ds_read_b64_tr_b16 a[170:171], v214 offset:0\n\t"
"v_mfma_f32_16x16x32_bf16 v[80:83], a[8:11], a[148:151], v[80:83]\n\t"
"ds_read_b64_tr_b16 a[172:173], v215 offset:0\n\t"
"v_mfma_f32_16x16x32_bf16 v[84:87], a[12:15], a[148:151], v[84:87]\n\t"
"s_waitcnt lgkmcnt(7)\n\t"
"ds_read_b64_tr_b16 a[174:175], v216 offset:0\n\t"
"v_mfma_f32_16x16x32_bf16 v[88:91], a[8:11], a[152:155], v[88:91]\n\t"
"buffer_load_dwordx4 v206, %[wq], s68 offen offset:2048 lds\n\t"
"v_mfma_f32_16x16x32_bf16 v[92:95], a[12:15], a[152:155], v[92:95]\n\t"
"ds_read_b64_tr_b16 a[176:177], v217 offset:0\n\t"
"v_mfma_f32_16x16x32_bf16 v[96:99], a[8:11], a[156:159], v[96:99]\n\t"
"ds_read_b64_tr_b16 a[178:179], v218 offset:0\n\t"
"v_mfma_f32_16x16x32_bf16 v[100:103], a[12:15], a[156:159], v[100:103]\n\t"
"s_waitcnt lgkmcnt(6)\n\t"
"ds_read_b64_tr_b16 a[180:181], v213 offset:8\n\t"
"ds_read_b64_tr_b16 a[182:183], v214 offset:8\n\t"
"v_mfma_f32_16x16x32_bf16 v[104:107], a[8:11], a[160:163], v[104:107]\n\t"
"ds_read_b64_tr_b16 a[184:185], v215 offset:8\n\t"
"v_mfma_f32_16x16x32_bf16 v[108:111], a[12:15], a[160:163], v[108:111]\n\t"
"ds_read_b64_tr_b16 a[186:187], v216 offset:8\n\t"
"v_mfma_f32_16x16x32_bf16 v[112:115], a[8:11], a[164:167], v[112:115]\n\t"
"ds_read_b64_tr_b16 a[188:189], v217 offset:8\n\t"
"v_mfma_f32_16x16x32_bf16 v[116:119], a[12:15], a[164:167], v[116:119]\n\t"
"s_waitcnt lgkmcnt(7)\n\t"
"ds_read_b64_tr_b16 a[190:191], v218 offset:8\n\t"
"v_mfma_f32_16x16x32_bf16 v[120:123], a[168:171], v[0:3], 0\n\t"
"ds_read_b64_tr_b16 a[192:193], v213 offset:16\n\t"
"v_mfma_f32_16x16x32_bf16 v[140:143], a[168:171], v[20:23], 0\n\t"
"ds_read_b64_tr_b16 a[194:195], v214 offset:16\n\t"
"v_mfma_f32_16x16x32_bf16 v[120:123], a[172:175], v[4:7], v[120:123]\n\t"
"ds_read_b64_tr_b16 a[196:197], v215 offset:16\n\t"
"v_mfma_f32_16x16x32_bf16 v[140:143], a[172:175], v[24:27], v[140:143]\n\t"
"s_waitcnt lgkmcnt(7)\n\t"
"ds_read_b64_tr_b16 a[198:199], v216 offset:16\n\t"
"v_mfma_f32_16x16x32_bf16 v[120:123], a[176:179], v[8:11], v[120:123]\n\t"
"buffer_load_dwordx4 v207, %[wq], s68 offen offset:3072 lds\n\t"
"v_mfma_f32_16x16x32_bf16 v[140:143], a[176:179], v[28:31], v[140:143]\n\t"
"ds_read_b64_tr_b16 a[200:201], v217 offset:16\n\t"
"v_mfma_f32_16x16x32_bf16 v[124:127], a[180:183], v[0:3], 0\n\t"
"ds_read_b64_tr_b16 a[202:203], v218 offset:16\n\t"
"v_mfma_f32_16x16x32_bf16 v[144:147], a[180:183], v[20:23], 0\n\t"
"s_waitcnt lgkmcnt(6)\n\t"
"ds_read_b64_tr_b16 a[204:205], v213 offset:24\n\t"
"ds_read_b64_tr_b16 a[206:207], v214 offset:24\n\t"
"v_mfma_f32_16x16x32_bf16 v[124:127], a[184:187], v[4:7], v[124:127]\n\t"
"ds_read_b64_tr_b16 a[144:145], v215 offset:24\n\t"
"v_mfma_f32_16x16x32_bf16 v[144:147], a[184:187], v[24:27], v[144:147]\n\t"
"ds_read_b64_tr_b16 a[146:147], v216 offset:24\n\t"
"v_mfma_f32_16x16x32_bf16 v[124:127], a[188:191], v[8:11], v[124:127]\n\t"
"s_add_u32 m0, s63, 8192\n\t"
"ds_read_b64_tr_b16 a[148:149], v217 offset:24\n\t"
"v_mfma_f32_16x16x32_bf16 v[144:147], a[188:191], v[28:31], v[144:147]\n\t"
"s_waitcnt lgkmcnt(7)\n\t"
"ds_read_b64_tr_b16 a[150:151], v218 offset:24\n\t"
"v_mfma_f32_16x16x32_bf16 v[128:131], a[192:195], v[0:3], 0\n\t"
"buffer_load_dwordx4 v208, %[wq], s68 offen offset:0 lds\n\t"
"v_mfma_f32_16x16x32_bf16 v[148:151], a[192:195], v[20:23], 0\n\t"
"ds_read_b64_tr_b16 a[152:153], v213 offset:32\n\t"
"v_mfma_f32_16x16x32_bf16 v[128:131], a[196:199], v[4:7], v[128:131]\n\t"
"ds_read_b64_tr_b16 a[154:155], v214 offset:32\n\t"
"v_mfma_f32_16x16x32_bf16 v[148:151], a[196:199], v[24:27], v[148:151]\n\t"
"s_waitcnt lgkmcnt(6)\n\t"
"ds_read_b64_tr_b16 a[156:157], v215 offset:32\n\t"
"ds_read_b64_tr_b16 a[158:159], v216 offset:32\n\t"
"v_mfma_f32_16x16x32_bf16 v[128:131], a[200:203], v[8:11], v[128:131]\n\t"
"ds_read_b64_tr_b16 a[160:161], v217 offset:32\n\t"
"v_mfma_f32_16x16x32_bf16 v[148:151], a[200:203], v[28:31], v[148:151]\n\t"
"ds_read_b64_tr_b16 a[162:163], v218 offset:32\n\t"
"v_mfma_f32_16x16x32_bf16 v[132:135], a[204:207], v[0:3], 0\n\t"
"s_waitcnt vmcnt(17)\n\t"
"v_mfma_f32_16x16x32_bf16 v[152:155], a[204:207], v[20:23], 0\n\t"
"s_waitcnt lgkmcnt(6)\n\t"
"s_barrier\n\t"
"v_mfma_f32_16x16x32_bf16 v[132:135], a[144:147], v[4:7], v[132:135]\n\t"
"ds_read_b64_tr_b16 a[164:165], v210 offset:25600\n\t"
"v_mfma_f32_16x16x32_bf16 v[152:155], a[144:147], v[24:27], v[152:155]\n\t"
"ds_read_b64_tr_b16 a[166:167], v210 offset:30208\n\t"
"v_mfma_f32_16x16x32_bf16 v[132:135], a[148:151], v[8:11], v[132:135]\n\t"
"ds_read_b64_tr_b16 a[168:169], v210 offset:25640\n\t"
"v_mfma_f32_16x16x32_bf16 v[152:155], a[148:151], v[28:31], v[152:155]\n\t"
"s_waitcnt lgkmcnt(5)\n\t"
"ds_read_b64_tr_b16 a[170:171], v210 offset:30248\n\t"
"ds_read_b64_tr_b16 a[172:173], v210 offset:25672\n\t"
"ds_read_b64_tr_b16 a[174:175], v210 offset:30280\n\t"
"v_mfma_f32_16x16x32_bf16 v[136:139], a[152:155], v[0:3], 0\n\t"
"ds_read_b64_tr_b16 a[176:177], v210 offset:25712\n\t"
"v_mfma_f32_16x16x32_bf16 v[156:159], a[152:155], v[20:23], 0\n\t"
"s_add_u32 s68, s44, 1108224\n\t"
"ds_read_b64_tr_b16 a[178:179], v210 offset:30320\n\t"
"v_mfma_f32_16x16x32_bf16 v[136:139], a[156:159], v[4:7], v[136:139]\n\t"
"s_add_u32 m0, s63, 37888\n\t"
"ds_read_b64_tr_b16 a[180:181], v212 offset:25600\n\t"
"v_mfma_f32_16x16x32_bf16 v[156:159], a[156:159], v[24:27], v[156:159]\n\t"
"s_waitcnt lgkmcnt(7)\n\t"
"ds_read_b64_tr_b16 a[182:183], v212 offset:30208\n\t"
"v_mfma_f32_16x16x32_bf16 v[136:139], a[160:163], v[8:11], v[136:139]\n\t"
"buffer_load_dwordx4 v200, %[wq], s68 offen offset:0 lds\n\t"
"v_mfma_f32_16x16x32_bf16 v[156:159], a[160:163], v[28:31], v[156:159]\n\t"
"ds_read_b64_tr_b16 a[184:185], v210 offset:25744\n\t"
"v_mfma_f32_16x16x32_bf16 v[40:43], a[16:19], a[164:167], v[40:43]\n\t"
"ds_read_b64_tr_b16 a[186:187], v210 offset:30352\n\t"
"v_mfma_f32_16x16x32_bf16 v[44:47], a[20:23], a[164:167], v[44:47]\n\t"
"s_waitcnt lgkmcnt(6)\n\t"
"ds_read_b64_tr_b16 a[188:189], v210 offset:25784\n\t"
"ds_read_b64_tr_b16 a[190:191], v210 offset:30392\n\t"
"buffer_load_dwordx4 v201, %[wq], s68 offen offset:1024 lds\n\t"
"v_mfma_f32_16x16x32_bf16 v[48:51], a[16:19], a[168:171], v[48:51]\n\t"
"ds_read_b64_tr_b16 a[192:193], v210 offset:25816\n\t"
"v_max3_f32 v178, v120, v121, v122\n\t"
"v_mfma_f32_16x16x32_bf16 v[52:55], a[20:23], a[168:171], v[52:55]\n\t"
"ds_read_b64_tr_b16 a[194:195], v210 offset:30424\n\t"
"v_min3_f32 v179, v120, v121, v122\n\t"
"v_mfma_f32_16x16x32_bf16 v[56:59], a[16:19], a[172:175], v[56:59]\n\t"
"s_waitcnt lgkmcnt(6)\n\t"
"ds_read_b64_tr_b16 a[196:197], v210 offset:25856\n\t"
"v_max3_f32 v180, v123, v124, v125\n\t"
"v_mfma_f32_16x16x32_bf16 v[60:63], a[20:23], a[172:175], v[60:63]\n\t"
"ds_read_b64_tr_b16 a[198:199], v210 offset:30464\n\t"
"v_min3_f32 v181, v123, v124, v125\n\t"
"v_mfma_f32_16x16x32_bf16 v[64:67], a[16:19], a[176:179], v[64:67]\n\t"
"ds_read_b64_tr_b16 a[200:201], v212 offset:25744\n\t"
"v_max3_f32 v178, v178, v126, v127\n\t"
"v_mfma_f32_16x16x32_bf16 v[68:71], a[20:23], a[176:179], v[68:71]\n\t"
"ds_read_b64_tr_b16 a[202:203], v212 offset:30352\n\t"
"v_min3_f32 v179, v179, v126, v127\n\t"
"v_mfma_f32_16x16x32_bf16 v[72:75], a[16:19], a[180:183], v[72:75]\n\t"
"s_waitcnt lgkmcnt(6)\n\t"
"ds_read_b64_tr_b16 a[204:205], v210 offset:34816\n\t"
"v_max3_f32 v180, v180, v128, v129\n\t"
"v_mfma_f32_16x16x32_bf16 v[76:79], a[20:23], a[180:183], v[76:79]\n\t"
"ds_read_b64_tr_b16 a[206:207], v210 offset:39424\n\t"
"v_min3_f32 v181, v181, v128, v129\n\t"
"v_mfma_f32_16x16x32_bf16 v[80:83], a[16:19], a[184:187], v[80:83]\n\t"
"v_max3_f32 v178, v178, v130, v131\n\t"
"v_min3_f32 v179, v179, v130, v131\n\t"
"v_mfma_f32_16x16x32_bf16 v[84:87], a[20:23], a[184:187], v[84:87]\n\t"
"ds_read_b64_tr_b16 a[144:145], v210 offset:34856\n\t"
"v_max3_f32 v180, v180, v132, v133\n\t"
"v_mfma_f32_16x16x32_bf16 v[88:91], a[16:19], a[188:191], v[88:91]\n\t"
"s_waitcnt lgkmcnt(5)\n\t"
"ds_read_b64_tr_b16 a[146:147], v210 offset:39464\n\t"
"v_min3_f32 v181, v181, v132, v133\n\t"
"v_mfma_f32_16x16x32_bf16 v[92:95], a[20:23], a[188:191], v[92:95]\n\t"
"ds_read_b64_tr_b16 a[148:149], v210 offset:34888\n\t"
"ds_read_b64_tr_b16 a[150:151], v210 offset:39496\n\t"
"v_max3_f32 v178, v178, v134, v135\n\t"
"v_mfma_f32_16x16x32_bf16 v[96:99], a[16:19], a[192:195], v[96:99]\n\t"
"v_min3_f32 v179, v179, v134, v135\n\t"
"v_max3_f32 v180, v180, v136, v137\n\t"
"v_mfma_f32_16x16x32_bf16 v[100:103], a[20:23], a[192:195], v[100:103]\n\t"
"v_min3_f32 v181, v181, v136, v137\n\t"
"v_max_f32 v178, v178, v180\n\t"
"v_mfma_f32_16x16x32_bf16 v[104:107], a[16:19], a[196:199], v[104:107]\n\t"
"s_waitcnt lgkmcnt(4)\n\t"
"v_min_f32 v179, v179, v181\n\t"
"v_mul_f32 v178, s72, v178\n\t"
"v_mfma_f32_16x16x32_bf16 v[108:111], a[20:23], a[196:199], v[108:111]\n\t"
"ds_read_b64_tr_b16 a[152:153], v210 offset:34928\n\t"
"ds_read_b64_tr_b16 a[154:155], v210 offset:39536\n\t"
"ds_read_b64_tr_b16 a[156:157], v212 offset:34816\n\t"
"ds_read_b64_tr_b16 a[158:159], v212 offset:39424\n\t"
"buffer_load_dwordx4 v202, %[wq], s68 offen offset:2048 lds\n\t"
"v_mul_f32 v179, s72, v179\n\t"
"v_mfma_f32_16x16x32_bf16 v[112:115], a[16:19], a[200:203], v[112:115]\n\t"
"v_fma_f32 v160, -v120, s72, v179\n\t"
"v_fma_f32 v120, v120, s72, -v178\n\t"
"v_mfma_f32_16x16x32_bf16 v[116:119], a[20:23], a[200:203], v[116:119]\n\t"
"ds_read_b64_tr_b16 a[160:161], v210 offset:34960\n\t"
"v_fma_f32 v161, -v121, s72, v179\n\t"
"v_mfma_f32_16x16x32_bf16 v[40:43], a[24:27], a[204:207], v[40:43]\n\t"
"s_waitcnt lgkmcnt(5)\n\t"
"v_exp_f32 v160, v160\n\t"
"v_mfma_f32_16x16x32_bf16 v[44:47], a[28:31], a[204:207], v[44:47]\n\t"
"ds_read_b64_tr_b16 a[162:163], v210 offset:39568\n\t"
"ds_read_b64_tr_b16 a[164:165], v210 offset:35000\n\t"
"ds_read_b64_tr_b16 a[166:167], v210 offset:39608\n\t"
"v_fma_f32 v121, v121, s72, -v178\n\t"
"v_mfma_f32_16x16x32_bf16 v[48:51], a[24:27], a[144:147], v[48:51]\n\t"
"v_exp_f32 v120, v120\n\t"
"v_mfma_f32_16x16x32_bf16 v[52:55], a[28:31], a[144:147], v[52:55]\n\t"
"ds_read_b64_tr_b16 a[168:169], v210 offset:35032\n\t"
"v_fma_f32 v162, -v122, s72, v179\n\t"
"v_mfma_f32_16x16x32_bf16 v[56:59], a[24:27], a[148:151], v[56:59]\n\t"
"s_waitcnt lgkmcnt(5)\n\t"
"v_exp_f32 v161, v161\n\t"
"v_mfma_f32_16x16x32_bf16 v[60:63], a[28:31], a[148:151], v[60:63]\n\t"
"ds_read_b64_tr_b16 a[170:171], v210 offset:39640\n\t"
"ds_read_b64_tr_b16 a[172:173], v210 offset:35072\n\t"
"ds_read_b64_tr_b16 a[174:175], v210 offset:39680\n\t"
"v_fma_f32 v122, v122, s72, -v178\n\t"
"v_mfma_f32_16x16x32_bf16 v[64:67], a[24:27], a[152:155], v[64:67]\n\t"
"buffer_load_dwordx4 v203, %[wq], s68 offen offset:3072 lds\n\t"
"v_exp_f32 v121, v121\n\t"
"v_fma_f32 v163, -v123, s72, v179\n\t"
"v_mfma_f32_16x16x32_bf16 v[68:71], a[28:31], a[152:155], v[68:71]\n\t"
"s_add_u32 m0, s63, 41984\n\t"
"v_exp_f32 v162, v162\n\t"
"v_mfma_f32_16x16x32_bf16 v[72:75], a[24:27], a[156:159], v[72:75]\n\t"
"s_waitcnt lgkmcnt(4)\n\t"
"ds_read_b64_tr_b16 a[176:177], v212 offset:34960\n\t"
"v_fma_f32 v123, v123, s72, -v178\n\t"
"v_mfma_f32_16x16x32_bf16 v[76:79], a[28:31], a[156:159], v[76:79]\n\t"
"ds_read_b64_tr_b16 a[178:179], v212 offset:39568\n\t"
"ds_read_b64_tr_b16 a[180:181], v210 offset:44032\n\t"
"ds_read_b64_tr_b16 a[182:183], v210 offset:48640\n\t"
"v_exp_f32 v122, v122\n\t"
"v_mfma_f32_16x16x32_bf16 v[80:83], a[24:27], a[160:163], v[80:83]\n\t"
"ds_read_b64_tr_b16 a[184:185], v210 offset:44072\n\t"
"v_fma_f32 v164, -v124, s72, v179\n\t"
"v_mfma_f32_16x16x32_bf16 v[84:87], a[28:31], a[160:163], v[84:87]\n\t"
"v_exp_f32 v163, v163\n\t"
"v_mfma_f32_16x16x32_bf16 v[88:91], a[24:27], a[164:167], v[88:91]\n\t"
"s_waitcnt lgkmcnt(5)\n\t"
"v_add_f32 v182, v160, v162\n\t"
"v_fma_f32 v124, v124, s72, -v178\n\t"
"v_mfma_f32_16x16x32_bf16 v[92:95], a[28:31], a[164:167], v[92:95]\n\t"
"ds_read_b64_tr_b16 a[186:187], v210 offset:48680\n\t"
"ds_read_b64_tr_b16 a[188:189], v210 offset:44104\n\t"
"ds_read_b64_tr_b16 a[190:191], v210 offset:48712\n\t"
"v_exp_f32 v123, v123\n\t"
"v_mfma_f32_16x16x32_bf16 v[96:99], a[24:27], a[168:171], v[96:99]\n\t"
"buffer_load_dwordx4 v204, %[wq], s68 offen offset:0 lds\n\t"
"v_add_f32 v180, v120, v122\n\t"
"v_mfma_f32_16x16x32_bf16 v[100:103], a[28:31], a[168:171], v[100:103]\n\t"
"ds_read_b64_tr_b16 a[192:193], v210 offset:44144\n\t"
"v_fma_f32 v165, -v125, s72, v179\n\t"
"v_mfma_f32_16x16x32_bf16 v[104:107], a[24:27], a[172:175], v[104:107]\n\t"
"s_waitcnt lgkmcnt(5)\n\t"
"v_exp_f32 v164, v164\n\t"
"v_add_f32 v183, v161, v163\n\t"
"v_mfma_f32_16x16x32_bf16 v[108:111], a[28:31], a[172:175], v[108:111]\n\t"
"ds_read_b64_tr_b16 a[194:195], v210 offset:48752\n\t"
"ds_read_b64_tr_b16 a[196:197], v212 offset:44032\n\t"
"ds_read_b64_tr_b16 a[198:199], v212 offset:48640\n\t"
"v_fma_f32 v125, v125, s72, -v178\n\t"
"v_mfma_f32_16x16x32_bf16 v[112:115], a[24:27], a[176:179], v[112:115]\n\t"
"v_exp_f32 v124, v124\n\t"
"v_mfma_f32_16x16x32_bf16 v[116:119], a[28:31], a[176:179], v[116:119]\n\t"
"v_add_f32 v181, v121, v123\n\t"
"v_fma_f32 v166, -v126, s72, v179\n\t"
"v_mfma_f32_16x16x32_bf16 v[40:43], a[32:35], a[180:183], v[40:43]\n\t"
"s_waitcnt lgkmcnt(4)\n\t"
"v_exp_f32 v165, v165\n\t"
"v_mfma_f32_16x16x32_bf16 v[44:47], a[36:39], a[180:183], v[44:47]\n\t"
"ds_read_b64_tr_b16 a[200:201], v210 offset:44176\n\t"
"ds_read_b64_tr_b16 a[202:203], v210 offset:48784\n\t"
"ds_read_b64_tr_b16 a[204:205], v210 offset:44216\n\t"
"ds_read_b64_tr_b16 a[206:207], v210 offset:48824\n\t"
"v_add_f32 v182, v182, v164\n\t"
"v_mfma_f32_16x16x32_bf16 v[48:51], a[32:35], a[184:187], v[48:51]\n\t"
"buffer_load_dwordx4 v205, %[wq], s68 offen offset:1024 lds\n\t"
"v_fma_f32 v126, v126, s72, -v178\n\t"
"v_mfma_f32_16x16x32_bf16 v[52:55], a[36:39], a[184:187], v[52:55]\n\t"
"v_exp_f32 v125, v125\n\t"
"v_mfma_f32_16x16x32_bf16 v[56:59], a[32:35], a[188:191], v[56:59]\n\t"
"s_waitcnt lgkmcnt(4)\n\t"
"v_add_f32 v180, v180, v124\n\t"
"v_fma_f32 v167, -v127, s72, v179\n\t"
"v_mfma_f32_16x16x32_bf16 v[60:63], a[36:39], a[188:191], v[60:63]\n\t"
"ds_read_b64_tr_b16 a[144:145], v210 offset:44248\n\t"
"ds_read_b64_tr_b16 a[146:147], v210 offset:48856\n\t"
"ds_read_b64_tr_b16 a[148:149], v210 offset:44288\n\t"
"ds_read_b64_tr_b16 a[150:151], v210 offset:48896\n\t"
"v_exp_f32 v166, v166\n\t"
"v_mfma_f32_16x16x32_bf16 v[64:67], a[32:35], a[192:195], v[64:67]\n\t"
"v_add_f32 v183, v183, v165\n\t"
"v_fma_f32 v127, v127, s72, -v178\n\t"
"v_mfma_f32_16x16x32_bf16 v[68:71], a[36:39], a[192:195], v[68:71]\n\t"
"v_exp_f32 v126, v126\n\t"
"v_mfma_f32_16x16x32_bf16 v[72:75], a[32:35], a[196:199], v[72:75]\n\t"
"s_waitcnt lgkmcnt(4)\n\t"
"ds_read_b64_tr_b16 a[152:153], v212 offset:44176\n\t"
"v_add_f32 v181, v181, v125\n\t"
"v_mfma_f32_16x16x32_bf16 v[76:79], a[36:39], a[196:199], v[76:79]\n\t"
"ds_read_b64_tr_b16 a[154:155], v212 offset:48784\n\t"
"ds_read_b64_tr_b16 a[156:157], v210 offset:53248\n\t"
"ds_read_b64_tr_b16 a[158:159], v210 offset:57856\n\t"
"v_fma_f32 v168, -v128, s72, v179\n\t"
"v_mfma_f32_16x16x32_bf16 v[80:83], a[32:35], a[200:203], v[80:83]\n\t"
"v_exp_f32 v167, v167\n\t"
"v_mfma_f32_16x16x32_bf16 v[84:87], a[36:39], a[200:203], v[84:87]\n\t"
"buffer_load_dwordx4 v206, %[wq], s68 offen offset:2048 lds\n\t"
"v_add_f32 v182, v182, v166\n\t"
"v_fma_f32 v128, v128, s72, -v178\n\t"
"v_mfma_f32_16x16x32_bf16 v[88:91], a[32:35], a[204:207], v[88:91]\n\t"
"s_waitcnt lgkmcnt(4)\n\t"
"v_exp_f32 v127, v127\n\t"
"v_mfma_f32_16x16x32_bf16 v[92:95], a[36:39], a[204:207], v[92:95]\n\t"
"ds_read_b64_tr_b16 a[160:161], v210 offset:53288\n\t"
"ds_read_b64_tr_b16 a[162:163], v210 offset:57896\n\t"
"ds_read_b64_tr_b16 a[164:165], v210 offset:53320\n\t"
"ds_read_b64_tr_b16 a[166:167], v210 offset:57928\n\t"
"v_add_f32 v180, v180, v126\n\t"
"v_mfma_f32_16x16x32_bf16 v[96:99], a[32:35], a[144:147], v[96:99]\n\t"
"ds_read_b64_tr_b16 a[168:169], v210 offset:53360\n\t"
"v_fma_f32 v169, -v129, s72, v179\n\t"
"v_mfma_f32_16x16x32_bf16 v[100:103], a[36:39], a[144:147], v[100:103]\n\t"
"v_exp_f32 v168, v168\n\t"
"v_add_f32 v183, v183, v167\n\t"
"v_mfma_f32_16x16x32_bf16 v[104:107], a[32:35], a[148:151], v[104:107]\n\t"
"s_waitcnt lgkmcnt(5)\n\t"
"ds_read_b64_tr_b16 a[170:171], v210 offset:57968\n\t"
"v_fma_f32 v129, v129, s72, -v178\n\t"
"v_mfma_f32_16x16x32_bf16 v[108:111], a[36:39], a[148:151], v[108:111]\n\t"
"ds_read_b64_tr_b16 a[172:173], v212 offset:53248\n\t"
"ds_read_b64_tr_b16 a[174:175], v212 offset:57856\n\t"
"buffer_load_dwordx4 v207, %[wq], s68 offen offset:3072 lds\n\t"
"v_exp_f32 v128, v128\n\t"
"v_mfma_f32_16x16x32_bf16 v[112:115], a[32:35], a[152:155], v[112:115]\n\t"
"s_add_u32 m0, s63, 46080\n\t"
"v_add_f32 v181, v181, v127\n\t"
"v_fma_f32 v170, -v130, s72, v179\n\t"
"v_mfma_f32_16x16x32_bf16 v[116:119], a[36:39], a[152:155], v[116:119]\n\t"
"v_exp_f32 v169, v169\n\t"
"v_mfma_f32_16x16x32_bf16 v[40:43], a[40:43], a[156:159], v[40:43]\n\t"
"s_waitcnt lgkmcnt(4)\n\t"
"ds_read_b64_tr_b16 a[176:177], v210 offset:53392\n\t"
"v_add_f32 v182, v182, v168\n\t"
"v_mfma_f32_16x16x32_bf16 v[44:47], a[44:47], a[156:159], v[44:47]\n\t"
"ds_read_b64_tr_b16 a[178:179], v210 offset:58000\n\t"
"ds_read_b64_tr_b16 a[180:181], v210 offset:53432\n\t"
"ds_read_b64_tr_b16 a[182:183], v210 offset:58040\n\t"
"v_fma_f32 v130, v130, s72, -v178\n\t"
"v_mfma_f32_16x16x32_bf16 v[48:51], a[40:43], a[160:163], v[48:51]\n\t"
"v_exp_f32 v129, v129\n\t"
"v_mfma_f32_16x16x32_bf16 v[52:55], a[44:47], a[160:163], v[52:55]\n\t"
"ds_read_b64_tr_b16 a[184:185], v210 offset:53464\n\t"
"ds_read_b64_tr_b16 a[186:187], v210 offset:58072\n\t"
"ds_read_b64_tr_b16 a[188:189], v210 offset:53504\n\t"
"ds_read_b64_tr_b16 a[190:191], v210 offset:58112\n\t"
"ds_read_b64_tr_b16 a[192:193], v212 offset:53392\n\t"
"ds_read_b64_tr_b16 a[194:195], v212 offset:58000\n\t"
"v_add_f32 v180, v180, v128\n\t"
"v_fma_f32 v171, -v131, s72, v179\n\t"
"v_mfma_f32_16x16x32_bf16 v[56:59], a[40:43], a[164:167], v[56:59]\n\t"
"buffer_load_dwordx4 v208, %[wq], s68 offen offset:0 lds\n\t"
"v_exp_f32 v170, v170\n\t"
"v_mfma_f32_16x16x32_bf16 v[60:63], a[44:47], a[164:167], v[60:63]\n\t"
"s_waitcnt lgkmcnt(10)\n\t"
"v_add_f32 v183, v183, v169\n\t"
"v_fma_f32 v131, v131, s72, -v178\n\t"
"v_mfma_f32_16x16x32_bf16 v[64:67], a[40:43], a[168:171], v[64:67]\n\t"
"s_waitcnt vmcnt(9)\n\t"
"v_exp_f32 v130, v130\n\t"
"v_mfma_f32_16x16x32_bf16 v[68:71], a[44:47], a[168:171], v[68:71]\n\t"
"s_waitcnt lgkmcnt(0)\n\t"
"v_add_f32 v181, v181, v129\n\t"
"v_fma_f32 v172, -v132, s72, v179\n\t"
"v_mfma_f32_16x16x32_bf16 v[72:75], a[40:43], a[172:175], v[72:75]\n\t"
"s_barrier\n\t"
"v_exp_f32 v171, v171\n\t"
"v_mfma_f32_16x16x32_bf16 v[76:79], a[44:47], a[172:175], v[76:79]\n\t"
"v_add_f32 v182, v182, v170\n\t"
"v_fma_f32 v132, v132, s72, -v178\n\t"
"v_mfma_f32_16x16x32_bf16 v[80:83], a[40:43], a[176:179], v[80:83]\n\t"
"s_add_u32 s68, s44, 1550592\n\t"
"buffer_store_dwordx2 v[232:233], v231, %[ws], s77 offen offset:0 sc1\n\t"
"ds_read_b64_tr_b16 a[196:197], v209 offset:0\n\t"
"ds_read_b64_tr_b16 a[198:199], v209 offset:4608\n\t"
"v_min_u32 v199, 88, v238\n\t"
"v_mfma_f32_16x16x32_bf16 v[84:87], a[44:47], a[176:179], v[84:87]\n\t"
"s_add_u32 m0, s63, 78848\n\t"
"v_exp_f32 v131, v131\n\t"
"v_mfma_f32_16x16x32_bf16 v[88:91], a[40:43], a[180:183], v[88:91]\n\t"
"ds_read_b64_tr_b16 a[200:201], v209 offset:40\n\t"
"v_add_f32 v180, v180, v130\n\t"
"v_mfma_f32_16x16x32_bf16 v[92:95], a[44:47], a[180:183], v[92:95]\n\t"
"ds_read_b64_tr_b16 a[202:203], v209 offset:4648\n\t"
"ds_read_b64_tr_b16 a[204:205], v209 offset:72\n\t"
"ds_read_b64_tr_b16 a[206:207], v209 offset:4680\n\t"
"v_fma_f32 v173, -v133, s72, v179\n\t"
"v_mfma_f32_16x16x32_bf16 v[96:99], a[40:43], a[184:187], v[96:99]\n\t"
"v_exp_f32 v172, v172\n\t"
"v_add_f32 v183, v183, v171\n\t"
"v_mfma_f32_16x16x32_bf16 v[100:103], a[44:47], a[184:187], v[100:103]\n\t"
"ds_read_b64_tr_b16 a[144:145], v209 offset:112\n\t"
"v_fma_f32 v133, v133, s72, -v178\n\t"
"v_mfma_f32_16x16x32_bf16 v[104:107], a[40:43], a[188:191], v[104:107]\n\t"
"s_waitcnt lgkmcnt(5)\n\t"
"v_exp_f32 v132, v132\n\t"
"v_mfma_f32_16x16x32_bf16 v[108:111], a[44:47], a[188:191], v[108:111]\n\t"
"ds_read_b64_tr_b16 a[146:147], v209 offset:4720\n\t"
"ds_read_b64_tr_b16 a[148:149], v211 offset:0\n\t"
"ds_read_b64_tr_b16 a[150:151], v211 offset:4608\n\t"
"v_add_f32 v181, v181, v131\n\t"
"v_fma_f32 v174, -v134, s72, v179\n\t"
"v_mfma_f32_16x16x32_bf16 v[112:115], a[40:43], a[192:195], v[112:115]\n\t"
"v_exp_f32 v173, v173\n\t"
"v_mfma_f32_16x16x32_bf16 v[116:119], a[44:47], a[192:195], v[116:119]\n\t"
"ds_read_b64_tr_b16 a[152:153], v209 offset:144\n\t"
"v_add_f32 v182, v182, v172\n\t"
"v_mfma_f32_16x16x32_bf16 v[40:43], a[48:51], a[196:199], v[40:43]\n\t"
"s_waitcnt lgkmcnt(5)\n\t"
"ds_read_b64_tr_b16 a[154:155], v209 offset:4752\n\t"
"v_fma_f32 v134, v134, s72, -v178\n\t"
"v_mfma_f32_16x16x32_bf16 v[44:47], a[52:55], a[196:199], v[44:47]\n\t"
"ds_read_b64_tr_b16 a[156:157], v209 offset:184\n\t"
"ds_read_b64_tr_b16 a[158:159], v209 offset:4792\n\t"
"v_exp_f32 v133, v133\n\t"
"v_mfma_f32_16x16x32_bf16 v[48:51], a[48:51], a[200:203], v[48:51]\n\t"
"v_add_f32 v180, v180, v132\n\t"
"v_fma_f32 v175, -v135, s72, v179\n\t"
"v_mfma_f32_16x16x32_bf16 v[52:55], a[52:55], a[200:203], v[52:55]\n\t"
"v_exp_f32 v174, v174\n\t"
"v_mfma_f32_16x16x32_bf16 v[56:59], a[48:51], a[204:207], v[56:59]\n\t"
"buffer_load_dwordx4 v200, %[wq], s68 offen offset:0 lds\n\t"
"v_add_f32 v183, v183, v173\n\t"
"v_fma_f32 v135, v135, s72, -v178\n\t"
"v_mfma_f32_16x16x32_bf16 v[60:63], a[52:55], a[204:207], v[60:63]\n\t"
"s_waitcnt lgkmcnt(4)\n\t"
"ds_read_b64_tr_b16 a[160:161], v209 offset:216\n\t"
"ds_read_b64_tr_b16 a[162:163], v209 offset:4824\n\t"
"ds_read_b64_tr_b16 a[164:165], v209 offset:256\n\t"
"ds_read_b64_tr_b16 a[166:167], v209 offset:4864\n\t"
"v_exp_f32 v134, v134\n\t"
"v_mfma_f32_16x16x32_bf16 v[64:67], a[48:51], a[144:147], v[64:67]\n\t"
"v_add_f32 v181, v181, v133\n\t"
"v_fma_f32 v176, -v136, s72, v179\n\t"
"v_mfma_f32_16x16x32_bf16 v[68:71], a[52:55], a[144:147], v[68:71]\n\t"
"v_exp_f32 v175, v175\n\t"
"v_mfma_f32_16x16x32_bf16 v[72:75], a[48:51], a[148:151], v[72:75]\n\t"
"s_waitcnt lgkmcnt(4)\n\t"
"ds_read_b64_tr_b16 a[168:169], v211 offset:144\n\t"
"v_add_f32 v182, v182, v174\n\t"
"v_mfma_f32_16x16x32_bf16 v[76:79], a[52:55], a[148:151], v[76:79]\n\t"
"ds_read_b64_tr_b16 a[170:171], v211 offset:4752\n\t"
"ds_read_b64_tr_b16 a[172:173], v209 offset:9216\n\t"
"ds_read_b64_tr_b16 a[174:175], v209 offset:13824\n\t"
"v_fma_f32 v136, v136, s72, -v178\n\t"
"v_mfma_f32_16x16x32_bf16 v[80:83], a[48:51], a[152:155], v[80:83]\n\t"
"v_exp_f32 v135, v135\n\t"
"v_mfma_f32_16x16x32_bf16 v[84:87], a[52:55], a[152:155], v[84:87]\n\t"
"v_add_f32 v180, v180, v134\n\t"
"v_fma_f32 v177, -v137, s72, v179\n\t"
"v_mfma_f32_16x16x32_bf16 v[88:91], a[48:51], a[156:159], v[88:91]\n\t"
"s_waitcnt lgkmcnt(4)\n\t"
"v_exp_f32 v176, v176\n\t"
"v_mfma_f32_16x16x32_bf16 v[92:95], a[52:55], a[156:159], v[92:95]\n\t"
"ds_read_b64_tr_b16 a[176:177], v209 offset:9256\n\t"
"ds_read_b64_tr_b16 a[178:179], v209 offset:13864\n\t"
"ds_read_b64_tr_b16 a[180:181], v209 offset:9288\n\t"
"ds_read_b64_tr_b16 a[182:183], v209 offset:13896\n\t"
"v_add_f32 v183, v183, v175\n\t"
"v_mfma_f32_16x16x32_bf16 v[96:99], a[48:51], a[160:163], v[96:99]\n\t"
"v_fma_f32 v137, v137, s72, -v178\n\t"
"v_exp_f32 v136, v136\n\t"
"v_mfma_f32_16x16x32_bf16 v[100:103], a[52:55], a[160:163], v[100:103]\n\t"
"buffer_load_dwordx4 v201, %[wq], s68 offen offset:1024 lds\n\t"
"v_mfma_f32_16x16x32_bf16 v[104:107], a[48:51], a[164:167], v[104:107]\n\t"
"s_waitcnt lgkmcnt(4)\n\t"
"ds_read_b64_tr_b16 a[184:185], v209 offset:9328\n\t"
"v_add_f32 v181, v181, v135\n\t"
"v_mfma_f32_16x16x32_bf16 v[108:111], a[52:55], a[164:167], v[108:111]\n\t"
"ds_read_b64_tr_b16 a[186:187], v209 offset:13936\n\t"
"ds_read_b64_tr_b16 a[188:189], v211 offset:9216\n\t"
"ds_read_b64_tr_b16 a[190:191], v211 offset:13824\n\t"
"v_exp_f32 v177, v177\n\t"
"v_mfma_f32_16x16x32_bf16 v[112:115], a[48:51], a[168:171], v[112:115]\n\t"
"ds_read_b64_tr_b16 a[192:193], v209 offset:9360\n\t"
"v_add_f32 v182, v182, v176\n\t"
"v_mfma_f32_16x16x32_bf16 v[116:119], a[52:55], a[168:171], v[116:119]\n\t"
"v_exp_f32 v137, v137\n\t"
"v_add_f32 v180, v180, v136\n\t"
"v_mfma_f32_16x16x32_bf16 v[40:43], a[56:59], a[172:175], v[40:43]\n\t"
"s_waitcnt lgkmcnt(5)\n\t"
"v_add_f32 v183, v183, v177\n\t"
"v_add_f32 v181, v181, v137\n\t"
"v_mfma_f32_16x16x32_bf16 v[44:47], a[60:63], a[172:175], v[44:47]\n\t"
"ds_read_b64_tr_b16 a[194:195], v209 offset:13968\n\t"
"ds_read_b64_tr_b16 a[196:197], v209 offset:9400\n\t"
"ds_read_b64_tr_b16 a[198:199], v209 offset:14008\n\t"
"v_add_f32 v180, v180, v181\n\t"
"v_mfma_f32_16x16x32_bf16 v[48:51], a[56:59], a[176:179], v[48:51]\n\t"
"ds_read_b64_tr_b16 a[200:201], v209 offset:9432\n\t"
"v_add_f32 v181, v182, v183\n\t"
"v_mfma_f32_16x16x32_bf16 v[52:55], a[60:63], a[176:179], v[52:55]\n\t"
"v_rcp_f32 v180, v180\n\t"
"v_mfma_f32_16x16x32_bf16 v[56:59], a[56:59], a[180:183], v[56:59]\n\t"
"s_waitcnt lgkmcnt(5)\n\t"
"v_rcp_f32 v181, v181\n\t"
"v_mfma_f32_16x16x32_bf16 v[60:63], a[60:63], a[180:183], v[60:63]\n\t"
"ds_read_b64_tr_b16 a[202:203], v209 offset:14040\n\t"
"ds_read_b64_tr_b16 a[204:205], v209 offset:9472\n\t"
"ds_read_b64_tr_b16 a[206:207], v209 offset:14080\n\t"
"v_mul_f32 v120, v180, v120\n\t"
"v_mul_f32 v121, v180, v121\n\t"
"v_mfma_f32_16x16x32_bf16 v[64:67], a[56:59], a[184:187], v[64:67]\n\t"
"v_mul_f32 v122, v180, v122\n\t"
"v_mul_f32 v123, v180, v123\n\t"
"v_mfma_f32_16x16x32_bf16 v[68:71], a[60:63], a[184:187], v[68:71]\n\t"
"buffer_load_dwordx4 v202, %[wq], s68 offen offset:2048 lds\n\t"
"v_cvt_pk_bf16_f32 v184, v120, v121\n\t"
"v_mfma_f32_16x16x32_bf16 v[72:75], a[56:59], a[188:191], v[72:75]\n\t"
"s_waitcnt lgkmcnt(4)\n\t"
"v_mul_f32 v124, v180, v124\n\t"
"v_mul_f32 v125, v180, v125\n\t"
"v_mfma_f32_16x16x32_bf16 v[76:79], a[60:63], a[188:191], v[76:79]\n\t"
"ds_read_b64_tr_b16 a[144:145], v211 offset:9360\n\t"
"ds_read_b64_tr_b16 a[146:147], v211 offset:13968\n\t"
"ds_read_b64_tr_b16 a[148:149], v209 offset:18432\n\t"
"ds_read_b64_tr_b16 a[150:151], v209 offset:23040\n\t"
"v_cvt_pk_bf16_f32 v185, v122, v123\n\t"
"v_mul_f32 v126, v180, v126\n\t"
"v_mfma_f32_16x16x32_bf16 v[80:83], a[56:59], a[192:195], v[80:83]\n\t"
"v_mul_f32 v127, v180, v127\n\t"
"v_cvt_pk_bf16_f32 v186, v124, v125\n\t"
"v_mfma_f32_16x16x32_bf16 v[84:87], a[60:63], a[192:195], v[84:87]\n\t"
"ds_write_b64 v225, v[184:185] offset:0\n\t"
"v_mfma_f32_16x16x32_bf16 v[88:91], a[56:59], a[196:199], v[88:91]\n\t"
"s_waitcnt lgkmcnt(5)\n\t"
"v_mul_f32 v128, v180, v128\n\t"
"v_mul_f32 v129, v180, v129\n\t"
"v_mfma_f32_16x16x32_bf16 v[92:95], a[60:63], a[196:199], v[92:95]\n\t"
"ds_read_b64_tr_b16 a[152:153], v209 offset:18472\n\t"
"ds_read_b64_tr_b16 a[154:155], v209 offset:23080\n\t"
"ds_read_b64_tr_b16 a[156:157], v209 offset:18504\n\t"
"ds_read_b64_tr_b16 a[158:159], v209 offset:23112\n\t"
"v_cvt_pk_bf16_f32 v187, v126, v127\n\t"
"v_mfma_f32_16x16x32_bf16 v[96:99], a[56:59], a[200:203], v[96:99]\n\t"
"v_mul_f32 v130, v180, v130\n\t"
"v_mul_f32 v131, v180, v131\n\t"
"v_mfma_f32_16x16x32_bf16 v[100:103], a[60:63], a[200:203], v[100:103]\n\t"
"ds_write_b64 v225, v[186:187] offset:8\n\t"
"v_cvt_pk_bf16_f32 v184, v128, v129\n\t"
"v_mfma_f32_16x16x32_bf16 v[104:107], a[56:59], a[204:207], v[104:107]\n\t"
"s_waitcnt lgkmcnt(6)\n\t"
"v_mul_f32 v132, v180, v132\n\t"
"v_mul_f32 v133, v180, v133\n\t"
"v_mfma_f32_16x16x32_bf16 v[108:111], a[60:63], a[204:207], v[108:111]\n\t"
"ds_read_b64_tr_b16 a[160:161], v209 offset:18544\n\t"
"ds_read_b64_tr_b16 a[162:163], v209 offset:23152\n\t"
"ds_read_b64_tr_b16 a[164:165], v211 offset:18432\n\t"
"ds_read_b64_tr_b16 a[166:167], v211 offset:23040\n\t"
"v_cvt_pk_bf16_f32 v185, v130, v131\n\t"
"v_mfma_f32_16x16x32_bf16 v[112:115], a[56:59], a[144:147], v[112:115]\n\t"
"v_mul_f32 v134, v180, v134\n\t"
"v_mul_f32 v135, v180, v135\n\t"
"v_mfma_f32_16x16x32_bf16 v[116:119], a[60:63], a[144:147], v[116:119]\n\t"
"buffer_load_dwordx4 v203, %[wq], s68 offen offset:3072 lds\n\t"
"v_cvt_pk_bf16_f32 v186, v132, v133\n\t"
"v_mfma_f32_16x16x32_bf16 v[40:43], a[64:67], a[148:151], v[40:43]\n\t"
"s_add_u32 m0, s63, 82944\n\t"
"ds_write_b64 v225, v[184:185] offset:16\n\t"
"v_mul_f32 v136, v180, v136\n\t"
"v_mfma_f32_16x16x32_bf16 v[44:47], a[68:71], a[148:151], v[44:47]\n\t"
"s_waitcnt lgkmcnt(6)\n\t"
"ds_read_b64_tr_b16 a[168:169], v209 offset:18576\n\t"
"ds_read_b64_tr_b16 a[170:171], v209 offset:23184\n\t"
"ds_read_b64_tr_b16 a[172:173], v209 offset:18616\n\t"
"ds_read_b64_tr_b16 a[174:175], v209 offset:23224\n\t"
"v_mul_f32 v137, v180, v137\n\t"
"v_cvt_pk_bf16_f32 v187, v134, v135\n\t"
"v_mfma_f32_16x16x32_bf16 v[48:51], a[64:67], a[152:155], v[48:51]\n\t"
"v_mul_f32 v160, v181, v160\n\t"
"v_mul_f32 v161, v181, v161\n\t"
"v_mfma_f32_16x16x32_bf16 v[52:55], a[68:71], a[152:155], v[52:55]\n\t"
"ds_write_b64 v225, v[186:187] offset:24\n\t"
"v_cvt_pk_bf16_f32 v184, v136, v137\n\t"
"v_mfma_f32_16x16x32_bf16 v[56:59], a[64:67], a[156:159], v[56:59]\n\t"
"s_waitcnt lgkmcnt(6)\n\t"
"v_mul_f32 v162, v181, v162\n\t"
"v_mul_f32 v163, v181, v163\n\t"
"v_mfma_f32_16x16x32_bf16 v[60:63], a[68:71], a[156:159], v[60:63]\n\t"
"ds_read_b64_tr_b16 a[176:177], v209 offset:18648\n\t"
"ds_read_b64_tr_b16 a[178:179], v209 offset:23256\n\t"
"ds_read_b64_tr_b16 a[180:181], v209 offset:18688\n\t"
"ds_read_b64_tr_b16 a[182:183], v209 offset:23296\n\t"
"v_cvt_pk_bf16_f32 v185, v160, v161\n\t"
"v_mfma_f32_16x16x32_bf16 v[64:67], a[64:67], a[160:163], v[64:67]\n\t"
"v_mul_f32 v164, v181, v164\n\t"
"v_mul_f32 v165, v181, v165\n\t"
"v_mfma_f32_16x16x32_bf16 v[68:71], a[68:71], a[160:163], v[68:71]\n\t"
"ds_read_b64_tr_b16 a[184:185], v211 offset:18576\n\t"
"v_cvt_pk_bf16_f32 v186, v162, v163\n\t"
"v_mfma_f32_16x16x32_bf16 v[72:75], a[64:67], a[164:167], v[72:75]\n\t"
"s_waitcnt lgkmcnt(6)\n\t"
"ds_write_b64 v225, v[184:185] offset:32\n\t"
"v_mul_f32 v166, v181, v166\n\t"
"v_mfma_f32_16x16x32_bf16 v[76:79], a[68:71], a[164:167], v[76:79]\n\t"
"ds_read_b64_tr_b16 a[186:187], v211 offset:23184\n\t"
"ds_read_b64_tr_b16 a[188:189], v219 offset:0\n\t"
"ds_read_b64_tr_b16 a[190:191], v220 offset:0\n\t"
"v_mul_f32 v167, v181, v167\n\t"
"v_mfma_f32_16x16x32_bf16 v[80:83], a[64:67], a[168:171], v[80:83]\n\t"
"buffer_load_dwordx4 v204, %[wq], s68 offen offset:0 lds\n\t"
"v_cvt_pk_bf16_f32 v187, v164, v165\n\t"
"v_mul_f32 v168, v181, v168\n\t"
"v_mfma_f32_16x16x32_bf16 v[84:87], a[68:71], a[168:171], v[84:87]\n\t"
"v_mul_f32 v169, v181, v169\n\t"
"v_cvt_pk_bf16_f32 v184, v166, v167\n\t"
"v_mfma_f32_16x16x32_bf16 v[88:91], a[64:67], a[172:175], v[88:91]\n\t"
"s_waitcnt lgkmcnt(5)\n\t"
"ds_write_b64 v225, v[186:187] offset:40\n\t"
"v_mfma_f32_16x16x32_bf16 v[92:95], a[68:71], a[172:175], v[92:95]\n\t"
"ds_read_b64_tr_b16 a[192:193], v221 offset:0\n\t"
"ds_read_b64_tr_b16 a[194:195], v222 offset:0\n\t"
"ds_read_b64_tr_b16 a[196:197], v223 offset:0\n\t"
"ds_read_b64_tr_b16 a[198:199], v224 offset:0\n\t"
"v_mul_f32 v170, v181, v170\n\t"
"v_mul_f32 v171, v181, v171\n\t"
"v_cvt_pk_bf16_f32 v185, v168, v169\n\t"
"v_mfma_f32_16x16x32_bf16 v[96:99], a[64:67], a[176:179], v[96:99]\n\t"
"ds_write_b64 v225, v[184:185] offset:48\n\t"
"v_mul_f32 v172, v181, v172\n\t"
"v_mul_f32 v173, v181, v173\n\t"
"v_cvt_pk_bf16_f32 v186, v170, v171\n\t"
"v_mfma_f32_16x16x32_bf16 v[100:103], a[68:71], a[176:179], v[100:103]\n\t"
"v_mul_f32 v174, v181, v174\n\t"
"v_mul_f32 v175, v181, v175\n\t"
"v_cvt_pk_bf16_f32 v187, v172, v173\n\t"
"v_mfma_f32_16x16x32_bf16 v[104:107], a[64:67], a[180:183], v[104:107]\n\t"
"s_waitcnt lgkmcnt(6)\n\t"
"ds_write_b64 v225, v[186:187] offset:56\n\t"
"v_mul_f32 v176, v181, v176\n\t"
"v_mul_f32 v177, v181, v177\n\t"
"v_cvt_pk_bf16_f32 v184, v174, v175\n\t"
"v_mfma_f32_16x16x32_bf16 v[108:111], a[68:71], a[180:183], v[108:111]\n\t"
"ds_read_b64_tr_b16 a[200:201], v219 offset:8\n\t"
"ds_read_b64_tr_b16 a[202:203], v220 offset:8\n\t"
"ds_read_b64_tr_b16 a[204:205], v221 offset:8\n\t"
"ds_read_b64_tr_b16 a[206:207], v222 offset:8\n\t"
"v_cvt_pk_bf16_f32 v185, v176, v177\n\t"
"v_mfma_f32_16x16x32_bf16 v[112:115], a[64:67], a[184:187], v[112:115]\n\t"
"ds_write_b64 v225, v[184:185] offset:64\n\t"
"v_mfma_f32_16x16x32_bf16 v[116:119], a[68:71], a[184:187], v[116:119]\n\t"
"ds_read_b64_tr_b16 a[144:145], v223 offset:8\n\t"
"v_max3_f32 v178, v140, v141, v142\n\t"
"v_min3_f32 v179, v140, v141, v142\n\t"
"v_max3_f32 v180, v143, v144, v145\n\t"
"v_min3_f32 v181, v143, v144, v145\n\t"
"v_max3_f32 v178, v178, v146, v147\n\t"
"v_min3_f32 v179, v179, v146, v147\n\t"
"v_max3_f32 v180, v180, v148, v149\n\t"
"v_min3_f32 v181, v181, v148, v149\n\t"
"v_max3_f32 v178, v178, v150, v151\n\t"
"v_min3_f32 v179, v179, v150, v151\n\t"
"v_max3_f32 v180, v180, v152, v153\n\t"
"v_min3_f32 v181, v181, v152, v153\n\t"
"v_max3_f32 v178, v178, v154, v155\n\t"
"v_min3_f32 v179, v179, v154, v155\n\t"
"v_max3_f32 v180, v180, v156, v157\n\t"
"v_min3_f32 v181, v181, v156, v157\n\t"
"v_max_f32 v178, v178, v180\n\t"
"v_min_f32 v179, v179, v181\n\t"
"v_mul_f32 v178, s72, v178\n\t"
"v_mul_f32 v179, s72, v179\n\t"
"v_pk_fma_f32 v[160:161], v[140:141], s[72:73], v[178:179] op_sel:[0,0,1] op_sel_hi:[1,1,1] neg_lo:[1,0,0] neg_hi:[1,0,0]\n\t"
"v_pk_fma_f32 v[140:141], v[140:141], s[72:73], v[178:179] op_sel:[0,0,0] op_sel_hi:[1,1,0] neg_lo:[0,0,1] neg_hi:[0,0,1]\n\t"
"v_exp_f32 v160, v160\n\t"
"v_exp_f32 v161, v161\n\t"
"v_pk_fma_f32 v[162:163], v[142:143], s[72:73], v[178:179] op_sel:[0,0,1] op_sel_hi:[1,1,1] neg_lo:[1,0,0] neg_hi:[1,0,0]\n\t"
"v_exp_f32 v140, v140\n\t"
"v_exp_f32 v141, v141\n\t"
"v_pk_fma_f32 v[142:143], v[142:143], s[72:73], v[178:179] op_sel:[0,0,0] op_sel_hi:[1,1,0] neg_lo:[0,0,1] neg_hi:[0,0,1]\n\t"
"v_exp_f32 v162, v162\n\t"
"v_exp_f32 v163, v163\n\t"
"v_pk_fma_f32 v[164:165], v[144:145], s[72:73], v[178:179] op_sel:[0,0,1] op_sel_hi:[1,1,1] neg_lo:[1,0,0] neg_hi:[1,0,0]\n\t"
"v_exp_f32 v142, v142\n\t"
"v_exp_f32 v143, v143\n\t"
"v_pk_fma_f32 v[144:145], v[144:145], s[72:73], v[178:179] op_sel:[0,0,0] op_sel_hi:[1,1,0] neg_lo:[0,0,1] neg_hi:[0,0,1]\n\t"
"v_exp_f32 v164, v164\n\t"
"v_exp_f32 v165, v165\n\t"
"v_pk_add_f32 v[182:183], v[160:161], v[162:163]\n\t"
"v_pk_fma_f32 v[166:167], v[146:147], s[72:73], v[178:179] op_sel:[0,0,1] op_sel_hi:[1,1,1] neg_lo:[1,0,0] neg_hi:[1,0,0]\n\t"
"v_exp_f32 v144, v144\n\t"
"v_exp_f32 v145, v145\n\t"
"v_pk_add_f32 v[180:181], v[140:141], v[142:143]\n\t"
"v_pk_fma_f32 v[146:147], v[146:147], s[72:73], v[178:179] op_sel:[0,0,0] op_sel_hi:[1,1,0] neg_lo:[0,0,1] neg_hi:[0,0,1]\n\t"
"v_exp_f32 v166, v166\n\t"
"v_exp_f32 v167, v167\n\t"
"v_pk_add_f32 v[182:183], v[182:183], v[164:165]\n\t"
"v_pk_fma_f32 v[168:169], v[148:149], s[72:73], v[178:179] op_sel:[0,0,1] op_sel_hi:[1,1,1] neg_lo:[1,0,0] neg_hi:[1,0,0]\n\t"
"v_exp_f32 v146, v146\n\t"
"v_exp_f32 v147, v147\n\t"
"v_pk_add_f32 v[180:181], v[180:181], v[144:145]\n\t"
"v_pk_fma_f32 v[148:149], v[148:149], s[72:73], v[178:179] op_sel:[0,0,0] op_sel_hi:[1,1,0] neg_lo:[0,0,1] neg_hi:[0,0,1]\n\t"
"v_exp_f32 v168, v168\n\t"
"v_exp_f32 v169, v169\n\t"
"v_pk_add_f32 v[182:183], v[182:183], v[166:167]\n\t"
"v_pk_fma_f32 v[170:171], v[150:151], s[72:73], v[178:179] op_sel:[0,0,1] op_sel_hi:[1,1,1] neg_lo:[1,0,0] neg_hi:[1,0,0]\n\t"
"v_exp_f32 v148, v148\n\t"
"v_exp_f32 v149, v149\n\t"
"v_pk_add_f32 v[180:181], v[180:181], v[146:147]\n\t"
"v_pk_fma_f32 v[150:151], v[150:151], s[72:73], v[178:179] op_sel:[0,0,0] op_sel_hi:[1,1,0] neg_lo:[0,0,1] neg_hi:[0,0,1]\n\t"
"v_exp_f32 v170, v170\n\t"
"v_exp_f32 v171, v171\n\t"
"v_pk_add_f32 v[182:183], v[182:183], v[168:169]\n\t"
"v_pk_fma_f32 v[172:173], v[152:153], s[72:73], v[178:179] op_sel:[0,0,1] op_sel_hi:[1,1,1] neg_lo:[1,0,0] neg_hi:[1,0,0]\n\t"
"v_exp_f32 v150, v150\n\t"
"v_exp_f32 v151, v151\n\t"
"v_pk_add_f32 v[180:181], v[180:181], v[148:149]\n\t"
"v_pk_fma_f32 v[152:153], v[152:153], s[72:73], v[178:179] op_sel:[0,0,0] op_sel_hi:[1,1,0] neg_lo:[0,0,1] neg_hi:[0,0,1]\n\t"
"v_exp_f32 v172, v172\n\t"
"v_exp_f32 v173, v173\n\t"
"v_pk_add_f32 v[182:183], v[182:183], v[170:171]\n\t"
"v_pk_fma_f32 v[174:175], v[154:155], s[72:73], v[178:179] op_sel:[0,0,1] op_sel_hi:[1,1,1] neg_lo:[1,0,0] neg_hi:[1,0,0]\n\t"
"v_exp_f32 v152, v152\n\t"
"v_exp_f32 v153, v153\n\t"
"v_pk_add_f32 v[180:181], v[180:181], v[150:151]\n\t"
"v_pk_fma_f32 v[154:155], v[154:155], s[72:73], v[178:179] op_sel:[0,0,0] op_sel_hi:[1,1,0] neg_lo:[0,0,1] neg_hi:[0,0,1]\n\t"
"v_exp_f32 v174, v174\n\t"
"v_exp_f32 v175, v175\n\t"
"v_pk_add_f32 v[182:183], v[182:183], v[172:173]\n\t"
"v_pk_fma_f32 v[176:177], v[156:157], s[72:73], v[178:179] op_sel:[0,0,1] op_sel_hi:[1,1,1] neg_lo:[1,0,0] neg_hi:[1,0,0]\n\t"
"v_exp_f32 v154, v154\n\t"
"v_exp_f32 v155, v155\n\t"
"v_pk_add_f32 v[180:181], v[180:181], v[152:153]\n\t"
"v_pk_fma_f32 v[156:157], v[156:157], s[72:73], v[178:179] op_sel:[0,0,0] op_sel_hi:[1,1,0] neg_lo:[0,0,1] neg_hi:[0,0,1]\n\t"
"v_exp_f32 v176, v176\n\t"
"v_exp_f32 v177, v177\n\t"
"v_pk_add_f32 v[182:183], v[182:183], v[174:175]\n\t"
"v_exp_f32 v156, v156\n\t"
"v_exp_f32 v157, v157\n\t"
"v_pk_add_f32 v[180:181], v[180:181], v[154:155]\n\t"
"v_pk_add_f32 v[182:183], v[182:183], v[176:177]\n\t"
"v_pk_add_f32 v[180:181], v[180:181], v[156:157]\n\t"
"v_add_f32 v180, v180, v181\n\t"
"v_add_f32 v181, v182, v183\n\t"
"v_rcp_f32 v180, v180\n\t"
"v_rcp_f32 v181, v181\n\t"
"s_nop 0\n\t"
"v_pk_mul_f32 v[140:141], v[140:141], v[180:181] op_sel:[0,0] op_sel_hi:[1,0]\n\t"
"v_pk_mul_f32 v[142:143], v[142:143], v[180:181] op_sel:[0,0] op_sel_hi:[1,0]\n\t"
"v_pk_mul_f32 v[144:145], v[144:145], v[180:181] op_sel:[0,0] op_sel_hi:[1,0]\n\t"
"v_cvt_pk_bf16_f32 v184, v140, v141\n\t"
"v_pk_mul_f32 v[146:147], v[146:147], v[180:181] op_sel:[0,0] op_sel_hi:[1,0]\n\t"
"v_cvt_pk_bf16_f32 v185, v142, v143\n\t"
"v_pk_mul_f32 v[148:149], v[148:149], v[180:181] op_sel:[0,0] op_sel_hi:[1,0]\n\t"
"v_cvt_pk_bf16_f32 v186, v144, v145\n\t"
"ds_write_b64 v225, v[184:185] offset:4736\n\t"
"v_pk_mul_f32 v[150:151], v[150:151], v[180:181] op_sel:[0,0] op_sel_hi:[1,0]\n\t"
"v_cvt_pk_bf16_f32 v187, v146, v147\n\t"
"v_pk_mul_f32 v[152:153], v[152:153], v[180:181] op_sel:[0,0] op_sel_hi:[1,0]\n\t"
"v_cvt_pk_bf16_f32 v184, v148, v149\n\t"
"ds_write_b64 v225, v[186:187] offset:4744\n\t"
"v_pk_mul_f32 v[154:155], v[154:155], v[180:181] op_sel:[0,0] op_sel_hi:[1,0]\n\t"
"v_cvt_pk_bf16_f32 v185, v150, v151\n\t"
"v_pk_mul_f32 v[156:157], v[156:157], v[180:181] op_sel:[0,0] op_sel_hi:[1,0]\n\t"
"v_cvt_pk_bf16_f32 v186, v152, v153\n\t"
"ds_write_b64 v225, v[184:185] offset:4752\n\t"
"v_pk_mul_f32 v[160:161], v[160:161], v[180:181] op_sel:[0,1] op_sel_hi:[1,1]\n\t"
"v_cvt_pk_bf16_f32 v187, v154, v155\n\t"
"v_pk_mul_f32 v[162:163], v[162:163], v[180:181] op_sel:[0,1] op_sel_hi:[1,1]\n\t"
"v_cvt_pk_bf16_f32 v184, v156, v157\n\t"
"ds_write_b64 v225, v[186:187] offset:4760\n\t"
"v_pk_mul_f32 v[164:165], v[164:165], v[180:181] op_sel:[0,1] op_sel_hi:[1,1]\n\t"
"v_cvt_pk_bf16_f32 v185, v160, v161\n\t"
"v_pk_mul_f32 v[166:167], v[166:167], v[180:181] op_sel:[0,1] op_sel_hi:[1,1]\n\t"
"v_cvt_pk_bf16_f32 v186, v162, v163\n\t"
"ds_write_b64 v225, v[184:185] offset:4768\n\t"
"v_pk_mul_f32 v[168:169], v[168:169], v[180:181] op_sel:[0,1] op_sel_hi:[1,1]\n\t"
"v_cvt_pk_bf16_f32 v187, v164, v165\n\t"
"v_pk_mul_f32 v[170:171], v[170:171], v[180:181] op_sel:[0,1] op_sel_hi:[1,1]\n\t"
"v_cvt_pk_bf16_f32 v184, v166, v167\n\t"
"ds_write_b64 v225, v[186:187] offset:4776\n\t"
"v_pk_mul_f32 v[172:173], v[172:173], v[180:181] op_sel:[0,1] op_sel_hi:[1,1]\n\t"
"v_cvt_pk_bf16_f32 v185, v168, v169\n\t"
"v_pk_mul_f32 v[174:175], v[174:175], v[180:181] op_sel:[0,1] op_sel_hi:[1,1]\n\t"
"v_cvt_pk_bf16_f32 v186, v170, v171\n\t"
"ds_write_b64 v225, v[184:185] offset:4784\n\t"
"v_pk_mul_f32 v[176:177], v[176:177], v[180:181] op_sel:[0,1] op_sel_hi:[1,1]\n\t"
"v_cvt_pk_bf16_f32 v187, v172, v173\n\t"
"v_cvt_pk_bf16_f32 v184, v174, v175\n\t"
"ds_write_b64 v225, v[186:187] offset:4792\n\t"
"v_cvt_pk_bf16_f32 v185, v176, v177\n\t"
"ds_write_b64 v225, v[184:185] offset:4800\n\t"
"v_mfma_f32_16x16x32_bf16 v[120:123], a[188:191], v[8:11], 0\n\t"
"ds_read_b64_tr_b16 a[146:147], v224 offset:8\n\t"
"v_mfma_f32_16x16x32_bf16 v[140:143], a[188:191], v[28:31], 0\n\t"
"s_waitcnt lgkmcnt(15)\n\t"
"ds_read_b64_tr_b16 a[148:149], v219 offset:16\n\t"
"ds_read_b64_tr_b16 a[150:151], v220 offset:16\n\t"
"v_mfma_f32_16x16x32_bf16 v[120:123], a[192:195], v[12:15], v[120:123]\n\t"
"buffer_load_dwordx4 v205, %[wq], s68 offen offset:1024 lds\n\t"
"v_mfma_f32_16x16x32_bf16 v[140:143], a[192:195], v[32:35], v[140:143]\n\t"
"ds_read_b64_tr_b16 a[152:153], v221 offset:16\n\t"
"v_mfma_f32_16x16x32_bf16 v[120:123], a[196:199], v[16:19], v[120:123]\n\t"
"ds_read_b64_tr_b16 a[154:155], v222 offset:16\n\t"
"v_mfma_f32_16x16x32_bf16 v[140:143], a[196:199], v[36:39], v[140:143]\n\t"
"s_waitcnt lgkmcnt(15)\n\t"
"ds_read_b64_tr_b16 a[156:157], v223 offset:16\n\t"
"ds_read_b64_tr_b16 a[158:159], v224 offset:16\n\t"
"v_mfma_f32_16x16x32_bf16 v[124:127], a[200:203], v[8:11], 0\n\t"
"ds_read_b64_tr_b16 a[160:161], v219 offset:24\n\t"
"v_mfma_f32_16x16x32_bf16 v[144:147], a[200:203], v[28:31], 0\n\t"
"ds_read_b64_tr_b16 a[162:163], v220 offset:24\n\t"
"v_mfma_f32_16x16x32_bf16 v[124:127], a[204:207], v[12:15], v[124:127]\n\t"
"ds_read_b64_tr_b16 a[164:165], v221 offset:24\n\t"
"v_mfma_f32_16x16x32_bf16 v[144:147], a[204:207], v[32:35], v[144:147]\n\t"
"s_waitcnt lgkmcnt(7)\n\t"
"ds_read_b64_tr_b16 a[166:167], v222 offset:24\n\t"
"v_mfma_f32_16x16x32_bf16 v[124:127], a[144:147], v[16:19], v[124:127]\n\t"
"ds_read_b64_tr_b16 a[168:169], v223 offset:24\n\t"
"v_mfma_f32_16x16x32_bf16 v[144:147], a[144:147], v[36:39], v[144:147]\n\t"
"ds_read_b64_tr_b16 a[170:171], v224 offset:24\n\t"
"v_mfma_f32_16x16x32_bf16 v[128:131], a[148:151], v[8:11], 0\n\t"
"ds_read_b64_tr_b16 a[172:173], v219 offset:32\n\t"
"v_mfma_f32_16x16x32_bf16 v[148:151], a[148:151], v[28:31], 0\n\t"
"s_waitcnt lgkmcnt(7)\n\t"
"ds_read_b64_tr_b16 a[174:175], v220 offset:32\n\t"
"v_mfma_f32_16x16x32_bf16 v[128:131], a[152:155], v[12:15], v[128:131]\n\t"
"buffer_load_dwordx4 v206, %[wq], s68 offen offset:2048 lds\n\t"
"v_mfma_f32_16x16x32_bf16 v[148:151], a[152:155], v[32:35], v[148:151]\n\t"
"ds_read_b64_tr_b16 a[176:177], v221 offset:32\n\t"
"v_mfma_f32_16x16x32_bf16 v[128:131], a[156:159], v[16:19], v[128:131]\n\t"
"ds_read_b64_tr_b16 a[178:179], v222 offset:32\n\t"
"v_mfma_f32_16x16x32_bf16 v[148:151], a[156:159], v[36:39], v[148:151]\n\t"
"s_waitcnt lgkmcnt(6)\n\t"
"ds_read_b64_tr_b16 a[180:181], v223 offset:32\n\t"
"ds_read_b64_tr_b16 a[182:183], v224 offset:32\n\t"
"v_mfma_f32_16x16x32_bf16 v[132:135], a[160:163], v[8:11], 0\n\t"
"ds_read_b64_tr_b16 a[184:185], v209 offset:27648\n\t"
"v_mfma_f32_16x16x32_bf16 v[152:155], a[160:163], v[28:31], 0\n\t"
"ds_read_b64_tr_b16 a[186:187], v209 offset:32256\n\t"
"v_mfma_f32_16x16x32_bf16 v[132:135], a[164:167], v[12:15], v[132:135]\n\t"
"ds_read_b64_tr_b16 a[188:189], v209 offset:27688\n\t"
"v_mfma_f32_16x16x32_bf16 v[152:155], a[164:167], v[32:35], v[152:155]\n\t"
"s_waitcnt lgkmcnt(7)\n\t"
"ds_read_b64_tr_b16 a[190:191], v209 offset:32296\n\t"
"v_mfma_f32_16x16x32_bf16 v[132:135], a[168:171], v[16:19], v[132:135]\n\t"
"buffer_load_dwordx4 v207, %[wq], s68 offen offset:3072 lds\n\t"
"v_mfma_f32_16x16x32_bf16 v[152:155], a[168:171], v[36:39], v[152:155]\n\t"
"ds_read_b64_tr_b16 a[192:193], v209 offset:27720\n\t"
"v_mfma_f32_16x16x32_bf16 v[136:139], a[172:175], v[8:11], 0\n\t"
"ds_read_b64_tr_b16 a[194:195], v209 offset:32328\n\t"
"v_mfma_f32_16x16x32_bf16 v[156:159], a[172:175], v[28:31], 0\n\t"
"s_waitcnt lgkmcnt(6)\n\t"
"ds_read_b64_tr_b16 a[196:197], v209 offset:27760\n\t"
"ds_read_b64_tr_b16 a[198:199], v209 offset:32368\n\t"
"v_mfma_f32_16x16x32_bf16 v[136:139], a[176:179], v[12:15], v[136:139]\n\t"
"ds_read_b64_tr_b16 a[200:201], v211 offset:27648\n\t"
"v_mfma_f32_16x16x32_bf16 v[156:159], a[176:179], v[32:35], v[156:159]\n\t"
"ds_read_b64_tr_b16 a[202:203], v211 offset:32256\n\t"
"v_mfma_f32_16x16x32_bf16 v[136:139], a[180:183], v[16:19], v[136:139]\n\t"
"s_add_u32 m0, s63, 87040\n\t"
"ds_read_b64_tr_b16 a[204:205], v209 offset:27792\n\t"
"v_mfma_f32_16x16x32_bf16 v[156:159], a[180:183], v[36:39], v[156:159]\n\t"
"s_waitcnt lgkmcnt(7)\n\t"
"ds_read_b64_tr_b16 a[206:207], v209 offset:32400\n\t"
"v_mfma_f32_16x16x32_bf16 v[40:43], a[72:75], a[184:187], v[40:43]\n\t"
"buffer_load_dwordx4 v208, %[wq], s68 offen offset:0 lds\n\t"
"v_mfma_f32_16x16x32_bf16 v[44:47], a[76:79], a[184:187], v[44:47]\n\t"
"ds_read_b64_tr_b16 a[144:145], v209 offset:27832\n\t"
"v_mfma_f32_16x16x32_bf16 v[48:51], a[72:75], a[188:191], v[48:51]\n\t"
"s_waitcnt lgkmcnt(5)\n\t"
"ds_read_b64_tr_b16 a[146:147], v209 offset:32440\n\t"
"v_max3_f32 v178, v120, v121, v122\n\t"
"v_mfma_f32_16x16x32_bf16 v[52:55], a[76:79], a[188:191], v[52:55]\n\t"
"ds_read_b64_tr_b16 a[148:149], v209 offset:27864\n\t"
"ds_read_b64_tr_b16 a[150:151], v209 offset:32472\n\t"
"ds_read_b64_tr_b16 a[152:153], v209 offset:27904\n\t"
"ds_read_b64_tr_b16 a[154:155], v209 offset:32512\n\t"
"ds_read_b64_tr_b16 a[156:157], v211 offset:27792\n\t"
"ds_read_b64_tr_b16 a[158:159], v211 offset:32400\n\t"
"v_min3_f32 v179, v120, v121, v122\n\t"
"v_mfma_f32_16x16x32_bf16 v[56:59], a[72:75], a[192:195], v[56:59]\n\t"
"v_max3_f32 v180, v123, v124, v125\n\t"
"v_min3_f32 v181, v123, v124, v125\n\t"
"v_mfma_f32_16x16x32_bf16 v[60:63], a[76:79], a[192:195], v[60:63]\n\t"
"v_max3_f32 v178, v178, v126, v127\n\t"
"v_min3_f32 v179, v179, v126, v127\n\t"
"v_mfma_f32_16x16x32_bf16 v[64:67], a[72:75], a[196:199], v[64:67]\n\t"
"s_waitcnt vmcnt(10)\n\t"
"v_max3_f32 v180, v180, v128, v129\n\t"
"v_min3_f32 v181, v181, v128, v129\n\t"
"v_mfma_f32_16x16x32_bf16 v[68:71], a[76:79], a[196:199], v[68:71]\n\t"
"s_waitcnt lgkmcnt(8)\n\t"
"v_max3_f32 v178, v178, v130, v131\n\t"
"v_min3_f32 v179, v179, v130, v131\n\t"
"v_mfma_f32_16x16x32_bf16 v[72:75], a[72:75], a[200:203], v[72:75]\n\t"
"s_waitcnt lgkmcnt(0)\n\t"
"v_max3_f32 v180, v180, v132, v133\n\t"
"v_min3_f32 v181, v181, v132, v133\n\t"
"v_mfma_f32_16x16x32_bf16 v[76:79], a[76:79], a[200:203], v[76:79]\n\t"
"s_barrier\n\t"
"v_max3_f32 v178, v178, v134, v135\n\t"
"v_min3_f32 v179, v179, v134, v135\n\t"
"v_mfma_f32_16x16x32_bf16 v[80:83], a[72:75], a[204:207], v[80:83]\n\t"
"ds_read_b64_tr_b16 a[160:161], v209 offset:37888\n\t"
"v_max3_f32 v180, v180, v136, v137\n\t"
"v_mfma_f32_16x16x32_bf16 v[84:87], a[76:79], a[204:207], v[84:87]\n\t"
"ds_read_b64_tr_b16 a[162:163], v209 offset:42496\n\t"
"ds_read_b64_tr_b16 a[164:165], v209 offset:37928\n\t"
"ds_read_b64_tr_b16 a[166:167], v209 offset:42536\n\t"
"v_min3_f32 v181, v181, v136, v137\n\t"
"v_mfma_f32_16x16x32_bf16 v[88:91], a[72:75], a[144:147], v[88:91]\n\t"
"ds_read_b64_tr_b16 a[168:169], v209 offset:37960\n\t"
"v_max_f32 v178, v178, v180\n\t"
"v_mfma_f32_16x16x32_bf16 v[92:95], a[76:79], a[144:147], v[92:95]\n\t"
"ds_read_b64_tr_b16 a[170:171], v209 offset:42568\n\t"
"v_min_f32 v179, v179, v181\n\t"
"v_mfma_f32_16x16x32_bf16 v[96:99], a[72:75], a[148:151], v[96:99]\n\t"
"ds_read_b64_tr_b16 a[172:173], v209 offset:38000\n\t"
"v_mul_f32 v178, s72, v178\n\t"
"v_mfma_f32_16x16x32_bf16 v[100:103], a[76:79], a[148:151], v[100:103]\n\t"
"ds_read_b64_tr_b16 a[174:175], v209 offset:42608\n\t"
"v_mul_f32 v179, s72, v179\n\t"
"v_mfma_f32_16x16x32_bf16 v[104:107], a[72:75], a[152:155], v[104:107]\n\t"
"ds_read_b64_tr_b16 a[176:177], v211 offset:37888\n\t"
"v_fma_f32 v160, -v120, s72, v179\n\t"
"v_mfma_f32_16x16x32_bf16 v[108:111], a[76:79], a[152:155], v[108:111]\n\t"
"buffer_load_dwordx2 v[242:243], v199, %[ws], s48 offen offset:128 sc1\n\t"
"v_mfma_f32_16x16x32_bf16 v[112:115], a[72:75], a[156:159], v[112:115]\n\t"
"s_waitcnt lgkmcnt(5)\n\t"
"ds_read_b64_tr_b16 a[178:179], v211 offset:42496\n\t"
"v_fma_f32 v120, v120, s72, -v178\n\t"
"v_mfma_f32_16x16x32_bf16 v[116:119], a[76:79], a[156:159], v[116:119]\n\t"
"ds_read_b64_tr_b16 a[180:181], v209 offset:38032\n\t"
"ds_read_b64_tr_b16 a[182:183], v209 offset:42640\n\t"
"s_getpc_b64 s[96:97]\n\t"
"71:\n\t"
"s_and_b32 s98, %[mi], 3\n\t"
"s_lshl_b32 s98, s98, 2\n\t"
"s_add_u32 s98, s98, %[wv]\n\t"
"s_lshl_b32 s98, s98, 10\n\t"
"v_add_u32 v198, s98, v230\n\t"
"v_min_u32 v198, 97f-71b, v198\n\t"
"global_load_dword v198, v198, s[96:97]\n\t"
"v_mfma_f32_16x16x32_bf16 v[40:43], a[80:83], a[160:163], v[40:43]\n\t"
"ds_read_b64_tr_b16 a[184:185], v209 offset:38072\n\t"
"v_fma_f32 v161, -v121, s72, v179\n\t"
"v_mfma_f32_16x16x32_bf16 v[44:47], a[84:87], a[160:163], v[44:47]\n\t"
"v_exp_f32 v160, v160\n\t"
"v_mfma_f32_16x16x32_bf16 v[48:51], a[80:83], a[164:167], v[48:51]\n\t"
"s_waitcnt lgkmcnt(5)\n\t"
"ds_read_b64_tr_b16 a[186:187], v209 offset:42680\n\t"
"v_fma_f32 v121, v121, s72, -v178\n\t"
"v_mfma_f32_16x16x32_bf16 v[52:55], a[84:87], a[164:167], v[52:55]\n\t"
"ds_read_b64_tr_b16 a[188:189], v209 offset:38104\n\t"
"ds_read_b64_tr_b16 a[190:191], v209 offset:42712\n\t"
"v_exp_f32 v120, v120\n\t"
"v_mfma_f32_16x16x32_bf16 v[56:59], a[80:83], a[168:171], v[56:59]\n\t"
"ds_read_b64_tr_b16 a[192:193], v209 offset:38144\n\t"
"v_fma_f32 v162, -v122, s72, v179\n\t"
"v_mfma_f32_16x16x32_bf16 v[60:63], a[84:87], a[168:171], v[60:63]\n\t"
"v_exp_f32 v161, v161\n\t"
"v_mfma_f32_16x16x32_bf16 v[64:67], a[80:83], a[172:175], v[64:67]\n\t"
"s_waitcnt lgkmcnt(5)\n\t"
"ds_read_b64_tr_b16 a[194:195], v209 offset:42752\n\t"
"v_fma_f32 v122, v122, s72, -v178\n\t"
"v_mfma_f32_16x16x32_bf16 v[68:71], a[84:87], a[172:175], v[68:71]\n\t"
"ds_read_b64_tr_b16 a[196:197], v211 offset:38032\n\t"
"ds_read_b64_tr_b16 a[198:199], v211 offset:42640\n\t"
"v_exp_f32 v121, v121\n\t"
"v_mfma_f32_16x16x32_bf16 v[72:75], a[80:83], a[176:179], v[72:75]\n\t"
"ds_read_b64_tr_b16 a[200:201], v209 offset:47104\n\t"
"v_fma_f32 v163, -v123, s72, v179\n\t"
"v_mfma_f32_16x16x32_bf16 v[76:79], a[84:87], a[176:179], v[76:79]\n\t"
"v_exp_f32 v162, v162\n\t"
"v_mfma_f32_16x16x32_bf16 v[80:83], a[80:83], a[180:183], v[80:83]\n\t"
"s_waitcnt lgkmcnt(5)\n\t"
"ds_read_b64_tr_b16 a[202:203], v209 offset:51712\n\t"
"v_fma_f32 v123, v123, s72, -v178\n\t"
"v_mfma_f32_16x16x32_bf16 v[84:87], a[84:87], a[180:183], v[84:87]\n\t"
"ds_read_b64_tr_b16 a[204:205], v209 offset:47144\n\t"
"ds_read_b64_tr_b16 a[206:207], v209 offset:51752\n\t"
"v_exp_f32 v122, v122\n\t"
"v_mfma_f32_16x16x32_bf16 v[88:91], a[80:83], a[184:187], v[88:91]\n\t"
"ds_read_b64_tr_b16 a[144:145], v209 offset:47176\n\t"
"v_fma_f32 v164, -v124, s72, v179\n\t"
"v_mfma_f32_16x16x32_bf16 v[92:95], a[84:87], a[184:187], v[92:95]\n\t"
"v_exp_f32 v163, v163\n\t"
"v_mfma_f32_16x16x32_bf16 v[96:99], a[80:83], a[188:191], v[96:99]\n\t"
"s_waitcnt lgkmcnt(5)\n\t"
"ds_read_b64_tr_b16 a[146:147], v209 offset:51784\n\t"
"v_add_f32 v182, v160, v162\n\t"
"v_mfma_f32_16x16x32_bf16 v[100:103], a[84:87], a[188:191], v[100:103]\n\t"
"ds_read_b64_tr_b16 a[148:149], v209 offset:47216\n\t"
"ds_read_b64_tr_b16 a[150:151], v209 offset:51824\n\t"
"v_fma_f32 v124, v124, s72, -v178\n\t"
"v_mfma_f32_16x16x32_bf16 v[104:107], a[80:83], a[192:195], v[104:107]\n\t"
"v_exp_f32 v123, v123\n\t"
"v_mfma_f32_16x16x32_bf16 v[108:111], a[84:87], a[192:195], v[108:111]\n\t"
"ds_read_b64_tr_b16 a[152:153], v211 offset:47104\n\t"
"v_add_f32 v180, v120, v122\n\t"
"v_mfma_f32_16x16x32_bf16 v[112:115], a[80:83], a[196:199], v[112:115]\n\t"
"s_waitcnt lgkmcnt(5)\n\t"
"ds_read_b64_tr_b16 a[154:155], v211 offset:51712\n\t"
"v_fma_f32 v165, -v125, s72, v179\n\t"
"v_mfma_f32_16x16x32_bf16 v[116:119], a[84:87], a[196:199], v[116:119]\n\t"
"ds_read_b64_tr_b16 a[156:157], v209 offset:47248\n\t"
"ds_read_b64_tr_b16 a[158:159], v209 offset:51856\n\t"
"v_exp_f32 v164, v164\n\t"
"v_mfma_f32_16x16x32_bf16 v[40:43], a[88:91], a[200:203], v[40:43]\n\t"
"ds_read_b64_tr_b16 a[160:161], v209 offset:47288\n\t"
"v_add_f32 v183, v161, v163\n\t"
"v_mfma_f32_16x16x32_bf16 v[44:47], a[92:95], a[200:203], v[44:47]\n\t"
"ds_read_b64_tr_b16 a[162:163], v209 offset:51896\n\t"
"v_fma_f32 v125, v125, s72, -v178\n\t"
"v_mfma_f32_16x16x32_bf16 v[48:51], a[88:91], a[204:207], v[48:51]\n\t"
"s_waitcnt lgkmcnt(6)\n\t"
"v_exp_f32 v124, v124\n\t"
"v_mfma_f32_16x16x32_bf16 v[52:55], a[92:95], a[204:207], v[52:55]\n\t"
"ds_read_b64_tr_b16 a[164:165], v209 offset:47320\n\t"
"ds_read_b64_tr_b16 a[166:167], v209 offset:51928\n\t"
"v_add_f32 v181, v121, v123\n\t"
"v_mfma_f32_16x16x32_bf16 v[56:59], a[88:91], a[144:147], v[56:59]\n\t"
"ds_read_b64_tr_b16 a[168:169], v209 offset:47360\n\t"
"v_fma_f32 v166, -v126, s72, v179\n\t"
"v_mfma_f32_16x16x32_bf16 v[60:63], a[92:95], a[144:147], v[60:63]\n\t"
"v_exp_f32 v165, v165\n\t"
"v_mfma_f32_16x16x32_bf16 v[64:67], a[88:91], a[148:151], v[64:67]\n\t"
"s_waitcnt lgkmcnt(5)\n\t"
"ds_read_b64_tr_b16 a[170:171], v209 offset:51968\n\t"
"v_add_f32 v182, v182, v164\n\t"
"v_mfma_f32_16x16x32_bf16 v[68:71], a[92:95], a[148:151], v[68:71]\n\t"
"ds_read_b64_tr_b16 a[172:173], v211 offset:47248\n\t"
"ds_read_b64_tr_b16 a[174:175], v211 offset:51856\n\t"
"v_fma_f32 v126, v126, s72, -v178\n\t"
"v_mfma_f32_16x16x32_bf16 v[72:75], a[88:91], a[152:155], v[72:75]\n\t"
"v_exp_f32 v125, v125\n\t"
"v_mfma_f32_16x16x32_bf16 v[76:79], a[92:95], a[152:155], v[76:79]\n\t"
"ds_read_b64_tr_b16 a[176:177], v210 offset:3072\n\t"
"v_add_f32 v180, v180, v124\n\t"
"v_mfma_f32_16x16x32_bf16 v[80:83], a[88:91], a[156:159], v[80:83]\n\t"
"s_waitcnt lgkmcnt(5)\n\t"
"ds_read_b64_tr_b16 a[178:179], v210 offset:7680\n\t"
"v_fma_f32 v167, -v127, s72, v179\n\t"
"v_mfma_f32_16x16x32_bf16 v[84:87], a[92:95], a[156:159], v[84:87]\n\t"
"ds_read_b64_tr_b16 a[180:181], v210 offset:3112\n\t"
"ds_read_b64_tr_b16 a[182:183], v210 offset:7720\n\t"
"v_exp_f32 v166, v166\n\t"
"v_mfma_f32_16x16x32_bf16 v[88:91], a[88:91], a[160:163], v[88:91]\n\t"
"ds_read_b64_tr_b16 a[184:185], v210 offset:3144\n\t"
"v_add_f32 v183, v183, v165\n\t"
"v_mfma_f32_16x16x32_bf16 v[92:95], a[92:95], a[160:163], v[92:95]\n\t"
"ds_read_b64_tr_b16 a[186:187], v210 offset:7752\n\t"
"v_fma_f32 v127, v127, s72, -v178\n\t"
"v_mfma_f32_16x16x32_bf16 v[96:99], a[88:91], a[164:167], v[96:99]\n\t"
"s_waitcnt lgkmcnt(6)\n\t"
"v_exp_f32 v126, v126\n\t"
"v_mfma_f32_16x16x32_bf16 v[100:103], a[92:95], a[164:167], v[100:103]\n\t"
"ds_read_b64_tr_b16 a[188:189], v210 offset:3184\n\t"
"ds_read_b64_tr_b16 a[190:191], v210 offset:7792\n\t"
"v_add_f32 v181, v181, v125\n\t"
"v_mfma_f32_16x16x32_bf16 v[104:107], a[88:91], a[168:171], v[104:107]\n\t"
"ds_read_b64_tr_b16 a[192:193], v212 offset:3072\n\t"
"v_fma_f32 v168, -v128, s72, v179\n\t"
"v_mfma_f32_16x16x32_bf16 v[108:111], a[92:95], a[168:171], v[108:111]\n\t"
"v_exp_f32 v167, v167\n\t"
"v_mfma_f32_16x16x32_bf16 v[112:115], a[88:91], a[172:175], v[112:115]\n\t"
"s_waitcnt lgkmcnt(5)\n\t"
"ds_read_b64_tr_b16 a[194:195], v212 offset:7680\n\t"
"v_add_f32 v182, v182, v166\n\t"
"v_mfma_f32_16x16x32_bf16 v[116:119], a[92:95], a[172:175], v[116:119]\n\t"
"ds_read_b64_tr_b16 a[196:197], v210 offset:3216\n\t"
"ds_read_b64_tr_b16 a[198:199], v210 offset:7824\n\t"
"v_fma_f32 v128, v128, s72, -v178\n\t"
"v_mfma_f32_16x16x32_bf16 v[40:43], a[96:99], a[176:179], v[40:43]\n\t"
"v_exp_f32 v127, v127\n\t"
"v_mfma_f32_16x16x32_bf16 v[44:47], a[100:103], a[176:179], v[44:47]\n\t"
"ds_read_b64_tr_b16 a[200:201], v210 offset:3256\n\t"
"v_add_f32 v180, v180, v126\n\t"
"v_mfma_f32_16x16x32_bf16 v[48:51], a[96:99], a[180:183], v[48:51]\n\t"
"s_waitcnt lgkmcnt(5)\n\t"
"ds_read_b64_tr_b16 a[202:203], v210 offset:7864\n\t"
"v_fma_f32 v169, -v129, s72, v179\n\t"
"v_mfma_f32_16x16x32_bf16 v[52:55], a[100:103], a[180:183], v[52:55]\n\t"
"ds_read_b64_tr_b16 a[204:205], v210 offset:3288\n\t"
"ds_read_b64_tr_b16 a[206:207], v210 offset:7896\n\t"
"v_exp_f32 v168, v168\n\t"
"v_mfma_f32_16x16x32_bf16 v[56:59], a[96:99], a[184:187], v[56:59]\n\t"
"ds_read_b64_tr_b16 a[144:145], v210 offset:3328\n\t"
"v_add_f32 v183, v183, v167\n\t"
"v_mfma_f32_16x16x32_bf16 v[60:63], a[100:103], a[184:187], v[60:63]\n\t"
"ds_read_b64_tr_b16 a[146:147], v210 offset:7936\n\t"
"v_fma_f32 v129, v129, s72, -v178\n\t"
"v_mfma_f32_16x16x32_bf16 v[64:67], a[96:99], a[188:191], v[64:67]\n\t"
"s_waitcnt lgkmcnt(6)\n\t"
"v_exp_f32 v128, v128\n\t"
"v_mfma_f32_16x16x32_bf16 v[68:71], a[100:103], a[188:191], v[68:71]\n\t"
"ds_read_b64_tr_b16 a[148:149], v212 offset:3216\n\t"
"ds_read_b64_tr_b16 a[150:151], v212 offset:7824\n\t"
"v_add_f32 v181, v181, v127\n\t"
"v_mfma_f32_16x16x32_bf16 v[72:75], a[96:99], a[192:195], v[72:75]\n\t"
"ds_read_b64_tr_b16 a[152:153], v210 offset:12288\n\t"
"v_fma_f32 v170, -v130, s72, v179\n\t"
"v_mfma_f32_16x16x32_bf16 v[76:79], a[100:103], a[192:195], v[76:79]\n\t"
"v_exp_f32 v169, v169\n\t"
"v_mfma_f32_16x16x32_bf16 v[80:83], a[96:99], a[196:199], v[80:83]\n\t"
"s_waitcnt lgkmcnt(5)\n\t"
"ds_read_b64_tr_b16 a[154:155], v210 offset:16896\n\t"
"v_add_f32 v182, v182, v168\n\t"
"v_mfma_f32_16x16x32_bf16 v[84:87], a[100:103], a[196:199], v[84:87]\n\t"
"ds_read_b64_tr_b16 a[156:157], v210 offset:12328\n\t"
"ds_read_b64_tr_b16 a[158:159], v210 offset:16936\n\t"
"v_fma_f32 v130, v130, s72, -v178\n\t"
"v_mfma_f32_16x16x32_bf16 v[88:91], a[96:99], a[200:203], v[88:91]\n\t"
"v_exp_f32 v129, v129\n\t"
"v_mfma_f32_16x16x32_bf16 v[92:95], a[100:103], a[200:203], v[92:95]\n\t"
"ds_read_b64_tr_b16 a[160:161], v210 offset:12360\n\t"
"v_add_f32 v180, v180, v128\n\t"
"v_mfma_f32_16x16x32_bf16 v[96:99], a[96:99], a[204:207], v[96:99]\n\t"
"s_waitcnt lgkmcnt(5)\n\t"
"ds_read_b64_tr_b16 a[162:163], v210 offset:16968\n\t"
"v_fma_f32 v171, -v131, s72, v179\n\t"
"v_mfma_f32_16x16x32_bf16 v[100:103], a[100:103], a[204:207], v[100:103]\n\t"
"ds_read_b64_tr_b16 a[164:165], v210 offset:12400\n\t"
"ds_read_b64_tr_b16 a[166:167], v210 offset:17008\n\t"
"v_exp_f32 v170, v170\n\t"
"v_mfma_f32_16x16x32_bf16 v[104:107], a[96:99], a[144:147], v[104:107]\n\t"
"ds_read_b64_tr_b16 a[168:169], v212 offset:12288\n\t"
"v_add_f32 v183, v183, v169\n\t"
"v_mfma_f32_16x16x32_bf16 v[108:111], a[100:103], a[144:147], v[108:111]\n\t"
"ds_read_b64_tr_b16 a[170:171], v212 offset:16896\n\t"
"v_fma_f32 v131, v131, s72, -v178\n\t"
"v_mfma_f32_16x16x32_bf16 v[112:115], a[96:99], a[148:151], v[112:115]\n\t"
"s_waitcnt lgkmcnt(6)\n\t"
"v_exp_f32 v130, v130\n\t"
"v_mfma_f32_16x16x32_bf16 v[116:119], a[100:103], a[148:151], v[116:119]\n\t"
"ds_read_b64_tr_b16 a[172:173], v210 offset:12432\n\t"
"ds_read_b64_tr_b16 a[174:175], v210 offset:17040\n\t"
"v_add_f32 v181, v181, v129\n\t"
"v_mfma_f32_16x16x32_bf16 v[40:43], a[104:107], a[152:155], v[40:43]\n\t"
"ds_read_b64_tr_b16 a[176:177], v210 offset:12472\n\t"
"v_fma_f32 v172, -v132, s72, v179\n\t"
"v_mfma_f32_16x16x32_bf16 v[44:47], a[108:111], a[152:155], v[44:47]\n\t"
"v_exp_f32 v171, v171\n\t"
"v_mfma_f32_16x16x32_bf16 v[48:51], a[104:107], a[156:159], v[48:51]\n\t"
"s_waitcnt lgkmcnt(5)\n\t"
"ds_read_b64_tr_b16 a[178:179], v210 offset:17080\n\t"
"v_add_f32 v182, v182, v170\n\t"
"v_mfma_f32_16x16x32_bf16 v[52:55], a[108:111], a[156:159], v[52:55]\n\t"
"ds_read_b64_tr_b16 a[180:181], v210 offset:12504\n\t"
"ds_read_b64_tr_b16 a[182:183], v210 offset:17112\n\t"
"ds_read_b64_tr_b16 a[184:185], v210 offset:12544\n\t"
"ds_read_b64_tr_b16 a[186:187], v210 offset:17152\n\t"
"ds_read_b64_tr_b16 a[188:189], v212 offset:12432\n\t"
"ds_read_b64_tr_b16 a[190:191], v212 offset:17040\n\t"
"v_fma_f32 v132, v132, s72, -v178\n\t"
"v_mfma_f32_16x16x32_bf16 v[56:59], a[104:107], a[160:163], v[56:59]\n\t"
"v_exp_f32 v131, v131\n\t"
"v_mfma_f32_16x16x32_bf16 v[60:63], a[108:111], a[160:163], v[60:63]\n\t"
"v_add_f32 v180, v180, v130\n\t"
"v_fma_f32 v173, -v133, s72, v179\n\t"
"v_mfma_f32_16x16x32_bf16 v[64:67], a[104:107], a[164:167], v[64:67]\n\t"
"s_waitcnt vmcnt(2)\n\t"
"v_exp_f32 v172, v172\n\t"
"v_mfma_f32_16x16x32_bf16 v[68:71], a[108:111], a[164:167], v[68:71]\n\t"
"s_waitcnt lgkmcnt(8)\n\t"
"v_add_f32 v183, v183, v171\n\t"
"v_fma_f32 v133, v133, s72, -v178\n\t"
"v_mfma_f32_16x16x32_bf16 v[72:75], a[104:107], a[168:171], v[72:75]\n\t"
"s_waitcnt lgkmcnt(0)\n\t"
"v_exp_f32 v132, v132\n\t"
"v_mfma_f32_16x16x32_bf16 v[76:79], a[108:111], a[168:171], v[76:79]\n\t"
"s_barrier\n\t"
"v_add_f32 v181, v181, v131\n\t"
"v_fma_f32 v174, -v134, s72, v179\n\t"
"v_mfma_f32_16x16x32_bf16 v[80:83], a[104:107], a[172:175], v[80:83]\n\t"
"v_exp_f32 v173, v173\n\t"
"v_mfma_f32_16x16x32_bf16 v[84:87], a[108:111], a[172:175], v[84:87]\n\t"
"ds_read_b64_tr_b16 a[192:193], v210 offset:25600\n\t"
"ds_read_b64_tr_b16 a[194:195], v210 offset:30208\n\t"
"ds_read_b64_tr_b16 a[196:197], v210 offset:25640\n\t"
"ds_read_b64_tr_b16 a[198:199], v210 offset:30248\n\t"
"v_add_f32 v182, v182, v172\n\t"
"v_mfma_f32_16x16x32_bf16 v[88:91], a[104:107], a[176:179], v[88:91]\n\t"
"ds_read_b64_tr_b16 a[200:201], v210 offset:25672\n\t"
"v_fma_f32 v134, v134, s72, -v178\n\t"
"v_mfma_f32_16x16x32_bf16 v[92:95], a[108:111], a[176:179], v[92:95]\n\t"
"v_exp_f32 v133, v133\n\t"
"v_mfma_f32_16x16x32_bf16 v[96:99], a[104:107], a[180:183], v[96:99]\n\t"
"ds_read_b64_tr_b16 a[202:203], v210 offset:30280\n\t"
"v_add_f32 v180, v180, v132\n\t"
"v_mfma_f32_16x16x32_bf16 v[100:103], a[108:111], a[180:183], v[100:103]\n\t"
"ds_read_b64_tr_b16 a[204:205], v210 offset:25712\n\t"
"ds_read_b64_tr_b16 a[206:207], v210 offset:30320\n\t"
"v_fma_f32 v175, -v135, s72, v179\n\t"
"v_mfma_f32_16x16x32_bf16 v[104:107], a[104:107], a[184:187], v[104:107]\n\t"
"v_exp_f32 v174, v174\n\t"
"v_mfma_f32_16x16x32_bf16 v[108:111], a[108:111], a[184:187], v[108:111]\n\t"
"s_waitcnt vmcnt(1)\n\t"
"v_cmp_eq_u64 vcc, v[242:243], v[254:255]\n\t"
"s_cmp_eq_u64 vcc, -1\n\t"
"s_cbranch_scc1 103f\n\t"
"s_mov_b32 s98, 65536\n\t"
"102:\n\t"
"s_sleep 3\n\t"
"buffer_load_dwordx2 v[242:243], v199, %[ws], s48 offen offset:128 sc1\n\t"
"s_waitcnt vmcnt(0)\n\t"
"v_cmp_eq_u64 vcc, v[242:243], v[254:255]\n\t"
"s_cmp_eq_u64 vcc, -1\n\t"
"s_cbranch_scc1 103f\n\t"
"s_sub_u32 s98, s98, 1\n\t"
"s_cmp_lg_u32 s98, 0\n\t"
"s_cbranch_scc1 102b\n\t"
"103:\n\t"
"v_mfma_f32_16x16x32_bf16 v[112:115], a[104:107], a[188:191], v[112:115]\n\t"
"s_waitcnt lgkmcnt(4)\n\t"
"ds_read_b64_tr_b16 a[144:145], v212 offset:25600\n\t"
"v_add_f32 v183, v183, v173\n\t"
"v_mfma_f32_16x16x32_bf16 v[116:119], a[108:111], a[188:191], v[116:119]\n\t"
"ds_read_b64_tr_b16 a[146:147], v212 offset:30208\n\t"
"ds_read_b64_tr_b16 a[148:149], v210 offset:25744\n\t"
"ds_read_b64_tr_b16 a[150:151], v210 offset:30352\n\t"
"v_fma_f32 v135, v135, s72, -v178\n\t"
"v_mfma_f32_16x16x32_bf16 v[40:43], a[112:115], a[192:195], v[40:43]\n\t"
"v_exp_f32 v134, v134\n\t"
"v_mfma_f32_16x16x32_bf16 v[44:47], a[116:119], a[192:195], v[44:47]\n\t"
"ds_read_b64_tr_b16 a[152:153], v210 offset:25784\n\t"
"v_add_f32 v181, v181, v133\n\t"
"v_mfma_f32_16x16x32_bf16 v[48:51], a[112:115], a[196:199], v[48:51]\n\t"
"s_waitcnt lgkmcnt(5)\n\t"
"ds_read_b64_tr_b16 a[154:155], v210 offset:30392\n\t"
"v_fma_f32 v176, -v136, s72, v179\n\t"
"v_mfma_f32_16x16x32_bf16 v[52:55], a[116:119], a[196:199], v[52:55]\n\t"
"ds_read_b64_tr_b16 a[156:157], v210 offset:25816\n\t"
"ds_read_b64_tr_b16 a[158:159], v210 offset:30424\n\t"
"v_exp_f32 v175, v175\n\t"
"v_mfma_f32_16x16x32_bf16 v[56:59], a[112:115], a[200:203], v[56:59]\n\t"
"ds_read_b64_tr_b16 a[160:161], v210 offset:25856\n\t"
"v_add_f32 v182, v182, v174\n\t"
"v_mfma_f32_16x16x32_bf16 v[60:63], a[116:119], a[200:203], v[60:63]\n\t"
"buffer_load_dwordx4 v[0:3], v230, %[ws], s47 offen offset:0 sc1\n\t"
"v_mfma_f32_16x16x32_bf16 v[64:67], a[112:115], a[204:207], v[64:67]\n\t"
"s_waitcnt lgkmcnt(5)\n\t"
"ds_read_b64_tr_b16 a[162:163], v210 offset:30464\n\t"
"v_fma_f32 v136, v136, s72, -v178\n\t"
"v_mfma_f32_16x16x32_bf16 v[68:71], a[116:119], a[204:207], v[68:71]\n\t"
"ds_read_b64_tr_b16 a[164:165], v212 offset:25744\n\t"
"ds_read_b64_tr_b16 a[166:167], v212 offset:30352\n\t"
"v_exp_f32 v135, v135\n\t"
"v_mfma_f32_16x16x32_bf16 v[72:75], a[112:115], a[144:147], v[72:75]\n\t"
"ds_read_b64_tr_b16 a[168:169], v210 offset:34816\n\t"
"v_add_f32 v180, v180, v134\n\t"
"v_mfma_f32_16x16x32_bf16 v[76:79], a[116:119], a[144:147], v[76:79]\n\t"
"buffer_load_dwordx4 v[4:7], v230, %[ws], s47 offen offset:1024 sc1\n\t"
"v_mfma_f32_16x16x32_bf16 v[80:83], a[112:115], a[148:151], v[80:83]\n\t"
"s_waitcnt lgkmcnt(5)\n\t"
"ds_read_b64_tr_b16 a[170:171], v210 offset:39424\n\t"
"v_fma_f32 v177, -v137, s72, v179\n\t"
"v_mfma_f32_16x16x32_bf16 v[84:87], a[116:119], a[148:151], v[84:87]\n\t"
"ds_read_b64_tr_b16 a[172:173], v210 offset:34856\n\t"
"ds_read_b64_tr_b16 a[174:175], v210 offset:39464\n\t"
"v_exp_f32 v176, v176\n\t"
"v_mfma_f32_16x16x32_bf16 v[88:91], a[112:115], a[152:155], v[88:91]\n\t"
"ds_read_b64_tr_b16 a[176:177], v210 offset:34888\n\t"
"v_add_f32 v183, v183, v175\n\t"
"v_mfma_f32_16x16x32_bf16 v[92:95], a[116:119], a[152:155], v[92:95]\n\t"
"buffer_load_dwordx4 v[8:11], v230, %[ws], s47 offen offset:2048 sc1\n\t"
"v_mfma_f32_16x16x32_bf16 v[96:99], a[112:115], a[156:159], v[96:99]\n\t"
"s_waitcnt lgkmcnt(5)\n\t"
"ds_read_b64_tr_b16 a[178:179], v210 offset:39496\n\t"
"v_fma_f32 v137, v137, s72, -v178\n\t"
"v_mfma_f32_16x16x32_bf16 v[100:103], a[116:119], a[156:159], v[100:103]\n\t"
"ds_read_b64_tr_b16 a[180:181], v210 offset:34928\n\t"
"ds_read_b64_tr_b16 a[182:183], v210 offset:39536\n\t"
"buffer_load_dwordx4 v[12:15], v230, %[ws], s47 offen offset:3072 sc1\n\t"
"v_mfma_f32_16x16x32_bf16 v[104:107], a[112:115], a[160:163], v[104:107]\n\t"
"s_add_u32 s64, s47, 4096\n\t"
"v_exp_f32 v136, v136\n\t"
"v_mfma_f32_16x16x32_bf16 v[108:111], a[116:119], a[160:163], v[108:111]\n\t"
"ds_read_b64_tr_b16 a[184:185], v212 offset:34816\n\t"
"v_add_f32 v181, v181, v135\n\t"
"v_mfma_f32_16x16x32_bf16 v[112:115], a[112:115], a[164:167], v[112:115]\n\t"
"s_waitcnt lgkmcnt(5)\n\t"
"v_exp_f32 v177, v177\n\t"
"v_mfma_f32_16x16x32_bf16 v[116:119], a[116:119], a[164:167], v[116:119]\n\t"
"ds_read_b64_tr_b16 a[186:187], v212 offset:39424\n\t"
"ds_read_b64_tr_b16 a[188:189], v210 offset:34960\n\t"
"ds_read_b64_tr_b16 a[190:191], v210 offset:39568\n\t"
"v_add_f32 v182, v182, v176\n\t"
"v_mfma_f32_16x16x32_bf16 v[40:43], a[120:123], a[168:171], v[40:43]\n\t"
"v_exp_f32 v137, v137\n\t"
"v_mfma_f32_16x16x32_bf16 v[44:47], a[124:127], a[168:171], v[44:47]\n\t"
"ds_read_b64_tr_b16 a[192:193], v210 offset:35000\n\t"
"v_add_f32 v180, v180, v136\n\t"
"v_mfma_f32_16x16x32_bf16 v[48:51], a[120:123], a[172:175], v[48:51]\n\t"
"s_waitcnt lgkmcnt(5)\n\t"
"ds_read_b64_tr_b16 a[194:195], v210 offset:39608\n\t"
"v_add_f32 v183, v183, v177\n\t"
"v_mfma_f32_16x16x32_bf16 v[52:55], a[124:127], a[172:175], v[52:55]\n\t"
"ds_read_b64_tr_b16 a[196:197], v210 offset:35032\n\t"
"ds_read_b64_tr_b16 a[198:199], v210 offset:39640\n\t"
"v_add_f32 v181, v181, v137\n\t"
"v_mfma_f32_16x16x32_bf16 v[56:59], a[120:123], a[176:179], v[56:59]\n\t"
"buffer_load_dwordx4 v[16:19], v230, %[ws], s64 offen offset:0 sc1\n\t"
"v_mfma_f32_16x16x32_bf16 v[60:63], a[124:127], a[176:179], v[60:63]\n\t"
"s_add_u32 s64, s47, 18432\n\t"
"ds_read_b64_tr_b16 a[200:201], v210 offset:35072\n\t"
"v_add_f32 v180, v180, v181\n\t"
"v_mfma_f32_16x16x32_bf16 v[64:67], a[120:123], a[180:183], v[64:67]\n\t"
"s_waitcnt lgkmcnt(5)\n\t"
"ds_read_b64_tr_b16 a[202:203], v210 offset:39680\n\t"
"v_add_f32 v181, v182, v183\n\t"
"v_mfma_f32_16x16x32_bf16 v[68:71], a[124:127], a[180:183], v[68:71]\n\t"
"ds_read_b64_tr_b16 a[204:205], v212 offset:34960\n\t"
"ds_read_b64_tr_b16 a[206:207], v212 offset:39568\n\t"
"v_rcp_f32 v180, v180\n\t"
"v_mfma_f32_16x16x32_bf16 v[72:75], a[120:123], a[184:187], v[72:75]\n\t"
"v_rcp_f32 v181, v181\n\t"
"v_mfma_f32_16x16x32_bf16 v[76:79], a[124:127], a[184:187], v[76:79]\n\t"
"ds_read_b64_tr_b16 a[144:145], v210 offset:44032\n\t"
"v_mul_f32 v120, v180, v120\n\t"
"v_mfma_f32_16x16x32_bf16 v[80:83], a[120:123], a[188:191], v[80:83]\n\t"
"s_waitcnt lgkmcnt(5)\n\t"
"ds_read_b64_tr_b16 a[146:147], v210 offset:48640\n\t"
"v_mul_f32 v121, v180, v121\n\t"
"v_mfma_f32_16x16x32_bf16 v[84:87], a[124:127], a[188:191], v[84:87]\n\t"
"ds_read_b64_tr_b16 a[148:149], v210 offset:44072\n\t"
"ds_read_b64_tr_b16 a[150:151], v210 offset:48680\n\t"
"v_mul_f32 v122, v180, v122\n\t"
"v_mfma_f32_16x16x32_bf16 v[88:91], a[120:123], a[192:195], v[88:91]\n\t"
"ds_read_b64_tr_b16 a[152:153], v210 offset:44104\n\t"
"v_mul_f32 v123, v180, v123\n\t"
"v_mfma_f32_16x16x32_bf16 v[92:95], a[124:127], a[192:195], v[92:95]\n\t"
"ds_read_b64_tr_b16 a[154:155], v210 offset:48712\n\t"
"v_cvt_pk_bf16_f32 v184, v120, v121\n\t"
"v_mfma_f32_16x16x32_bf16 v[96:99], a[120:123], a[196:199], v[96:99]\n\t"
"s_waitcnt lgkmcnt(6)\n\t"
"ds_read_b64_tr_b16 a[156:157], v210 offset:44144\n\t"
"v_mul_f32 v124, v180, v124\n\t"
"v_mfma_f32_16x16x32_bf16 v[100:103], a[124:127], a[196:199], v[100:103]\n\t"
"ds_read_b64_tr_b16 a[158:159], v210 offset:48752\n\t"
"buffer_load_dwordx4 v[20:23], v230, %[ws], s64 offen offset:0 sc1\n\t"
"v_mfma_f32_16x16x32_bf16 v[104:107], a[120:123], a[200:203], v[104:107]\n\t"
"ds_read_b64_tr_b16 a[160:161], v212 offset:44032\n\t"
"v_mul_f32 v125, v180, v125\n\t"
"v_mfma_f32_16x16x32_bf16 v[108:111], a[124:127], a[200:203], v[108:111]\n\t"
"ds_read_b64_tr_b16 a[162:163], v212 offset:48640\n\t"
"v_cvt_pk_bf16_f32 v185, v122, v123\n\t"
"v_mfma_f32_16x16x32_bf16 v[112:115], a[120:123], a[204:207], v[112:115]\n\t"
"s_waitcnt lgkmcnt(6)\n\t"
"ds_read_b64_tr_b16 a[164:165], v210 offset:44176\n\t"
"v_mul_f32 v126, v180, v126\n\t"
"v_mfma_f32_16x16x32_bf16 v[116:119], a[124:127], a[204:207], v[116:119]\n\t"
"ds_read_b64_tr_b16 a[166:167], v210 offset:48784\n\t"
"v_mul_f32 v127, v180, v127\n\t"
"v_mfma_f32_16x16x32_bf16 v[40:43], a[128:131], a[144:147], v[40:43]\n\t"
"buffer_load_dwordx4 v[24:27], v230, %[ws], s64 offen offset:1024 sc1\n\t"
"v_mfma_f32_16x16x32_bf16 v[44:47], a[132:135], a[144:147], v[44:47]\n\t"
"ds_read_b64_tr_b16 a[168:169], v210 offset:44216\n\t"
"v_cvt_pk_bf16_f32 v186, v124, v125\n\t"
"v_mfma_f32_16x16x32_bf16 v[48:51], a[128:131], a[148:151], v[48:51]\n\t"
"s_waitcnt lgkmcnt(5)\n\t"
"ds_write_b64 v250, v[184:185] offset:0\n\t"
"v_mul_f32 v128, v180, v128\n\t"
"v_mfma_f32_16x16x32_bf16 v[52:55], a[132:135], a[148:151], v[52:55]\n\t"
"ds_read_b64_tr_b16 a[170:171], v210 offset:48824\n\t"
"ds_read_b64_tr_b16 a[172:173], v210 offset:44248\n\t"
"ds_read_b64_tr_b16 a[174:175], v210 offset:48856\n\t"
"v_mul_f32 v129, v180, v129\n\t"
"v_mfma_f32_16x16x32_bf16 v[56:59], a[128:131], a[152:155], v[56:59]\n\t"
"buffer_load_dwordx4 v[28:31], v230, %[ws], s64 offen offset:2048 sc1\n\t"
"v_cvt_pk_bf16_f32 v187, v126, v127\n\t"
"v_mfma_f32_16x16x32_bf16 v[60:63], a[132:135], a[152:155], v[60:63]\n\t"
"v_mul_f32 v130, v180, v130\n\t"
"v_mul_f32 v131, v180, v131\n\t"
"v_mfma_f32_16x16x32_bf16 v[64:67], a[128:131], a[156:159], v[64:67]\n\t"
"s_waitcnt lgkmcnt(5)\n\t"
"ds_read_b64_tr_b16 a[176:177], v210 offset:44288\n\t"
"v_cvt_pk_bf16_f32 v184, v128, v129\n\t"
"v_mfma_f32_16x16x32_bf16 v[68:71], a[132:135], a[156:159], v[68:71]\n\t"
"ds_read_b64_tr_b16 a[178:179], v210 offset:48896\n\t"
"ds_read_b64_tr_b16 a[180:181], v212 offset:44176\n\t"
"ds_read_b64_tr_b16 a[182:183], v212 offset:48784\n\t"
"buffer_load_dwordx4 v[32:35], v230, %[ws], s64 offen offset:3072 sc1\n\t"
"ds_write_b64 v250, v[186:187] offset:8\n\t"
"v_mfma_f32_16x16x32_bf16 v[72:75], a[128:131], a[160:163], v[72:75]\n\t"
"s_add_u32 s64, s47, 22528\n\t"
"v_mul_f32 v132, v180, v132\n\t"
"v_mul_f32 v133, v180, v133\n\t"
"v_mfma_f32_16x16x32_bf16 v[76:79], a[132:135], a[160:163], v[76:79]\n\t"
"v_cvt_pk_bf16_f32 v185, v130, v131\n\t"
"v_mul_f32 v134, v180, v134\n\t"
"v_mfma_f32_16x16x32_bf16 v[80:83], a[128:131], a[164:167], v[80:83]\n\t"
"s_waitcnt lgkmcnt(5)\n\t"
"ds_read_b64_tr_b16 a[184:185], v210 offset:53248\n\t"
"v_mul_f32 v135, v180, v135\n\t"
"v_mfma_f32_16x16x32_bf16 v[84:87], a[132:135], a[164:167], v[84:87]\n\t"
"ds_read_b64_tr_b16 a[186:187], v210 offset:57856\n\t"
"ds_read_b64_tr_b16 a[188:189], v210 offset:53288\n\t"
"ds_read_b64_tr_b16 a[190:191], v210 offset:57896\n\t"
"v_cvt_pk_bf16_f32 v186, v132, v133\n\t"
"v_mfma_f32_16x16x32_bf16 v[88:91], a[128:131], a[168:171], v[88:91]\n\t"
"ds_write_b64 v250, v[184:185] offset:16\n\t"
"v_mfma_f32_16x16x32_bf16 v[92:95], a[132:135], a[168:171], v[92:95]\n\t"
"v_mul_f32 v136, v180, v136\n\t"
"v_mul_f32 v137, v180, v137\n\t"
"v_mfma_f32_16x16x32_bf16 v[96:99], a[128:131], a[172:175], v[96:99]\n\t"
"s_waitcnt lgkmcnt(6)\n\t"
"ds_read_b64_tr_b16 a[192:193], v210 offset:53320\n\t"
"v_cvt_pk_bf16_f32 v187, v134, v135\n\t"
"v_mfma_f32_16x16x32_bf16 v[100:103], a[132:135], a[172:175], v[100:103]\n\t"
"ds_read_b64_tr_b16 a[194:195], v210 offset:57928\n\t"
"ds_read_b64_tr_b16 a[196:197], v210 offset:53360\n\t"
"ds_read_b64_tr_b16 a[198:199], v210 offset:57968\n\t"
"v_mul_f32 v160, v181, v160\n\t"
"v_mfma_f32_16x16x32_bf16 v[104:107], a[128:131], a[176:179], v[104:107]\n\t"
"v_mul_f32 v161, v181, v161\n\t"
"v_cvt_pk_bf16_f32 v184, v136, v137\n\t"
"v_mfma_f32_16x16x32_bf16 v[108:111], a[132:135], a[176:179], v[108:111]\n\t"
"buffer_load_dwordx4 v[36:39], v230, %[ws], s64 offen offset:0 sc1\n\t"
"v_mfma_f32_16x16x32_bf16 v[112:115], a[128:131], a[180:183], v[112:115]\n\t"
"s_waitcnt lgkmcnt(5)\n\t"
"ds_write_b64 v250, v[186:187] offset:24\n\t"
"v_mul_f32 v162, v181, v162\n\t"
"v_mfma_f32_16x16x32_bf16 v[116:119], a[132:135], a[180:183], v[116:119]\n\t"
"ds_read_b64_tr_b16 a[200:201], v212 offset:53248\n\t"
"ds_read_b64_tr_b16 a[202:203], v212 offset:57856\n\t"
"ds_read_b64_tr_b16 a[204:205], v210 offset:53392\n\t"
"ds_read_b64_tr_b16 a[206:207], v210 offset:58000\n\t"
"buffer_load_dwordx2 v[246:247], v238, %[ws], s56 offen offset:0 sc1\n\t"
"v_mul_f32 v163, v181, v163\n\t"
"v_mfma_f32_16x16x32_bf16 v[40:43], a[136:139], a[184:187], v[40:43]\n\t"
"v_cvt_pk_bf16_f32 v185, v160, v161\n\t"
"v_mul_f32 v164, v181, v164\n\t"
"v_mfma_f32_16x16x32_bf16 v[44:47], a[140:143], a[184:187], v[44:47]\n\t"
"ds_read_b64_tr_b16 a[144:145], v210 offset:53432\n\t"
"v_mul_f32 v165, v181, v165\n\t"
"v_mfma_f32_16x16x32_bf16 v[48:51], a[136:139], a[188:191], v[48:51]\n\t"
"s_waitcnt lgkmcnt(6)\n\t"
"ds_read_b64_tr_b16 a[146:147], v210 offset:58040\n\t"
"v_cvt_pk_bf16_f32 v186, v162, v163\n\t"
"v_mfma_f32_16x16x32_bf16 v[52:55], a[140:143], a[188:191], v[52:55]\n\t"
"ds_read_b64_tr_b16 a[148:149], v210 offset:53464\n\t"
"ds_read_b64_tr_b16 a[150:151], v210 offset:58072\n\t"
"ds_write_b64 v250, v[184:185] offset:32\n\t"
"v_mfma_f32_16x16x32_bf16 v[56:59], a[136:139], a[192:195], v[56:59]\n\t"
"v_mul_f32 v166, v181, v166\n\t"
"v_mul_f32 v167, v181, v167\n\t"
"v_mfma_f32_16x16x32_bf16 v[60:63], a[140:143], a[192:195], v[60:63]\n\t"
"ds_read_b64_tr_b16 a[152:153], v210 offset:53504\n\t"
"v_cvt_pk_bf16_f32 v187, v164, v165\n\t"
"v_mfma_f32_16x16x32_bf16 v[64:67], a[136:139], a[196:199], v[64:67]\n\t"
"ds_read_b64_tr_b16 a[154:155], v210 offset:58112\n\t"
"v_mul_f32 v168, v181, v168\n\t"
"v_mfma_f32_16x16x32_bf16 v[68:71], a[140:143], a[196:199], v[68:71]\n\t"
"s_waitcnt lgkmcnt(7)\n\t"
"ds_read_b64_tr_b16 a[156:157], v212 offset:53392\n\t"
"ds_read_b64_tr_b16 a[158:159], v212 offset:58000\n\t"
"v_mul_f32 v169, v181, v169\n\t"
"v_mfma_f32_16x16x32_bf16 v[72:75], a[136:139], a[200:203], v[72:75]\n\t"
"ds_write_b64 v250, v[186:187] offset:40\n\t"
"v_cvt_pk_bf16_f32 v184, v166, v167\n\t"
"v_mfma_f32_16x16x32_bf16 v[76:79], a[140:143], a[200:203], v[76:79]\n\t"
"v_mul_f32 v170, v181, v170\n\t"
"v_mul_f32 v171, v181, v171\n\t"
"v_mfma_f32_16x16x32_bf16 v[80:83], a[136:139], a[204:207], v[80:83]\n\t"
"v_cvt_pk_bf16_f32 v185, v168, v169\n\t"
"v_mul_f32 v172, v181, v172\n\t"
"v_mfma_f32_16x16x32_bf16 v[84:87], a[140:143], a[204:207], v[84:87]\n\t"
"s_waitcnt lgkmcnt(6)\n\t"
"v_mul_f32 v173, v181, v173\n\t"
"v_cvt_pk_bf16_f32 v186, v170, v171\n\t"
"v_mfma_f32_16x16x32_bf16 v[88:91], a[136:139], a[144:147], v[88:91]\n\t"
"ds_write_b64 v250, v[184:185] offset:48\n\t"
"v_mfma_f32_16x16x32_bf16 v[92:95], a[140:143], a[144:147], v[92:95]\n\t"
"v_mul_f32 v174, v181, v174\n\t"
"v_mul_f32 v175, v181, v175\n\t"
"v_mfma_f32_16x16x32_bf16 v[96:99], a[136:139], a[148:151], v[96:99]\n\t"
"v_cvt_pk_bf16_f32 v187, v172, v173\n\t"
"v_mul_f32 v176, v181, v176\n\t"
"v_mfma_f32_16x16x32_bf16 v[100:103], a[140:143], a[148:151], v[100:103]\n\t"
"s_waitcnt lgkmcnt(2)\n\t"
"v_mul_f32 v177, v181, v177\n\t"
"v_cvt_pk_bf16_f32 v184, v174, v175\n\t"
"v_mfma_f32_16x16x32_bf16 v[104:107], a[136:139], a[152:155], v[104:107]\n\t"
"ds_write_b64 v250, v[186:187] offset:56\n\t"
"v_cvt_pk_bf16_f32 v185, v176, v177\n\t"
"v_mfma_f32_16x16x32_bf16 v[108:111], a[140:143], a[152:155], v[108:111]\n\t"
"ds_write_b64 v250, v[184:185] offset:64\n\t"
"v_mfma_f32_16x16x32_bf16 v[112:115], a[136:139], a[156:159], v[112:115]\n\t"
"v_max3_f32 v178, v140, v141, v142\n\t"
"v_min3_f32 v179, v140, v141, v142\n\t"
"v_max3_f32 v180, v143, v144, v145\n\t"
"v_min3_f32 v181, v143, v144, v145\n\t"
"v_max3_f32 v178, v178, v146, v147\n\t"
"v_min3_f32 v179, v179, v146, v147\n\t"
"v_max3_f32 v180, v180, v148, v149\n\t"
"v_min3_f32 v181, v181, v148, v149\n\t"
"v_max3_f32 v178, v178, v150, v151\n\t"
"v_min3_f32 v179, v179, v150, v151\n\t"
"v_max3_f32 v180, v180, v152, v153\n\t"
"v_min3_f32 v181, v181, v152, v153\n\t"
"v_max3_f32 v178, v178, v154, v155\n\t"
"v_min3_f32 v179, v179, v154, v155\n\t"
"v_max3_f32 v180, v180, v156, v157\n\t"
"v_min3_f32 v181, v181, v156, v157\n\t"
"v_max_f32 v178, v178, v180\n\t"
"v_min_f32 v179, v179, v181\n\t"
"v_mul_f32 v178, s72, v178\n\t"
"v_mul_f32 v179, s72, v179\n\t"
"v_pk_fma_f32 v[160:161], v[140:141], s[72:73], v[178:179] op_sel:[0,0,1] op_sel_hi:[1,1,1] neg_lo:[1,0,0] neg_hi:[1,0,0]\n\t"
"v_pk_fma_f32 v[140:141], v[140:141], s[72:73], v[178:179] op_sel:[0,0,0] op_sel_hi:[1,1,0] neg_lo:[0,0,1] neg_hi:[0,0,1]\n\t"
"v_exp_f32 v160, v160\n\t"
"v_exp_f32 v161, v161\n\t"
"v_pk_fma_f32 v[162:163], v[142:143], s[72:73], v[178:179] op_sel:[0,0,1] op_sel_hi:[1,1,1] neg_lo:[1,0,0] neg_hi:[1,0,0]\n\t"
"v_exp_f32 v140, v140\n\t"
"v_exp_f32 v141, v141\n\t"
"v_pk_fma_f32 v[142:143], v[142:143], s[72:73], v[178:179] op_sel:[0,0,0] op_sel_hi:[1,1,0] neg_lo:[0,0,1] neg_hi:[0,0,1]\n\t"
"v_exp_f32 v162, v162\n\t"
"v_exp_f32 v163, v163\n\t"
"v_pk_fma_f32 v[164:165], v[144:145], s[72:73], v[178:179] op_sel:[0,0,1] op_sel_hi:[1,1,1] neg_lo:[1,0,0] neg_hi:[1,0,0]\n\t"
"v_exp_f32 v142, v142\n\t"
"v_exp_f32 v143, v143\n\t"
"v_pk_fma_f32 v[144:145], v[144:145], s[72:73], v[178:179] op_sel:[0,0,0] op_sel_hi:[1,1,0] neg_lo:[0,0,1] neg_hi:[0,0,1]\n\t"
"v_exp_f32 v164, v164\n\t"
"v_exp_f32 v165, v165\n\t"
"v_pk_add_f32 v[182:183], v[160:161], v[162:163]\n\t"
"v_pk_fma_f32 v[166:167], v[146:147], s[72:73], v[178:179] op_sel:[0,0,1] op_sel_hi:[1,1,1] neg_lo:[1,0,0] neg_hi:[1,0,0]\n\t"
"v_exp_f32 v144, v144\n\t"
"v_exp_f32 v145, v145\n\t"
"v_pk_add_f32 v[180:181], v[140:141], v[142:143]\n\t"
"v_pk_fma_f32 v[146:147], v[146:147], s[72:73], v[178:179] op_sel:[0,0,0] op_sel_hi:[1,1,0] neg_lo:[0,0,1] neg_hi:[0,0,1]\n\t"
"v_exp_f32 v166, v166\n\t"
"v_exp_f32 v167, v167\n\t"
"v_pk_add_f32 v[182:183], v[182:183], v[164:165]\n\t"
"v_pk_fma_f32 v[168:169], v[148:149], s[72:73], v[178:179] op_sel:[0,0,1] op_sel_hi:[1,1,1] neg_lo:[1,0,0] neg_hi:[1,0,0]\n\t"
"v_exp_f32 v146, v146\n\t"
"v_exp_f32 v147, v147\n\t"
"v_pk_add_f32 v[180:181], v[180:181], v[144:145]\n\t"
"v_pk_fma_f32 v[148:149], v[148:149], s[72:73], v[178:179] op_sel:[0,0,0] op_sel_hi:[1,1,0] neg_lo:[0,0,1] neg_hi:[0,0,1]\n\t"
"v_exp_f32 v168, v168\n\t"
"v_exp_f32 v169, v169\n\t"
"v_pk_add_f32 v[182:183], v[182:183], v[166:167]\n\t"
"v_pk_fma_f32 v[170:171], v[150:151], s[72:73], v[178:179] op_sel:[0,0,1] op_sel_hi:[1,1,1] neg_lo:[1,0,0] neg_hi:[1,0,0]\n\t"
"v_exp_f32 v148, v148\n\t"
"v_exp_f32 v149, v149\n\t"
"v_pk_add_f32 v[180:181], v[180:181], v[146:147]\n\t"
"v_pk_fma_f32 v[150:151], v[150:151], s[72:73], v[178:179] op_sel:[0,0,0] op_sel_hi:[1,1,0] neg_lo:[0,0,1] neg_hi:[0,0,1]\n\t"
"v_exp_f32 v170, v170\n\t"
"v_exp_f32 v171, v171\n\t"
"v_pk_add_f32 v[182:183], v[182:183], v[168:169]\n\t"
"v_pk_fma_f32 v[172:173], v[152:153], s[72:73], v[178:179] op_sel:[0,0,1] op_sel_hi:[1,1,1] neg_lo:[1,0,0] neg_hi:[1,0,0]\n\t"
"v_exp_f32 v150, v150\n\t"
"v_exp_f32 v151, v151\n\t"
"v_pk_add_f32 v[180:181], v[180:181], v[148:149]\n\t"
"v_pk_fma_f32 v[152:153], v[152:153], s[72:73], v[178:179] op_sel:[0,0,0] op_sel_hi:[1,1,0] neg_lo:[0,0,1] neg_hi:[0,0,1]\n\t"
"v_exp_f32 v172, v172\n\t"
"v_exp_f32 v173, v173\n\t"
"v_pk_add_f32 v[182:183], v[182:183], v[170:171]\n\t"
"v_pk_fma_f32 v[174:175], v[154:155], s[72:73], v[178:179] op_sel:[0,0,1] op_sel_hi:[1,1,1] neg_lo:[1,0,0] neg_hi:[1,0,0]\n\t"
"v_exp_f32 v152, v152\n\t"
"v_exp_f32 v153, v153\n\t"
"v_pk_add_f32 v[180:181], v[180:181], v[150:151]\n\t"
"v_pk_fma_f32 v[154:155], v[154:155], s[72:73], v[178:179] op_sel:[0,0,0] op_sel_hi:[1,1,0] neg_lo:[0,0,1] neg_hi:[0,0,1]\n\t"
"v_exp_f32 v174, v174\n\t"
"v_exp_f32 v175, v175\n\t"
"v_pk_add_f32 v[182:183], v[182:183], v[172:173]\n\t"
"v_pk_fma_f32 v[176:177], v[156:157], s[72:73], v[178:179] op_sel:[0,0,1] op_sel_hi:[1,1,1] neg_lo:[1,0,0] neg_hi:[1,0,0]\n\t"
"v_exp_f32 v154, v154\n\t"
"v_exp_f32 v155, v155\n\t"
"v_pk_add_f32 v[180:181], v[180:181], v[152:153]\n\t"
"v_pk_fma_f32 v[156:157], v[156:157], s[72:73], v[178:179] op_sel:[0,0,0] op_sel_hi:[1,1,0] neg_lo:[0,0,1] neg_hi:[0,0,1]\n\t"
"v_exp_f32 v176, v176\n\t"
"v_exp_f32 v177, v177\n\t"
"v_pk_add_f32 v[182:183], v[182:183], v[174:175]\n\t"
"v_exp_f32 v156, v156\n\t"
"v_exp_f32 v157, v157\n\t"
"v_pk_add_f32 v[180:181], v[180:181], v[154:155]\n\t"
"v_pk_add_f32 v[182:183], v[182:183], v[176:177]\n\t"
"v_pk_add_f32 v[180:181], v[180:181], v[156:157]\n\t"
"v_add_f32 v180, v180, v181\n\t"
"v_add_f32 v181, v182, v183\n\t"
"v_rcp_f32 v180, v180\n\t"
"v_rcp_f32 v181, v181\n\t"
"s_nop 0\n\t"
"v_pk_mul_f32 v[140:141], v[140:141], v[180:181] op_sel:[0,0] op_sel_hi:[1,0]\n\t"
"v_pk_mul_f32 v[142:143], v[142:143], v[180:181] op_sel:[0,0] op_sel_hi:[1,0]\n\t"
"v_pk_mul_f32 v[144:145], v[144:145], v[180:181] op_sel:[0,0] op_sel_hi:[1,0]\n\t"
"v_cvt_pk_bf16_f32 v184, v140, v141\n\t"
"v_pk_mul_f32 v[146:147], v[146:147], v[180:181] op_sel:[0,0] op_sel_hi:[1,0]\n\t"
"v_cvt_pk_bf16_f32 v185, v142, v143\n\t"
"v_pk_mul_f32 v[148:149], v[148:149], v[180:181] op_sel:[0,0] op_sel_hi:[1,0]\n\t"
"v_cvt_pk_bf16_f32 v186, v144, v145\n\t"
"ds_write_b64 v250, v[184:185] offset:4736\n\t"
"v_pk_mul_f32 v[150:151], v[150:151], v[180:181] op_sel:[0,0] op_sel_hi:[1,0]\n\t"
"v_cvt_pk_bf16_f32 v187, v146, v147\n\t"
"v_pk_mul_f32 v[152:153], v[152:153], v[180:181] op_sel:[0,0] op_sel_hi:[1,0]\n\t"
"v_cvt_pk_bf16_f32 v184, v148, v149\n\t"
"ds_write_b64 v250, v[186:187] offset:4744\n\t"
"v_pk_mul_f32 v[154:155], v[154:155], v[180:181] op_sel:[0,0] op_sel_hi:[1,0]\n\t"
"v_cvt_pk_bf16_f32 v185, v150, v151\n\t"
"v_pk_mul_f32 v[156:157], v[156:157], v[180:181] op_sel:[0,0] op_sel_hi:[1,0]\n\t"
"v_cvt_pk_bf16_f32 v186, v152, v153\n\t"
"ds_write_b64 v250, v[184:185] offset:4752\n\t"
"v_pk_mul_f32 v[160:161], v[160:161], v[180:181] op_sel:[0,1] op_sel_hi:[1,1]\n\t"
"v_cvt_pk_bf16_f32 v187, v154, v155\n\t"
"v_pk_mul_f32 v[162:163], v[162:163], v[180:181] op_sel:[0,1] op_sel_hi:[1,1]\n\t"
"v_cvt_pk_bf16_f32 v184, v156, v157\n\t"
"ds_write_b64 v250, v[186:187] offset:4760\n\t"
"v_pk_mul_f32 v[164:165], v[164:165], v[180:181] op_sel:[0,1] op_sel_hi:[1,1]\n\t"
"v_cvt_pk_bf16_f32 v185, v160, v161\n\t"
"v_pk_mul_f32 v[166:167], v[166:167], v[180:181] op_sel:[0,1] op_sel_hi:[1,1]\n\t"
"v_cvt_pk_bf16_f32 v186, v162, v163\n\t"
"ds_write_b64 v250, v[184:185] offset:4768\n\t"
"v_pk_mul_f32 v[168:169], v[168:169], v[180:181] op_sel:[0,1] op_sel_hi:[1,1]\n\t"
"v_cvt_pk_bf16_f32 v187, v164, v165\n\t"
"v_pk_mul_f32 v[170:171], v[170:171], v[180:181] op_sel:[0,1] op_sel_hi:[1,1]\n\t"
"v_cvt_pk_bf16_f32 v184, v166, v167\n\t"
"ds_write_b64 v250, v[186:187] offset:4776\n\t"
"v_pk_mul_f32 v[172:173], v[172:173], v[180:181] op_sel:[0,1] op_sel_hi:[1,1]\n\t"
"v_cvt_pk_bf16_f32 v185, v168, v169\n\t"
"v_pk_mul_f32 v[174:175], v[174:175], v[180:181] op_sel:[0,1] op_sel_hi:[1,1]\n\t"
"v_cvt_pk_bf16_f32 v186, v170, v171\n\t"
"ds_write_b64 v250, v[184:185] offset:4784\n\t"
"v_pk_mul_f32 v[176:177], v[176:177], v[180:181] op_sel:[0,1] op_sel_hi:[1,1]\n\t"
"v_cvt_pk_bf16_f32 v187, v172, v173\n\t"
"v_cvt_pk_bf16_f32 v184, v174, v175\n\t"
"ds_write_b64 v250, v[186:187] offset:4792\n\t"
"v_cvt_pk_bf16_f32 v185, v176, v177\n\t"
"ds_write_b64 v250, v[184:185] offset:4800\n\t"
"v_mfma_f32_16x16x32_bf16 v[116:119], a[140:143], a[156:159], v[116:119]\n\t"
"v_cvt_pk_bf16_f32 v184, v40, v41\n\t"
"v_cvt_pk_bf16_f32 v185, v42, v43\n\t"
"v_cvt_pk_bf16_f32 v186, v44, v45\n\t"
"v_cvt_pk_bf16_f32 v187, v46, v47\n\t"
"v_cvt_pk_bf16_f32 v188, v48, v49\n\t"
"v_cvt_pk_bf16_f32 v189, v50, v51\n\t"
"v_cvt_pk_bf16_f32 v190, v52, v53\n\t"
"v_cvt_pk_bf16_f32 v191, v54, v55\n\t"
"ds_write_b128 v227, v[184:187] offset:0\n\t"
"v_cvt_pk_bf16_f32 v192, v56, v57\n\t"
"v_cvt_pk_bf16_f32 v193, v58, v59\n\t"
"v_cvt_pk_bf16_f32 v194, v60, v61\n\t"
"v_cvt_pk_bf16_f32 v195, v62, v63\n\t"
"ds_write_b128 v227, v[188:191] offset:1024\n\t"
"v_cvt_pk_bf16_f32 v184, v64, v65\n\t"
"v_cvt_pk_bf16_f32 v185, v66, v67\n\t"
"v_cvt_pk_bf16_f32 v186, v68, v69\n\t"
"v_cvt_pk_bf16_f32 v187, v70, v71\n\t"
"ds_write_b128 v227, v[192:195] offset:2048\n\t"
"v_cvt_pk_bf16_f32 v188, v72, v73\n\t"
"v_cvt_pk_bf16_f32 v189, v74, v75\n\t"
"v_cvt_pk_bf16_f32 v190, v76, v77\n\t"
"v_cvt_pk_bf16_f32 v191, v78, v79\n\t"
"ds_write_b128 v227, v[184:187] offset:3072\n\t"
"v_cvt_pk_bf16_f32 v192, v80, v81\n\t"
"v_cvt_pk_bf16_f32 v193, v82, v83\n\t"
"v_cvt_pk_bf16_f32 v194, v84, v85\n\t"
"v_cvt_pk_bf16_f32 v195, v86, v87\n\t"
"v_cndmask_b32 v188, v188, v244, s[74:75]\n\t"
"v_cndmask_b32 v189, v189, v244, s[74:75]\n\t"
"v_cndmask_b32 v190, v190, v244, s[74:75]\n\t"
"v_cndmask_b32 v191, v191, v244, s[74:75]\n\t"
"ds_write_b128 v227, v[188:191] offset:4096\n\t"
"v_cvt_pk_bf16_f32 v184, v88, v89\n\t"
"v_cvt_pk_bf16_f32 v185, v90, v91\n\t"
"v_cvt_pk_bf16_f32 v186, v92, v93\n\t"
"v_cvt_pk_bf16_f32 v187, v94, v95\n\t"
"ds_write_b128 v227, v[192:195] offset:5120\n\t"
"v_cvt_pk_bf16_f32 v188, v96, v97\n\t"
"v_cvt_pk_bf16_f32 v189, v98, v99\n\t"
"v_cvt_pk_bf16_f32 v190, v100, v101\n\t"
"v_cvt_pk_bf16_f32 v191, v102, v103\n\t"
"ds_write_b128 v227, v[184:187] offset:6144\n\t"
"v_cvt_pk_bf16_f32 v192, v104, v105\n\t"
"v_cvt_pk_bf16_f32 v193, v106, v107\n\t"
"v_cvt_pk_bf16_f32 v194, v108, v109\n\t"
"v_cvt_pk_bf16_f32 v195, v110, v111\n\t"
"ds_write_b128 v227, v[188:191] offset:7168\n\t"
"v_cvt_pk_bf16_f32 v184, v112, v113\n\t"
"v_cvt_pk_bf16_f32 v185, v114, v115\n\t"
"v_cvt_pk_bf16_f32 v186, v116, v117\n\t"
"v_cvt_pk_bf16_f32 v187, v118, v119\n\t"
"ds_write_b128 v227, v[192:195] offset:8192\n\t"
"v_cndmask_b32 v184, v184, v244, s[74:75]\n\t"
"v_cndmask_b32 v185, v185, v244, s[74:75]\n\t"
"v_cndmask_b32 v186, v186, v244, s[74:75]\n\t"
"v_cndmask_b32 v187, v187, v244, s[74:75]\n\t"
"ds_write_b128 v227, v[184:187] offset:9216\n\t"
"s_waitcnt lgkmcnt(0)\n\t"
"s_barrier\n\t"
"ds_read_b128 a[0:3], v228 offset:0\n\t"
"ds_read_b64_tr_b16 a[4:5], v226 offset:0\n\t"
"ds_read_b64_tr_b16 a[6:7], v226 offset:4736\n\t"
"ds_read_b128 a[8:11], v229 offset:4096\n\t"
"ds_read_b64_tr_b16 a[12:13], v226 offset:32\n\t"
"ds_read_b64_tr_b16 a[14:15], v226 offset:4768\n\t"
"ds_read_b64_tr_b16 a[16:17], v226 offset:64\n\t"
"ds_read_b64_tr_b16 a[18:19], v226 offset:4800\n\t"
"ds_read_b128 a[20:23], v228 offset:10240\n\t"
"ds_read_b64_tr_b16 a[24:25], v226 offset:9472\n\t"
"ds_read_b64_tr_b16 a[26:27], v226 offset:14208\n\t"
"ds_read_b128 a[28:31], v229 offset:14336\n\t"
"ds_read_b64_tr_b16 a[32:33], v226 offset:9504\n\t"
"ds_read_b64_tr_b16 a[34:35], v226 offset:14240\n\t"
"ds_read_b64_tr_b16 a[36:37], v226 offset:9536\n\t"
"ds_read_b64_tr_b16 a[38:39], v226 offset:14272\n\t"
"s_waitcnt lgkmcnt(13)\n\t"
"v_mfma_f32_16x16x32_bf16 v[120:123], a[0:3], a[4:7], 0\n\t"
"s_waitcnt lgkmcnt(10)\n\t"
"ds_read_b128 a[40:43], v228 offset:20480\n\t"
"v_lshrrev_b32 v189, 2, v245\n\t"
"v_mfma_f32_16x16x32_bf16 v[124:127], a[8:11], a[4:7], 0\n\t"
"s_add_u32 s67, %[lds], 160448\n\t"
"ds_read_b64_tr_b16 a[44:45], v226 offset:18944\n\t"
"v_and_b32 v184, 3, v245\n\t"
"v_mfma_f32_16x16x32_bf16 v[128:131], a[0:3], a[12:15], 0\n\t"
"s_waitcnt lgkmcnt(9)\n\t"
"v_mov_b32 v186, s67\n\t"
"s_and_b32 s67, %[a], 1\n\t"
"v_mfma_f32_16x16x32_bf16 v[132:135], a[8:11], a[12:15], 0\n\t"
"s_lshl_b32 s67, s67, 4\n\t"
"v_subrev_u32 v189, s67, v189\n\t"
"v_mul_u32_u24 v184, 18, v184\n\t"
"v_mfma_f32_16x16x32_bf16 v[136:139], a[0:3], a[16:19], 0\n\t"
"s_add_u32 s67, %[lds], 153600\n\t"
"ds_read_b64_tr_b16 a[46:47], v226 offset:23680\n\t"
"v_sub_u32 v184, v189, v184\n\t"
"v_mfma_f32_16x16x32_bf16 v[140:143], a[8:11], a[16:19], 0\n\t"
"s_waitcnt lgkmcnt(7)\n\t"
"ds_read_b128 a[48:51], v229 offset:24576\n\t"
"ds_read_b64_tr_b16 a[52:53], v226 offset:18976\n\t"
"ds_read_b64_tr_b16 a[54:55], v226 offset:23712\n\t"
"v_mul_i32_i24 v185, 40, v189\n\t"
"v_mfma_f32_16x16x32_bf16 v[120:123], a[20:23], a[24:27], v[120:123]\n\t"
"s_waitcnt lgkmcnt(6)\n\t"
"v_add_u32 v185, s67, v185\n\t"
"v_add_u32 v187, 0, v184\n\t"
"v_mfma_f32_16x16x32_bf16 v[124:127], a[28:31], a[24:27], v[124:127]\n\t"
"v_cmp_gt_u32 vcc, 18, v187\n\t"
"v_add_u32 v188, 0, v185\n\t"
"s_nop 0\n\t"
"v_cndmask_b32 v213, v186, v188, vcc\n\t"
"v_mfma_f32_16x16x32_bf16 v[128:131], a[20:23], a[32:35], v[128:131]\n\t"
"ds_read_b64_tr_b16 a[56:57], v226 offset:19008\n\t"
"ds_read_b64_tr_b16 a[58:59], v226 offset:23744\n\t"
"ds_read_b128 a[60:63], v228 offset:30720\n\t"
"v_add_u32 v187, 16, v184\n\t"
"v_cmp_gt_u32 vcc, 18, v187\n\t"
"v_mfma_f32_16x16x32_bf16 v[132:135], a[28:31], a[32:35], v[132:135]\n\t"
"v_add_u32 v188, 640, v185\n\t"
"v_cndmask_b32 v214, v186, v188, vcc\n\t"
"v_mfma_f32_16x16x32_bf16 v[136:139], a[20:23], a[36:39], v[136:139]\n\t"
"s_waitcnt lgkmcnt(6)\n\t"
"ds_read_b64_tr_b16 a[64:65], v226 offset:28416\n\t"
"ds_read_b64_tr_b16 a[66:67], v226 offset:33152\n\t"
"ds_read_b128 a[68:71], v229 offset:34816\n\t"
"v_add_u32 v187, 32, v184\n\t"
"v_cmp_gt_u32 vcc, 18, v187\n\t"
"v_add_u32 v188, 1280, v185\n\t"
"v_mfma_f32_16x16x32_bf16 v[140:143], a[28:31], a[36:39], v[140:143]\n\t"
"v_cndmask_b32 v215, v186, v188, vcc\n\t"
"v_add_u32 v187, 48, v184\n\t"
"v_mfma_f32_16x16x32_bf16 v[120:123], a[40:43], a[44:47], v[120:123]\n\t"
"s_waitcnt lgkmcnt(6)\n\t"
"ds_read_b64_tr_b16 a[72:73], v226 offset:28448\n\t"
"ds_read_b64_tr_b16 a[74:75], v226 offset:33184\n\t"
"ds_read_b64_tr_b16 a[76:77], v226 offset:28480\n\t"
"ds_read_b64_tr_b16 a[78:79], v226 offset:33216\n\t"
"v_cmp_gt_u32 vcc, 18, v187\n\t"
"v_add_u32 v188, 1920, v185\n\t"
"v_mfma_f32_16x16x32_bf16 v[124:127], a[48:51], a[44:47], v[124:127]\n\t"
"v_cndmask_b32 v216, v186, v188, vcc\n\t"
"v_add_u32 v187, 64, v184\n\t"
"v_mfma_f32_16x16x32_bf16 v[128:131], a[40:43], a[52:55], v[128:131]\n\t"
"s_waitcnt lgkmcnt(7)\n\t"
"v_cmp_gt_u32 vcc, 18, v187\n\t"
"v_add_u32 v188, 2560, v185\n\t"
"s_nop 0\n\t"
"v_cndmask_b32 v217, v186, v188, vcc\n\t"
"v_mfma_f32_16x16x32_bf16 v[132:135], a[48:51], a[52:55], v[132:135]\n\t"
"v_add_u32 v187, 80, v184\n\t"
"v_cmp_gt_u32 vcc, 18, v187\n\t"
"v_mfma_f32_16x16x32_bf16 v[136:139], a[40:43], a[56:59], v[136:139]\n\t"
"s_waitcnt lgkmcnt(4)\n\t"
"ds_read_b128 a[80:83], v228 offset:5120\n\t"
"ds_read_b64_tr_b16 a[84:85], v251 offset:0\n\t"
"ds_read_b64_tr_b16 a[86:87], v251 offset:4736\n\t"
"v_add_u32 v188, 3200, v185\n\t"
"v_cndmask_b32 v218, v186, v188, vcc\n\t"
"v_mfma_f32_16x16x32_bf16 v[140:143], a[48:51], a[56:59], v[140:143]\n\t"
"ds_read_b128 a[88:91], v229 offset:9216\n\t"
"ds_read_b64_tr_b16 a[92:93], v251 offset:32\n\t"
"ds_read_b64_tr_b16 a[94:95], v251 offset:4768\n\t"
"v_add_u32 v187, -8, v184\n\t"
"v_cmp_gt_u32 vcc, 18, v187\n\t"
"v_add_u32 v188, 2560, v185\n\t"
"v_mfma_f32_16x16x32_bf16 v[120:123], a[60:63], a[64:67], v[120:123]\n\t"
"v_cndmask_b32 v219, v186, v188, vcc\n\t"
"v_add_u32 v187, 8, v184\n\t"
"v_mfma_f32_16x16x32_bf16 v[124:127], a[68:71], a[64:67], v[124:127]\n\t"
"s_waitcnt lgkmcnt(6)\n\t"
"v_cmp_gt_u32 vcc, 18, v187\n\t"
"v_add_u32 v188, 3200, v185\n\t"
"v_mfma_f32_16x16x32_bf16 v[128:131], a[60:63], a[72:75], v[128:131]\n\t"
"ds_read_b64_tr_b16 a[0:1], v251 offset:64\n\t"
"ds_read_b64_tr_b16 a[2:3], v251 offset:4800\n\t"
"ds_read_b128 a[4:7], v228 offset:15360\n\t"
"v_cndmask_b32 v220, v186, v188, vcc\n\t"
"v_add_u32 v187, 24, v184\n\t"
"v_cmp_gt_u32 vcc, 18, v187\n\t"
"v_mfma_f32_16x16x32_bf16 v[132:135], a[68:71], a[72:75], v[132:135]\n\t"
"v_add_u32 v188, 3840, v185\n\t"
"v_cndmask_b32 v221, v186, v188, vcc\n\t"
"v_mfma_f32_16x16x32_bf16 v[136:139], a[60:63], a[76:79], v[136:139]\n\t"
"s_waitcnt lgkmcnt(6)\n\t"
"ds_read_b64_tr_b16 a[8:9], v251 offset:9472\n\t"
"ds_read_b64_tr_b16 a[10:11], v251 offset:14208\n\t"
"ds_read_b128 a[12:15], v229 offset:19456\n\t"
"v_add_u32 v187, 40, v184\n\t"
"v_cmp_gt_u32 vcc, 18, v187\n\t"
"v_mfma_f32_16x16x32_bf16 v[140:143], a[68:71], a[76:79], v[140:143]\n\t"
"v_add_u32 v188, 4480, v185\n\t"
"v_cndmask_b32 v222, v186, v188, vcc\n\t"
"v_add_u32 v187, 56, v184\n\t"
"v_mfma_f32_16x16x32_bf16 v[144:147], a[80:83], a[84:87], 0\n\t"
"s_waitcnt lgkmcnt(6)\n\t"
"ds_read_b64_tr_b16 a[16:17], v251 offset:9504\n\t"
"ds_read_b64_tr_b16 a[18:19], v251 offset:14240\n\t"
"ds_read_b64_tr_b16 a[20:21], v251 offset:9536\n\t"
"ds_read_b64_tr_b16 a[22:23], v251 offset:14272\n\t"
"v_cmp_gt_u32 vcc, 18, v187\n\t"
"v_add_u32 v188, 5120, v185\n\t"
"v_mfma_f32_16x16x32_bf16 v[148:151], a[88:91], a[84:87], 0\n\t"
"v_cndmask_b32 v223, v186, v188, vcc\n\t"
"v_add_u32 v187, 72, v184\n\t"
"v_mfma_f32_16x16x32_bf16 v[152:155], a[80:83], a[92:95], 0\n\t"
"v_cmp_gt_u32 vcc, 18, v187\n\t"
"v_add_u32 v188, 5760, v185\n\t"
"s_nop 0\n\t"
"v_cndmask_b32 v224, v186, v188, vcc\n\t"
"v_mfma_f32_16x16x32_bf16 v[156:159], a[88:91], a[92:95], 0\n\t"
"s_waitcnt lgkmcnt(7)\n\t"
"ds_read_b128 a[24:27], v228 offset:25600\n\t"
"v_mfma_f32_16x16x32_bf16 v[160:163], a[80:83], a[0:3], 0\n\t"
"s_waitcnt lgkmcnt(5)\n\t"
"ds_read_b64_tr_b16 a[28:29], v251 offset:18944\n\t"
"ds_read_b64_tr_b16 a[30:31], v251 offset:23680\n\t"
"v_mfma_f32_16x16x32_bf16 v[164:167], a[88:91], a[0:3], 0\n\t"
"buffer_store_dwordx4 v[120:123], v230, %[ws], s76 offen offset:0 sc1\n\t"
"buffer_store_dwordx4 v[124:127], v236, %[ws], s76 offen offset:2304 sc1\n\t"
"buffer_store_dwordx4 v[128:131], v230, %[ws], s76 offen offset:1024 sc1\n\t"
"ds_read_b128 a[32:35], v229 offset:29696\n\t"
"ds_read_b64_tr_b16 a[36:37], v251 offset:18976\n\t"
"ds_read_b64_tr_b16 a[38:39], v251 offset:23712\n\t"
"v_mfma_f32_16x16x32_bf16 v[144:147], a[4:7], a[8:11], v[144:147]\n\t"
"ds_read_b64_tr_b16 a[40:41], v251 offset:19008\n\t"
"v_mfma_f32_16x16x32_bf16 v[148:151], a[12:15], a[8:11], v[148:151]\n\t"
"s_waitcnt lgkmcnt(7)\n\t"
"ds_read_b64_tr_b16 a[42:43], v251 offset:23744\n\t"
"v_mfma_f32_16x16x32_bf16 v[152:155], a[4:7], a[16:19], v[152:155]\n\t"
"ds_read_b128 a[44:47], v228 offset:35840\n\t"
"v_mfma_f32_16x16x32_bf16 v[156:159], a[12:15], a[16:19], v[156:159]\n\t"
"ds_read_b64_tr_b16 a[48:49], v251 offset:28416\n\t"
"v_mfma_f32_16x16x32_bf16 v[160:163], a[4:7], a[20:23], v[160:163]\n\t"
"s_waitcnt lgkmcnt(7)\n\t"
"ds_read_b64_tr_b16 a[50:51], v251 offset:33152\n\t"
"ds_read_b128 a[52:55], v229 offset:39936\n\t"
"v_mfma_f32_16x16x32_bf16 v[164:167], a[12:15], a[20:23], v[164:167]\n\t"
"s_waitcnt lgkmcnt(6)\n\t"
"ds_read_b64_tr_b16 a[56:57], v251 offset:28448\n\t"
"v_mfma_f32_16x16x32_bf16 v[144:147], a[24:27], a[28:31], v[144:147]\n\t"
"buffer_store_dwordx4 v[132:135], v236, %[ws], s76 offen offset:2560 sc1\n\t"
"buffer_store_dwordx4 v[136:139], v235, %[ws], s76 offen offset:2048 sc1\n\t"
"buffer_store_dwordx4 v[140:143], v237, %[ws], s76 offen offset:2816 sc1\n\t"
"ds_read_b64_tr_b16 a[58:59], v251 offset:33184\n\t"
"ds_read_b64_tr_b16 a[60:61], v251 offset:28480\n\t"
"ds_read_b64_tr_b16 a[62:63], v251 offset:33216\n\t"
"v_mfma_f32_16x16x32_bf16 v[148:151], a[32:35], a[28:31], v[148:151]\n\t"
"v_mfma_f32_16x16x32_bf16 v[152:155], a[24:27], a[36:39], v[152:155]\n\t"
"v_mfma_f32_16x16x32_bf16 v[156:159], a[32:35], a[36:39], v[156:159]\n\t"
"s_waitcnt lgkmcnt(7)\n\t"
"v_mfma_f32_16x16x32_bf16 v[160:163], a[24:27], a[40:43], v[160:163]\n\t"
"v_mfma_f32_16x16x32_bf16 v[164:167], a[32:35], a[40:43], v[164:167]\n\t"
"s_waitcnt lgkmcnt(4)\n\t"
"v_mfma_f32_16x16x32_bf16 v[144:147], a[44:47], a[48:51], v[144:147]\n\t"
"v_mfma_f32_16x16x32_bf16 v[148:151], a[52:55], a[48:51], v[148:151]\n\t"
"s_waitcnt lgkmcnt(0)\n\t"
"v_mfma_f32_16x16x32_bf16 v[152:155], a[44:47], a[56:59], v[152:155]\n\t"
"v_mfma_f32_16x16x32_bf16 v[156:159], a[52:55], a[56:59], v[156:159]\n\t"
"v_mfma_f32_16x16x32_bf16 v[160:163], a[44:47], a[60:63], v[160:163]\n\t"
"v_mfma_f32_16x16x32_bf16 v[164:167], a[52:55], a[60:63], v[164:167]\n\t"
"s_add_u32 s76, s76, 11520\n\t"
"s_nop 0\n\t"
"buffer_store_dwordx4 v[144:147], v230, %[ws], s76 offen offset:0 sc1\n\t"
"buffer_store_dwordx4 v[148:151], v236, %[ws], s76 offen offset:2304 sc1\n\t"
"s_nop 0\n\t"
"buffer_store_dwordx4 v[152:155], v230, %[ws], s76 offen offset:1024 sc1\n\t"
"buffer_store_dwordx4 v[156:159], v236, %[ws], s76 offen offset:2560 sc1\n\t"
"buffer_store_dwordx4 v[160:163], v235, %[ws], s76 offen offset:2048 sc1\n\t"
"buffer_store_dwordx4 v[164:167], v237, %[ws], s76 offen offset:2816 sc1\n\t"
"s_barrier\n\t"
"s_waitcnt vmcnt(12)\n\t"
"v_cmp_eq_u64 vcc, v[246:247], v[232:233]\n\t"
"s_cmp_eq_u64 vcc, -1\n\t"
"s_cbranch_scc1 105f\n\t"
"s_mov_b32 s98, 65536\n\t"
"104:\n\t"
"s_sleep 3\n\t"
"buffer_load_dwordx2 v[246:247], v238, %[ws], s56 offen offset:0 sc1\n\t"
"s_waitcnt vmcnt(0)\n\t"
"v_cmp_eq_u64 vcc, v[246:247], v[232:233]\n\t"
"s_cmp_eq_u64 vcc, -1\n\t"
"s_cbranch_scc1 105f\n\t"
"s_sub_u32 s98, s98, 1\n\t"
"s_cmp_lg_u32 s98, 0\n\t"
"s_cbranch_scc1 104b\n\t"
"105:\n\t"
"buffer_store_dwordx2 v[254:255], v231, %[ws], s60 offen offset:0 sc1\n\t"
"s_getpc_b64 s[96:97]\n\t"
"73:\n\t"
"s_and_b32 s98, %[mi], 3\n\t"
"s_lshl_b32 s98, s98, 2\n\t"
"s_add_u32 s98, s98, %[wv]\n\t"
"s_lshl_b32 s98, s98, 10\n\t"
"v_add_u32 v198, s98, v230\n\t"
"v_min_u32 v198, 97f-73b, v198\n\t"
"global_load_dword v198, v198, s[96:97]\n\t"
"ds_read_b64_tr_b16 a[0:1], v213 offset:0\n\t"
"ds_read_b64_tr_b16 a[2:3], v214 offset:0\n\t"
"ds_read_b64_tr_b16 a[4:5], v215 offset:0\n\t"
"ds_read_b64_tr_b16 a[6:7], v216 offset:0\n\t"
"ds_read_b64_tr_b16 a[8:9], v217 offset:0\n\t"
"ds_read_b64_tr_b16 a[10:11], v218 offset:0\n\t"
"ds_read_b64_tr_b16 a[12:13], v213 offset:8\n\t"
"ds_read_b64_tr_b16 a[14:15], v214 offset:8\n\t"
"ds_read_b64_tr_b16 a[16:17], v215 offset:8\n\t"
"ds_read_b64_tr_b16 a[18:19], v216 offset:8\n\t"
"ds_read_b64_tr_b16 a[20:21], v217 offset:8\n\t"
"ds_read_b64_tr_b16 a[22:23], v218 offset:8\n\t"
"s_waitcnt lgkmcnt(8)\n\t"
"v_mfma_f32_16x16x32_bf16 v[40:43], a[0:3], v[0:3], 0\n\t"
"s_lshl_b32 s64, %[wv], 6\n\t"
"ds_read_b64_tr_b16 a[24:25], v213 offset:16\n\t"
"v_add_u32 v190, s64, v245\n\t"
"v_mfma_f32_16x16x32_bf16 v[60:63], a[0:3], v[20:23], 0\n\t"
"s_add_u32 s64, %[lds], 115712\n\t"
"ds_read_b64_tr_b16 a[26:27], v214 offset:16\n\t"
"v_mov_b32 v184, 2130706432\n\t"
"v_mfma_f32_16x16x32_bf16 v[40:43], a[4:7], v[4:7], v[40:43]\n\t"
"s_add_u32 s65, s64, 11520\n\t"
"v_add_u32 v185, 0, v190\n\t"
"v_mul_u32_u24 v186, 1366, v185\n\t"
"v_mfma_f32_16x16x32_bf16 v[60:63], a[4:7], v[24:27], v[60:63]\n\t"
"s_waitcnt lgkmcnt(6)\n\t"
"ds_read_b64_tr_b16 a[28:29], v215 offset:16\n\t"
"v_lshrrev_b32 v186, 16, v186\n\t"
"v_mfma_f32_16x16x32_bf16 v[40:43], a[8:11], v[8:11], v[40:43]\n\t"
"ds_read_b64_tr_b16 a[30:31], v216 offset:16\n\t"
"v_mul_u32_u24 v187, 48, v186\n\t"
"v_mfma_f32_16x16x32_bf16 v[60:63], a[8:11], v[28:31], v[60:63]\n\t"
"v_sub_u32 v188, v185, v187\n\t"
"v_and_b32 v193, 1, v186\n\t"
"v_mfma_f32_16x16x32_bf16 v[44:47], a[12:15], v[0:3], 0\n\t"
"s_waitcnt lgkmcnt(4)\n\t"
"v_mul_u32_u24 v193, 45, v193\n\t"
"v_add_lshl_u32 v193, v193, v188, 1\n\t"
"v_mfma_f32_16x16x32_bf16 v[64:67], a[12:15], v[20:23], 0\n\t"
"v_lshrrev_b32 v194, 6, v193\n\t"
"v_and_b32 v195, 15, v193\n\t"
"v_mfma_f32_16x16x32_bf16 v[44:47], a[16:19], v[4:7], v[44:47]\n\t"
"ds_read_b64_tr_b16 a[32:33], v217 offset:16\n\t"
"ds_read_b64_tr_b16 a[34:35], v218 offset:16\n\t"
"ds_read_b64_tr_b16 a[36:37], v213 offset:24\n\t"
"ds_read_b64_tr_b16 a[38:39], v214 offset:24\n\t"
"v_lshl_add_u32 v194, v194, 4, v195\n\t"
"v_mul_u32_u24 v194, 40, v194\n\t"
"v_mfma_f32_16x16x32_bf16 v[64:67], a[16:19], v[24:27], v[64:67]\n\t"
"v_bfe_u32 v196, v193, 4, 2\n\t"
"v_lshl_add_u32 v194, v196, 3, v194\n\t"
"v_mfma_f32_16x16x32_bf16 v[44:47], a[20:23], v[8:11], v[44:47]\n\t"
"s_waitcnt lgkmcnt(4)\n\t"
"v_and_b32 v196, 3, v193\n\t"
"v_mul_u32_u24 v196, 40, v196\n\t"
"v_mfma_f32_16x16x32_bf16 v[64:67], a[20:23], v[28:31], v[64:67]\n\t"
"v_lshrrev_b32 v195, 2, v195\n\t"
"v_lshl_add_u32 v196, v195, 3, v196\n\t"
"v_mfma_f32_16x16x32_bf16 v[48:51], a[24:27], v[0:3], 0\n\t"
"ds_read_b64_tr_b16 a[40:41], v215 offset:24\n\t"
"ds_read_b64_tr_b16 a[42:43], v216 offset:24\n\t"
"ds_read_b64_tr_b16 a[44:45], v217 offset:24\n\t"
"ds_read_b64_tr_b16 a[46:47], v218 offset:24\n\t"
"v_cmp_lt_u32 vcc, 127, v193\n\t"
"v_add_u32 v196, 1280, v196\n\t"
"v_mfma_f32_16x16x32_bf16 v[68:71], a[24:27], v[20:23], 0\n\t"
"v_mul_u32_u24 v195, 40, v193\n\t"
"v_cndmask_b32 v194, v194, v196, vcc\n\t"
"v_cmp_lt_u32 vcc, 143, v193\n\t"
"v_mfma_f32_16x16x32_bf16 v[48:51], a[28:31], v[4:7], v[48:51]\n\t"
"s_waitcnt lgkmcnt(4)\n\t"
"v_subrev_u32 v195, 5728, v195\n\t"
"v_lshrrev_b32 v196, 1, v186\n\t"
"v_mfma_f32_16x16x32_bf16 v[68:71], a[28:31], v[24:27], v[68:71]\n\t"
"v_cndmask_b32 v194, v194, v195, vcc\n\t"
"v_cmp_gt_u32 vcc, 45, v188\n\t"
"v_mfma_f32_16x16x32_bf16 v[48:51], a[32:35], v[8:11], v[48:51]\n\t"
"ds_read_b64_tr_b16 a[48:49], v213 offset:32\n\t"
"ds_read_b64_tr_b16 a[50:51], v214 offset:32\n\t"
"ds_read_b64_tr_b16 a[52:53], v215 offset:32\n\t"
"ds_read_b64_tr_b16 a[54:55], v216 offset:32\n\t"
"v_mul_u32_u24 v196, 1440, v196\n\t"
"v_add3_u32 v187, v194, v196, s64\n\t"
"v_mfma_f32_16x16x32_bf16 v[68:71], a[32:35], v[28:31], v[68:71]\n\t"
"v_lshl_add_u32 v191, v186, 3, s65\n\t"
"v_lshlrev_b32 v185, 4, v185\n\t"
"v_mfma_f32_16x16x32_bf16 v[52:55], a[36:39], v[0:3], 0\n\t"
"s_waitcnt lgkmcnt(4)\n\t"
"v_cndmask_b32 v225, v184, v185, vcc\n\t"
"v_cndmask_b32 v246, v191, v187, vcc\n\t"
"v_mfma_f32_16x16x32_bf16 v[72:75], a[36:39], v[20:23], 0\n\t"
"v_add_u32 v185, 256, v190\n\t"
"v_mul_u32_u24 v186, 1366, v185\n\t"
"v_mfma_f32_16x16x32_bf16 v[52:55], a[40:43], v[4:7], v[52:55]\n\t"
"ds_read_b64_tr_b16 a[56:57], v217 offset:32\n\t"
"ds_read_b64_tr_b16 a[58:59], v218 offset:32\n\t"
"ds_read_b64_tr_b16 a[60:61], v219 offset:0\n\t"
"ds_read_b64_tr_b16 a[62:63], v220 offset:0\n\t"
"v_lshrrev_b32 v186, 16, v186\n\t"
"v_mul_u32_u24 v187, 48, v186\n\t"
"v_sub_u32 v188, v185, v187\n\t"
"v_mfma_f32_16x16x32_bf16 v[72:75], a[40:43], v[24:27], v[72:75]\n\t"
"v_and_b32 v193, 1, v186\n\t"
"v_mul_u32_u24 v193, 45, v193\n\t"
"v_mfma_f32_16x16x32_bf16 v[52:55], a[44:47], v[8:11], v[52:55]\n\t"
"s_waitcnt lgkmcnt(4)\n\t"
"v_add_lshl_u32 v193, v193, v188, 1\n\t"
"v_lshrrev_b32 v194, 6, v193\n\t"
"v_mfma_f32_16x16x32_bf16 v[72:75], a[44:47], v[28:31], v[72:75]\n\t"
"v_and_b32 v195, 15, v193\n\t"
"v_lshl_add_u32 v194, v194, 4, v195\n\t"
"v_mfma_f32_16x16x32_bf16 v[56:59], a[48:51], v[0:3], 0\n\t"
"ds_read_b64_tr_b16 a[0:1], v221 offset:0\n\t"
"ds_read_b64_tr_b16 a[2:3], v222 offset:0\n\t"
"ds_read_b64_tr_b16 a[4:5], v223 offset:0\n\t"
"ds_read_b64_tr_b16 a[6:7], v224 offset:0\n\t"
"v_mul_u32_u24 v194, 40, v194\n\t"
"v_bfe_u32 v196, v193, 4, 2\n\t"
"v_mfma_f32_16x16x32_bf16 v[76:79], a[48:51], v[20:23], 0\n\t"
"v_lshl_add_u32 v194, v196, 3, v194\n\t"
"v_and_b32 v196, 3, v193\n\t"
"v_mfma_f32_16x16x32_bf16 v[56:59], a[52:55], v[4:7], v[56:59]\n\t"
"s_waitcnt lgkmcnt(4)\n\t"
"v_mul_u32_u24 v196, 40, v196\n\t"
"v_lshrrev_b32 v195, 2, v195\n\t"
"v_lshl_add_u32 v196, v195, 3, v196\n\t"
"v_mfma_f32_16x16x32_bf16 v[76:79], a[52:55], v[24:27], v[76:79]\n\t"
"v_cmp_lt_u32 vcc, 127, v193\n\t"
"v_add_u32 v196, 1280, v196\n\t"
"v_mfma_f32_16x16x32_bf16 v[56:59], a[56:59], v[8:11], v[56:59]\n\t"
"ds_read_b64_tr_b16 a[8:9], v219 offset:8\n\t"
"ds_read_b64_tr_b16 a[10:11], v220 offset:8\n\t"
"ds_read_b64_tr_b16 a[12:13], v221 offset:8\n\t"
"ds_read_b64_tr_b16 a[14:15], v222 offset:8\n\t"
"v_mul_u32_u24 v195, 40, v193\n\t"
"v_cndmask_b32 v194, v194, v196, vcc\n\t"
"v_mfma_f32_16x16x32_bf16 v[76:79], a[56:59], v[28:31], v[76:79]\n\t"
"v_cmp_lt_u32 vcc, 143, v193\n\t"
"v_subrev_u32 v195, 5728, v195\n\t"
"v_mfma_f32_16x16x32_bf16 v[80:83], a[60:63], v[8:11], 0\n\t"
"s_waitcnt lgkmcnt(4)\n\t"
"v_lshrrev_b32 v196, 1, v186\n\t"
"v_cndmask_b32 v194, v194, v195, vcc\n\t"
"v_mfma_f32_16x16x32_bf16 v[100:103], a[60:63], v[28:31], 0\n\t"
"v_cmp_gt_u32 vcc, 45, v188\n\t"
"v_mul_u32_u24 v196, 1440, v196\n\t"
"v_mfma_f32_16x16x32_bf16 v[80:83], a[0:3], v[12:15], v[80:83]\n\t"
"ds_read_b64_tr_b16 a[16:17], v223 offset:8\n\t"
"ds_read_b64_tr_b16 a[18:19], v224 offset:8\n\t"
"ds_read_b64_tr_b16 a[20:21], v219 offset:16\n\t"
"ds_read_b64_tr_b16 a[22:23], v220 offset:16\n\t"
"v_add3_u32 v187, v194, v196, s64\n\t"
"v_lshl_add_u32 v191, v186, 3, s65\n\t"
"v_mfma_f32_16x16x32_bf16 v[100:103], a[0:3], v[32:35], v[100:103]\n\t"
"v_lshlrev_b32 v185, 4, v185\n\t"
"v_cndmask_b32 v226, v184, v185, vcc\n\t"
"v_cndmask_b32 v247, v191, v187, vcc\n\t"
"v_mfma_f32_16x16x32_bf16 v[80:83], a[4:7], v[16:19], v[80:83]\n\t"
"s_waitcnt lgkmcnt(4)\n\t"
"v_add_u32 v185, 512, v190\n\t"
"v_mul_u32_u24 v186, 1366, v185\n\t"
"v_mfma_f32_16x16x32_bf16 v[100:103], a[4:7], v[36:39], v[100:103]\n\t"
"v_lshrrev_b32 v186, 16, v186\n\t"
"v_mul_u32_u24 v187, 48, v186\n\t"
"v_mfma_f32_16x16x32_bf16 v[84:87], a[8:11], v[8:11], 0\n\t"
"ds_read_b64_tr_b16 a[24:25], v221 offset:16\n\t"
"ds_read_b64_tr_b16 a[26:27], v222 offset:16\n\t"
"ds_read_b64_tr_b16 a[28:29], v223 offset:16\n\t"
"ds_read_b64_tr_b16 a[30:31], v224 offset:16\n\t"
"v_sub_u32 v188, v185, v187\n\t"
"v_and_b32 v193, 1, v186\n\t"
"v_mfma_f32_16x16x32_bf16 v[104:107], a[8:11], v[28:31], 0\n\t"
"v_mul_u32_u24 v193, 45, v193\n\t"
"v_add_lshl_u32 v193, v193, v188, 1\n\t"
"v_mfma_f32_16x16x32_bf16 v[84:87], a[12:15], v[12:15], v[84:87]\n\t"
"s_waitcnt lgkmcnt(4)\n\t"
"v_lshrrev_b32 v194, 6, v193\n\t"
"v_and_b32 v195, 15, v193\n\t"
"v_mfma_f32_16x16x32_bf16 v[104:107], a[12:15], v[32:35], v[104:107]\n\t"
"v_lshl_add_u32 v194, v194, 4, v195\n\t"
"v_mul_u32_u24 v194, 40, v194\n\t"
"v_bfe_u32 v196, v193, 4, 2\n\t"
"v_mfma_f32_16x16x32_bf16 v[84:87], a[16:19], v[16:19], v[84:87]\n\t"
"ds_read_b64_tr_b16 a[32:33], v219 offset:24\n\t"
"ds_read_b64_tr_b16 a[34:35], v220 offset:24\n\t"
"ds_read_b64_tr_b16 a[36:37], v221 offset:24\n\t"
"ds_read_b64_tr_b16 a[38:39], v222 offset:24\n\t"
"v_lshl_add_u32 v194, v196, 3, v194\n\t"
"v_and_b32 v196, 3, v193\n\t"
"v_mfma_f32_16x16x32_bf16 v[104:107], a[16:19], v[36:39], v[104:107]\n\t"
"v_mul_u32_u24 v196, 40, v196\n\t"
"v_lshrrev_b32 v195, 2, v195\n\t"
"v_mfma_f32_16x16x32_bf16 v[88:91], a[20:23], v[8:11], 0\n\t"
"s_waitcnt lgkmcnt(4)\n\t"
"v_lshl_add_u32 v196, v195, 3, v196\n\t"
"v_cmp_lt_u32 vcc, 127, v193\n\t"
"v_mfma_f32_16x16x32_bf16 v[108:111], a[20:23], v[28:31], 0\n\t"
"v_add_u32 v196, 1280, v196\n\t"
"v_mul_u32_u24 v195, 40, v193\n\t"
"v_mfma_f32_16x16x32_bf16 v[88:91], a[24:27], v[12:15], v[88:91]\n\t"
"ds_read_b64_tr_b16 a[40:41], v223 offset:24\n\t"
"ds_read_b64_tr_b16 a[42:43], v224 offset:24\n\t"
"ds_read_b64_tr_b16 a[44:45], v219 offset:32\n\t"
"ds_read_b64_tr_b16 a[46:47], v220 offset:32\n\t"
"v_cndmask_b32 v194, v194, v196, vcc\n\t"
"v_cmp_lt_u32 vcc, 143, v193\n\t"
"v_mfma_f32_16x16x32_bf16 v[108:111], a[24:27], v[32:35], v[108:111]\n\t"
"v_subrev_u32 v195, 5728, v195\n\t"
"v_lshrrev_b32 v196, 1, v186\n\t"
"v_mfma_f32_16x16x32_bf16 v[88:91], a[28:31], v[16:19], v[88:91]\n\t"
"v_cndmask_b32 v194, v194, v195, vcc\n\t"
"v_cmp_gt_u32 vcc, 45, v188\n\t"
"v_mul_u32_u24 v196, 1440, v196\n\t"
"v_mfma_f32_16x16x32_bf16 v[108:111], a[28:31], v[36:39], v[108:111]\n\t"
"s_waitcnt lgkmcnt(4)\n\t"
"v_add3_u32 v187, v194, v196, s64\n\t"
"v_lshl_add_u32 v191, v186, 3, s65\n\t"
"v_mfma_f32_16x16x32_bf16 v[92:95], a[32:35], v[8:11], 0\n\t"
"ds_read_b64_tr_b16 a[48:49], v221 offset:32\n\t"
"ds_read_b64_tr_b16 a[50:51], v222 offset:32\n\t"
"ds_read_b64_tr_b16 a[52:53], v223 offset:32\n\t"
"ds_read_b64_tr_b16 a[54:55], v224 offset:32\n\t"
"v_lshlrev_b32 v185, 4, v185\n\t"
"v_cndmask_b32 v227, v184, v185, vcc\n\t"
"v_mfma_f32_16x16x32_bf16 v[112:115], a[32:35], v[28:31], 0\n\t"
"s_lshl_b32 s65, %[wv], 9\n\t"
"v_cndmask_b32 v248, v191, v187, vcc\n\t"
"v_mfma_f32_16x16x32_bf16 v[92:95], a[36:39], v[12:15], v[92:95]\n\t"
"s_add_u32 s65, s65, s64\n\t"
"v_add_u32 v249, s65, v239\n\t"
"v_lshrrev_b32 v184, 4, v245\n\t"
"v_mfma_f32_16x16x32_bf16 v[112:115], a[36:39], v[32:35], v[112:115]\n\t"
"s_waitcnt lgkmcnt(4)\n\t"
"v_bfe_u32 v185, v245, 2, 2\n\t"
"v_and_b32 v186, 3, v245\n\t"
"v_mfma_f32_16x16x32_bf16 v[92:95], a[40:43], v[16:19], v[92:95]\n\t"
"v_cmp_eq_u32 vcc, v184, v186\n\t"
"v_mul_u32_u24 v184, 36, v184\n\t"
"v_mfma_f32_16x16x32_bf16 v[112:115], a[40:43], v[36:39], v[112:115]\n\t"
"v_add_u32 v184, v184, v185\n\t"
"v_mul_u32_u24 v184, 40, v184\n\t"
"v_mfma_f32_16x16x32_bf16 v[96:99], a[44:47], v[8:11], 0\n\t"
"s_add_u32 s65, s64, 12288\n\t"
"v_add_u32 v184, s64, v184\n\t"
"v_mfma_f32_16x16x32_bf16 v[116:119], a[44:47], v[28:31], 0\n\t"
"s_waitcnt lgkmcnt(0)\n\t"
"v_mov_b32 v185, s65\n\t"
"v_add_u32 v186, 5760, v184\n\t"
"v_mfma_f32_16x16x32_bf16 v[96:99], a[48:51], v[12:15], v[96:99]\n\t"
"v_cndmask_b32 v250, v185, v184, vcc\n\t"
"v_cndmask_b32 v251, v185, v186, vcc\n\t"
"v_mfma_f32_16x16x32_bf16 v[116:119], a[48:51], v[32:35], v[116:119]\n\t"
"v_cmp_eq_u32 vcc, 0, v245\n\t"
"v_mov_b32 v184, 2130706432\n\t"
"v_mfma_f32_16x16x32_bf16 v[96:99], a[52:55], v[16:19], v[96:99]\n\t"
"s_lshl_b32 s64, %[wv], 5\n\t"
"s_add_u32 s60, s60, s64\n\t"
"v_cndmask_b32 v231, v184, v245, vcc\n\t"
"v_mfma_f32_16x16x32_bf16 v[116:119], a[52:55], v[36:39], v[116:119]\n\t"
"v_max3_f32 v178, v40, v41, v42\n\t"
"v_min3_f32 v179, v40, v41, v42\n\t"
"v_max3_f32 v180, v43, v44, v45\n\t"
"v_min3_f32 v181, v43, v44, v45\n\t"
"v_max3_f32 v178, v178, v46, v47\n\t"
"v_min3_f32 v179, v179, v46, v47\n\t"
"v_max3_f32 v180, v180, v48, v49\n\t"
"v_min3_f32 v181, v181, v48, v49\n\t"
"v_max3_f32 v178, v178, v50, v51\n\t"
"v_min3_f32 v179, v179, v50, v51\n\t"
"v_max3_f32 v180, v180, v52, v53\n\t"
"v_min3_f32 v181, v181, v52, v53\n\t"
"v_max3_f32 v178, v178, v54, v55\n\t"
"v_min3_f32 v179, v179, v54, v55\n\t"
"v_max3_f32 v180, v180, v56, v57\n\t"
"v_min3_f32 v181, v181, v56, v57\n\t"
"v_max_f32 v178, v178, v180\n\t"
"v_min_f32 v179, v179, v181\n\t"
"v_pk_add_f32 v[160:161], v[178:179], v[40:41] op_sel:[1,0] op_sel_hi:[1,1] neg_lo:[0,1] neg_hi:[0,1]\n\t"
"v_pk_add_f32 v[40:41], v[40:41], v[178:179] op_sel:[0,0] op_sel_hi:[1,0] neg_lo:[0,1] neg_hi:[0,1]\n\t"
"v_exp_f32 v160, v160\n\t"
"v_exp_f32 v161, v161\n\t"
"v_pk_add_f32 v[162:163], v[178:179], v[42:43] op_sel:[1,0] op_sel_hi:[1,1] neg_lo:[0,1] neg_hi:[0,1]\n\t"
"v_exp_f32 v40, v40\n\t"
"v_exp_f32 v41, v41\n\t"
"v_pk_add_f32 v[42:43], v[42:43], v[178:179] op_sel:[0,0] op_sel_hi:[1,0] neg_lo:[0,1] neg_hi:[0,1]\n\t"
"v_exp_f32 v162, v162\n\t"
"v_exp_f32 v163, v163\n\t"
"v_pk_add_f32 v[164:165], v[178:179], v[44:45] op_sel:[1,0] op_sel_hi:[1,1] neg_lo:[0,1] neg_hi:[0,1]\n\t"
"v_exp_f32 v42, v42\n\t"
"v_exp_f32 v43, v43\n\t"
"v_pk_add_f32 v[44:45], v[44:45], v[178:179] op_sel:[0,0] op_sel_hi:[1,0] neg_lo:[0,1] neg_hi:[0,1]\n\t"
"v_exp_f32 v164, v164\n\t"
"v_exp_f32 v165, v165\n\t"
"v_pk_add_f32 v[182:183], v[160:161], v[162:163]\n\t"
"v_pk_add_f32 v[166:167], v[178:179], v[46:47] op_sel:[1,0] op_sel_hi:[1,1] neg_lo:[0,1] neg_hi:[0,1]\n\t"
"v_exp_f32 v44, v44\n\t"
"v_exp_f32 v45, v45\n\t"
"v_pk_add_f32 v[180:181], v[40:41], v[42:43]\n\t"
"v_pk_add_f32 v[46:47], v[46:47], v[178:179] op_sel:[0,0] op_sel_hi:[1,0] neg_lo:[0,1] neg_hi:[0,1]\n\t"
"v_exp_f32 v166, v166\n\t"
"v_exp_f32 v167, v167\n\t"
"v_pk_add_f32 v[182:183], v[182:183], v[164:165]\n\t"
"v_pk_add_f32 v[168:169], v[178:179], v[48:49] op_sel:[1,0] op_sel_hi:[1,1] neg_lo:[0,1] neg_hi:[0,1]\n\t"
"v_exp_f32 v46, v46\n\t"
"v_exp_f32 v47, v47\n\t"
"v_pk_add_f32 v[180:181], v[180:181], v[44:45]\n\t"
"v_pk_add_f32 v[48:49], v[48:49], v[178:179] op_sel:[0,0] op_sel_hi:[1,0] neg_lo:[0,1] neg_hi:[0,1]\n\t"
"v_exp_f32 v168, v168\n\t"
"v_exp_f32 v169, v169\n\t"
"v_pk_add_f32 v[182:183], v[182:183], v[166:167]\n\t"
"v_pk_add_f32 v[170:171], v[178:179], v[50:51] op_sel:[1,0] op_sel_hi:[1,1] neg_lo:[0,1] neg_hi:[0,1]\n\t"
"v_exp_f32 v48, v48\n\t"
"v_exp_f32 v49, v49\n\t"
"v_pk_add_f32 v[180:181], v[180:181], v[46:47]\n\t"
"v_pk_add_f32 v[50:51], v[50:51], v[178:179] op_sel:[0,0] op_sel_hi:[1,0] neg_lo:[0,1] neg_hi:[0,1]\n\t"
"v_exp_f32 v170, v170\n\t"
"v_exp_f32 v171, v171\n\t"
"v_pk_add_f32 v[182:183], v[182:183], v[168:169]\n\t"
"v_pk_add_f32 v[172:173], v[178:179], v[52:53] op_sel:[1,0] op_sel_hi:[1,1] neg_lo:[0,1] neg_hi:[0,1]\n\t"
"v_exp_f32 v50, v50\n\t"
"v_exp_f32 v51, v51\n\t"
"v_pk_add_f32 v[180:181], v[180:181], v[48:49]\n\t"
"v_pk_add_f32 v[52:53], v[52:53], v[178:179] op_sel:[0,0] op_sel_hi:[1,0] neg_lo:[0,1] neg_hi:[0,1]\n\t"
"v_exp_f32 v172, v172\n\t"
"v_exp_f32 v173, v173\n\t"
"v_pk_add_f32 v[182:183], v[182:183], v[170:171]\n\t"
"v_pk_add_f32 v[174:175], v[178:179], v[54:55] op_sel:[1,0] op_sel_hi:[1,1] neg_lo:[0,1] neg_hi:[0,1]\n\t"
"v_exp_f32 v52, v52\n\t"
"v_exp_f32 v53, v53\n\t"
"buffer_load_dwordx2 v[120:121], v240, %[ws], s78 offen sc1\n\t"
"s_add_u32 s65, s78, 23040\n\t"
"buffer_load_dwordx2 v[122:123], v240, %[ws], s65 offen sc1\n\t"
"s_add_u32 s65, s78, 46080\n\t"
"buffer_load_dwordx2 v[124:125], v240, %[ws], s65 offen sc1\n\t"
"s_add_u32 s65, s78, 69120\n\t"
"buffer_load_dwordx2 v[126:127], v240, %[ws], s65 offen sc1\n\t"
"s_add_u32 s65, s78, 92160\n\t"
"buffer_load_dwordx2 v[128:129], v240, %[ws], s65 offen sc1\n\t"
"s_add_u32 s65, s78, 115200\n\t"
"buffer_load_dwordx2 v[130:131], v240, %[ws], s65 offen sc1\n\t"
"s_add_u32 s65, s78, 138240\n\t"
"buffer_load_dwordx2 v[132:133], v240, %[ws], s65 offen sc1\n\t"
"s_add_u32 s65, s78, 161280\n\t"
"buffer_load_dwordx2 v[134:135], v240, %[ws], s65 offen sc1\n\t"
"s_add_u32 s65, s78, 184320\n\t"
"buffer_load_dwordx2 v[136:137], v240, %[ws], s65 offen sc1\n\t"
"s_add_u32 s65, s78, 207360\n\t"
"buffer_load_dwordx2 v[138:139], v240, %[ws], s65 offen sc1\n\t"
"s_add_u32 s65, s78, 230400\n\t"
"buffer_load_dwordx2 v[140:141], v240, %[ws], s65 offen sc1\n\t"
"s_add_u32 s65, s78, 253440\n\t"
"buffer_load_dwordx2 v[142:143], v240, %[ws], s65 offen sc1\n\t"
"s_add_u32 s65, s78, 276480\n\t"
"buffer_load_dwordx2 v[144:145], v240, %[ws], s65 offen sc1\n\t"
"s_add_u32 s65, s78, 299520\n\t"
"buffer_load_dwordx2 v[146:147], v240, %[ws], s65 offen sc1\n\t"
"s_add_u32 s65, s78, 322560\n\t"
"buffer_load_dwordx2 v[148:149], v240, %[ws], s65 offen sc1\n\t"
"s_add_u32 s65, s78, 345600\n\t"
"buffer_load_dwordx2 v[150:151], v240, %[ws], s65 offen sc1\n\t"
"v_pk_add_f32 v[180:181], v[180:181], v[50:51]\n\t"
"v_pk_add_f32 v[54:55], v[54:55], v[178:179] op_sel:[0,0] op_sel_hi:[1,0] neg_lo:[0,1] neg_hi:[0,1]\n\t"
"v_exp_f32 v174, v174\n\t"
"v_exp_f32 v175, v175\n\t"
"v_pk_add_f32 v[182:183], v[182:183], v[172:173]\n\t"
"v_pk_add_f32 v[176:177], v[178:179], v[56:57] op_sel:[1,0] op_sel_hi:[1,1] neg_lo:[0,1] neg_hi:[0,1]\n\t"
"v_exp_f32 v54, v54\n\t"
"v_exp_f32 v55, v55\n\t"
"v_pk_add_f32 v[180:181], v[180:181], v[52:53]\n\t"
"v_pk_add_f32 v[56:57], v[56:57], v[178:179] op_sel:[0,0] op_sel_hi:[1,0] neg_lo:[0,1] neg_hi:[0,1]\n\t"
"v_exp_f32 v176, v176\n\t"
"v_exp_f32 v177, v177\n\t"
"v_pk_add_f32 v[182:183], v[182:183], v[174:175]\n\t"
"v_exp_f32 v56, v56\n\t"
"v_exp_f32 v57, v57\n\t"
"v_pk_add_f32 v[180:181], v[180:181], v[54:55]\n\t"
"v_pk_add_f32 v[182:183], v[182:183], v[176:177]\n\t"
"v_pk_add_f32 v[180:181], v[180:181], v[56:57]\n\t"
"v_add_f32 v180, v180, v181\n\t"
"v_add_f32 v181, v182, v183\n\t"
"v_rcp_f32 v180, v180\n\t"
"v_rcp_f32 v181, v181\n\t"
"s_nop 0\n\t"
"v_pk_mul_f32 v[160:161], v[160:161], v[180:181] op_sel:[0,1] op_sel_hi:[1,1]\n\t"
"v_pk_mul_f32 v[162:163], v[162:163], v[180:181] op_sel:[0,1] op_sel_hi:[1,1]\n\t"
"v_pk_mul_f32 v[164:165], v[164:165], v[180:181] op_sel:[0,1] op_sel_hi:[1,1]\n\t"
"v_cvt_pk_bf16_f32 v0, v160, v161\n\t"
"v_pk_mul_f32 v[166:167], v[166:167], v[180:181] op_sel:[0,1] op_sel_hi:[1,1]\n\t"
"v_cvt_pk_bf16_f32 v1, v162, v163\n\t"
"v_pk_mul_f32 v[168:169], v[168:169], v[180:181] op_sel:[0,1] op_sel_hi:[1,1]\n\t"
"v_cvt_pk_bf16_f32 v2, v164, v165\n\t"
"v_pk_mul_f32 v[170:171], v[170:171], v[180:181] op_sel:[0,1] op_sel_hi:[1,1]\n\t"
"v_cvt_pk_bf16_f32 v3, v166, v167\n\t"
"v_pk_mul_f32 v[172:173], v[172:173], v[180:181] op_sel:[0,1] op_sel_hi:[1,1]\n\t"
"v_cvt_pk_bf16_f32 v4, v168, v169\n\t"
"v_pk_mul_f32 v[174:175], v[174:175], v[180:181] op_sel:[0,1] op_sel_hi:[1,1]\n\t"
"v_cvt_pk_bf16_f32 v5, v170, v171\n\t"
"v_pk_mul_f32 v[176:177], v[176:177], v[180:181] op_sel:[0,1] op_sel_hi:[1,1]\n\t"
"v_cvt_pk_bf16_f32 v6, v172, v173\n\t"
"v_pk_mul_f32 v[40:41], v[40:41], v[180:181] op_sel:[0,0] op_sel_hi:[1,0]\n\t"
"v_cvt_pk_bf16_f32 v7, v174, v175\n\t"
"v_pk_mul_f32 v[42:43], v[42:43], v[180:181] op_sel:[0,0] op_sel_hi:[1,0]\n\t"
"v_cvt_pk_bf16_f32 v8, v176, v177\n\t"
"v_pk_mul_f32 v[44:45], v[44:45], v[180:181] op_sel:[0,0] op_sel_hi:[1,0]\n\t"
"v_cvt_pk_bf16_f32 v9, v40, v41\n\t"
"v_pk_mul_f32 v[46:47], v[46:47], v[180:181] op_sel:[0,0] op_sel_hi:[1,0]\n\t"
"v_cvt_pk_bf16_f32 v10, v42, v43\n\t"
"v_pk_mul_f32 v[48:49], v[48:49], v[180:181] op_sel:[0,0] op_sel_hi:[1,0]\n\t"
"v_cvt_pk_bf16_f32 v11, v44, v45\n\t"
"v_pk_mul_f32 v[50:51], v[50:51], v[180:181] op_sel:[0,0] op_sel_hi:[1,0]\n\t"
"v_cvt_pk_bf16_f32 v12, v46, v47\n\t"
"v_pk_mul_f32 v[52:53], v[52:53], v[180:181] op_sel:[0,0] op_sel_hi:[1,0]\n\t"
"v_cvt_pk_bf16_f32 v13, v48, v49\n\t"
"v_pk_mul_f32 v[54:55], v[54:55], v[180:181] op_sel:[0,0] op_sel_hi:[1,0]\n\t"
"v_cvt_pk_bf16_f32 v14, v50, v51\n\t"
"v_pk_mul_f32 v[56:57], v[56:57], v[180:181] op_sel:[0,0] op_sel_hi:[1,0]\n\t"
"v_cvt_pk_bf16_f32 v15, v52, v53\n\t"
"v_cvt_pk_bf16_f32 v16, v54, v55\n\t"
"v_cvt_pk_bf16_f32 v17, v56, v57\n\t"
"v_max3_f32 v178, v60, v61, v62\n\t"
"v_min3_f32 v179, v60, v61, v62\n\t"
"v_max3_f32 v180, v63, v64, v65\n\t"
"v_min3_f32 v181, v63, v64, v65\n\t"
"v_max3_f32 v178, v178, v66, v67\n\t"
"v_min3_f32 v179, v179, v66, v67\n\t"
"v_max3_f32 v180, v180, v68, v69\n\t"
"v_min3_f32 v181, v181, v68, v69\n\t"
"v_max3_f32 v178, v178, v70, v71\n\t"
"v_min3_f32 v179, v179, v70, v71\n\t"
"v_max3_f32 v180, v180, v72, v73\n\t"
"v_min3_f32 v181, v181, v72, v73\n\t"
"v_max3_f32 v178, v178, v74, v75\n\t"
"v_min3_f32 v179, v179, v74, v75\n\t"
"v_max3_f32 v180, v180, v76, v77\n\t"
"v_min3_f32 v181, v181, v76, v77\n\t"
"v_max_f32 v178, v178, v180\n\t"
"v_min_f32 v179, v179, v181\n\t"
"v_pk_add_f32 v[160:161], v[178:179], v[60:61] op_sel:[1,0] op_sel_hi:[1,1] neg_lo:[0,1] neg_hi:[0,1]\n\t"
"v_pk_add_f32 v[60:61], v[60:61], v[178:179] op_sel:[0,0] op_sel_hi:[1,0] neg_lo:[0,1] neg_hi:[0,1]\n\t"
"v_exp_f32 v160, v160\n\t"
"v_exp_f32 v161, v161\n\t"
"v_pk_add_f32 v[162:163], v[178:179], v[62:63] op_sel:[1,0] op_sel_hi:[1,1] neg_lo:[0,1] neg_hi:[0,1]\n\t"
"v_exp_f32 v60, v60\n\t"
"v_exp_f32 v61, v61\n\t"
"v_pk_add_f32 v[62:63], v[62:63], v[178:179] op_sel:[0,0] op_sel_hi:[1,0] neg_lo:[0,1] neg_hi:[0,1]\n\t"
"v_exp_f32 v162, v162\n\t"
"v_exp_f32 v163, v163\n\t"
"v_pk_add_f32 v[164:165], v[178:179], v[64:65] op_sel:[1,0] op_sel_hi:[1,1] neg_lo:[0,1] neg_hi:[0,1]\n\t"
"v_exp_f32 v62, v62\n\t"
"v_exp_f32 v63, v63\n\t"
"v_pk_add_f32 v[64:65], v[64:65], v[178:179] op_sel:[0,0] op_sel_hi:[1,0] neg_lo:[0,1] neg_hi:[0,1]\n\t"
"v_exp_f32 v164, v164\n\t"
"v_exp_f32 v165, v165\n\t"
"v_pk_add_f32 v[182:183], v[160:161], v[162:163]\n\t"
"v_pk_add_f32 v[166:167], v[178:179], v[66:67] op_sel:[1,0] op_sel_hi:[1,1] neg_lo:[0,1] neg_hi:[0,1]\n\t"
"v_exp_f32 v64, v64\n\t"
"v_exp_f32 v65, v65\n\t"
"v_pk_add_f32 v[180:181], v[60:61], v[62:63]\n\t"
"v_pk_add_f32 v[66:67], v[66:67], v[178:179] op_sel:[0,0] op_sel_hi:[1,0] neg_lo:[0,1] neg_hi:[0,1]\n\t"
"v_exp_f32 v166, v166\n\t"
"v_exp_f32 v167, v167\n\t"
"v_pk_add_f32 v[182:183], v[182:183], v[164:165]\n\t"
"v_pk_add_f32 v[168:169], v[178:179], v[68:69] op_sel:[1,0] op_sel_hi:[1,1] neg_lo:[0,1] neg_hi:[0,1]\n\t"
"v_exp_f32 v66, v66\n\t"
"v_exp_f32 v67, v67\n\t"
"v_pk_add_f32 v[180:181], v[180:181], v[64:65]\n\t"
"v_pk_add_f32 v[68:69], v[68:69], v[178:179] op_sel:[0,0] op_sel_hi:[1,0] neg_lo:[0,1] neg_hi:[0,1]\n\t"
"v_exp_f32 v168, v168\n\t"
"v_exp_f32 v169, v169\n\t"
"v_pk_add_f32 v[182:183], v[182:183], v[166:167]\n\t"
"v_pk_add_f32 v[170:171], v[178:179], v[70:71] op_sel:[1,0] op_sel_hi:[1,1] neg_lo:[0,1] neg_hi:[0,1]\n\t"
"v_exp_f32 v68, v68\n\t"
"v_exp_f32 v69, v69\n\t"
"v_pk_add_f32 v[180:181], v[180:181], v[66:67]\n\t"
"v_pk_add_f32 v[70:71], v[70:71], v[178:179] op_sel:[0,0] op_sel_hi:[1,0] neg_lo:[0,1] neg_hi:[0,1]\n\t"
"v_exp_f32 v170, v170\n\t"
"v_exp_f32 v171, v171\n\t"
"v_pk_add_f32 v[182:183], v[182:183], v[168:169]\n\t"
"v_pk_add_f32 v[172:173], v[178:179], v[72:73] op_sel:[1,0] op_sel_hi:[1,1] neg_lo:[0,1] neg_hi:[0,1]\n\t"
"v_exp_f32 v70, v70\n\t"
"v_exp_f32 v71, v71\n\t"
"v_pk_add_f32 v[180:181], v[180:181], v[68:69]\n\t"
"v_pk_add_f32 v[72:73], v[72:73], v[178:179] op_sel:[0,0] op_sel_hi:[1,0] neg_lo:[0,1] neg_hi:[0,1]\n\t"
"v_exp_f32 v172, v172\n\t"
"v_exp_f32 v173, v173\n\t"
"v_pk_add_f32 v[182:183], v[182:183], v[170:171]\n\t"
"v_pk_add_f32 v[174:175], v[178:179], v[74:75] op_sel:[1,0] op_sel_hi:[1,1] neg_lo:[0,1] neg_hi:[0,1]\n\t"
"v_exp_f32 v72, v72\n\t"
"v_exp_f32 v73, v73\n\t"
"v_pk_add_f32 v[180:181], v[180:181], v[70:71]\n\t"
"v_pk_add_f32 v[74:75], v[74:75], v[178:179] op_sel:[0,0] op_sel_hi:[1,0] neg_lo:[0,1] neg_hi:[0,1]\n\t"
"v_exp_f32 v174, v174\n\t"
"v_exp_f32 v175, v175\n\t"
"v_pk_add_f32 v[182:183], v[182:183], v[172:173]\n\t"
"v_pk_add_f32 v[176:177], v[178:179], v[76:77] op_sel:[1,0] op_sel_hi:[1,1] neg_lo:[0,1] neg_hi:[0,1]\n\t"
"v_exp_f32 v74, v74\n\t"
"v_exp_f32 v75, v75\n\t"
"v_pk_add_f32 v[180:181], v[180:181], v[72:73]\n\t"
"v_pk_add_f32 v[76:77], v[76:77], v[178:179] op_sel:[0,0] op_sel_hi:[1,0] neg_lo:[0,1] neg_hi:[0,1]\n\t"
"v_exp_f32 v176, v176\n\t"
"v_exp_f32 v177, v177\n\t"
"v_pk_add_f32 v[182:183], v[182:183], v[174:175]\n\t"
"v_exp_f32 v76, v76\n\t"
"v_exp_f32 v77, v77\n\t"
"v_pk_add_f32 v[180:181], v[180:181], v[74:75]\n\t"
"v_pk_add_f32 v[182:183], v[182:183], v[176:177]\n\t"
"v_pk_add_f32 v[180:181], v[180:181], v[76:77]\n\t"
"v_add_f32 v180, v180, v181\n\t"
"v_add_f32 v181, v182, v183\n\t"
"v_rcp_f32 v180, v180\n\t"
"v_rcp_f32 v181, v181\n\t"
"s_nop 0\n\t"
"v_pk_mul_f32 v[160:161], v[160:161], v[180:181] op_sel:[0,1] op_sel_hi:[1,1]\n\t"
"v_pk_mul_f32 v[162:163], v[162:163], v[180:181] op_sel:[0,1] op_sel_hi:[1,1]\n\t"
"v_pk_mul_f32 v[164:165], v[164:165], v[180:181] op_sel:[0,1] op_sel_hi:[1,1]\n\t"
"v_cvt_pk_bf16_f32 v20, v160, v161\n\t"
"v_pk_mul_f32 v[166:167], v[166:167], v[180:181] op_sel:[0,1] op_sel_hi:[1,1]\n\t"
"v_cvt_pk_bf16_f32 v21, v162, v163\n\t"
"v_pk_mul_f32 v[168:169], v[168:169], v[180:181] op_sel:[0,1] op_sel_hi:[1,1]\n\t"
"v_cvt_pk_bf16_f32 v22, v164, v165\n\t"
"v_pk_mul_f32 v[170:171], v[170:171], v[180:181] op_sel:[0,1] op_sel_hi:[1,1]\n\t"
"v_cvt_pk_bf16_f32 v23, v166, v167\n\t"
"s_waitcnt vmcnt(14)\n\t"
"v_pk_add_f32 v[152:153], v[120:121], v[122:123]\n\t"
"s_waitcnt vmcnt(13)\n\t"
"v_pk_add_f32 v[152:153], v[152:153], v[124:125]\n\t"
"s_waitcnt vmcnt(12)\n\t"
"v_pk_add_f32 v[152:153], v[152:153], v[126:127]\n\t"
"s_waitcnt vmcnt(11)\n\t"
"v_pk_add_f32 v[152:153], v[152:153], v[128:129]\n\t"
"s_waitcnt vmcnt(10)\n\t"
"v_pk_add_f32 v[152:153], v[152:153], v[130:131]\n\t"
"s_waitcnt vmcnt(9)\n\t"
"v_pk_add_f32 v[152:153], v[152:153], v[132:133]\n\t"
"s_waitcnt vmcnt(8)\n\t"
"v_pk_add_f32 v[152:153], v[152:153], v[134:135]\n\t"
"s_waitcnt vmcnt(7)\n\t"
"v_pk_add_f32 v[152:153], v[152:153], v[136:137]\n\t"
"s_waitcnt vmcnt(6)\n\t"
"v_pk_add_f32 v[152:153], v[152:153], v[138:139]\n\t"
"s_waitcnt vmcnt(5)\n\t"
"v_pk_add_f32 v[152:153], v[152:153], v[140:141]\n\t"
"s_waitcnt vmcnt(4)\n\t"
"v_pk_add_f32 v[152:153], v[152:153], v[142:143]\n\t"
"s_waitcnt vmcnt(3)\n\t"
"v_pk_add_f32 v[152:153], v[152:153], v[144:145]\n\t"
"s_waitcnt vmcnt(2)\n\t"
"v_pk_add_f32 v[152:153], v[152:153], v[146:147]\n\t"
"s_waitcnt vmcnt(1)\n\t"
"v_pk_add_f32 v[152:153], v[152:153], v[148:149]\n\t"
"s_waitcnt vmcnt(0)\n\t"
"v_pk_add_f32 v[152:153], v[152:153], v[150:151]\n\t"
"v_cmp_u_f32 vcc, v152, v153\n\t"
"s_cmp_eq_u64 vcc, 0\n\t"
"s_cbranch_scc1 107f\n\t"
"s_mov_b32 s98, 65536\n\t"
"106:\n\t"
"s_sleep 3\n\t"
"s_nop 7\n\t"
"buffer_load_dwordx2 v[120:121], v240, %[ws], s78 offen sc1\n\t"
"s_add_u32 s65, s78, 23040\n\t"
"buffer_load_dwordx2 v[122:123], v240, %[ws], s65 offen sc1\n\t"
"s_add_u32 s65, s78, 46080\n\t"
"buffer_load_dwordx2 v[124:125], v240, %[ws], s65 offen sc1\n\t"
"s_add_u32 s65, s78, 69120\n\t"
"buffer_load_dwordx2 v[126:127], v240, %[ws], s65 offen sc1\n\t"
"s_add_u32 s65, s78, 92160\n\t"
"buffer_load_dwordx2 v[128:129], v240, %[ws], s65 offen sc1\n\t"
"s_add_u32 s65, s78, 115200\n\t"
"buffer_load_dwordx2 v[130:131], v240, %[ws], s65 offen sc1\n\t"
"s_add_u32 s65, s78, 138240\n\t"
"buffer_load_dwordx2 v[132:133], v240, %[ws], s65 offen sc1\n\t"
"s_add_u32 s65, s78, 161280\n\t"
"buffer_load_dwordx2 v[134:135], v240, %[ws], s65 offen sc1\n\t"
"s_add_u32 s65, s78, 184320\n\t"
"buffer_load_dwordx2 v[136:137], v240, %[ws], s65 offen sc1\n\t"
"s_add_u32 s65, s78, 207360\n\t"
"buffer_load_dwordx2 v[138:139], v240, %[ws], s65 offen sc1\n\t"
"s_add_u32 s65, s78, 230400\n\t"
"buffer_load_dwordx2 v[140:141], v240, %[ws], s65 offen sc1\n\t"
"s_add_u32 s65, s78, 253440\n\t"
"buffer_load_dwordx2 v[142:143], v240, %[ws], s65 offen sc1\n\t"
"s_add_u32 s65, s78, 276480\n\t"
"buffer_load_dwordx2 v[144:145], v240, %[ws], s65 offen sc1\n\t"
"s_add_u32 s65, s78, 299520\n\t"
"buffer_load_dwordx2 v[146:147], v240, %[ws], s65 offen sc1\n\t"
"s_add_u32 s65, s78, 322560\n\t"
"buffer_load_dwordx2 v[148:149], v240, %[ws], s65 offen sc1\n\t"
"s_add_u32 s65, s78, 345600\n\t"
"buffer_load_dwordx2 v[150:151], v240, %[ws], s65 offen sc1\n\t"
"s_waitcnt vmcnt(0)\n\t"
"v_pk_add_f32 v[152:153], v[120:121], v[122:123]\n\t"
"v_pk_add_f32 v[152:153], v[152:153], v[124:125]\n\t"
"v_pk_add_f32 v[152:153], v[152:153], v[126:127]\n\t"
"v_pk_add_f32 v[152:153], v[152:153], v[128:129]\n\t"
"v_pk_add_f32 v[152:153], v[152:153], v[130:131]\n\t"
"v_pk_add_f32 v[152:153], v[152:153], v[132:133]\n\t"
"v_pk_add_f32 v[152:153], v[152:153], v[134:135]\n\t"
"v_pk_add_f32 v[152:153], v[152:153], v[136:137]\n\t"
"v_pk_add_f32 v[152:153], v[152:153], v[138:139]\n\t"
"v_pk_add_f32 v[152:153], v[152:153], v[140:141]\n\t"
"v_pk_add_f32 v[152:153], v[152:153], v[142:143]\n\t"
"v_pk_add_f32 v[152:153], v[152:153], v[144:145]\n\t"
"v_pk_add_f32 v[152:153], v[152:153], v[146:147]\n\t"
"v_pk_add_f32 v[152:153], v[152:153], v[148:149]\n\t"
"v_pk_add_f32 v[152:153], v[152:153], v[150:151]\n\t"
"v_cmp_u_f32 vcc, v152, v153\n\t"
"s_cmp_eq_u64 vcc, 0\n\t"
"s_cbranch_scc1 107f\n\t"
"s_sub_u32 s98, s98, 1\n\t"
"s_cmp_lg_u32 s98, 0\n\t"
"s_cbranch_scc1 106b\n\t"
"107:\n\t"
"s_nop 1\n\t"
"v_cvt_pk_bf16_f32 v192, v152, v153\n\t"
"buffer_store_dword v192, v241, %[ws], s57 offen sc1\n\t"
"v_mov_b32 v184, 0\n\t"
"v_mov_b32 v185, 0\n\t"
"ds_write_b64 v249, v[184:185] offset:12288\n\t"
"v_pk_mul_f32 v[172:173], v[172:173], v[180:181] op_sel:[0,1] op_sel_hi:[1,1]\n\t"
"v_cvt_pk_bf16_f32 v24, v168, v169\n\t"
"v_pk_mul_f32 v[174:175], v[174:175], v[180:181] op_sel:[0,1] op_sel_hi:[1,1]\n\t"
"v_cvt_pk_bf16_f32 v25, v170, v171\n\t"
"v_pk_mul_f32 v[176:177], v[176:177], v[180:181] op_sel:[0,1] op_sel_hi:[1,1]\n\t"
"v_cvt_pk_bf16_f32 v26, v172, v173\n\t"
"v_pk_mul_f32 v[60:61], v[60:61], v[180:181] op_sel:[0,0] op_sel_hi:[1,0]\n\t"
"v_cvt_pk_bf16_f32 v27, v174, v175\n\t"
"v_pk_mul_f32 v[62:63], v[62:63], v[180:181] op_sel:[0,0] op_sel_hi:[1,0]\n\t"
"v_cvt_pk_bf16_f32 v28, v176, v177\n\t"
"v_pk_mul_f32 v[64:65], v[64:65], v[180:181] op_sel:[0,0] op_sel_hi:[1,0]\n\t"
"v_cvt_pk_bf16_f32 v29, v60, v61\n\t"
"v_pk_mul_f32 v[66:67], v[66:67], v[180:181] op_sel:[0,0] op_sel_hi:[1,0]\n\t"
"v_cvt_pk_bf16_f32 v30, v62, v63\n\t"
"v_pk_mul_f32 v[68:69], v[68:69], v[180:181] op_sel:[0,0] op_sel_hi:[1,0]\n\t"
"v_cvt_pk_bf16_f32 v31, v64, v65\n\t"
"v_pk_mul_f32 v[70:71], v[70:71], v[180:181] op_sel:[0,0] op_sel_hi:[1,0]\n\t"
"v_cvt_pk_bf16_f32 v32, v66, v67\n\t"
"v_pk_mul_f32 v[72:73], v[72:73], v[180:181] op_sel:[0,0] op_sel_hi:[1,0]\n\t"
"v_cvt_pk_bf16_f32 v33, v68, v69\n\t"
"v_pk_mul_f32 v[74:75], v[74:75], v[180:181] op_sel:[0,0] op_sel_hi:[1,0]\n\t"
"v_cvt_pk_bf16_f32 v34, v70, v71\n\t"
"v_pk_mul_f32 v[76:77], v[76:77], v[180:181] op_sel:[0,0] op_sel_hi:[1,0]\n\t"
"v_cvt_pk_bf16_f32 v35, v72, v73\n\t"
"v_cvt_pk_bf16_f32 v36, v74, v75\n\t"
"v_cvt_pk_bf16_f32 v37, v76, v77\n\t"
"v_max3_f32 v178, v80, v81, v82\n\t"
"v_min3_f32 v179, v80, v81, v82\n\t"
"v_max3_f32 v180, v83, v84, v85\n\t"
"v_min3_f32 v181, v83, v84, v85\n\t"
"v_max3_f32 v178, v178, v86, v87\n\t"
"v_min3_f32 v179, v179, v86, v87\n\t"
"v_max3_f32 v180, v180, v88, v89\n\t"
"v_min3_f32 v181, v181, v88, v89\n\t"
"v_max3_f32 v178, v178, v90, v91\n\t"
"v_min3_f32 v179, v179, v90, v91\n\t"
"v_max3_f32 v180, v180, v92, v93\n\t"
"v_min3_f32 v181, v181, v92, v93\n\t"
"v_max3_f32 v178, v178, v94, v95\n\t"
"v_min3_f32 v179, v179, v94, v95\n\t"
"v_max3_f32 v180, v180, v96, v97\n\t"
"v_min3_f32 v181, v181, v96, v97\n\t"
"v_max_f32 v178, v178, v180\n\t"
"v_min_f32 v179, v179, v181\n\t"
"v_pk_add_f32 v[160:161], v[178:179], v[80:81] op_sel:[1,0] op_sel_hi:[1,1] neg_lo:[0,1] neg_hi:[0,1]\n\t"
"v_pk_add_f32 v[80:81], v[80:81], v[178:179] op_sel:[0,0] op_sel_hi:[1,0] neg_lo:[0,1] neg_hi:[0,1]\n\t"
"v_exp_f32 v160, v160\n\t"
"v_exp_f32 v161, v161\n\t"
"v_pk_add_f32 v[162:163], v[178:179], v[82:83] op_sel:[1,0] op_sel_hi:[1,1] neg_lo:[0,1] neg_hi:[0,1]\n\t"
"v_exp_f32 v80, v80\n\t"
"v_exp_f32 v81, v81\n\t"
"v_pk_add_f32 v[82:83], v[82:83], v[178:179] op_sel:[0,0] op_sel_hi:[1,0] neg_lo:[0,1] neg_hi:[0,1]\n\t"
"v_exp_f32 v162, v162\n\t"
"v_exp_f32 v163, v163\n\t"
"v_pk_add_f32 v[164:165], v[178:179], v[84:85] op_sel:[1,0] op_sel_hi:[1,1] neg_lo:[0,1] neg_hi:[0,1]\n\t"
"v_exp_f32 v82, v82\n\t"
"v_exp_f32 v83, v83\n\t"
"v_pk_add_f32 v[84:85], v[84:85], v[178:179] op_sel:[0,0] op_sel_hi:[1,0] neg_lo:[0,1] neg_hi:[0,1]\n\t"
"v_exp_f32 v164, v164\n\t"
"v_exp_f32 v165, v165\n\t"
"v_pk_add_f32 v[182:183], v[160:161], v[162:163]\n\t"
"v_pk_add_f32 v[166:167], v[178:179], v[86:87] op_sel:[1,0] op_sel_hi:[1,1] neg_lo:[0,1] neg_hi:[0,1]\n\t"
"v_exp_f32 v84, v84\n\t"
"v_exp_f32 v85, v85\n\t"
"v_pk_add_f32 v[180:181], v[80:81], v[82:83]\n\t"
"v_pk_add_f32 v[86:87], v[86:87], v[178:179] op_sel:[0,0] op_sel_hi:[1,0] neg_lo:[0,1] neg_hi:[0,1]\n\t"
"v_exp_f32 v166, v166\n\t"
"v_exp_f32 v167, v167\n\t"
"v_pk_add_f32 v[182:183], v[182:183], v[164:165]\n\t"
"v_pk_add_f32 v[168:169], v[178:179], v[88:89] op_sel:[1,0] op_sel_hi:[1,1] neg_lo:[0,1] neg_hi:[0,1]\n\t"
"v_exp_f32 v86, v86\n\t"
"v_exp_f32 v87, v87\n\t"
"v_pk_add_f32 v[180:181], v[180:181], v[84:85]\n\t"
"v_pk_add_f32 v[88:89], v[88:89], v[178:179] op_sel:[0,0] op_sel_hi:[1,0] neg_lo:[0,1] neg_hi:[0,1]\n\t"
"v_exp_f32 v168, v168\n\t"
"v_exp_f32 v169, v169\n\t"
"v_pk_add_f32 v[182:183], v[182:183], v[166:167]\n\t"
"v_pk_add_f32 v[170:171], v[178:179], v[90:91] op_sel:[1,0] op_sel_hi:[1,1] neg_lo:[0,1] neg_hi:[0,1]\n\t"
"v_exp_f32 v88, v88\n\t"
"v_exp_f32 v89, v89\n\t"
"v_pk_add_f32 v[180:181], v[180:181], v[86:87]\n\t"
"v_pk_add_f32 v[90:91], v[90:91], v[178:179] op_sel:[0,0] op_sel_hi:[1,0] neg_lo:[0,1] neg_hi:[0,1]\n\t"
"v_exp_f32 v170, v170\n\t"
"v_exp_f32 v171, v171\n\t"
"v_pk_add_f32 v[182:183], v[182:183], v[168:169]\n\t"
"v_pk_add_f32 v[172:173], v[178:179], v[92:93] op_sel:[1,0] op_sel_hi:[1,1] neg_lo:[0,1] neg_hi:[0,1]\n\t"
"v_exp_f32 v90, v90\n\t"
"v_exp_f32 v91, v91\n\t"
"v_pk_add_f32 v[180:181], v[180:181], v[88:89]\n\t"
"v_pk_add_f32 v[92:93], v[92:93], v[178:179] op_sel:[0,0] op_sel_hi:[1,0] neg_lo:[0,1] neg_hi:[0,1]\n\t"
"v_exp_f32 v172, v172\n\t"
"v_exp_f32 v173, v173\n\t"
"v_pk_add_f32 v[182:183], v[182:183], v[170:171]\n\t"
"v_pk_add_f32 v[174:175], v[178:179], v[94:95] op_sel:[1,0] op_sel_hi:[1,1] neg_lo:[0,1] neg_hi:[0,1]\n\t"
"v_exp_f32 v92, v92\n\t"
"v_exp_f32 v93, v93\n\t"
"v_pk_add_f32 v[180:181], v[180:181], v[90:91]\n\t"
"v_pk_add_f32 v[94:95], v[94:95], v[178:179] op_sel:[0,0] op_sel_hi:[1,0] neg_lo:[0,1] neg_hi:[0,1]\n\t"
"v_exp_f32 v174, v174\n\t"
"v_exp_f32 v175, v175\n\t"
"v_pk_add_f32 v[182:183], v[182:183], v[172:173]\n\t"
"v_pk_add_f32 v[176:177], v[178:179], v[96:97] op_sel:[1,0] op_sel_hi:[1,1] neg_lo:[0,1] neg_hi:[0,1]\n\t"
"v_exp_f32 v94, v94\n\t"
"v_exp_f32 v95, v95\n\t"
"v_pk_add_f32 v[180:181], v[180:181], v[92:93]\n\t"
"v_pk_add_f32 v[96:97], v[96:97], v[178:179] op_sel:[0,0] op_sel_hi:[1,0] neg_lo:[0,1] neg_hi:[0,1]\n\t"
"v_exp_f32 v176, v176\n\t"
"v_exp_f32 v177, v177\n\t"
"v_pk_add_f32 v[182:183], v[182:183], v[174:175]\n\t"
"v_exp_f32 v96, v96\n\t"
"v_exp_f32 v97, v97\n\t"
"v_pk_add_f32 v[180:181], v[180:181], v[94:95]\n\t"
"v_pk_add_f32 v[182:183], v[182:183], v[176:177]\n\t"
"v_pk_add_f32 v[180:181], v[180:181], v[96:97]\n\t"
"v_add_f32 v180, v180, v181\n\t"
"v_add_f32 v181, v182, v183\n\t"
"v_rcp_f32 v180, v180\n\t"
"v_rcp_f32 v181, v181\n\t"
"s_nop 0\n\t"
"v_pk_mul_f32 v[160:161], v[160:161], v[180:181] op_sel:[0,1] op_sel_hi:[1,1]\n\t"
"v_pk_mul_f32 v[162:163], v[162:163], v[180:181] op_sel:[0,1] op_sel_hi:[1,1]\n\t"
"v_pk_mul_f32 v[164:165], v[164:165], v[180:181] op_sel:[0,1] op_sel_hi:[1,1]\n\t"
"v_cvt_pk_bf16_f32 v120, v160, v161\n\t"
"v_pk_mul_f32 v[166:167], v[166:167], v[180:181] op_sel:[0,1] op_sel_hi:[1,1]\n\t"
"v_cvt_pk_bf16_f32 v121, v162, v163\n\t"
"v_pk_mul_f32 v[168:169], v[168:169], v[180:181] op_sel:[0,1] op_sel_hi:[1,1]\n\t"
"v_cvt_pk_bf16_f32 v122, v164, v165\n\t"
"v_pk_mul_f32 v[170:171], v[170:171], v[180:181] op_sel:[0,1] op_sel_hi:[1,1]\n\t"
"v_cvt_pk_bf16_f32 v123, v166, v167\n\t"
"v_pk_mul_f32 v[172:173], v[172:173], v[180:181] op_sel:[0,1] op_sel_hi:[1,1]\n\t"
"v_cvt_pk_bf16_f32 v124, v168, v169\n\t"
"v_pk_mul_f32 v[174:175], v[174:175], v[180:181] op_sel:[0,1] op_sel_hi:[1,1]\n\t"
"v_cvt_pk_bf16_f32 v125, v170, v171\n\t"
"v_pk_mul_f32 v[176:177], v[176:177], v[180:181] op_sel:[0,1] op_sel_hi:[1,1]\n\t"
"v_cvt_pk_bf16_f32 v126, v172, v173\n\t"
"v_pk_mul_f32 v[80:81], v[80:81], v[180:181] op_sel:[0,0] op_sel_hi:[1,0]\n\t"
"v_cvt_pk_bf16_f32 v127, v174, v175\n\t"
"v_pk_mul_f32 v[82:83], v[82:83], v[180:181] op_sel:[0,0] op_sel_hi:[1,0]\n\t"
"v_cvt_pk_bf16_f32 v128, v176, v177\n\t"
"v_pk_mul_f32 v[84:85], v[84:85], v[180:181] op_sel:[0,0] op_sel_hi:[1,0]\n\t"
"v_cvt_pk_bf16_f32 v129, v80, v81\n\t"
"v_pk_mul_f32 v[86:87], v[86:87], v[180:181] op_sel:[0,0] op_sel_hi:[1,0]\n\t"
"v_cvt_pk_bf16_f32 v130, v82, v83\n\t"
"v_pk_mul_f32 v[88:89], v[88:89], v[180:181] op_sel:[0,0] op_sel_hi:[1,0]\n\t"
"v_cvt_pk_bf16_f32 v131, v84, v85\n\t"
"v_pk_mul_f32 v[90:91], v[90:91], v[180:181] op_sel:[0,0] op_sel_hi:[1,0]\n\t"
"v_cvt_pk_bf16_f32 v132, v86, v87\n\t"
"v_pk_mul_f32 v[92:93], v[92:93], v[180:181] op_sel:[0,0] op_sel_hi:[1,0]\n\t"
"v_cvt_pk_bf16_f32 v133, v88, v89\n\t"
"v_pk_mul_f32 v[94:95], v[94:95], v[180:181] op_sel:[0,0] op_sel_hi:[1,0]\n\t"
"v_cvt_pk_bf16_f32 v134, v90, v91\n\t"
"v_pk_mul_f32 v[96:97], v[96:97], v[180:181] op_sel:[0,0] op_sel_hi:[1,0]\n\t"
"v_cvt_pk_bf16_f32 v135, v92, v93\n\t"
"v_cvt_pk_bf16_f32 v136, v94, v95\n\t"
"v_cvt_pk_bf16_f32 v137, v96, v97\n\t"
"v_max3_f32 v178, v100, v101, v102\n\t"
"v_min3_f32 v179, v100, v101, v102\n\t"
"v_max3_f32 v180, v103, v104, v105\n\t"
"v_min3_f32 v181, v103, v104, v105\n\t"
"v_max3_f32 v178, v178, v106, v107\n\t"
"v_min3_f32 v179, v179, v106, v107\n\t"
"v_max3_f32 v180, v180, v108, v109\n\t"
"v_min3_f32 v181, v181, v108, v109\n\t"
"v_max3_f32 v178, v178, v110, v111\n\t"
"v_min3_f32 v179, v179, v110, v111\n\t"
"v_max3_f32 v180, v180, v112, v113\n\t"
"v_min3_f32 v181, v181, v112, v113\n\t"
"v_max3_f32 v178, v178, v114, v115\n\t"
"v_min3_f32 v179, v179, v114, v115\n\t"
"v_max3_f32 v180, v180, v116, v117\n\t"
"v_min3_f32 v181, v181, v116, v117\n\t"
"v_max_f32 v178, v178, v180\n\t"
"v_min_f32 v179, v179, v181\n\t"
"v_pk_add_f32 v[160:161], v[178:179], v[100:101] op_sel:[1,0] op_sel_hi:[1,1] neg_lo:[0,1] neg_hi:[0,1]\n\t"
"v_pk_add_f32 v[100:101], v[100:101], v[178:179] op_sel:[0,0] op_sel_hi:[1,0] neg_lo:[0,1] neg_hi:[0,1]\n\t"
"v_exp_f32 v160, v160\n\t"
"v_exp_f32 v161, v161\n\t"
"v_pk_add_f32 v[162:163], v[178:179], v[102:103] op_sel:[1,0] op_sel_hi:[1,1] neg_lo:[0,1] neg_hi:[0,1]\n\t"
"v_exp_f32 v100, v100\n\t"
"v_exp_f32 v101, v101\n\t"
"v_pk_add_f32 v[102:103], v[102:103], v[178:179] op_sel:[0,0] op_sel_hi:[1,0] neg_lo:[0,1] neg_hi:[0,1]\n\t"
"v_exp_f32 v162, v162\n\t"
"v_exp_f32 v163, v163\n\t"
"v_pk_add_f32 v[164:165], v[178:179], v[104:105] op_sel:[1,0] op_sel_hi:[1,1] neg_lo:[0,1] neg_hi:[0,1]\n\t"
"v_exp_f32 v102, v102\n\t"
"v_exp_f32 v103, v103\n\t"
"v_pk_add_f32 v[104:105], v[104:105], v[178:179] op_sel:[0,0] op_sel_hi:[1,0] neg_lo:[0,1] neg_hi:[0,1]\n\t"
"v_exp_f32 v164, v164\n\t"
"v_exp_f32 v165, v165\n\t"
"v_pk_add_f32 v[182:183], v[160:161], v[162:163]\n\t"
"v_pk_add_f32 v[166:167], v[178:179], v[106:107] op_sel:[1,0] op_sel_hi:[1,1] neg_lo:[0,1] neg_hi:[0,1]\n\t"
"v_exp_f32 v104, v104\n\t"
"v_exp_f32 v105, v105\n\t"
"v_pk_add_f32 v[180:181], v[100:101], v[102:103]\n\t"
"v_pk_add_f32 v[106:107], v[106:107], v[178:179] op_sel:[0,0] op_sel_hi:[1,0] neg_lo:[0,1] neg_hi:[0,1]\n\t"
"buffer_load_dwordx4 v[184:187], v225, %[ws], s61 offen sc1\n\t"
"buffer_load_dwordx4 v[188:191], v226, %[ws], s61 offen sc1\n\t"
"buffer_load_dwordx4 v[192:195], v227, %[ws], s61 offen sc1\n\t"
"s_getpc_b64 s[96:97]\n\t"
"74:\n\t"
"s_and_b32 s98, %[mi], 3\n\t"
"s_lshl_b32 s98, s98, 2\n\t"
"s_add_u32 s98, s98, %[wv]\n\t"
"s_lshl_b32 s98, s98, 10\n\t"
"v_add_u32 v228, s98, v230\n\t"
"v_min_u32 v228, 97f-74b, v228\n\t"
"global_load_dword v228, v228, s[96:97]\n\t"
"v_exp_f32 v166, v166\n\t"
"v_exp_f32 v167, v167\n\t"
"v_pk_add_f32 v[182:183], v[182:183], v[164:165]\n\t"
"v_pk_add_f32 v[168:169], v[178:179], v[108:109] op_sel:[1,0] op_sel_hi:[1,1] neg_lo:[0,1] neg_hi:[0,1]\n\t"
"v_exp_f32 v106, v106\n\t"
"v_exp_f32 v107, v107\n\t"
"v_pk_add_f32 v[180:181], v[180:181], v[104:105]\n\t"
"v_pk_add_f32 v[108:109], v[108:109], v[178:179] op_sel:[0,0] op_sel_hi:[1,0] neg_lo:[0,1] neg_hi:[0,1]\n\t"
"v_exp_f32 v168, v168\n\t"
"v_exp_f32 v169, v169\n\t"
"v_pk_add_f32 v[182:183], v[182:183], v[166:167]\n\t"
"v_pk_add_f32 v[170:171], v[178:179], v[110:111] op_sel:[1,0] op_sel_hi:[1,1] neg_lo:[0,1] neg_hi:[0,1]\n\t"
"v_exp_f32 v108, v108\n\t"
"v_exp_f32 v109, v109\n\t"
"v_pk_add_f32 v[180:181], v[180:181], v[106:107]\n\t"
"v_pk_add_f32 v[110:111], v[110:111], v[178:179] op_sel:[0,0] op_sel_hi:[1,0] neg_lo:[0,1] neg_hi:[0,1]\n\t"
"v_exp_f32 v170, v170\n\t"
"v_exp_f32 v171, v171\n\t"
"v_pk_add_f32 v[182:183], v[182:183], v[168:169]\n\t"
"v_pk_add_f32 v[172:173], v[178:179], v[112:113] op_sel:[1,0] op_sel_hi:[1,1] neg_lo:[0,1] neg_hi:[0,1]\n\t"
"v_exp_f32 v110, v110\n\t"
"v_exp_f32 v111, v111\n\t"
"v_pk_add_f32 v[180:181], v[180:181], v[108:109]\n\t"
"v_pk_add_f32 v[112:113], v[112:113], v[178:179] op_sel:[0,0] op_sel_hi:[1,0] neg_lo:[0,1] neg_hi:[0,1]\n\t"
"v_exp_f32 v172, v172\n\t"
"v_exp_f32 v173, v173\n\t"
"v_pk_add_f32 v[182:183], v[182:183], v[170:171]\n\t"
"v_pk_add_f32 v[174:175], v[178:179], v[114:115] op_sel:[1,0] op_sel_hi:[1,1] neg_lo:[0,1] neg_hi:[0,1]\n\t"
"v_exp_f32 v112, v112\n\t"
"v_exp_f32 v113, v113\n\t"
"v_pk_add_f32 v[180:181], v[180:181], v[110:111]\n\t"
"v_pk_add_f32 v[114:115], v[114:115], v[178:179] op_sel:[0,0] op_sel_hi:[1,0] neg_lo:[0,1] neg_hi:[0,1]\n\t"
"v_exp_f32 v174, v174\n\t"
"v_exp_f32 v175, v175\n\t"
"v_pk_add_f32 v[182:183], v[182:183], v[172:173]\n\t"
"v_pk_add_f32 v[176:177], v[178:179], v[116:117] op_sel:[1,0] op_sel_hi:[1,1] neg_lo:[0,1] neg_hi:[0,1]\n\t"
"v_exp_f32 v114, v114\n\t"
"v_exp_f32 v115, v115\n\t"
"v_pk_add_f32 v[180:181], v[180:181], v[112:113]\n\t"
"v_pk_add_f32 v[116:117], v[116:117], v[178:179] op_sel:[0,0] op_sel_hi:[1,0] neg_lo:[0,1] neg_hi:[0,1]\n\t"
"v_exp_f32 v176, v176\n\t"
"v_exp_f32 v177, v177\n\t"
"v_pk_add_f32 v[182:183], v[182:183], v[174:175]\n\t"
"v_exp_f32 v116, v116\n\t"
"v_exp_f32 v117, v117\n\t"
"v_pk_add_f32 v[180:181], v[180:181], v[114:115]\n\t"
"v_pk_add_f32 v[182:183], v[182:183], v[176:177]\n\t"
"v_pk_add_f32 v[180:181], v[180:181], v[116:117]\n\t"
"v_add_f32 v180, v180, v181\n\t"
"v_add_f32 v181, v182, v183\n\t"
"v_rcp_f32 v180, v180\n\t"
"v_rcp_f32 v181, v181\n\t"
"s_nop 0\n\t"
"v_pk_mul_f32 v[160:161], v[160:161], v[180:181] op_sel:[0,1] op_sel_hi:[1,1]\n\t"
"v_pk_mul_f32 v[162:163], v[162:163], v[180:181] op_sel:[0,1] op_sel_hi:[1,1]\n\t"
"v_pk_mul_f32 v[164:165], v[164:165], v[180:181] op_sel:[0,1] op_sel_hi:[1,1]\n\t"
"v_cvt_pk_bf16_f32 v140, v160, v161\n\t"
"v_pk_mul_f32 v[166:167], v[166:167], v[180:181] op_sel:[0,1] op_sel_hi:[1,1]\n\t"
"v_cvt_pk_bf16_f32 v141, v162, v163\n\t"
"v_pk_mul_f32 v[168:169], v[168:169], v[180:181] op_sel:[0,1] op_sel_hi:[1,1]\n\t"
"v_cvt_pk_bf16_f32 v142, v164, v165\n\t"
"v_pk_mul_f32 v[170:171], v[170:171], v[180:181] op_sel:[0,1] op_sel_hi:[1,1]\n\t"
"v_cvt_pk_bf16_f32 v143, v166, v167\n\t"
"v_pk_mul_f32 v[172:173], v[172:173], v[180:181] op_sel:[0,1] op_sel_hi:[1,1]\n\t"
"v_cvt_pk_bf16_f32 v144, v168, v169\n\t"
"v_pk_mul_f32 v[174:175], v[174:175], v[180:181] op_sel:[0,1] op_sel_hi:[1,1]\n\t"
"v_cvt_pk_bf16_f32 v145, v170, v171\n\t"
"v_pk_mul_f32 v[176:177], v[176:177], v[180:181] op_sel:[0,1] op_sel_hi:[1,1]\n\t"
"v_cvt_pk_bf16_f32 v146, v172, v173\n\t"
"v_pk_mul_f32 v[100:101], v[100:101], v[180:181] op_sel:[0,0] op_sel_hi:[1,0]\n\t"
"v_cvt_pk_bf16_f32 v147, v174, v175\n\t"
"v_pk_mul_f32 v[102:103], v[102:103], v[180:181] op_sel:[0,0] op_sel_hi:[1,0]\n\t"
"v_cvt_pk_bf16_f32 v148, v176, v177\n\t"
"v_pk_mul_f32 v[104:105], v[104:105], v[180:181] op_sel:[0,0] op_sel_hi:[1,0]\n\t"
"v_cvt_pk_bf16_f32 v149, v100, v101\n\t"
"v_pk_mul_f32 v[106:107], v[106:107], v[180:181] op_sel:[0,0] op_sel_hi:[1,0]\n\t"
"v_cvt_pk_bf16_f32 v150, v102, v103\n\t"
"v_pk_mul_f32 v[108:109], v[108:109], v[180:181] op_sel:[0,0] op_sel_hi:[1,0]\n\t"
"v_cvt_pk_bf16_f32 v151, v104, v105\n\t"
"v_pk_mul_f32 v[110:111], v[110:111], v[180:181] op_sel:[0,0] op_sel_hi:[1,0]\n\t"
"v_cvt_pk_bf16_f32 v152, v106, v107\n\t"
"v_pk_mul_f32 v[112:113], v[112:113], v[180:181] op_sel:[0,0] op_sel_hi:[1,0]\n\t"
"v_cvt_pk_bf16_f32 v153, v108, v109\n\t"
"v_pk_mul_f32 v[114:115], v[114:115], v[180:181] op_sel:[0,0] op_sel_hi:[1,0]\n\t"
"v_cvt_pk_bf16_f32 v154, v110, v111\n\t"
"v_pk_mul_f32 v[116:117], v[116:117], v[180:181] op_sel:[0,0] op_sel_hi:[1,0]\n\t"
"v_cvt_pk_bf16_f32 v155, v112, v113\n\t"
"v_cvt_pk_bf16_f32 v156, v114, v115\n\t"
"v_cvt_pk_bf16_f32 v157, v116, v117\n\t"
"v_mov_b32 v18, 0\n\t"
"v_mov_b32 v19, 0\n\t"
"v_mov_b32 v38, 0\n\t"
"v_mov_b32 v39, 0\n\t"
"v_mov_b32 v138, 0\n\t"
"v_mov_b32 v139, 0\n\t"
"v_mov_b32 v158, 0\n\t"
"v_mov_b32 v159, 0\n\t"
"s_waitcnt vmcnt(3)\n\t"
"v_max3_u32 v160, v184, v185, v186\n\t"
"s_waitcnt vmcnt(2)\n\t"
"v_max3_u32 v160, v160, v187, v188\n\t"
"v_max3_u32 v160, v160, v189, v190\n\t"
"s_waitcnt vmcnt(1)\n\t"
"v_max3_u32 v160, v160, v191, v192\n\t"
"v_max3_u32 v160, v160, v193, v194\n\t"
"v_max_u32 v160, v160, v195\n\t"
"v_cmp_eq_u32 vcc, -1, v160\n\t"
"s_cmp_eq_u64 vcc, 0\n\t"
"s_cbranch_scc1 109f\n\t"
"s_mov_b32 s98, 65536\n\t"
"108:\n\t"
"s_sleep 3\n\t"
"s_nop 7\n\t"
"buffer_load_dwordx4 v[184:187], v225, %[ws], s61 offen sc1\n\t"
"buffer_load_dwordx4 v[188:191], v226, %[ws], s61 offen sc1\n\t"
"buffer_load_dwordx4 v[192:195], v227, %[ws], s61 offen sc1\n\t"
"s_waitcnt vmcnt(0)\n\t"
"v_max3_u32 v160, v184, v185, v186\n\t"
"v_max3_u32 v160, v160, v187, v188\n\t"
"v_max3_u32 v160, v160, v189, v190\n\t"
"v_max3_u32 v160, v160, v191, v192\n\t"
"v_max3_u32 v160, v160, v193, v194\n\t"
"v_max_u32 v160, v160, v195\n\t"
"v_cmp_eq_u32 vcc, -1, v160\n\t"
"s_cmp_eq_u64 vcc, 0\n\t"
"s_cbranch_scc1 109f\n\t"
"s_sub_u32 s98, s98, 1\n\t"
"s_cmp_lg_u32 s98, 0\n\t"
"s_cbranch_scc1 108b\n\t"
"109:\n\t"
"s_nop 1\n\t"
"ds_write2_b64 v246, v[184:185], v[186:187] offset0:0 offset1:5\n\t"
"ds_write2_b64 v247, v[188:189], v[190:191] offset0:0 offset1:5\n\t"
"ds_write2_b64 v248, v[192:193], v[194:195] offset0:0 offset1:5\n\t"
"s_waitcnt lgkmcnt(0)\n\t"
"s_barrier\n\t"
"ds_read_b64_tr_b16 a[0:1], v250 offset:0\n\t"
"ds_read_b64_tr_b16 a[2:3], v250 offset:160\n\t"
"ds_read_b64_tr_b16 a[4:5], v250 offset:320\n\t"
"ds_read_b64_tr_b16 a[6:7], v250 offset:480\n\t"
"ds_read_b64_tr_b16 a[8:9], v250 offset:640\n\t"
"ds_read_b64_tr_b16 a[10:11], v250 offset:800\n\t"
"ds_read_b64_tr_b16 a[12:13], v250 offset:960\n\t"
"ds_read_b64_tr_b16 a[14:15], v250 offset:1120\n\t"
"ds_read_b64_tr_b16 a[16:17], v250 offset:1280\n\t"
"ds_read_b64_tr_b16 a[18:19], v250 offset:1440\n\t"
"ds_read_b64_tr_b16 a[20:21], v250 offset:8\n\t"
"ds_read_b64_tr_b16 a[22:23], v250 offset:168\n\t"
"s_waitcnt lgkmcnt(8)\n\t"
"v_mfma_f32_16x16x32_bf16 v[56:59], a[0:3], v[0:3], 0\n\t"
"ds_read_b64_tr_b16 a[24:25], v250 offset:328\n\t"
"v_mfma_f32_16x16x32_bf16 v[76:79], a[0:3], v[20:23], 0\n\t"
"ds_read_b64_tr_b16 a[26:27], v250 offset:488\n\t"
"v_mfma_f32_16x16x32_bf16 v[56:59], a[4:7], v[4:7], v[56:59]\n\t"
"ds_read_b64_tr_b16 a[28:29], v250 offset:648\n\t"
"v_mfma_f32_16x16x32_bf16 v[76:79], a[4:7], v[24:27], v[76:79]\n\t"
"s_waitcnt lgkmcnt(7)\n\t"
"ds_read_b64_tr_b16 a[30:31], v250 offset:808\n\t"
"v_mfma_f32_16x16x32_bf16 v[56:59], a[8:11], v[8:11], v[56:59]\n\t"
"ds_read_b64_tr_b16 a[32:33], v250 offset:968\n\t"
"v_mfma_f32_16x16x32_bf16 v[76:79], a[8:11], v[28:31], v[76:79]\n\t"
"ds_read_b64_tr_b16 a[34:35], v250 offset:1128\n\t"
"v_mfma_f32_16x16x32_bf16 v[56:59], a[12:15], v[12:15], v[56:59]\n\t"
"ds_read_b64_tr_b16 a[36:37], v250 offset:1288\n\t"
"v_mfma_f32_16x16x32_bf16 v[76:79], a[12:15], v[32:35], v[76:79]\n\t"
"s_waitcnt lgkmcnt(7)\n\t"
"ds_read_b64_tr_b16 a[38:39], v250 offset:1448\n\t"
"v_mfma_f32_16x16x32_bf16 v[56:59], a[16:19], v[16:19], v[56:59]\n\t"
"ds_read_b64_tr_b16 a[40:41], v250 offset:16\n\t"
"v_mfma_f32_16x16x32_bf16 v[76:79], a[16:19], v[36:39], v[76:79]\n\t"
"ds_read_b64_tr_b16 a[42:43], v250 offset:176\n\t"
"v_mfma_f32_16x16x32_bf16 v[52:55], a[20:23], v[0:3], 0\n\t"
"ds_read_b64_tr_b16 a[44:45], v250 offset:336\n\t"
"v_mfma_f32_16x16x32_bf16 v[72:75], a[20:23], v[20:23], 0\n\t"
"s_waitcnt lgkmcnt(7)\n\t"
"ds_read_b64_tr_b16 a[46:47], v250 offset:496\n\t"
"v_mfma_f32_16x16x32_bf16 v[52:55], a[24:27], v[4:7], v[52:55]\n\t"
"ds_read_b64_tr_b16 a[48:49], v250 offset:656\n\t"
"v_mfma_f32_16x16x32_bf16 v[72:75], a[24:27], v[24:27], v[72:75]\n\t"
"ds_read_b64_tr_b16 a[50:51], v250 offset:816\n\t"
"v_mfma_f32_16x16x32_bf16 v[52:55], a[28:31], v[8:11], v[52:55]\n\t"
"ds_read_b64_tr_b16 a[52:53], v250 offset:976\n\t"
"v_mfma_f32_16x16x32_bf16 v[72:75], a[28:31], v[28:31], v[72:75]\n\t"
"s_waitcnt lgkmcnt(7)\n\t"
"ds_read_b64_tr_b16 a[54:55], v250 offset:1136\n\t"
"v_mfma_f32_16x16x32_bf16 v[52:55], a[32:35], v[12:15], v[52:55]\n\t"
"ds_read_b64_tr_b16 a[56:57], v250 offset:1296\n\t"
"v_mfma_f32_16x16x32_bf16 v[72:75], a[32:35], v[32:35], v[72:75]\n\t"
"ds_read_b64_tr_b16 a[58:59], v250 offset:1456\n\t"
"v_mfma_f32_16x16x32_bf16 v[52:55], a[36:39], v[16:19], v[52:55]\n\t"
"ds_read_b64_tr_b16 a[60:61], v250 offset:24\n\t"
"v_mfma_f32_16x16x32_bf16 v[72:75], a[36:39], v[36:39], v[72:75]\n\t"
"s_waitcnt lgkmcnt(7)\n\t"
"ds_read_b64_tr_b16 a[62:63], v250 offset:184\n\t"
"v_mfma_f32_16x16x32_bf16 v[48:51], a[40:43], v[0:3], 0\n\t"
"ds_read_b64_tr_b16 a[0:1], v250 offset:344\n\t"
"v_mfma_f32_16x16x32_bf16 v[68:71], a[40:43], v[20:23], 0\n\t"
"ds_read_b64_tr_b16 a[2:3], v250 offset:504\n\t"
"v_mfma_f32_16x16x32_bf16 v[48:51], a[44:47], v[4:7], v[48:51]\n\t"
"ds_read_b64_tr_b16 a[4:5], v250 offset:664\n\t"
"v_mfma_f32_16x16x32_bf16 v[68:71], a[44:47], v[24:27], v[68:71]\n\t"
"s_waitcnt lgkmcnt(7)\n\t"
"ds_read_b64_tr_b16 a[6:7], v250 offset:824\n\t"
"v_mfma_f32_16x16x32_bf16 v[48:51], a[48:51], v[8:11], v[48:51]\n\t"
"ds_read_b64_tr_b16 a[8:9], v250 offset:984\n\t"
"v_mfma_f32_16x16x32_bf16 v[68:71], a[48:51], v[28:31], v[68:71]\n\t"
"ds_read_b64_tr_b16 a[10:11], v250 offset:1144\n\t"
"v_mfma_f32_16x16x32_bf16 v[48:51], a[52:55], v[12:15], v[48:51]\n\t"
"ds_read_b64_tr_b16 a[12:13], v250 offset:1304\n\t"
"v_mfma_f32_16x16x32_bf16 v[68:71], a[52:55], v[32:35], v[68:71]\n\t"
"s_waitcnt lgkmcnt(7)\n\t"
"ds_read_b64_tr_b16 a[14:15], v250 offset:1464\n\t"
"v_mfma_f32_16x16x32_bf16 v[48:51], a[56:59], v[16:19], v[48:51]\n\t"
"ds_read_b64_tr_b16 a[16:17], v250 offset:32\n\t"
"v_mfma_f32_16x16x32_bf16 v[68:71], a[56:59], v[36:39], v[68:71]\n\t"
"ds_read_b64_tr_b16 a[18:19], v250 offset:192\n\t"
"v_mfma_f32_16x16x32_bf16 v[44:47], a[60:63], v[0:3], 0\n\t"
"ds_read_b64_tr_b16 a[20:21], v250 offset:352\n\t"
"v_mfma_f32_16x16x32_bf16 v[64:67], a[60:63], v[20:23], 0\n\t"
"s_waitcnt lgkmcnt(7)\n\t"
"ds_read_b64_tr_b16 a[22:23], v250 offset:512\n\t"
"v_mfma_f32_16x16x32_bf16 v[44:47], a[0:3], v[4:7], v[44:47]\n\t"
"ds_read_b64_tr_b16 a[24:25], v250 offset:672\n\t"
"v_mfma_f32_16x16x32_bf16 v[64:67], a[0:3], v[24:27], v[64:67]\n\t"
"ds_read_b64_tr_b16 a[26:27], v250 offset:832\n\t"
"v_mfma_f32_16x16x32_bf16 v[44:47], a[4:7], v[8:11], v[44:47]\n\t"
"ds_read_b64_tr_b16 a[28:29], v250 offset:992\n\t"
"v_mfma_f32_16x16x32_bf16 v[64:67], a[4:7], v[28:31], v[64:67]\n\t"
"s_waitcnt lgkmcnt(7)\n\t"
"ds_read_b64_tr_b16 a[30:31], v250 offset:1152\n\t"
"v_mfma_f32_16x16x32_bf16 v[44:47], a[8:11], v[12:15], v[44:47]\n\t"
"ds_read_b64_tr_b16 a[32:33], v250 offset:1312\n\t"
"v_mfma_f32_16x16x32_bf16 v[64:67], a[8:11], v[32:35], v[64:67]\n\t"
"ds_read_b64_tr_b16 a[34:35], v250 offset:1472\n\t"
"v_mfma_f32_16x16x32_bf16 v[44:47], a[12:15], v[16:19], v[44:47]\n\t"
"ds_read_b64_tr_b16 a[36:37], v251 offset:0\n\t"
"v_mfma_f32_16x16x32_bf16 v[64:67], a[12:15], v[36:39], v[64:67]\n\t"
"s_waitcnt lgkmcnt(7)\n\t"
"ds_read_b64_tr_b16 a[38:39], v251 offset:160\n\t"
"v_mfma_f32_16x16x32_bf16 v[40:43], a[16:19], v[0:3], 0\n\t"
"ds_read_b64_tr_b16 a[40:41], v251 offset:320\n\t"
"v_mfma_f32_16x16x32_bf16 v[60:63], a[16:19], v[20:23], 0\n\t"
"ds_read_b64_tr_b16 a[42:43], v251 offset:480\n\t"
"v_mfma_f32_16x16x32_bf16 v[40:43], a[20:23], v[4:7], v[40:43]\n\t"
"ds_read_b64_tr_b16 a[44:45], v251 offset:640\n\t"
"v_mfma_f32_16x16x32_bf16 v[60:63], a[20:23], v[24:27], v[60:63]\n\t"
"s_waitcnt lgkmcnt(7)\n\t"
"ds_read_b64_tr_b16 a[46:47], v251 offset:800\n\t"
"v_mfma_f32_16x16x32_bf16 v[40:43], a[24:27], v[8:11], v[40:43]\n\t"
"ds_read_b64_tr_b16 a[48:49], v251 offset:960\n\t"
"v_mfma_f32_16x16x32_bf16 v[60:63], a[24:27], v[28:31], v[60:63]\n\t"
"ds_read_b64_tr_b16 a[50:51], v251 offset:1120\n\t"
"v_mfma_f32_16x16x32_bf16 v[40:43], a[28:31], v[12:15], v[40:43]\n\t"
"ds_read_b64_tr_b16 a[52:53], v251 offset:1280\n\t"
"v_mfma_f32_16x16x32_bf16 v[60:63], a[28:31], v[32:35], v[60:63]\n\t"
"s_waitcnt lgkmcnt(7)\n\t"
"ds_read_b64_tr_b16 a[54:55], v251 offset:1440\n\t"
"v_mfma_f32_16x16x32_bf16 v[40:43], a[32:35], v[16:19], v[40:43]\n\t"
"ds_read_b64_tr_b16 a[56:57], v251 offset:8\n\t"
"v_mfma_f32_16x16x32_bf16 v[60:63], a[32:35], v[36:39], v[60:63]\n\t"
"ds_read_b64_tr_b16 a[58:59], v251 offset:168\n\t"
"v_mfma_f32_16x16x32_bf16 v[96:99], a[36:39], v[120:123], 0\n\t"
"ds_read_b64_tr_b16 a[60:61], v251 offset:328\n\t"
"v_mfma_f32_16x16x32_bf16 v[116:119], a[36:39], v[140:143], 0\n\t"
"s_waitcnt lgkmcnt(7)\n\t"
"ds_read_b64_tr_b16 a[62:63], v251 offset:488\n\t"
"v_mfma_f32_16x16x32_bf16 v[96:99], a[40:43], v[124:127], v[96:99]\n\t"
"ds_read_b64_tr_b16 a[0:1], v251 offset:648\n\t"
"v_mfma_f32_16x16x32_bf16 v[116:119], a[40:43], v[144:147], v[116:119]\n\t"
"ds_read_b64_tr_b16 a[2:3], v251 offset:808\n\t"
"v_cndmask_b32 v213, v42, v40, s[54:55]\n\t"
"v_mfma_f32_16x16x32_bf16 v[96:99], a[44:47], v[128:131], v[96:99]\n\t"
"s_waitcnt lgkmcnt(6)\n\t"
"ds_read_b64_tr_b16 a[4:5], v251 offset:968\n\t"
"v_cndmask_b32 v214, v62, v60, s[54:55]\n\t"
"v_mfma_f32_16x16x32_bf16 v[116:119], a[44:47], v[148:151], v[116:119]\n\t"
"ds_read_b64_tr_b16 a[6:7], v251 offset:1128\n\t"
"v_add_f32 v213, 0x358637bd, v213\n\t"
"v_mfma_f32_16x16x32_bf16 v[96:99], a[48:51], v[132:135], v[96:99]\n\t"
"ds_read_b64_tr_b16 a[8:9], v251 offset:1288\n\t"
"v_add_f32 v214, 0x358637bd, v214\n\t"
"v_mfma_f32_16x16x32_bf16 v[116:119], a[48:51], v[152:155], v[116:119]\n\t"
"v_rcp_f32 v213, v213\n\t"
"v_mfma_f32_16x16x32_bf16 v[96:99], a[52:55], v[136:139], v[96:99]\n\t"
"s_waitcnt lgkmcnt(5)\n\t"
"v_rcp_f32 v214, v214\n\t"
"v_mfma_f32_16x16x32_bf16 v[116:119], a[52:55], v[156:159], v[116:119]\n\t"
"v_mul_f32 v56, v213, v56\n\t"
"v_mul_f32 v57, v213, v57\n\t"
"v_mfma_f32_16x16x32_bf16 v[92:95], a[56:59], v[120:123], 0\n\t"
"ds_read_b64_tr_b16 a[10:11], v251 offset:1448\n\t"
"ds_read_b64_tr_b16 a[12:13], v251 offset:16\n\t"
"ds_read_b64_tr_b16 a[14:15], v251 offset:176\n\t"
"v_mul_f32 v58, v213, v58\n\t"
"v_mfma_f32_16x16x32_bf16 v[112:115], a[56:59], v[140:143], 0\n\t"
"v_mul_f32 v59, v213, v59\n\t"
"v_mul_f32 v76, v214, v76\n\t"
"v_mfma_f32_16x16x32_bf16 v[92:95], a[60:63], v[124:127], v[92:95]\n\t"
"s_waitcnt lgkmcnt(4)\n\t"
"v_mul_f32 v77, v214, v77\n\t"
"v_mul_f32 v78, v214, v78\n\t"
"v_mfma_f32_16x16x32_bf16 v[112:115], a[60:63], v[144:147], v[112:115]\n\t"
"v_mul_f32 v79, v214, v79\n\t"
"v_cvt_pk_bf16_f32 v160, v56, v57\n\t"
"v_mfma_f32_16x16x32_bf16 v[92:95], a[0:3], v[128:131], v[92:95]\n\t"
"ds_read_b64_tr_b16 a[16:17], v251 offset:336\n\t"
"ds_read_b64_tr_b16 a[18:19], v251 offset:496\n\t"
"ds_read_b64_tr_b16 a[20:21], v251 offset:656\n\t"
"ds_read_b64_tr_b16 a[22:23], v251 offset:816\n\t"
"v_cvt_pk_bf16_f32 v161, v58, v59\n\t"
"v_mfma_f32_16x16x32_bf16 v[112:115], a[0:3], v[148:151], v[112:115]\n\t"
"v_cvt_pk_bf16_f32 v180, v76, v77\n\t"
"v_cvt_pk_bf16_f32 v181, v78, v79\n\t"
"v_mfma_f32_16x16x32_bf16 v[92:95], a[4:7], v[132:135], v[92:95]\n\t"
"s_waitcnt lgkmcnt(4)\n\t"
"v_mul_f32 v52, v213, v52\n\t"
"v_mul_f32 v53, v213, v53\n\t"
"v_mfma_f32_16x16x32_bf16 v[112:115], a[4:7], v[152:155], v[112:115]\n\t"
"ds_read_b64_tr_b16 a[24:25], v251 offset:976\n\t"
"v_mul_f32 v54, v213, v54\n\t"
"v_mfma_f32_16x16x32_bf16 v[92:95], a[8:11], v[136:139], v[92:95]\n\t"
"ds_read_b64_tr_b16 a[26:27], v251 offset:1136\n\t"
"ds_read_b64_tr_b16 a[28:29], v251 offset:1296\n\t"
"ds_read_b64_tr_b16 a[30:31], v251 offset:1456\n\t"
"v_mul_f32 v55, v213, v55\n\t"
"v_mfma_f32_16x16x32_bf16 v[112:115], a[8:11], v[156:159], v[112:115]\n\t"
"v_mul_f32 v72, v214, v72\n\t"
"v_mul_f32 v73, v214, v73\n\t"
"v_mfma_f32_16x16x32_bf16 v[88:91], a[12:15], v[120:123], 0\n\t"
"s_waitcnt lgkmcnt(4)\n\t"
"v_mul_f32 v74, v214, v74\n\t"
"v_mul_f32 v75, v214, v75\n\t"
"v_mfma_f32_16x16x32_bf16 v[108:111], a[12:15], v[140:143], 0\n\t"
"v_cvt_pk_bf16_f32 v162, v52, v53\n\t"
"v_cvt_pk_bf16_f32 v163, v54, v55\n\t"
"v_mfma_f32_16x16x32_bf16 v[88:91], a[16:19], v[124:127], v[88:91]\n\t"
"ds_read_b64_tr_b16 a[32:33], v251 offset:24\n\t"
"ds_read_b64_tr_b16 a[34:35], v251 offset:184\n\t"
"ds_read_b64_tr_b16 a[36:37], v251 offset:344\n\t"
"ds_read_b64_tr_b16 a[38:39], v251 offset:504\n\t"
"v_cvt_pk_bf16_f32 v182, v72, v73\n\t"
"v_mfma_f32_16x16x32_bf16 v[108:111], a[16:19], v[144:147], v[108:111]\n\t"
"v_cvt_pk_bf16_f32 v183, v74, v75\n\t"
"v_mul_f32 v48, v213, v48\n\t"
"v_mfma_f32_16x16x32_bf16 v[88:91], a[20:23], v[128:131], v[88:91]\n\t"
"s_waitcnt lgkmcnt(4)\n\t"
"v_mul_f32 v49, v213, v49\n\t"
"v_mul_f32 v50, v213, v50\n\t"
"v_mfma_f32_16x16x32_bf16 v[108:111], a[20:23], v[148:151], v[108:111]\n\t"
"ds_read_b64_tr_b16 a[40:41], v251 offset:664\n\t"
"v_mul_f32 v51, v213, v51\n\t"
"v_mfma_f32_16x16x32_bf16 v[88:91], a[24:27], v[132:135], v[88:91]\n\t"
"ds_read_b64_tr_b16 a[42:43], v251 offset:824\n\t"
"ds_read_b64_tr_b16 a[44:45], v251 offset:984\n\t"
"ds_read_b64_tr_b16 a[46:47], v251 offset:1144\n\t"
"v_mul_f32 v68, v214, v68\n\t"
"v_mfma_f32_16x16x32_bf16 v[108:111], a[24:27], v[152:155], v[108:111]\n\t"
"v_mul_f32 v69, v214, v69\n\t"
"v_mul_f32 v70, v214, v70\n\t"
"v_mfma_f32_16x16x32_bf16 v[88:91], a[28:31], v[136:139], v[88:91]\n\t"
"s_waitcnt lgkmcnt(4)\n\t"
"v_mul_f32 v71, v214, v71\n\t"
"v_cvt_pk_bf16_f32 v164, v48, v49\n\t"
"v_mfma_f32_16x16x32_bf16 v[108:111], a[28:31], v[156:159], v[108:111]\n\t"
"v_cvt_pk_bf16_f32 v165, v50, v51\n\t"
"v_cvt_pk_bf16_f32 v184, v68, v69\n\t"
"v_mfma_f32_16x16x32_bf16 v[84:87], a[32:35], v[120:123], 0\n\t"
"ds_read_b64_tr_b16 a[48:49], v251 offset:1304\n\t"
"ds_read_b64_tr_b16 a[50:51], v251 offset:1464\n\t"
"ds_read_b64_tr_b16 a[52:53], v251 offset:32\n\t"
"ds_read_b64_tr_b16 a[54:55], v251 offset:192\n\t"
"v_cvt_pk_bf16_f32 v185, v70, v71\n\t"
"v_mfma_f32_16x16x32_bf16 v[104:107], a[32:35], v[140:143], 0\n\t"
"v_mul_f32 v44, v213, v44\n\t"
"v_mul_f32 v45, v213, v45\n\t"
"v_mfma_f32_16x16x32_bf16 v[84:87], a[36:39], v[124:127], v[84:87]\n\t"
"s_waitcnt lgkmcnt(4)\n\t"
"v_mul_f32 v46, v213, v46\n\t"
"v_mul_f32 v47, v213, v47\n\t"
"v_mfma_f32_16x16x32_bf16 v[104:107], a[36:39], v[144:147], v[104:107]\n\t"
"v_mul_f32 v64, v214, v64\n\t"
"v_mul_f32 v65, v214, v65\n\t"
"v_mfma_f32_16x16x32_bf16 v[84:87], a[40:43], v[128:131], v[84:87]\n\t"
"ds_read_b64_tr_b16 a[56:57], v251 offset:352\n\t"
"ds_read_b64_tr_b16 a[58:59], v251 offset:512\n\t"
"ds_read_b64_tr_b16 a[60:61], v251 offset:672\n\t"
"ds_read_b64_tr_b16 a[62:63], v251 offset:832\n\t"
"v_mul_f32 v66, v214, v66\n\t"
"v_mfma_f32_16x16x32_bf16 v[104:107], a[40:43], v[148:151], v[104:107]\n\t"
"v_mul_f32 v67, v214, v67\n\t"
"v_cvt_pk_bf16_f32 v166, v44, v45\n\t"
"v_mfma_f32_16x16x32_bf16 v[84:87], a[44:47], v[132:135], v[84:87]\n\t"
"v_cvt_pk_bf16_f32 v167, v46, v47\n\t"
"v_cvt_pk_bf16_f32 v186, v64, v65\n\t"
"v_mfma_f32_16x16x32_bf16 v[104:107], a[44:47], v[152:155], v[104:107]\n\t"
"s_waitcnt lgkmcnt(4)\n\t"
"ds_read_b64_tr_b16 a[0:1], v251 offset:992\n\t"
"v_cvt_pk_bf16_f32 v187, v66, v67\n\t"
"v_mfma_f32_16x16x32_bf16 v[84:87], a[48:51], v[136:139], v[84:87]\n\t"
"ds_read_b64_tr_b16 a[2:3], v251 offset:1152\n\t"
"ds_read_b64_tr_b16 a[4:5], v251 offset:1312\n\t"
"ds_read_b64_tr_b16 a[6:7], v251 offset:1472\n\t"
"v_mul_f32 v40, v213, v40\n\t"
"v_mfma_f32_16x16x32_bf16 v[104:107], a[48:51], v[156:159], v[104:107]\n\t"
"v_mul_f32 v41, v213, v41\n\t"
"v_mul_f32 v42, v213, v42\n\t"
"v_mfma_f32_16x16x32_bf16 v[80:83], a[52:55], v[120:123], 0\n\t"
"v_mul_f32 v43, v213, v43\n\t"
"v_mul_f32 v60, v214, v60\n\t"
"v_mfma_f32_16x16x32_bf16 v[100:103], a[52:55], v[140:143], 0\n\t"
"s_waitcnt lgkmcnt(4)\n\t"
"v_mul_f32 v61, v214, v61\n\t"
"v_mfma_f32_16x16x32_bf16 v[80:83], a[56:59], v[124:127], v[80:83]\n\t"
"v_mul_f32 v62, v214, v62\n\t"
"v_mul_f32 v63, v214, v63\n\t"
"v_mfma_f32_16x16x32_bf16 v[100:103], a[56:59], v[144:147], v[100:103]\n\t"
"v_cvt_pk_bf16_f32 v176, v40, v41\n\t"
"v_cvt_pk_bf16_f32 v177, v42, v43\n\t"
"v_mfma_f32_16x16x32_bf16 v[80:83], a[60:63], v[128:131], v[80:83]\n\t"
"v_cvt_pk_bf16_f32 v196, v60, v61\n\t"
"v_mfma_f32_16x16x32_bf16 v[100:103], a[60:63], v[148:151], v[100:103]\n\t"
"s_waitcnt lgkmcnt(0)\n\t"
"s_add_u32 s64, s59, 0\n\t"
"v_cvt_pk_bf16_f32 v197, v62, v63\n\t"
"v_mfma_f32_16x16x32_bf16 v[80:83], a[0:3], v[132:135], v[80:83]\n\t"
"buffer_store_dwordx4 v[160:163], v230, %[ws], s64 offen sc1\n\t"
"s_add_u32 s64, s59, 1024\n\t"
"v_mfma_f32_16x16x32_bf16 v[100:103], a[0:3], v[152:155], v[100:103]\n\t"
"buffer_store_dwordx4 v[164:167], v230, %[ws], s64 offen sc1\n\t"
"s_add_u32 s64, s59, 20480\n\t"
"v_mfma_f32_16x16x32_bf16 v[80:83], a[4:7], v[136:139], v[80:83]\n\t"
"buffer_store_dwordx4 v[180:183], v230, %[ws], s64 offen sc1\n\t"
"s_add_u32 s64, s59, 21504\n\t"
"v_mfma_f32_16x16x32_bf16 v[100:103], a[4:7], v[156:159], v[100:103]\n\t"
"buffer_store_dwordx4 v[184:187], v230, %[ws], s64 offen sc1\n\t"
"s_nop 3\n\t"
"v_cndmask_b32 v213, v82, v80, s[54:55]\n\t"
"s_nop 1\n\t"
"v_cndmask_b32 v214, v102, v100, s[54:55]\n\t"
"v_add_f32 v213, 0x358637bd, v213\n\t"
"v_add_f32 v214, 0x358637bd, v214\n\t"
"v_rcp_f32 v213, v213\n\t"
"v_rcp_f32 v214, v214\n\t"
"v_mul_f32 v96, v213, v96\n\t"
"v_mul_f32 v97, v213, v97\n\t"
"v_mul_f32 v98, v213, v98\n\t"
"v_mul_f32 v99, v213, v99\n\t"
"v_mul_f32 v116, v214, v116\n\t"
"v_mul_f32 v117, v214, v117\n\t"
"v_mul_f32 v118, v214, v118\n\t"
"v_mul_f32 v119, v214, v119\n\t"
"v_cvt_pk_bf16_f32 v168, v96, v97\n\t"
"v_cvt_pk_bf16_f32 v169, v98, v99\n\t"
"v_cvt_pk_bf16_f32 v188, v116, v117\n\t"
"v_cvt_pk_bf16_f32 v189, v118, v119\n\t"
"v_mul_f32 v92, v213, v92\n\t"
"v_mul_f32 v93, v213, v93\n\t"
"v_mul_f32 v94, v213, v94\n\t"
"v_mul_f32 v95, v213, v95\n\t"
"v_mul_f32 v112, v214, v112\n\t"
"v_mul_f32 v113, v214, v113\n\t"
"v_mul_f32 v114, v214, v114\n\t"
"v_mul_f32 v115, v214, v115\n\t"
"v_cvt_pk_bf16_f32 v170, v92, v93\n\t"
"v_cvt_pk_bf16_f32 v171, v94, v95\n\t"
"v_cvt_pk_bf16_f32 v190, v112, v113\n\t"
"v_cvt_pk_bf16_f32 v191, v114, v115\n\t"
"v_mul_f32 v88, v213, v88\n\t"
"v_mul_f32 v89, v213, v89\n\t"
"v_mul_f32 v90, v213, v90\n\t"
"v_mul_f32 v91, v213, v91\n\t"
"v_mul_f32 v108, v214, v108\n\t"
"v_mul_f32 v109, v214, v109\n\t"
"v_mul_f32 v110, v214, v110\n\t"
"v_mul_f32 v111, v214, v111\n\t"
"v_cvt_pk_bf16_f32 v172, v88, v89\n\t"
"v_cvt_pk_bf16_f32 v173, v90, v91\n\t"
"v_cvt_pk_bf16_f32 v192, v108, v109\n\t"
"v_cvt_pk_bf16_f32 v193, v110, v111\n\t"
"v_mul_f32 v84, v213, v84\n\t"
"v_mul_f32 v85, v213, v85\n\t"
"v_mul_f32 v86, v213, v86\n\t"
"v_mul_f32 v87, v213, v87\n\t"
"v_mul_f32 v104, v214, v104\n\t"
"v_mul_f32 v105, v214, v105\n\t"
"v_mul_f32 v106, v214, v106\n\t"
"v_mul_f32 v107, v214, v107\n\t"
"v_cvt_pk_bf16_f32 v174, v84, v85\n\t"
"v_cvt_pk_bf16_f32 v175, v86, v87\n\t"
"v_cvt_pk_bf16_f32 v194, v104, v105\n\t"
"v_cvt_pk_bf16_f32 v195, v106, v107\n\t"
"v_mul_f32 v80, v213, v80\n\t"
"v_mul_f32 v81, v213, v81\n\t"
"v_mul_f32 v82, v213, v82\n\t"
"v_mul_f32 v83, v213, v83\n\t"
"v_mul_f32 v100, v214, v100\n\t"
"v_mul_f32 v101, v214, v101\n\t"
"v_mul_f32 v102, v214, v102\n\t"
"v_mul_f32 v103, v214, v103\n\t"
"v_cvt_pk_bf16_f32 v178, v80, v81\n\t"
"v_cvt_pk_bf16_f32 v179, v82, v83\n\t"
"v_cvt_pk_bf16_f32 v198, v100, v101\n\t"
"v_cvt_pk_bf16_f32 v199, v102, v103\n\t"
"s_add_u32 s64, s59, 2048\n\t"
"buffer_store_dwordx4 v[168:171], v230, %[ws], s64 offen sc1\n\t"
"s_add_u32 s64, s59, 3072\n\t"
"buffer_store_dwordx4 v[172:175], v230, %[ws], s64 offen sc1\n\t"
"s_add_u32 s64, s59, 4096\n\t"
"buffer_store_dwordx4 v[176:179], v230, %[ws], s64 offen sc1\n\t"
"s_add_u32 s64, s59, 22528\n\t"
"buffer_store_dwordx4 v[188:191], v230, %[ws], s64 offen sc1\n\t"
"s_add_u32 s64, s59, 23552\n\t"
"buffer_store_dwordx4 v[192:195], v230, %[ws], s64 offen sc1\n\t"
"s_add_u32 s64, s59, 24576\n\t"
"buffer_store_dwordx4 v[196:199], v230, %[ws], s64 offen sc1\n\t"
"s_waitcnt vmcnt(0)\n\t"
"buffer_store_dwordx2 v[254:255], v231, %[ws], s60 offen offset:128 sc1\n\t"
"s_or_b32 s64, %[mi], %[wv]\n\t"
"s_cmp_lg_u32 s64, 0\n\t"
"s_cbranch_scc1 98f\n\t"
"s_lshl_b32 s64, %[a], 7\n\t"
"s_add_u32 s64, s64, 49831936\n\t"
"buffer_store_dwordx2 v[232:233], v231, %[ws], s64 offen offset:0 sc1\n\t"
"98:\n\t"
"s_waitcnt vmcnt(0)\n\t"
"97:\n\t"
"s_nop 0\n\t"
".Lfused_kernel_b:\n\ts_nop 0\n"
    :: [x]"s"(rx), [wq]"s"(rq), [wp]"s"(rp), [wo]"s"(ro), [w2]"s"(r2), [nc0]"s"(nc0), [ws]"s"(rw), [wv]"s"(wv), [lds]"s"(ldsb), [mi]"s"(mi), [a]"s"(a)
    : CLOBBERS);
  }
}

extern "C" void kernel_launch(void* const* d_in, const int* in_sizes, int n_in, void* d_out, int out_size, void* d_ws, size_t ws_size, hipStream_t stream) {
  (void)in_sizes; (void)n_in; (void)out_size; (void)ws_size;
  (void)hipFuncSetAttribute((const void*)fused_kernel, hipFuncAttributeMaxDynamicSharedMemorySize, 160 * 1024);
  fused_kernel<<<256, 256, 160 * 1024, stream>>>((const char*)d_in[0], (const char*)d_in[1], (const char*)d_in[2], (const char*)d_in[3],
      (const char*)d_in[5], (const char*)d_in[6], (char*)d_out, (char*)d_ws);
}
```
